```python
import torch

def _softmax(x, axis=-1):
    return torch.softmax(x, dim=axis)
def _gelu(x):
    return torch.nn.functional.gelu(x, approximate="tanh")
def _silu(x):
    return torch.nn.functional.silu(x)
def _sigmoid(x):
    return torch.sigmoid(x)
def _layernorm(x, axis=-1, eps=1e-5):
    m = x.mean(dim=axis, keepdim=True)
    v = x.var(dim=axis, keepdim=True, unbiased=False)
    return (x - m) / torch.sqrt(v + eps)
def _scatter_add(tgt, idx, src, axis=0):
    return tgt.clone().index_add_(axis, idx, src)
def _segment_sum(src, seg, n):
    out = torch.zeros((n,) + src.shape[1:], dtype=src.dtype)
    return out.index_add_(0, seg, src)
def _pad(x, pw):
    flat = tuple(v for pair in reversed(pw) for v in pair)
    return torch.nn.functional.pad(x, flat)

kernel_name = 'parallel_sum__ssm_conv_h3__moe_arch_mod__prompt'

def setup_inputs():

    _w = (lambda a: (lambda t: t.to(torch.bfloat16) if t.is_floating_point() else t)(torch.as_tensor(a)))
    def _A_setup():
        B, S, D = 1, 2048, 576
        ks = 4
        return {
            "q": _w((lambda *s: torch.randn(*s, dtype=torch.bfloat16) * 0.05)(B, S, D)),
            "k": _w((lambda *s: torch.randn(*s, dtype=torch.bfloat16) * 0.8)(B, S, D)),
            "v": _w((lambda *s: torch.randn(*s, dtype=torch.bfloat16) * 0.8)(B, S, D)),
            "shift_k": _w((lambda *s: torch.randn(*s, dtype=torch.bfloat16) * 0.8)(D, ks) * 0.5),
            "ssm_k": _w((lambda *s: torch.randn(*s, dtype=torch.bfloat16) * 0.8)(D, S) * (1.0 / S ** 0.5)),
            "ssm_D": _w((lambda *s: torch.randn(*s, dtype=torch.bfloat16) * 0.8)(D)),
        }
    _A_in = _A_setup()

    def _B_setup():
        B, S, D, F, C = 1, 2048, 576, 4608, 8
        return {
            "x": _w((lambda *s: torch.randn(*s, dtype=torch.bfloat16) * 0.05)(B, S, D)),
            "w_r": _w((lambda *s: torch.randn(*s, dtype=torch.bfloat16) * 0.05)(D)),
            "w_gate": _w((lambda *s: torch.randn(*s, dtype=torch.bfloat16) * 0.05)(D, F)),
            "w_up": _w((lambda *s: torch.randn(*s, dtype=torch.bfloat16) * 0.05)(D, F)),
            "w_down": _w((lambda *s: torch.randn(*s, dtype=torch.bfloat16) * 0.05)(F, D)),
        }
    _B_in = _B_setup()

    return {
        "A_q": _A_in["q"],
        "A_k": _A_in["k"],
        "A_v": _A_in["v"],
        "A_shift_k": _A_in["shift_k"],
        "A_ssm_k": _A_in["ssm_k"],
        "A_ssm_D": _A_in["ssm_D"],
        "B_x": _B_in["x"],
        "B_w_r": _B_in["w_r"],
        "B_w_gate": _B_in["w_gate"],
        "B_w_up": _B_in["w_up"],
        "B_w_down": _B_in["w_down"],
    }

def reference(A_q, A_k, A_v, A_shift_k, A_ssm_k, A_ssm_D, B_x, B_w_r, B_w_gate, B_w_up, B_w_down):
    def _A_ref(q, k, v, shift_k, ssm_k, ssm_D):
        B, S, D = q.shape
        ks = shift_k.shape[-1]
        k_pad = _pad(k, ((0, 0), (ks - 1, 0), (0, 0)))
        k_shift = torch.zeros_like(k)
        for t in range(ks):
            k_shift = k_shift + k_pad[:, t:t + S, :] * shift_k[:, ks - 1 - t][None, None, :]
        kv = k_shift * v
        L = 2 * S
        kv_pad = _pad(kv, ((0, 0), (0, S), (0, 0)))
        kf_pad = _pad(ssm_k, ((0, 0), (0, S)))
        kv_f = (lambda x, axes=(-2,-1): torch.fft.rfft2(x.float(), dim=axes))(kv_pad[:, None, :, :], axes=(1, 2))
        kf_f = (lambda x, axes=(-2,-1): torch.fft.rfft2(x.float(), dim=axes))(kf_pad[:, None, :], axes=(1, 2))
        kf_f = (lambda x, *d: x.permute(*d))(kf_f, 1, 2, 0)
        y_f = kv_f * kf_f[None, :, :, :]
        y = (lambda x, s, axes=(-2,-1): torch.fft.irfft2(x, s=s, dim=axes).to(torch.bfloat16))(y_f, (1, L), axes=(1, 2))[:, 0, :S, :]
        y = y + kv * ssm_D[None, None, :]
        return q * y

    def _B_ref(x, w_r, w_gate, w_up, w_down):
        B, S, D = x.shape
        C = 8
        r = x @ w_r
        idx = torch.argsort(-r, axis=-1)[:, :C]
        idx_e = (lambda x, n, axis: torch.repeat_interleave(x, n, dim=axis))(idx.reshape(B, C, 1), D, axis=2)
        x_sel = (lambda x, i, axis: torch.take_along_dim(x, i, dim=axis))(x, idx_e, axis=1)
        g = _silu(x_sel @ w_gate)
        u = x_sel @ w_up
        y = (g * u) @ w_down
        r_sel = _sigmoid((lambda x, i, axis: torch.take_along_dim(x, i, dim=axis))(r, idx, axis=1))
        y = y * r_sel.reshape(B, C, 1)
        b_off = (torch.arange(B) * S).reshape(B, 1)
        flat_idx = (idx + b_off).reshape(B * C)
        zero = (x * 0.0).reshape(B * S, D)
        upd = _scatter_add(zero, flat_idx, y.reshape(B * C, D), axis=0)
        return x + upd.reshape(B, S, D)

    x = A_q
    xn = _layernorm(x, axis=-1)
    return x + _A_ref(xn, A_k, A_v, A_shift_k, A_ssm_k, A_ssm_D) + _B_ref(xn, B_w_r, B_w_gate, B_w_up, B_w_down)
```

```cpp
#include <hip/hip_runtime.h>
#include <hip/hip_bf16.h>

typedef unsigned int u32;
typedef unsigned long long u64;
typedef unsigned short u16;
typedef u32 v4u __attribute__((ext_vector_type(4)));
typedef u32 v2u __attribute__((ext_vector_type(2)));
typedef float f4 __attribute__((ext_vector_type(4)));
typedef __bf16 bf8 __attribute__((ext_vector_type(8)));
typedef __bf16 bf2 __attribute__((ext_vector_type(2)));
#define LAS __attribute__((address_space(3)))
#define GAS __attribute__((address_space(1)))

namespace {

constexpr int S = 2048, D = 576, F = 4608;
constexpr int NWG = 256, NTHR = 512, NCONV = 144, NMOE = 72;
constexpr int AUX_SC1 = 16;
constexpr u32 CLR = 0xFFFFFFFFu, SAME = 0xFFFFFFFEu, MAXV = 0xFFFFFFFDu;
constexpr u32 SPIN_CAP = 1u << 16;

constexpr int WS_E = 0;
constexpr int WS_R = 4096;
constexpr int WS_U = WS_R + S * 8;
constexpr int TILE_B = D * 16;
constexpr int WS_P = WS_U + NWG * TILE_B;
constexpr int PART_B = NCONV * 128;
constexpr int WS_END = WS_P + NMOE * PART_B;
constexpr int WS_DBG = (WS_END + 4095) / 4096 * 4096;

constexpr int HC_B = 2080 * 16;
constexpr int L_H = 0;
constexpr int UL_B = 290 * 16;
constexpr int L_U = L_H + 4 * HC_B;
constexpr int L_MISC = L_U + 4 * UL_B;
constexpr int L_CNT = L_MISC;
constexpr int L_CAND = L_CNT + 64;
constexpr int L_SEL = L_CAND + 336;
constexpr int L_TAU = L_SEL + 32;
constexpr int L_STAT = L_TAU + 16;
constexpr int L_SSTAT = L_STAT + 64;
constexpr int L_END = L_SSTAT + 64;
constexpr int XS_STRIDE = 1184;
constexpr int L_XS = 0;
constexpr int L_RED = L_XS + 8 * XS_STRIDE;
constexpr int L_HB = L_RED + 6 * 8 * 128 * 4;
constexpr int HB_STRIDE = 160;
static_assert(L_END <= 160 * 1024, "lds");
static_assert(L_HB + 8 * HB_STRIDE <= L_U, "lds");

enum { C_CLR = 0, C_STAT, C_SWEEP, C_TAU, C_CAND, C_NCAND, C_SEL, C_H, C_U, C_Y, C_XS, C_RED, C_HB, C_SSTAT, C_FB };

__device__ __forceinline__ float lo16(u32 x) { return __uint_as_float(x << 16); }
__device__ __forceinline__ float hi16(u32 x) { return __uint_as_float(x & 0xFFFF0000u); }
__device__ __forceinline__ u32 pk2(float a, float b) {
  bf2 p = {(__bf16)a, (__bf16)b};
  return __builtin_bit_cast(u32, p);
}
__device__ __forceinline__ float rbf(float a) { return lo16(pk2(a, a)); }

template <int CTRL> __device__ __forceinline__ u32 dppu(u32 x) {
  return (u32)__builtin_amdgcn_mov_dpp((int)x, CTRL, 0xf, 0xf, true);
}
template <int CTRL> __device__ __forceinline__ float dppf(float x) { return __uint_as_float(dppu<CTRL>(__float_as_uint(x))); }
__device__ __forceinline__ float xrow_sum(float x) {
  auto s = __builtin_amdgcn_permlane16_swap(__float_as_uint(x), __float_as_uint(x), false, false);
  x = __uint_as_float(s[0]) + __uint_as_float(s[1]);
  auto t = __builtin_amdgcn_permlane32_swap(__float_as_uint(x), __float_as_uint(x), false, false);
  return __uint_as_float(t[0]) + __uint_as_float(t[1]);
}
__device__ __forceinline__ float wave_sum(float x) {
  x += dppf<0xB1>(x);
  x += dppf<0x4E>(x);
  x += dppf<0x141>(x);
  x += dppf<0x140>(x);
  return xrow_sum(x);
}
__device__ __forceinline__ u32 umax(u32 a, u32 b) { return a > b ? a : b; }
__device__ __forceinline__ u32 umin(u32 a, u32 b) { return a < b ? a : b; }
__device__ __forceinline__ u32 row_umax(u32 x) {
  x = umax(x, dppu<0xB1>(x));
  x = umax(x, dppu<0x4E>(x));
  x = umax(x, dppu<0x141>(x));
  x = umax(x, dppu<0x140>(x));
  return x;
}
__device__ __forceinline__ u32 xrow_umin(u32 x) {
  auto s = __builtin_amdgcn_permlane16_swap(x, x, false, false);
  x = umin(s[0], s[1]);
  auto t = __builtin_amdgcn_permlane32_swap(x, x, false, false);
  return umin(t[0], t[1]);
}
__device__ __forceinline__ u32 xrow_umax(u32 x) {
  auto s = __builtin_amdgcn_permlane16_swap(x, x, false, false);
  x = umax(s[0], s[1]);
  auto t = __builtin_amdgcn_permlane32_swap(x, x, false, false);
  return umax(t[0], t[1]);
}

__device__ __forceinline__ void lds_signal(LAS char* sm, int c) {
  u64 keep;
  asm volatile("s_waitcnt lgkmcnt(0)\n\ts_mov_b64 %0, exec\n\ts_mov_b64 exec, 1\n\tds_add_u32 %1, %2\n\ts_mov_b64 exec, %0"
               : "=&s"(keep) : "v"((u32)(size_t)(sm + L_CNT + 4 * c)), "v"(1u) : "memory");
}
__device__ __forceinline__ void lds_wait(LAS char* sm, int c, u32 target) {
  asm volatile("" ::: "memory");
  while ((u32)__builtin_amdgcn_readfirstlane(
             __hip_atomic_load((LAS u32*)(sm + L_CNT) + c, __ATOMIC_RELAXED, __HIP_MEMORY_SCOPE_WORKGROUP)) < target)
    __builtin_amdgcn_s_sleep(1);
  asm volatile("" ::: "memory");
}
__device__ __forceinline__ void wg_barrier() { asm volatile("s_waitcnt lgkmcnt(0)\n\ts_barrier" ::: "memory"); }
__device__ __forceinline__ u32 umax3(u32 a, u32 b, u32 c) { return umax(umax(a, b), c); }
__device__ __forceinline__ u32 vmax4(v4u g, u32 acc) { return umax3(umax3(g.x, g.y, acc), g.z, g.w); }

struct Row { v2u a0, a1; u32 a2; };
__device__ __forceinline__ Row load_row(const u16* p, int lane) {
  Row r;
  r.a0 = *(const GAS v2u*)((const GAS char*)p + lane * 8);
  r.a1 = *(const GAS v2u*)((const GAS char*)p + 512 + lane * 8);
  r.a2 = *(const GAS u16*)((const GAS char*)p + 1024 + lane * 2);
  return r;
}
__device__ __forceinline__ void unpack_row(Row r, float (&x)[9]) {
  asm volatile("" : "+v"(r.a2));
  x[0] = lo16(r.a0.x); x[1] = hi16(r.a0.x); x[2] = lo16(r.a0.y); x[3] = hi16(r.a0.y);
  x[4] = lo16(r.a1.x); x[5] = hi16(r.a1.x); x[6] = lo16(r.a1.y); x[7] = hi16(r.a1.y);
  x[8] = lo16(r.a2);
}
__device__ __forceinline__ void ln_stats(const float (&x)[9], float& m, float& rs) {
  float s = ((x[0] + x[1]) + (x[2] + x[3])) + ((x[4] + x[5]) + (x[6] + x[7])) + x[8];
  float mean = wave_sum(s) * (1.0f / D);
  float q = 0.f;
#pragma unroll
  for (int i = 0; i < 9; i++) { float d = x[i] - mean; q = fmaf(d, d, q); }
  float var = wave_sum(q) * (1.0f / D);
  m = rbf(mean);
  float sd = rbf(__builtin_amdgcn_sqrtf(rbf(rbf(var) + 1e-5f)));
  rs = __builtin_amdgcn_rcpf(sd);
}
__device__ __forceinline__ float ln_norm(float x, float m, float rs) { return rbf(rbf(x - m) * rs); }

__device__ __forceinline__ f4 mfma16(v4u a, v4u b, f4 c) {
  return __builtin_amdgcn_mfma_f32_16x16x32_bf16(__builtin_bit_cast(bf8, a), __builtin_bit_cast(bf8, b), c, 0, 0, 0);
}
__device__ __forceinline__ u64 load_epoch(char* ws) {
  return __hip_atomic_load((GAS u64*)(ws + WS_E), __ATOMIC_RELAXED, __HIP_MEMORY_SCOPE_AGENT);
}

#define STAMP(i) do {} while (0)
#define STAMPC(i) do {} while (0)
#define FLUSH() do {} while (0)
constexpr int L_TOTAL = L_END;

struct Ctx {
  const u16 *q, *k, *v, *shk, *hk, *dsk, *wr, *wg, *wu, *wd;
  u16* out;
  char* ws;
  __amdgpu_buffer_rsrc_t rws;
  LAS char* sm;
  int tid, lane, wave, C, cg, mw;
  bool is_conv;
};
constexpr int OOB = 0x7FFFFF00;

struct HCopy {
  v4u lo[7], hi[7];
  __device__ __forceinline__ void load(const Ctx& P, int i0, int nr) {
#pragma unroll
    for (int r = 0; r < 7; r++) {
      if (r < 5 || nr > 5) {
        const int i = min(i0 + r * 64 + P.lane, 1031), ch = i / 258, m = i - ch * 258 - 2;
        const GAS char* hp = (const GAS char*)P.hk + (size_t)(4 * P.cg + ch) * (S * 2);
        lo[r] = *(const GAS v4u*)(hp + max(m, 0) * 16);
        hi[r] = *(const GAS v4u*)(hp + min(max(m + 1, 0), 255) * 16);
      }
    }
  }
  __device__ __forceinline__ void store(const Ctx& P, int i0, int nr) {
#pragma unroll
    for (int r = 0; r < 7; r++) {
      if (r < 5 || nr > 5) {
        const int i = min(i0 + r * 64 + P.lane, 1031), ch = i / 258, m = i - ch * 258 - 2;
        u32 d[8], o[7];
#pragma unroll
        for (int e = 0; e < 4; e++) { d[e] = m >= 0 ? lo[r][e] : 0u; d[4 + e] = m >= -1 ? hi[r][e] : 0u; }
#pragma unroll
        for (int e = 0; e < 7; e++) o[e] = __builtin_amdgcn_alignbit(d[e + 1], d[e], 16);
        LAS char* dst = P.sm + L_H + ch * HC_B + (m + 2) * 16;
        constexpr int BASE[8] = {0, 260, 519, 778, 1037, 1296, 1556, 1815};
#pragma unroll
        for (int e = 0; e < 4; e++) {
          v4u a = {d[e], d[e + 1], d[e + 2], d[e + 3]}, c = {o[e], o[e + 1], o[e + 2], o[e + 3]};
          *(LAS v4u*)(dst + BASE[2 * e] * 16) = a;
          *(LAS v4u*)(dst + BASE[2 * e + 1] * 16) = c;
        }
      }
    }
  }
};

__device__ __forceinline__ void ffn_act(const Ctx& P, int tk) {
  LAS char* sm = P.sm;
  lds_wait(sm, C_RED, 6);
  float G = 0.f, U = 0.f;
#pragma unroll
  for (int w = 0; w < 6; w++) {
    G += *(const LAS float*)(sm + L_RED + ((w * 8 + tk) * 128 + P.lane) * 4);
    U += *(const LAS float*)(sm + L_RED + ((w * 8 + tk) * 128 + 64 + P.lane) * 4);
  }
  G = rbf(G);
  const float sg = rbf(G * __builtin_amdgcn_rcpf(1.0f + __expf(-G)));
  *(LAS u16*)(sm + L_HB + tk * HB_STRIDE + P.lane * 2) = (u16)pk2(sg * rbf(U), 0.f);
  lds_signal(sm, C_HB);
}

struct Ffn {
  static constexpr int ND = 2;
  v4u W1[3][8];
  v2u W2[ND][2][8];
  __device__ __forceinline__ void load1(const Ctx& P) {
    const int n16 = P.lane & 15, g4 = P.lane >> 4;
    const GAS char* wp = (const GAS char*)(n16 < 8 ? P.wg : P.wu) + ((size_t)(96 * P.wave + 8 * g4) * F + 64 * P.mw + 8 * (n16 & 7)) * 2;
#pragma unroll
    for (int i = 0; i < 3; i++)
#pragma unroll
      for (int jj = 0; jj < 8; jj++) W1[i][jj] = *(const GAS v4u*)(wp + (size_t)(32 * i + jj) * (F * 2));
  }
  __device__ __forceinline__ void load2(const Ctx& P, int cu0) {
    const int n16 = P.lane & 15, g4 = P.lane >> 4;
    const GAS char* wp = (const GAS char*)P.wd + ((size_t)(64 * P.mw + 8 * g4) * D + 64 * cu0 + 4 * n16) * 2;
#pragma unroll
    for (int ci = 0; ci < ND; ci++)
      if (ci == 0 || P.wave >= 3) {
#pragma unroll
        for (int ks = 0; ks < 2; ks++)
#pragma unroll
          for (int jj = 0; jj < 8; jj++) W2[ci][ks][jj] = *(const GAS v2u*)(wp + (size_t)(32 * ks + jj) * (D * 2) + ci * 128);
      }
  }
  __device__ __forceinline__ void run(const Ctx& P, int cu0) {
    LAS char* sm = P.sm;
    const int lane = P.lane, wave = P.wave, n16 = lane & 15, g4 = lane >> 4;
    v4u Bf[3][8];
#pragma unroll
    for (int i = 0; i < 3; i++)
#pragma unroll
      for (int e = 0; e < 8; e++)
#pragma unroll
        for (int d = 0; d < 4; d++)
          Bf[i][e][d] = __builtin_amdgcn_perm(W1[i][2 * d + 1][e >> 1], W1[i][2 * d][e >> 1], (e & 1) ? 0x07060302u : 0x05040100u);
#pragma unroll
    for (int i = 0; i < 3; i++)
#pragma unroll
      for (int e = 0; e < 8; e++) asm volatile("" : "+v"(Bf[i][e]));
    STAMP(3);
    lds_wait(sm, C_XS, 2);
    STAMP(4);
    f4 acc[8];
#pragma unroll
    for (int e = 0; e < 8; e++) acc[e] = (f4){0.f, 0.f, 0.f, 0.f};
#pragma unroll
    for (int i = 0; i < 3; i++) {
      const v4u A = *(const LAS v4u*)(sm + L_XS + (n16 & 7) * XS_STRIDE + (96 * wave + 32 * i + 8 * g4) * 2);
#pragma unroll
      for (int e = 0; e < 8; e++) acc[e] = mfma16(A, Bf[i][e], acc[e]);
    }
    if (g4 < 2) {
#pragma unroll
      for (int r = 0; r < 4; r++) {
        LAS char* dst = sm + L_RED + ((wave * 8 + 4 * g4 + r) * 128 + 8 * n16) * 4;
        f4 a = {acc[0][r], acc[1][r], acc[2][r], acc[3][r]}, c = {acc[4][r], acc[5][r], acc[6][r], acc[7][r]};
        *(LAS f4*)dst = a;
        *(LAS f4*)(dst + 16) = c;
      }
    }
    lds_signal(sm, C_RED);
    ffn_act(P, wave);
    v4u Bd[ND][2][4];
#pragma unroll
    for (int ci = 0; ci < ND; ci++)
      if (ci == 0 || wave >= 3) {
#pragma unroll
        for (int ks = 0; ks < 2; ks++)
#pragma unroll
          for (int e = 0; e < 4; e++) {
#pragma unroll
            for (int d = 0; d < 4; d++)
              Bd[ci][ks][e][d] = __builtin_amdgcn_perm(W2[ci][ks][2 * d + 1][e >> 1], W2[ci][ks][2 * d][e >> 1], (e & 1) ? 0x07060302u : 0x05040100u);
            asm volatile("" : "+v"(Bd[ci][ks][e]));
          }
      }
    lds_wait(sm, C_HB, 8);
    STAMP(5);
    const v4u A0 = *(const LAS v4u*)(sm + L_HB + (n16 & 7) * HB_STRIDE + g4 * 16);
    const v4u A1 = *(const LAS v4u*)(sm + L_HB + (n16 & 7) * HB_STRIDE + 64 + g4 * 16);
#pragma unroll
    for (int ci = 0; ci < ND; ci++) {
      if (ci == 1 && wave < 3) break;
      f4 ad[4];
#pragma unroll
      for (int e = 0; e < 4; e++) ad[e] = mfma16(A0, Bd[ci][0][e], (f4){0.f, 0.f, 0.f, 0.f});
#pragma unroll
      for (int e = 0; e < 4; e++) ad[e] = mfma16(A1, Bd[ci][1][e], ad[e]);
      v4u s0, s1;
#pragma unroll
      for (int e = 0; e < 4; e++) {
        s0[e] = umin(__float_as_uint(g4 >= 2 ? ad[e][2] : ad[e][0]), SAME);
        s1[e] = umin(__float_as_uint(g4 >= 2 ? ad[e][3] : ad[e][1]), SAME);
      }
      const int off = WS_P + P.mw * PART_B + (16 * (cu0 + ci) + n16) * 128 + g4 * 16;
      __builtin_amdgcn_raw_buffer_store_b128(s0, P.rws, off, 0, AUX_SC1);
      __builtin_amdgcn_raw_buffer_store_b128(s1, P.rws, off + 64, 0, AUX_SC1);
    }
    STAMP(6);
  }
};

__device__ __forceinline__ void conv_phase(const Ctx& P) {
  LAS char* sm = P.sm;
  const int tid = P.tid, lane = P.lane, wave = P.wave, n16 = lane & 15, g4 = lane >> 4, gch = tid & 3;
  const int goff = WS_U + (tid >> 2) * TILE_B + (4 * P.cg + gch) * 16;
  lds_wait(sm, C_SWEEP, 2);
  STAMP(3);
  {
    v4u g[4];
    _Pragma("nounroll") for (u32 spin = 0; spin < SPIN_CAP; spin++) {
      asm volatile("" ::: "memory");
#pragma unroll
      for (int it = 0; it < 4; it++) g[it] = __builtin_amdgcn_raw_buffer_load_b128(P.rws, goff + it * 64 * TILE_B, 0, AUX_SC1);
      u32 mx = 0;
#pragma unroll
      for (int it = 0; it < 4; it++) mx = vmax4(g[it], mx);
      if (__all(mx != CLR)) break;
      __builtin_amdgcn_s_sleep(1);
    }
#pragma unroll
    for (int it = 0; it < 4; it++) *(LAS v4u*)(sm + L_U + gch * UL_B + (32 + it * 64 + (tid >> 2)) * 16) = g[it];
  }
  lds_signal(sm, C_U);
  STAMP(4);
  lds_wait(sm, C_H, 3);
  lds_wait(sm, C_U, 4);
  STAMP(5);
  {
    const int p1 = n16 + 1, kap = p1 & 7;
    const int base16 = 258 * kap + ((0x98654320u >> (4 * kap)) & 15);
    const LAS char* pa = sm + L_H + wave * HC_B + (base16 + g4 + (p1 >> 3)) * 16;
    const LAS char* pb = sm + L_U + wave * UL_B + (32 + 2 * n16 + 1 - g4 - 28) * 16;
    f4 acc[8];
#pragma unroll
    for (int t = 0; t < 8; t++) acc[t] = (f4){0.f, 0.f, 0.f, 0.f};
#pragma unroll
    for (int bb = 0; bb < 8; bb++) {
      v4u A[8], B[8];
#pragma unroll
      for (int a = 0; a < 8; a++) {
        A[a] = *(const LAS v4u*)(pa + a * 512 + bb * 64);
        B[a] = *(const LAS v4u*)(pb + a * 512 + (7 - bb) * 64);
      }
#pragma unroll
      for (int t = 0; t < 8; t++)
#pragma unroll
        for (int a = 0; a <= t; a++) acc[t] = mfma16(A[a], B[t - a], acc[t]);
    }
    LAS char* yb = sm + L_H + wave * (HC_B + 64) + n16 * 32 + g4 * 8;
#pragma unroll
    for (int t = 0; t < 8; t++) {
      v2u y2 = {pk2(acc[t][0], acc[t][1]), pk2(acc[t][2], acc[t][3])};
      *(LAS v2u*)(yb + t * 512) = y2;
    }
  }
  lds_signal(sm, C_Y);
  STAMP(6);
  lds_wait(sm, C_Y, 4);
#pragma unroll
  for (int it = 0; it < 4; it++) {
    const int cc = it * 64 + (tid >> 2);
    const v4u y4 = *(const LAS v4u*)(sm + L_H + gch * (HC_B + 64) + cc * 16);
    const v4u u4 = *(const LAS v4u*)(sm + L_U + gch * UL_B + (32 + cc) * 16);
    v4u w4;
#pragma unroll
    for (int e = 0; e < 4; e++) { const u32 y = umin(y4[e], MAXV); w4[e] = (y == u4[e]) ? SAME : y; }
    __builtin_amdgcn_raw_buffer_store_b128(w4, P.rws, goff + it * 64 * TILE_B, 0, AUX_SC1);
  }
  STAMP(7);
}

template <bool MOE> __device__ __forceinline__ void tile_wave(const Ctx& P) {
  LAS char* sm = P.sm;
  const int tid = P.tid, lane = P.lane, wave = P.wave, C = P.C;
  const bool LN = wave < 4, TILE = !MOE || wave < 5;
  const bool act = tid < 288;
  const int j = min(tid, 287);
  const int cu0 = wave < 3 ? wave : 2 * wave - 3;
  Row qr0, qr1, wrr;
  u64 epoch;
  u32 kk[11], vv[8], qq[8], dd;
  v4u sk;
  if (TILE) {
    if (LN) {
      qr0 = load_row(P.q + (size_t)(8 * C + 2 * wave) * D, lane);
      qr1 = load_row(P.q + (size_t)(8 * C + 2 * wave + 1) * D, lane);
      wrr = load_row(P.wr, lane);
      epoch = load_epoch(P.ws);
    }
    const __amdgpu_buffer_rsrc_t rk = __builtin_amdgcn_make_buffer_rsrc((void*)P.k, 0, S * D * 2, 0x00020000);
#pragma unroll
    for (int i = 0; i < 11; i++) kk[i] = __builtin_amdgcn_raw_buffer_load_b32(rk, (8 * C - 3 + i) * (D * 2) + j * 4, 0, 0);
#pragma unroll
    for (int i = 0; i < 8; i++) vv[i] = *(const GAS u32*)((const GAS char*)P.v + (size_t)(8 * C + i) * (D * 2) + j * 4);
    sk = *(const GAS v4u*)((const GAS char*)P.shk + j * 16);
#pragma unroll
    for (int i = 0; i < 8; i++) qq[i] = *(const GAS u32*)((const GAS char*)P.q + (size_t)(8 * C + i) * (D * 2) + j * 4);
    dd = *(const GAS u32*)((const GAS char*)P.dsk + j * 4);
  } else {
    ((LAS u32*)(sm + L_MISC))[lane] = 0u;
    ((LAS u32*)(sm + L_MISC))[64 + lane] = 0u;
  }
  Ffn ffn;
  if (MOE) ffn.load1(P);
  wg_barrier();

  if (LN) {
    float x0[9], x1[9], w9[9], m0, rs0, m1, rs1;
    unpack_row(qr0, x0); unpack_row(qr1, x1); unpack_row(wrr, w9);
    ln_stats(x0, m0, rs0); ln_stats(x1, m1, rs1);
    float r0 = 0.f, r1 = 0.f;
#pragma unroll
    for (int i = 0; i < 9; i++) { r0 = fmaf(ln_norm(x0[i], m0, rs0), w9[i], r0); r1 = fmaf(ln_norm(x1[i], m1, rs1), w9[i], r1); }
    r0 = wave_sum(r0); r1 = wave_sum(r1);
    lds_wait(sm, C_CLR, 2);
    {
      asm volatile("" : "+v"(epoch));
      epoch += 1;
      const u32 rr = pk2(r0, r1);
      const u32 tlo = (u32)epoch << 16, thi = (u32)(epoch >> 16);
      v4u gr = {tlo | (rr & 0xFFFFu), thi, tlo | (rr >> 16), thi};
      __builtin_amdgcn_raw_buffer_store_b128(gr, P.rws, lane == 0 ? WS_R + (8 * C + 2 * wave) * 8 : OOB, 0, AUX_SC1);
    }
    if (lane == 0) { f4 st = {m0, rs0, m1, rs1}; *(LAS f4*)(sm + L_STAT + wave * 16) = st; }
    lds_signal(sm, C_STAT);
    STAMP(1);
  }
  u32 up[2][4];
  const int uoff = WS_U + C * TILE_B + j * 32;
  if (TILE) {
    float w0[4] = {lo16(sk.x), hi16(sk.x), lo16(sk.y), hi16(sk.y)};
    float w1[4] = {lo16(sk.z), hi16(sk.z), lo16(sk.w), hi16(sk.w)};
    float u0[8], u1[8];
#pragma unroll
    for (int s = 0; s < 8; s++) {
      float a = lo16(kk[s]) * w0[3], c = hi16(kk[s]) * w1[3];
      a = fmaf(lo16(kk[s + 1]), w0[2], a); c = fmaf(hi16(kk[s + 1]), w1[2], c);
      a = fmaf(lo16(kk[s + 2]), w0[1], a); c = fmaf(hi16(kk[s + 2]), w1[1], c);
      a = fmaf(lo16(kk[s + 3]), w0[0], a); c = fmaf(hi16(kk[s + 3]), w1[0], c);
      u0[s] = a * lo16(vv[s]); u1[s] = c * hi16(vv[s]);
    }
#pragma unroll
    for (int i = 0; i < 4; i++) {
      up[0][i] = umin(pk2(u0[7 - 2 * i], u0[6 - 2 * i]), MAXV);
      up[1][i] = umin(pk2(u1[7 - 2 * i], u1[6 - 2 * i]), MAXV);
    }
    if (!LN) lds_wait(sm, C_CLR, 2);
    v4u p0 = {up[0][0], up[0][1], up[0][2], up[0][3]}, p1 = {up[1][0], up[1][1], up[1][2], up[1][3]};
    __builtin_amdgcn_raw_buffer_store_b128(p0, P.rws, act ? uoff : OOB, 0, AUX_SC1);
    __builtin_amdgcn_raw_buffer_store_b128(p1, P.rws, act ? uoff + 16 : OOB, 0, AUX_SC1);
  }
  STAMP(2);
  float xn[2][8], c0[2][8];
  if (TILE) {
    lds_wait(sm, C_STAT, 4);
    const float d0 = lo16(dd), d1 = hi16(dd);
#pragma unroll
    for (int s2 = 0; s2 < 4; s2++) {
      const f4 st = *(const LAS f4*)(sm + L_STAT + s2 * 16);
#pragma unroll
      for (int e = 0; e < 2; e++) {
        const int s = 2 * s2 + e;
        const float m = st[2 * e], rs = st[2 * e + 1];
        const float xa = lo16(qq[s]), xb = hi16(qq[s]);
        const float na = ln_norm(xa, m, rs), nb = ln_norm(xb, m, rs);
        const u32 pa = up[0][(7 - s) >> 1], pb = up[1][(7 - s) >> 1];
        const float ua = ((7 - s) & 1) ? hi16(pa) : lo16(pa), ub = ((7 - s) & 1) ? hi16(pb) : lo16(pb);
        xn[0][s] = na; xn[1][s] = nb;
        c0[0][s] = fmaf(na, fmaf(ua, d0, 1.0f), xa);
        c0[1][s] = fmaf(nb, fmaf(ub, d1, 1.0f), xb);
        asm volatile("" : "+v"(xn[0][s]), "+v"(xn[1][s]), "+v"(c0[0][s]), "+v"(c0[1][s]));
      }
    }
  }
  if (MOE) {
    ffn.load2(P, cu0);
    ffn.run(P, cu0);
    if (!TILE) return;
  } else if (LN && P.is_conv) conv_phase(P);

  asm volatile("s_waitcnt vmcnt(0)" ::: "memory");
  lds_wait(sm, C_SEL, 2);
  u32 skip = 0;
  {
    const v4u s0 = *(const LAS v4u*)(sm + L_SEL), s1 = *(const LAS v4u*)(sm + L_SEL + 16);
#pragma unroll
    for (int e = 0; e < 4; e++) {
      const u32 t0 = 2047u - (s0[e] & 0xFFFFu), t1 = 2047u - (s1[e] & 0xFFFFu);
      skip |= ((int)(t0 >> 3) == C ? 1u : 0u) << (t0 & 7);
      skip |= ((int)(t1 >> 3) == C ? 1u : 0u) << (t1 & 7);
    }
    if (!act) skip = 0xFF;
  }
  const __amdgpu_buffer_rsrc_t ro = __builtin_amdgcn_make_buffer_rsrc((void*)P.out, 0, S * D * 2, 0x00020000);
  int ooff[8];
#pragma unroll
  for (int s = 0; s < 8; s++) ooff[s] = ((skip >> s) & 1) ? OOB : (8 * C + s) * (D * 2) + j * 4;
  v4u y0, y1;
  _Pragma("nounroll") for (u32 spin = 0; spin < SPIN_CAP; spin++) {
    asm volatile("" ::: "memory");
    y0 = __builtin_amdgcn_raw_buffer_load_b128(P.rws, uoff, 0, AUX_SC1);
    y1 = __builtin_amdgcn_raw_buffer_load_b128(P.rws, uoff + 16, 0, AUX_SC1);
    bool ok = vmax4(y0, vmax4(y1, 0u)) != CLR;
#pragma unroll
    for (int e = 0; e < 4; e++) ok = ok & (y0[e] != up[0][e]) & (y1[e] != up[1][e]);
    if (__all(ok)) break;
    __builtin_amdgcn_s_sleep(1);
  }
  STAMP(8);
#pragma unroll
  for (int e = 0; e < 4; e++) {
    const u32 a = y0[e] == SAME ? up[0][e] : y0[e], c = y1[e] == SAME ? up[1][e] : y1[e];
    const u32 o0 = pk2(fmaf(xn[0][2 * e], lo16(a), c0[0][2 * e]), fmaf(xn[1][2 * e], lo16(c), c0[1][2 * e]));
    const u32 o1 = pk2(fmaf(xn[0][2 * e + 1], hi16(a), c0[0][2 * e + 1]), fmaf(xn[1][2 * e + 1], hi16(c), c0[1][2 * e + 1]));
    __builtin_amdgcn_raw_buffer_store_b32(o0, ro, ooff[2 * e], 0, 0);
    __builtin_amdgcn_raw_buffer_store_b32(o1, ro, ooff[2 * e + 1], 0, 0);
  }
  STAMP(9);
}

__device__ __forceinline__ void control_wave(const Ctx& P, const int ROLE) {
  LAS char* sm = P.sm;
  const int lane = P.lane, wave = P.wave, cw = wave - 6, ct = P.tid - 384, C = P.C, cg = P.cg;
  if (wave == 5) {
    ((LAS u32*)(sm + L_MISC))[lane] = 0u;
    ((LAS u32*)(sm + L_MISC))[64 + lane] = 0u;
    const v4u z = {0u, 0u, 0u, 0u};
    *(LAS v4u*)(sm + L_U + (lane >> 4) * UL_B + (lane & 15) * 16) = z;
    *(LAS v4u*)(sm + L_U + (lane >> 4) * UL_B + 256 + (lane & 15) * 16) = z;
  } else {
    const v4u c4 = {CLR, CLR, CLR, CLR};
#pragma unroll
    for (int i = 0; i < 5; i++)
      __builtin_amdgcn_raw_buffer_store_b128(c4, P.rws, (i < 4 || ct < 64) ? WS_U + C * TILE_B + (i * 128 + ct) * 16 : OOB, 0, AUX_SC1);
    if (ROLE == 1) {
#pragma unroll
      for (int i = 0; i < 9; i++) __builtin_amdgcn_raw_buffer_store_b128(c4, P.rws, WS_P + P.mw * PART_B + (i * 128 + ct) * 16, 0, AUX_SC1);
    }
  }
  u64 epoch = load_epoch(P.ws);
  HCopy hc;
  const int hi0 = wave == 5 ? 640 : cw * 320, hnr = wave == 5 ? 7 : 5;
  if (ROLE == 0) hc.load(P, hi0, hnr);
  wg_barrier();
  asm volatile("s_waitcnt vmcnt(0)" : "+v"(epoch) :: "memory");
  if (wave != 5) lds_signal(sm, C_CLR);
  epoch += 1;
  STAMP(1);
  if (ROLE == 0) { hc.store(P, hi0, hnr); lds_signal(sm, C_H); }
  STAMP(2);
  if (wave == 5) return;
  u32 key[16];
  {
    const u32 tlo = (u32)epoch << 16, thi = (u32)(epoch >> 16);
    v4u g[8];
    _Pragma("nounroll") for (u32 spin = 0; spin < SPIN_CAP; spin++) {
      asm volatile("" ::: "memory");
#pragma unroll
      for (int i = 0; i < 8; i++) g[i] = __builtin_amdgcn_raw_buffer_load_b128(P.rws, WS_R + (cw * 512 + i * 64 + lane) * 16, 0, AUX_SC1);
      u32 bh = 0, bl = 0;
#pragma unroll
      for (int i = 0; i < 8; i++) { bh |= (g[i].y ^ thi) | (g[i].w ^ thi); bl |= (g[i].x ^ tlo) | (g[i].z ^ tlo); }
      if (__all((bh | (bl >> 16)) == 0)) break;
      __builtin_amdgcn_s_sleep(1);
    }
    lds_signal(sm, C_SWEEP);
    STAMP(3);
#pragma unroll
    for (int i = 0; i < 8; i++) {
      const u32 tok = 2 * (cw * 512 + i * 64 + lane);
      const u32 a = g[i].x << 16, c = g[i].z << 16;
      key[2 * i] = ((a ^ (((int)a >> 31) | 0x80000000u)) & 0xFFFF0000u) | (2047u - tok);
      key[2 * i + 1] = ((c ^ (((int)c >> 31) | 0x80000000u)) & 0xFFFF0000u) | (2046u - tok);
    }
  }
  {
    LAS u32* cand = (LAS u32*)(sm + L_CAND);
    u32 mx = key[0];
#pragma unroll
    for (int i = 1; i < 16; i++) mx = umax(mx, key[i]);
    u32 tau = xrow_umin(row_umax(mx));
    if (lane == 0) ((LAS u32*)(sm + L_TAU))[cw] = tau;
    lds_signal(sm, C_TAU);
    lds_wait(sm, C_TAU, 2);
    if (blockIdx.x == 0 && cw == 0 && lane == 0)
      __hip_atomic_store((GAS u64*)(P.ws + WS_E), epoch, __ATOMIC_RELAXED, __HIP_MEMORY_SCOPE_AGENT);
    tau = umin(((LAS u32*)(sm + L_TAU))[0], ((LAS u32*)(sm + L_TAU))[1]);
    u32 cnt = 0;
#pragma unroll
    for (int i = 0; i < 16; i++) cnt += key[i] >= tau;
    u32 pos = 0, tot = 0;
#pragma unroll
    for (int bit = 0; bit < 5; bit++) {
      const u64 bm = __ballot((cnt >> bit) & 1);
      pos += __builtin_amdgcn_mbcnt_hi((u32)(bm >> 32), __builtin_amdgcn_mbcnt_lo((u32)bm, 0u)) << bit;
      tot += (u32)__builtin_popcountll(bm) << bit;
    }
#pragma unroll
    for (int i = 0; i < 16; i++) {
      const bool c = key[i] >= tau;
      cand[(c && pos < 32) ? cw * 32 + pos : 80 + cw] = key[i];
      pos += c;
    }
    if (lane == 0) ((LAS u32*)(sm + L_TAU))[2 + cw] = tot;
    lds_signal(sm, C_CAND);
    lds_wait(sm, C_CAND, 2);
    const u32 n0 = __builtin_amdgcn_readfirstlane(((LAS u32*)(sm + L_TAU))[2]);
    const u32 n1 = __builtin_amdgcn_readfirstlane(((LAS u32*)(sm + L_TAU))[3]);
    u32 mine, rank = 0;
    if (n0 > 32 || n1 > 32) {
      _Pragma("nounroll") for (int r = 0; r < 8; r++) {
        u32 m2 = key[0];
#pragma unroll
        for (int i = 1; i < 16; i++) m2 = umax(m2, key[i]);
        m2 = xrow_umax(row_umax(m2));
#pragma unroll
        for (int i = 0; i < 16; i++) key[i] = key[i] == m2 ? 0u : key[i];
        if (lane == 0) cand[64 + cw * 8 + r] = m2;
      }
      lds_signal(sm, C_FB);
      lds_wait(sm, C_FB, 2);
      mine = lane < 16 ? cand[64 + lane] : 0u;
      _Pragma("nounroll") for (u32 jj = 0; jj < 16; jj++) rank += cand[64 + jj] > mine;
      if (lane >= 16) rank = 99;
    } else {
      mine = cand[lane];
      const u32 nmax = umax(n0, n1);
      _Pragma("nounroll") for (u32 jj = 0; jj < nmax; jj += 4) {
        const v4u c4 = *(const LAS v4u*)(cand + jj), d4 = *(const LAS v4u*)(cand + 32 + jj);
        rank += (c4.x > mine) + (c4.y > mine) + (c4.z > mine) + (c4.w > mine);
        rank += (d4.x > mine) + (d4.y > mine) + (d4.z > mine) + (d4.w > mine);
      }
      if ((u32)(lane & 31) >= (lane < 32 ? n0 : n1)) rank = 99;
    }
    if (rank < 8) ((LAS u32*)(sm + L_SEL))[rank] = mine;
    lds_signal(sm, C_SEL);
  }
  STAMP(4);
  if (ROLE == 2) return;
  {
    const v4u s4 = *(const LAS v4u*)(sm + L_SEL + cw * 16);
    Row rr[4];
#pragma unroll
    for (int e = 0; e < 4; e++) {
      const int tok = 2047 - (int)(__builtin_amdgcn_readfirstlane(s4[e]) & 2047u);
      rr[e] = load_row(P.q + (size_t)tok * D, lane);
    }
    float x[4][9], m[4], rs[4];
#pragma unroll
    for (int e = 0; e < 4; e++) { unpack_row(rr[e], x[e]); ln_stats(x[e], m[e], rs[e]); }
    if (ROLE == 1) {
#pragma unroll
      for (int e = 0; e < 4; e++) {
        LAS char* dst = sm + L_XS + (cw * 4 + e) * XS_STRIDE;
        float y[9];
#pragma unroll
        for (int i = 0; i < 9; i++) y[i] = rbf(x[e][i] - m[e]) * rs[e];
        v2u a = {pk2(y[0], y[1]), pk2(y[2], y[3])}, c = {pk2(y[4], y[5]), pk2(y[6], y[7])};
        *(LAS v2u*)(dst + lane * 8) = a;
        *(LAS v2u*)(dst + 512 + lane * 8) = c;
        *(LAS u16*)(dst + 1024 + lane * 2) = (u16)pk2(y[8], 0.f);
      }
    } else if (lane == 0) {
      f4 s0 = {m[0], rs[0], m[1], rs[1]}, s1 = {m[2], rs[2], m[3], rs[3]};
      *(LAS f4*)(sm + L_SSTAT + cw * 32) = s0;
      *(LAS f4*)(sm + L_SSTAT + cw * 32 + 16) = s1;
    }
    lds_signal(sm, ROLE == 1 ? C_XS : C_SSTAT);
    STAMP(5);
  }
  if (ROLE == 1) {
    ffn_act(P, wave);
  } else if (cw == 1) {
    const int pi = lane & 7, ti = 4 * (pi & 1) + (pi & 2) + (pi >> 2);
    lds_wait(sm, C_SSTAT, 2);
    const u32 kx = ((LAS u32*)(sm + L_SEL))[ti];
    const int tok = 2047 - (int)(kx & 2047u);
    const float m = ((LAS float*)(sm + L_SSTAT))[2 * ti], rs = ((LAS float*)(sm + L_SSTAT))[2 * ti + 1];
    const v2u x2 = *(const GAS v2u*)((const GAS char*)P.q + (size_t)tok * (D * 2) + cg * 8);
    const v2u d2 = *(const GAS v2u*)((const GAS char*)P.dsk + cg * 8);
    const u32 rb = kx & 0xFFFF0000u;
    const float r = __uint_as_float((rb & 0x80000000u) ? (rb ^ 0x80000000u) : (~rb & 0xFFFF0000u));
    const float rsig = rbf(__builtin_amdgcn_rcpf(1.0f + __expf(-r)));
    v4u g[9];
    _Pragma("nounroll") for (u32 spin = 0; spin < SPIN_CAP; spin++) {
      asm volatile("" ::: "memory");
#pragma unroll
      for (int i = 0; i < 9; i++)
        g[i] = __builtin_amdgcn_raw_buffer_load_b128(P.rws, WS_P + ((lane >> 3) + 8 * i) * PART_B + cg * 128 + pi * 16, 0, AUX_SC1);
      u32 mx = 0;
#pragma unroll
      for (int i = 0; i < 9; i++) mx = vmax4(g[i], mx);
      if (__all(mx != CLR)) break;
      __builtin_amdgcn_s_sleep(2);
    }
    STAMP(6);
    float sum[4];
#pragma unroll
    for (int e = 0; e < 4; e++) {
      float s = __uint_as_float(g[0][e]);
#pragma unroll
      for (int i = 1; i < 9; i++) s += __uint_as_float(g[i][e]);
      s += dppf<0x128>(s);
      sum[e] = xrow_sum(s);
    }
    lds_wait(sm, C_Y, 4);
    float o[4];
#pragma unroll
    for (int e = 0; e < 4; e++) {
      const float y = lo16(*(const LAS u16*)(sm + L_H + e * (HC_B + 64) + tok * 2));
      const float u = lo16(*(const LAS u16*)(sm + L_U + e * UL_B + (32 + (tok >> 3)) * 16 + (7 - (tok & 7)) * 2));
      const float x = (e & 1) ? hi16(x2[e >> 1]) : lo16(x2[e >> 1]);
      const float dk = (e & 1) ? hi16(d2[e >> 1]) : lo16(d2[e >> 1]);
      const float n = ln_norm(x, m, rs);
      const float upd = rbf(rbf(sum[e]) * rsig);
      o[e] = fmaf(n, fmaf(u, dk, y), x) + (n + upd);
    }
    if (lane < 8) {
      v2u o2 = {pk2(o[0], o[1]), pk2(o[2], o[3])};
      *(GAS v2u*)((GAS char*)P.out + (size_t)tok * (D * 2) + cg * 8) = o2;
    }
    STAMP(7);
  }
}

__global__ __launch_bounds__(NTHR, 2) void fused_kernel(
    const u16* __restrict__ q, const u16* __restrict__ k, const u16* __restrict__ v, const u16* __restrict__ shk,
    const u16* __restrict__ hk, const u16* __restrict__ dsk, const u16* __restrict__ wr, const u16* __restrict__ wg,
    const u16* __restrict__ wu, const u16* __restrict__ wd, u16* __restrict__ out, char* ws) {
  extern __shared__ __attribute__((aligned(16))) char smem_[];
  Ctx P;
  P.q = q; P.k = k; P.v = v; P.shk = shk; P.hk = hk; P.dsk = dsk; P.wr = wr; P.wg = wg; P.wu = wu; P.wd = wd;
  P.out = out; P.ws = ws;
  P.sm = (LAS char*)smem_;
  P.tid = threadIdx.x; P.lane = P.tid & 63;
  P.wave = __builtin_amdgcn_readfirstlane(P.tid >> 6);
  const int b = blockIdx.x;
  P.C = (b & 7) * 32 + (b >> 3);
  P.is_conv = b < NCONV;
  const bool is_moe = !P.is_conv && b < NCONV + NMOE;
  P.cg = (b & 7) * 18 + (b >> 3);
  P.mw = b - NCONV;
  P.rws = __builtin_amdgcn_make_buffer_rsrc(ws, 0, WS_END, 0x00020000);
  const int lane = P.lane, wave = P.wave;
  STAMP(0); STAMPC(14);
  if (is_moe) {
    if (wave < 6) { tile_wave<true>(P); FLUSH(); return; }
  } else if (wave < 5) {
    tile_wave<false>(P);
    FLUSH();
    return;
  }
  control_wave(P, P.is_conv ? 0 : (is_moe ? 1 : 2));
  FLUSH();
}

}

extern "C" void kernel_launch(void* const* d_in, const int* in_sizes, int n_in, void* d_out, int out_size, void* d_ws,
                              size_t ws_size, hipStream_t stream) {
  (void)in_sizes; (void)n_in; (void)out_size; (void)ws_size;
  (void)hipFuncSetAttribute((const void*)fused_kernel, hipFuncAttributeMaxDynamicSharedMemorySize, L_TOTAL);
  fused_kernel<<<NWG, NTHR, L_TOTAL, stream>>>(
      (const u16*)d_in[0], (const u16*)d_in[1], (const u16*)d_in[2], (const u16*)d_in[3], (const u16*)d_in[4],
      (const u16*)d_in[5], (const u16*)d_in[7], (const u16*)d_in[8], (const u16*)d_in[9], (const u16*)d_in[10],
      (u16*)d_out, (char*)d_ws);
}
```

```cpp
#include <hip/hip_runtime.h>
#include <hip/hip_bf16.h>

typedef unsigned int u32;
typedef unsigned long long u64;
typedef unsigned short u16;
typedef u32 v4u __attribute__((ext_vector_type(4)));
typedef u32 v2u __attribute__((ext_vector_type(2)));
typedef float f4 __attribute__((ext_vector_type(4)));
typedef float f2 __attribute__((ext_vector_type(2)));
typedef __bf16 bf8 __attribute__((ext_vector_type(8)));
typedef __bf16 bf2 __attribute__((ext_vector_type(2)));
#define LAS __attribute__((address_space(3)))
#define GAS __attribute__((address_space(1)))

namespace {

constexpr int S = 2048, D = 576, F = 4608;
constexpr int NWG = 256, NTHR = 512, NCONV = 144, NMOE = 72;
constexpr int AUX_SC1 = 16, AUX_NT = 0;
constexpr u32 CLR = 0xFFFFFFFFu, SAME = 0xFFFFFFFEu, MAXV = 0xFFFFFFFDu;
constexpr u32 SPIN_CAP = 1u << 16, GATE_CAP = 96;
constexpr u32 GMAGIC = 0x5AC3E900u;
constexpr int OOB = 0x7FFFFF00;
#define U_POST 0
#define WS_AT_END 0
#define H_DELAY 12
#define HELPERS 1

constexpr int WS_E = 0;
constexpr int WS_G = 4096;
constexpr int GATE_B = 256 * 256;
constexpr int WS_R = WS_G + 4 * GATE_B;
constexpr int WS_M = WS_R + S * 8;
constexpr int WS_U = WS_M + S * 8;
constexpr int TILE_B = D * 16;
constexpr int WS_P = WS_U + NWG * TILE_B;
constexpr int PART_B = NCONV * 128;
constexpr int WS_END = WS_P + NMOE * PART_B;
constexpr int WS_DBG = (WS_END + 4095) / 4096 * 4096;
enum { G_R = 0, G_U, G_Y, G_P };
#define ONES8 0x3F803F80u, 0x3F803F80u, 0x3F803F80u, 0x3F803F80u, 0x3F803F80u, 0x3F803F80u, 0x3F803F80u, 0x3F803F80u
#define ONES32 ONES8, ONES8, ONES8, ONES8
__device__ const u32 kOnes[288] __attribute__((aligned(128))) = {ONES32, ONES32, ONES32, ONES32, ONES32, ONES32, ONES32, ONES32, ONES32};

constexpr int HC_B = 2080 * 16;
constexpr int L_H = 0;
constexpr int UL_B = 290 * 16;
constexpr int L_U = L_H + 4 * HC_B;
constexpr int L_STG = L_U + 4 * UL_B;
constexpr int L_MISC = L_STG + TILE_B;
constexpr int L_CNT = L_MISC;
constexpr int L_CAND = L_CNT + 64;
constexpr int L_SEL = L_CAND + 288;
constexpr int L_REC = L_SEL + 32;
constexpr int L_STAT = L_REC + 64;
constexpr int L_END = L_STAT + 64;
constexpr int XS_STRIDE = 1184;
constexpr int L_XS = 0;
constexpr int L_RED = L_XS + 8 * XS_STRIDE;
constexpr int L_HB = L_RED + 6 * 8 * 128 * 4;
constexpr int HB_STRIDE = 160;
static_assert(L_END + 1024 <= 160 * 1024, "lds");
static_assert(L_HB + 8 * HB_STRIDE <= L_U, "lds");
static_assert(L_END - L_MISC == 512, "zero fill");

__device__ __forceinline__ constexpr int xrow_off(int t) { return L_U + (t / 3) * UL_B + 512 + (t % 3) * (D * 2); }
enum { C_CLR = 0, C_STAT, C_TRUST, C_GU, C_SEL, C_H, C_U, C_Y, C_XS, C_RED, C_HB, C_GY, C_PUB, C_TAG, C_QQ };

__device__ __forceinline__ float lo16(u32 x) { return __uint_as_float(x << 16); }
__device__ __forceinline__ float hi16(u32 x) { return __uint_as_float(x & 0xFFFF0000u); }
__device__ __forceinline__ u32 pk2(float a, float b) {
  bf2 p = {(__bf16)a, (__bf16)b};
  return __builtin_bit_cast(u32, p);
}
__device__ __forceinline__ float rbf(float a) { return lo16(pk2(a, a)); }
__device__ __forceinline__ float dot2(u32 a, u32 b, float c) {
  return __builtin_amdgcn_fdot2_f32_bf16(__builtin_bit_cast(bf2, a), __builtin_bit_cast(bf2, b), c, false);
}
template <int CTRL> __device__ __forceinline__ u32 dppu(u32 x) {
  return (u32)__builtin_amdgcn_mov_dpp((int)x, CTRL, 0xf, 0xf, true);
}
template <int CTRL> __device__ __forceinline__ float dppf(float x) { return __uint_as_float(dppu<CTRL>(__float_as_uint(x))); }
__device__ __forceinline__ float xrow_sum(float x) {
  auto s = __builtin_amdgcn_permlane16_swap(__float_as_uint(x), __float_as_uint(x), false, false);
  x = __uint_as_float(s[0]) + __uint_as_float(s[1]);
  auto t = __builtin_amdgcn_permlane32_swap(__float_as_uint(x), __float_as_uint(x), false, false);
  return __uint_as_float(t[0]) + __uint_as_float(t[1]);
}
__device__ __forceinline__ u32 umax(u32 a, u32 b) { return a > b ? a : b; }
__device__ __forceinline__ u32 umin(u32 a, u32 b) { return a < b ? a : b; }
__device__ __forceinline__ u32 umed3(u32 a, u32 b, u32 c) { return umax(umin(a, b), umin(umax(a, b), c)); }
__device__ __forceinline__ u32 xrow_umin(u32 x) {
  auto s = __builtin_amdgcn_permlane16_swap(x, x, false, false);
  x = umin(s[0], s[1]);
  auto t = __builtin_amdgcn_permlane32_swap(x, x, false, false);
  return umin(t[0], t[1]);
}
__device__ __forceinline__ u32 wave_umax(u32 x) {
  x = umax(x, dppu<0xB1>(x));
  x = umax(x, dppu<0x4E>(x));
  x = umax(x, dppu<0x141>(x));
  x = umax(x, dppu<0x140>(x));
  auto s = __builtin_amdgcn_permlane16_swap(x, x, false, false);
  x = umax(s[0], s[1]);
  auto t = __builtin_amdgcn_permlane32_swap(x, x, false, false);
  return umax(t[0], t[1]);
}
__device__ __forceinline__ void lds_signal(LAS char* sm, int c) {
  u64 keep;
  asm volatile("s_waitcnt lgkmcnt(0)\n\ts_mov_b64 %0, exec\n\ts_mov_b64 exec, 1\n\tds_add_u32 %1, %2\n\ts_mov_b64 exec, %0"
               : "=&s"(keep) : "v"((u32)(size_t)(sm + L_CNT + 4 * c)), "v"(1u) : "memory");
}
__device__ __forceinline__ void lds_wait(LAS char* sm, int c, u32 target) {
  asm volatile("" ::: "memory");
  while ((u32)__builtin_amdgcn_readfirstlane(
             __hip_atomic_load((LAS u32*)(sm + L_CNT) + c, __ATOMIC_RELAXED, __HIP_MEMORY_SCOPE_WORKGROUP)) < target)
    __builtin_amdgcn_s_sleep(1);
  asm volatile("" ::: "memory");
}
__device__ __forceinline__ void wg_barrier() { asm volatile("s_waitcnt lgkmcnt(0)\n\ts_barrier" ::: "memory"); }
__device__ __forceinline__ u32 umax3(u32 a, u32 b, u32 c) { return umax(umax(a, b), c); }
__device__ __forceinline__ u32 vmax4(v4u g, u32 acc) { return umax3(umax3(g.x, g.y, acc), g.z, g.w); }
__device__ __forceinline__ f4 mfma16(v4u a, v4u b, f4 c) {
  return __builtin_amdgcn_mfma_f32_16x16x32_bf16(__builtin_bit_cast(bf8, a), __builtin_bit_cast(bf8, b), c, 0, 0, 0);
}
__device__ __forceinline__ __amdgpu_buffer_rsrc_t mkrsrc(const void* p, int bytes) {
  return __builtin_amdgcn_make_buffer_rsrc((void*)p, 0, bytes, 0x00020000);
}

#define STAMP(i) do {} while (0)
#define STAMPC(i) do {} while (0)
#define FLUSH() do {} while (0)
#define NOTE(i, v) do {} while (0)
constexpr int L_TOTAL = L_END;

struct Ctx {
  const u16 *q, *k, *v, *shk, *hk, *dsk, *wr, *wg, *wu, *wd;
  u16* out;
  char* ws;
  __amdgpu_buffer_rsrc_t rws;
  LAS char* sm;
  int tid, lane, wave, b, C, cg, mw;
  bool is_conv;
};

__device__ __forceinline__ int gate_off(int gate, int rep, int shard) { return WS_G + gate * GATE_B + (rep * 16 + shard) * 256; }
__device__ __forceinline__ u32 gate_add(const Ctx& P, int off, int val) {
  return (u32)__builtin_amdgcn_raw_ptr_buffer_atomic_add_i32(val, P.rws, off, 0, 0);
}
__device__ __forceinline__ u32 gate_post(const Ctx& P, int gate, bool on, u32 inc) {
  return gate_add(P, (on && P.lane < 16) ? gate_off(gate, P.lane, P.b & 15) : OOB, (int)inc);
}
__device__ __forceinline__ void gate_confirm(const Ctx& P, int gate, u32 old, u32 inc) {
  bool bad = P.lane < 16;
  _Pragma("nounroll") for (u32 spin = 0; spin < GATE_CAP; spin++) {
    const u32 d = old - GMAGIC;
    bad = bad && ((d & 0xF0u) != 0u || d != (d & 0xFu) * inc);
    if (!__any(bad)) break;
    NOTE(26, spin + 1);
    __builtin_amdgcn_s_sleep(2);
    old = gate_add(P, bad ? gate_off(gate, P.lane, P.b & 15) : OOB, (int)inc);
  }
}
__device__ __forceinline__ void gate_wait(const Ctx& P, int gate, u32 expect, u32 inc) {
  const int off = P.lane < 16 ? gate_off(gate, P.b >> 4, P.lane) : OOB;
  const u32 want = P.lane < 16 ? GMAGIC + expect * inc : 0u;
  _Pragma("nounroll") for (u32 spin = 0; spin < GATE_CAP; spin++)
    if (__all(gate_add(P, off, 0) == want)) break;
}
__device__ __forceinline__ u32 gate_inc(u32 tag) { return 1u + (tag << 8); }

struct Row { v2u a0, a1; u32 a2; };
template <int AUX> __device__ __forceinline__ Row load_row(__amdgpu_buffer_rsrc_t r, int soff, int lane) {
  Row x;
  x.a0 = __builtin_amdgcn_raw_buffer_load_b64(r, lane * 8, soff, AUX);
  x.a1 = __builtin_amdgcn_raw_buffer_load_b64(r, lane * 8 + 512, soff, AUX);
  x.a2 = __builtin_amdgcn_raw_buffer_load_b16(r, lane * 2 + 1024, soff, AUX);
  return x;
}
__device__ __forceinline__ void hcopy_store(LAS char* dst, v4u lo, v4u hi, bool first) {
  u32 d[8], o[7];
#pragma unroll
  for (int e = 0; e < 4; e++) { d[e] = lo[e]; d[4 + e] = hi[e]; }
#pragma unroll
  for (int e = 0; e < 7; e++) o[e] = __builtin_amdgcn_alignbit(d[e + 1], d[e], 16);
  constexpr int BASE[8] = {0, 260, 519, 778, 1037, 1296, 1556, 1815};
#pragma unroll
  for (int e = 0; e < 4; e++) {
    v4u a = {d[e], d[e + 1], d[e + 2], d[e + 3]}, c = {o[e], o[e + 1], o[e + 2], o[e + 3]};
    if (e > 0 || first) *(LAS v4u*)(dst + BASE[2 * e] * 16) = a;
    *(LAS v4u*)(dst + BASE[2 * e + 1] * 16) = c;
  }
}
struct HCopy {
  v4u lo[8];
  __device__ __forceinline__ void load(const Ctx& P, int ch0) {
    const __amdgpu_buffer_rsrc_t rh = mkrsrc(P.hk + (size_t)(4 * P.cg + ch0) * S, 2 * S * 2);
#pragma unroll
    for (int r = 0; r < 8; r++) lo[r] = __builtin_amdgcn_raw_buffer_load_b128(rh, P.lane * 16 + (r & 3) * 1024, (r >> 2) * 4096, AUX_NT);
  }
  __device__ __forceinline__ void store(const Ctx& P, int ch0) {
    LAS char* dst = P.sm + L_H + ch0 * HC_B + (P.lane + 2) * 16;
#pragma unroll
    for (int r = 0; r < 8; r++) *(LAS v4u*)(dst + (r >> 2) * HC_B + (r & 3) * 1024) = lo[r];
    {
      const int c = P.lane & 1, m1 = (P.lane >> 1) & 1;
      const v4u z = {0u, 0u, 0u, 0u};
      v4u h0 = *(const LAS v4u*)(P.sm + L_H + (ch0 + c) * HC_B + 32);
      h0 = m1 ? h0 : z;
      if (P.lane < 4) hcopy_store(P.sm + L_H + (ch0 + c) * HC_B + m1 * 16, z, h0, true);
    }
    v4u hi[8];
#pragma unroll
    for (int r = 0; r < 8; r++) hi[r] = *(const LAS v4u*)(dst + (r >> 2) * HC_B + (r & 3) * 1024 + 16);
#pragma unroll
    for (int r = 0; r < 8; r++) hcopy_store(dst + (r >> 2) * HC_B + (r & 3) * 1024, lo[r], hi[r], false);
    lds_signal(P.sm, C_H);
  }
};

__device__ __forceinline__ void ffn_act(const Ctx& P, int tk) {
  LAS char* sm = P.sm;
  lds_wait(sm, C_RED, 6);
  float G = 0.f, U = 0.f;
#pragma unroll
  for (int w = 0; w < 6; w++) {
    G += *(const LAS float*)(sm + L_RED + ((w * 8 + tk) * 128 + P.lane) * 4);
    U += *(const LAS float*)(sm + L_RED + ((w * 8 + tk) * 128 + 64 + P.lane) * 4);
  }
  G = rbf(G);
  const float sg = rbf(G * __builtin_amdgcn_rcpf(1.0f + __expf(-G)));
  *(LAS u16*)(sm + L_HB + tk * HB_STRIDE + P.lane * 2) = (u16)pk2(sg * rbf(U), 0.f);
  lds_signal(sm, C_HB);
}

struct Ffn {
  v4u W1[3][8];
  v2u W2[2][2][8];
  __device__ __forceinline__ void load(const Ctx& P, int cu0) {
    const int n16 = P.lane & 15, g4 = P.lane >> 4;
    const GAS char* wp = (const GAS char*)(n16 < 8 ? P.wg : P.wu) + ((size_t)(96 * P.wave + 8 * g4) * F + 64 * P.mw + 8 * (n16 & 7)) * 2;
#pragma unroll
    for (int i = 0; i < 3; i++)
#pragma unroll
      for (int jj = 0; jj < 8; jj++) W1[i][jj] = *(const GAS v4u*)(wp + (size_t)(32 * i + jj) * (F * 2));
    __builtin_amdgcn_sched_barrier(0);
    const __amdgpu_buffer_rsrc_t rd = mkrsrc(P.wd + (size_t)64 * P.mw * D, 64 * D * 2);
    const int o2 = (8 * g4 * D + 64 * cu0 + 4 * n16) * 2;
#pragma unroll
    for (int ci = 0; ci < 2; ci++)
      if (ci == 0 || P.wave >= 3) {
#pragma unroll
        for (int ks = 0; ks < 2; ks++)
#pragma unroll
          for (int jj = 0; jj < 8; jj++)
            W2[ci][ks][jj] = __builtin_amdgcn_raw_buffer_load_b64(rd, o2 + (jj & 3) * (D * 2) + ci * 128, (32 * ks + (jj & 4)) * (D * 2), AUX_NT);
      }
  }
  __device__ __forceinline__ void run(const Ctx& P, int cu0) {
    LAS char* sm = P.sm;
    const int lane = P.lane, wave = P.wave, n16 = lane & 15, g4 = lane >> 4;
    v4u Bf[3][8];
#pragma unroll
    for (int i = 0; i < 3; i++)
#pragma unroll
      for (int e = 0; e < 8; e++) {
#pragma unroll
        for (int d = 0; d < 4; d++)
          Bf[i][e][d] = __builtin_amdgcn_perm(W1[i][2 * d + 1][e >> 1], W1[i][2 * d][e >> 1], (e & 1) ? 0x07060302u : 0x05040100u);
        asm volatile("" : "+v"(Bf[i][e]));
      }
    STAMP(4);
    lds_wait(sm, C_XS, 2);
    STAMP(5);
    const u32 tag = *(const LAS u32*)(sm + L_REC), tag12 = tag & 0xFFFu;
    f4 acc[8];
#pragma unroll
    for (int e = 0; e < 8; e++) acc[e] = (f4){0.f, 0.f, 0.f, 0.f};
#pragma unroll
    for (int i = 0; i < 3; i++) {
      const v4u A = *(const LAS v4u*)(sm + L_XS + (n16 & 7) * XS_STRIDE + (96 * wave + 32 * i + 8 * g4) * 2);
#pragma unroll
      for (int e = 0; e < 8; e++) acc[e] = mfma16(A, Bf[i][e], acc[e]);
    }
    {
      LAS char* dst = sm + L_RED + ((wave * 8 + 4 * (g4 & 1) + (g4 & 2)) * 128 + 8 * n16) * 4;
#pragma unroll
      for (int r = 0; r < 2; r++) {
        f4 a, c;
#pragma unroll
        for (int e = 0; e < 4; e++) { a[e] = g4 >= 2 ? acc[e][2 + r] : acc[e][r]; c[e] = g4 >= 2 ? acc[4 + e][2 + r] : acc[4 + e][r]; }
        *(LAS f4*)(dst + r * 512) = a;
        *(LAS f4*)(dst + r * 512 + 16) = c;
      }
    }
    lds_signal(sm, C_RED);
    v4u Bd[2][2][4];
#pragma unroll
    for (int ci = 0; ci < 2; ci++)
      if (ci == 0 || wave >= 3) {
#pragma unroll
        for (int ks = 0; ks < 2; ks++)
#pragma unroll
          for (int e = 0; e < 4; e++) {
#pragma unroll
            for (int d = 0; d < 4; d++)
              Bd[ci][ks][e][d] = __builtin_amdgcn_perm(W2[ci][ks][2 * d + 1][e >> 1], W2[ci][ks][2 * d][e >> 1], (e & 1) ? 0x07060302u : 0x05040100u);
            asm volatile("" : "+v"(Bd[ci][ks][e]));
          }
      }
    asm volatile("s_waitcnt vmcnt(0)" ::: "memory");
    (void)gate_post(P, G_P, wave == 5, gate_inc(tag));
    ffn_act(P, wave);
    STAMP(6);
    lds_wait(sm, C_HB, 8);
    const v4u A0 = *(const LAS v4u*)(sm + L_HB + (n16 & 7) * HB_STRIDE + g4 * 16);
    const v4u A1 = *(const LAS v4u*)(sm + L_HB + (n16 & 7) * HB_STRIDE + 64 + g4 * 16);
#pragma unroll
    for (int ci = 0; ci < 2; ci++) {
      if (ci == 1 && wave < 3) break;
      f4 ad[4];
#pragma unroll
      for (int e = 0; e < 4; e++) ad[e] = mfma16(A0, Bd[ci][0][e], (f4){0.f, 0.f, 0.f, 0.f});
#pragma unroll
      for (int e = 0; e < 4; e++) ad[e] = mfma16(A1, Bd[ci][1][e], ad[e]);
      v4u s0, s1;
#pragma unroll
      for (int e = 0; e < 4; e++) {
        const u32 x = ((__float_as_uint(g4 >= 2 ? ad[e][2] : ad[e][0]) + 0x800u) & 0xFFFFF000u) | tag12;
        const u32 y = ((__float_as_uint(g4 >= 2 ? ad[e][3] : ad[e][1]) + 0x800u) & 0xFFFFF000u) | tag12;
        s0[e] = (u32)__builtin_amdgcn_update_dpp((int)x, (int)y, 0x128, 0xf, 0xc, false);
        s1[e] = (u32)__builtin_amdgcn_update_dpp((int)y, (int)x, 0x128, 0xf, 0x3, false);
      }
      const int off = WS_P + P.mw * PART_B + (16 * (cu0 + ci) + (n16 & 7)) * 128 + (n16 >> 3) * 64 + g4 * 16;
      __builtin_amdgcn_raw_buffer_store_b128(s0, P.rws, off, 0, AUX_SC1);
      __builtin_amdgcn_raw_buffer_store_b128(s1, P.rws, off + 1024, 0, AUX_SC1);
    }
    STAMP(7);
  }
};

__device__ __forceinline__ void helper_prefetch(const Ctx& P) {
  const int g = P.b & 7, idx = (P.b - NCONV - NMOE) >> 3;
  v4u acc = {0u, 0u, 0u, 0u};
  v4u t[19];
#pragma unroll
  for (int i = 0; i < 19; i++) {
    const int u = (i * 5 + idx) * 7 + P.wave;
    const int sl = u / 72;
    const __amdgpu_buffer_rsrc_t rd = mkrsrc(P.wd, F * D * 2);
    t[i] = __builtin_amdgcn_raw_buffer_load_b128(rd, u < 648 ? P.lane * 16 : OOB, (sl * 8 + g) * (64 * D * 2) + (u - sl * 72) * 1024, AUX_NT);
  }
#pragma unroll
  for (int i = 0; i < 19; i++) acc |= t[i];
  asm volatile("" :: "v"(acc));
}

__device__ __forceinline__ void conv_phase(const Ctx& P) {
  LAS char* sm = P.sm;
  const int tid = P.tid, lane = P.lane, wave = P.wave, n16 = lane & 15, g4 = lane >> 4, gch = tid & 3;
  const int goff = WS_U + (tid >> 2) * TILE_B + (4 * P.cg + gch) * 16;
  const u32 inc = gate_inc(*(const LAS u32*)(sm + L_REC));
  if (wave == 0) {
    gate_wait(P, G_U, 16u, inc);
    lds_signal(sm, C_GU);
  } else {
    lds_wait(sm, C_GU, 1);
  }
  STAMP(3);
  lds_wait(sm, C_TRUST, 1);
  STAMP(4);
  {
    v4u g[4];
    u32 spin = 0;
    _Pragma("nounroll") for (; spin < SPIN_CAP; spin++) {
      asm volatile("" ::: "memory");
#pragma unroll
      for (int it = 0; it < 4; it++) g[it] = __builtin_amdgcn_raw_buffer_load_b128(P.rws, goff, it * 64 * TILE_B, AUX_SC1);
      u32 mx = 0;
#pragma unroll
      for (int it = 0; it < 4; it++) mx = vmax4(g[it], mx);
      if (__all(mx != CLR)) break;
      __builtin_amdgcn_s_sleep(4);
    }
    NOTE(24, spin + 1);
    asm volatile("s_waitcnt vmcnt(0)" ::: "memory");
    lds_wait(sm, C_QQ, 5);
#pragma unroll
    for (int it = 0; it < 4; it++) *(LAS v4u*)(sm + L_U + gch * UL_B + (32 + it * 64 + (tid >> 2)) * 16) = g[it];
  }
  lds_signal(sm, C_U);
  STAMP(5);
  lds_wait(sm, C_H, 2);
  lds_wait(sm, C_U, 4);
  STAMP(6);
  {
    const int p1 = n16 + 1, kap = p1 & 7;
    const int base16 = 258 * kap + ((0x98654320u >> (4 * kap)) & 15);
    const LAS char* pa = sm + L_H + wave * HC_B + (base16 + g4 + (p1 >> 3)) * 16;
    const LAS char* pb = sm + L_U + wave * UL_B + (32 + 2 * n16 + 1 - g4 - 28) * 16;
    f4 acc[8];
#pragma unroll
    for (int t = 0; t < 8; t++) acc[t] = (f4){0.f, 0.f, 0.f, 0.f};
    v4u A[2][8], B[2][8];
#pragma unroll
    for (int a = 0; a < 8; a++) {
      A[0][a] = *(const LAS v4u*)(pa + a * 512);
      B[0][a] = *(const LAS v4u*)(pb + a * 512 + 7 * 64);
    }
#pragma unroll
    for (int bb = 0; bb < 8; bb++) {
      __builtin_amdgcn_sched_barrier(0);
      if (bb < 7) {
#pragma unroll
        for (int a = 0; a < 8; a++) {
          A[(bb + 1) & 1][a] = *(const LAS v4u*)(pa + a * 512 + (bb + 1) * 64);
          B[(bb + 1) & 1][a] = *(const LAS v4u*)(pb + a * 512 + (6 - bb) * 64);
        }
      }
#pragma unroll
      for (int t = 0; t < 8; t++)
#pragma unroll
        for (int a = 0; a <= t; a++) acc[t] = mfma16(A[bb & 1][a], B[bb & 1][t - a], acc[t]);
      if (bb < 7) {
#pragma unroll
        for (int i = 0; i < 16; i++) {
          __builtin_amdgcn_sched_group_barrier(0x8, 2, 0);
          __builtin_amdgcn_sched_group_barrier(0x100, 1, 0);
        }
        __builtin_amdgcn_sched_group_barrier(0x8, 4, 0);
      }
    }
    __builtin_amdgcn_sched_barrier(0);
    (void)gate_post(P, G_Y, wave == 0, inc);
    LAS char* yb = sm + L_H + wave * (HC_B + 64) + n16 * 32 + g4 * 8;
#pragma unroll
    for (int t = 0; t < 8; t++) {
      v2u y2 = {umin(pk2(acc[t][0], acc[t][1]), MAXV), umin(pk2(acc[t][2], acc[t][3]), MAXV)};
      *(LAS v2u*)(yb + t * 512) = y2;
    }
  }
  lds_signal(sm, C_Y);
  STAMP(7);
  v4u u4[4];
#pragma unroll
  for (int it = 0; it < 4; it++) u4[it] = *(const LAS v4u*)(sm + L_U + gch * UL_B + (32 + it * 64 + (tid >> 2)) * 16);
  lds_wait(sm, C_Y, 4);
#pragma unroll
  for (int it = 0; it < 4; it++) {
    const v4u y4 = *(const LAS v4u*)(sm + L_H + gch * (HC_B + 64) + (it * 64 + (tid >> 2)) * 16);
    v4u w4;
#pragma unroll
    for (int e = 0; e < 4; e++) w4[e] = (y4[e] == u4[it][e]) ? SAME : y4[e];
    __builtin_amdgcn_raw_buffer_store_b128(w4, P.rws, goff, it * 64 * TILE_B, AUX_SC1);
  }
  STAMP(8);
}

template <bool MOE> __device__ __forceinline__ void tile_wave(const Ctx& P) {
  LAS char* sm = P.sm;
  const int tid = P.tid, lane = P.lane, wave = P.wave, C = P.C;
  const bool act = tid < 288;
  const int j4 = min(tid, 287) * 4;
  const __amdgpu_buffer_rsrc_t rk = mkrsrc(P.k, S * D * 2), rv = mkrsrc(P.v, S * D * 2);
  const int row0 = 8 * C * (D * 2);
  u32 kk[11], vv[8], qq[8];
#pragma unroll
  for (int i = 0; i < 3; i++) kk[i] = __builtin_amdgcn_raw_buffer_load_b32(rk, row0 + (i - 3) * (D * 2) + j4, 0, AUX_NT);
#pragma unroll
  for (int i = 0; i < 8; i++) {
    kk[3 + i] = __builtin_amdgcn_raw_buffer_load_b32(rk, j4 + (i & 3) * (D * 2), row0 + (i & 4) * (D * 2), AUX_NT);
    vv[i] = __builtin_amdgcn_raw_buffer_load_b32(rv, j4 + (i & 3) * (D * 2), row0 + (i & 4) * (D * 2), AUX_NT);
  }
  const v4u sk = *(const GAS v4u*)((const GAS char*)P.shk + j4 * 4);
  STAMP(10);
  lds_wait(sm, C_TAG, 1);
  const u32 uinc = gate_inc(*(const LAS u32*)(sm + L_REC));
  __builtin_amdgcn_sched_barrier(0);
  u32 gold;
  asm volatile("" :: "v"(sk) : "memory");
  gold = gate_post(P, G_U, wave == 4, uinc);
  STAMP(12);
  u32 up[2][4];
  {
    float w0[4] = {lo16(sk.x), hi16(sk.x), lo16(sk.y), hi16(sk.y)};
    float w1[4] = {lo16(sk.z), hi16(sk.z), lo16(sk.w), hi16(sk.w)};
    float u0[8], u1[8];
#pragma unroll
    for (int s = 0; s < 8; s++) {
      float a = lo16(kk[s]) * w0[3], c = hi16(kk[s]) * w1[3];
      a = fmaf(lo16(kk[s + 1]), w0[2], a); c = fmaf(hi16(kk[s + 1]), w1[2], c);
      a = fmaf(lo16(kk[s + 2]), w0[1], a); c = fmaf(hi16(kk[s + 2]), w1[1], c);
      a = fmaf(lo16(kk[s + 3]), w0[0], a); c = fmaf(hi16(kk[s + 3]), w1[0], c);
      u0[s] = a * lo16(vv[s]); u1[s] = c * hi16(vv[s]);
    }
#pragma unroll
    for (int i = 0; i < 4; i++) {
      up[0][i] = umin(pk2(u0[7 - 2 * i], u0[6 - 2 * i]), MAXV);
      up[1][i] = umin(pk2(u1[7 - 2 * i], u1[6 - 2 * i]), MAXV);
    }
    STAMP(1);
    LAS char* stg = sm + L_STG + wave * 2048;
    v4u p0 = {up[0][0], up[0][1], up[0][2], up[0][3]}, p1 = {up[1][0], up[1][1], up[1][2], up[1][3]};
    if (act) { *(LAS v4u*)(stg + lane * 32) = p0; *(LAS v4u*)(stg + lane * 32 + 16) = p1; }
    asm volatile("" ::: "memory");
    p0 = *(const LAS v4u*)(stg + lane * 16);
    p1 = *(const LAS v4u*)(stg + (wave < 4 ? 1024 : 0) + lane * 16);
    STAMP(13);
    lds_wait(sm, C_CLR, 1);
    const int so = WS_U + C * TILE_B + wave * 2048 + lane * 16;
    __builtin_amdgcn_raw_buffer_store_b128(p0, P.rws, so, 0, AUX_SC1);
    __builtin_amdgcn_raw_buffer_store_b128(p1, P.rws, wave < 4 ? so + 1024 : OOB, 0, AUX_SC1);
  }
  __builtin_amdgcn_sched_barrier(0);
  if (MOE) lds_signal(sm, C_PUB);
  STAMP(2);
  lds_wait(sm, C_STAT, 1);
#pragma unroll
  for (int i = 0; i < 8; i++) qq[i] = *(const LAS u32*)(sm + xrow_off(i) + j4);
  lds_signal(sm, C_QQ);
#pragma unroll
  for (int i = 0; i < 8; i++) asm volatile("" : "+v"(qq[i]));
  const u32 dd = *(const GAS u32*)((const GAS char*)P.dsk + j4);
  if (wave == 4) gate_confirm(P, G_U, gold, uinc);
  if (MOE) {
    Ffn ffn;
    const int cu0 = wave < 3 ? wave : 2 * wave - 3;
    lds_wait(sm, C_PUB, 5);
    ffn.load(P, cu0);
    STAMP(3);
    ffn.run(P, cu0);
  } else if (P.is_conv) {
    if (wave < 4) conv_phase(P);
  } else {
    lds_wait(sm, C_PUB, 1);
    helper_prefetch(P);
  }

  float xn[2][8], c0[2][8];
  {
    const float d0 = lo16(dd), d1 = hi16(dd);
#pragma unroll
    for (int s2 = 0; s2 < 4; s2++) {
      const f4 st = *(const LAS f4*)(sm + L_STAT + s2 * 16);
#pragma unroll
      for (int e = 0; e < 2; e++) {
        const int s = 2 * s2 + e;
        const float xa = lo16(qq[s]), xb = hi16(qq[s]);
        const float na = fmaf(xa, st[2 * e], st[2 * e + 1]), nb = fmaf(xb, st[2 * e], st[2 * e + 1]);
        const u32 pa = up[0][(7 - s) >> 1], pb = up[1][(7 - s) >> 1];
        const float ua = ((7 - s) & 1) ? hi16(pa) : lo16(pa), ub = ((7 - s) & 1) ? hi16(pb) : lo16(pb);
        xn[0][s] = na; xn[1][s] = nb;
        c0[0][s] = fmaf(na, fmaf(ua, d0, 1.0f), xa);
        c0[1][s] = fmaf(nb, fmaf(ub, d1, 1.0f), xb);
        asm volatile("" : "+v"(xn[0][s]), "+v"(xn[1][s]), "+v"(c0[0][s]), "+v"(c0[1][s]));
      }
    }
  }
  lds_wait(sm, C_SEL, 1);
  u32 skip = 0;
  {
    const v4u s0 = *(const LAS v4u*)(sm + L_SEL), s1 = *(const LAS v4u*)(sm + L_SEL + 16);
#pragma unroll
    for (int e = 0; e < 4; e++) {
      const u32 t0 = 2047u - (s0[e] & 2047u), t1 = 2047u - (s1[e] & 2047u);
      skip |= ((int)(t0 >> 3) == C ? 1u : 0u) << (t0 & 7);
      skip |= ((int)(t1 >> 3) == C ? 1u : 0u) << (t1 & 7);
    }
    if (!act) skip = 0xFF;
  }
  const __amdgpu_buffer_rsrc_t ro = mkrsrc(P.out, S * D * 2);
  int ooff[8];
#pragma unroll
  for (int s = 0; s < 8; s++) { ooff[s] = ((skip >> s) & 1) ? OOB : row0 + s * (D * 2) + j4; asm volatile("" : "+v"(ooff[s])); }
  const int uoff = WS_U + C * TILE_B + j4 * 8;
  if (!MOE && !(P.is_conv && wave < 4)) asm volatile("s_waitcnt vmcnt(0)" ::: "memory");
  lds_wait(sm, C_GY, 1);
  STAMP(9);
  v4u y0, y1;
  u32 spin = 0;
  _Pragma("nounroll") for (; spin < SPIN_CAP; spin++) {
    asm volatile("" ::: "memory");
    y0 = __builtin_amdgcn_raw_buffer_load_b128(P.rws, uoff, 0, AUX_SC1);
    y1 = __builtin_amdgcn_raw_buffer_load_b128(P.rws, uoff + 16, 0, AUX_SC1);
    bool ok = vmax4(y0, vmax4(y1, 0u)) != CLR;
#pragma unroll
    for (int e = 0; e < 4; e++) ok = ok & (y0[e] != up[0][e]) & (y1[e] != up[1][e]);
    if (__all(ok)) break;
    __builtin_amdgcn_s_sleep(4);
  }
  NOTE(25, spin + 1);
#pragma unroll
  for (int e = 0; e < 4; e++) {
    const u32 a = y0[e] == SAME ? up[0][e] : y0[e], c = y1[e] == SAME ? up[1][e] : y1[e];
    const u32 o0 = pk2(fmaf(xn[0][2 * e], lo16(a), c0[0][2 * e]), fmaf(xn[1][2 * e], lo16(c), c0[1][2 * e]));
    const u32 o1 = pk2(fmaf(xn[0][2 * e + 1], hi16(a), c0[0][2 * e + 1]), fmaf(xn[1][2 * e + 1], hi16(c), c0[1][2 * e + 1]));
    __builtin_amdgcn_raw_buffer_store_b32(o0, ro, ooff[2 * e], 0, 0);
    __builtin_amdgcn_raw_buffer_store_b32(o1, ro, ooff[2 * e + 1], 0, 0);
  }
}

__device__ __forceinline__ void route(const Ctx& P, u32 tag, u64 epoch) {
  LAS char* sm = P.sm;
  const int lane = P.lane, wave = P.wave;
  (void)wave;
  gate_wait(P, G_R, 16u, gate_inc(tag));
  STAMP(3);
  v4u g[16];
  u32 spin = 0;
  _Pragma("nounroll") for (; spin < SPIN_CAP; spin++) {
    asm volatile("" ::: "memory");
#pragma unroll
    for (int i = 0; i < 16; i++)
      g[i] = __builtin_amdgcn_raw_buffer_load_b128(P.rws, WS_R + lane * 16 + (i & 3) * 1024, (i >> 2) * 4096, AUX_SC1);
    u32 bad = 0;
#pragma unroll
    for (int i = 0; i < 16; i++) {
      bad = __builtin_amdgcn_bitop3_b32(bad, g[i].y, tag, 0xF6);
      bad = __builtin_amdgcn_bitop3_b32(bad, g[i].w, tag, 0xF6);
    }
    if (__all(bad == 0)) break;
    __builtin_amdgcn_s_sleep(4);
  }
  NOTE(24, spin + 1);
  lds_signal(sm, C_TRUST);
  STAMP(4);
  if (P.b == 0 && lane == 0) __hip_atomic_store((GAS u64*)(P.ws + WS_E), epoch, __ATOMIC_RELAXED, __HIP_MEMORY_SCOPE_AGENT);
  u32 k1 = umax(g[0].x, g[0].z), k2 = umin(g[0].x, g[0].z), k3 = 0;
#pragma unroll
  for (int i = 2; i < 32; i++) {
    const u32 x = (i & 1) ? g[i >> 1].z : g[i >> 1].x;
    k3 = umed3(k2, k3, x);
    k2 = umed3(k1, k2, x);
    k1 = umax(k1, x);
  }
  u32 tau = umax(k1, dppu<0xB1>(k1));
  tau = umax(tau, dppu<0x4E>(tau));
  tau = umax(tau, dppu<0x141>(tau));
  tau = umin(tau, dppu<0x140>(tau));
  tau = xrow_umin(tau);
  LAS u32* cand = (LAS u32*)(sm + L_CAND);
  const u64 m1 = __ballot(k1 >= tau), m2 = __ballot(k2 >= tau), m3 = __ballot(k3 >= tau);
  const u32 n1 = __builtin_popcountll(m1), n2 = n1 + __builtin_popcountll(m2), n = n2 + __builtin_popcountll(m3);
  bool slow = n > 64;
  if (!slow) {
    const u32 p1 = __builtin_amdgcn_mbcnt_hi((u32)(m1 >> 32), __builtin_amdgcn_mbcnt_lo((u32)m1, 0u));
    const u32 p2 = __builtin_amdgcn_mbcnt_hi((u32)(m2 >> 32), __builtin_amdgcn_mbcnt_lo((u32)m2, n1));
    const u32 p3 = __builtin_amdgcn_mbcnt_hi((u32)(m3 >> 32), __builtin_amdgcn_mbcnt_lo((u32)m3, n2));
    cand[k1 >= tau ? p1 : 64] = k1;
    cand[k2 >= tau ? p2 : 64] = k2;
    cand[k3 >= tau ? p3 : 64] = k3;
    asm volatile("" ::: "memory");
    const u32 mine = cand[lane];
    u32 rank = 0;
    _Pragma("nounroll") for (u32 jj = 0; jj < n; jj += 8) {
      const v4u c4 = *(const LAS v4u*)(cand + jj), d4 = *(const LAS v4u*)(cand + jj + 4);
      rank += (c4.x > mine) + (c4.y > mine) + (c4.z > mine) + (c4.w > mine);
      rank += (d4.x > mine) + (d4.y > mine) + (d4.z > mine) + (d4.w > mine);
    }
    const bool in = (u32)lane < n && rank < 8;
    if (in) ((LAS u32*)(sm + L_SEL))[rank] = mine;
    slow = __any(in && (u32)lane >= n2);
  }
  if (slow) {
    _Pragma("nounroll") for (int r = 0; r < 8; r++) {
      u32 mx = 0;
#pragma unroll
      for (int i = 0; i < 16; i++) mx = umax3(mx, g[i].x, g[i].z);
      mx = wave_umax(mx);
#pragma unroll
      for (int i = 0; i < 16; i++) { g[i].x = g[i].x == mx ? 0u : g[i].x; g[i].z = g[i].z == mx ? 0u : g[i].z; }
      if (lane == 0) ((LAS u32*)(sm + L_SEL))[r] = mx;
    }
  }
  lds_signal(sm, C_SEL);
  STAMP(5);
}

__device__ __forceinline__ u32 load_msd(const Ctx& P, int tok, u32 tag) {
  v2u g;
  _Pragma("nounroll") for (u32 spin = 0; spin < SPIN_CAP; spin++) {
    asm volatile("" ::: "memory");
    g = __builtin_amdgcn_raw_buffer_load_b64(P.rws, WS_M + tok * 8, 0, AUX_SC1);
    if (__all(g.y == tag)) break;
    __builtin_amdgcn_s_sleep(4);
  }
  return g.x;
}

__device__ __forceinline__ void side_wave(const Ctx& P, const int ROLE) {
  LAS char* sm = P.sm;
  const int lane = P.lane, wave = P.wave, C = P.C, cg = P.cg;
  const __amdgpu_buffer_rsrc_t rq = mkrsrc(P.q, S * D * 2);
  u32 tag;
  if (wave != 7) {
    if (wave == 5) {
      const v4u g4 = {GMAGIC, GMAGIC, GMAGIC, GMAGIC};
      __builtin_amdgcn_raw_buffer_store_b128(g4, P.rws, lane < 32 ? gate_off(lane >> 3, P.b >> 4, P.b & 15) + (lane & 7) * 16 : OOB, 0, AUX_SC1);
      const v4u c4 = {CLR, CLR, CLR, CLR};
#pragma unroll
      for (int i = 0; i < 9; i++) __builtin_amdgcn_raw_buffer_store_b128(c4, P.rws, WS_U + lane * 16 + (i & 3) * 1024, C * TILE_B + (i >> 2) * 4096, AUX_SC1);
      STAMP(10);
      asm volatile("s_waitcnt vmcnt(0)" ::: "memory");
      lds_signal(sm, C_CLR);
      STAMP(13);
    } else {
      lds_wait(sm, C_TAG, 1);
    }
    if (ROLE == 0) {
      HCopy hc;
      hc.load(P, 2 * (wave - 5));
      STAMP(12);
      hc.store(P, 2 * (wave - 5));
      STAMP(3);
    } else if (ROLE == 2) {
      lds_wait(sm, C_PUB, 1);
      helper_prefetch(P);
    }
    if (wave == 5) {
      if (ROLE == 1) {
        Ffn ffn;
        lds_wait(sm, C_STAT, 1);
        ffn.load(P, 7);
        STAMP(3);
        ffn.run(P, 7);
      }
      return;
    }
    tag = *(const LAS u32*)(sm + L_REC);
    if (ROLE == 1) { lds_wait(sm, C_SEL, 1); __builtin_amdgcn_s_setprio(3); }
  } else {
    __builtin_amdgcn_s_setprio(3);
    const int m = lane & 15, g4 = lane >> 4;
    u64 epoch = __hip_atomic_load((GAS u64*)(P.ws + WS_E), __ATOMIC_RELAXED, __HIP_MEMORY_SCOPE_AGENT);
    __builtin_amdgcn_sched_barrier(0);
    v4u xa[9], wb[9];
    const int xo = (m & 7) * (D * 2) + (m >> 3) * 64 + g4 * 16;
#pragma unroll
    for (int i = 0; i < 9; i++) xa[i] = __builtin_amdgcn_raw_buffer_load_b128(rq, xo + i * 128, 8 * C * (D * 2), AUX_NT);
    __builtin_amdgcn_sched_barrier(0);
    const size_t pw = (size_t)P.wr + m * 64, po = (size_t)kOnes;
    const GAS char* wp = (const GAS char*)(m < 2 ? pw : po) + g4 * 16;
#pragma unroll
    for (int i = 0; i < 9; i++) wb[i] = *(const GAS v4u*)(wp + i * 128);
    STAMP(10);
    __builtin_amdgcn_sched_barrier(0);
    asm volatile("" : "+v"(epoch) :: "memory");
    epoch += 1;
    tag = (u32)epoch;
    if (lane == 0) *(LAS u32*)(sm + L_REC) = tag;
    lds_signal(sm, C_TAG);
    STAMP(1);
    f4 gx = {0.f, 0.f, 0.f, 0.f}, gw = {0.f, 0.f, 0.f, 0.f}, gs = {0.f, 0.f, 0.f, 0.f};
#pragma unroll
    for (int i = 0; i < 9; i++) gx = mfma16(xa[i], xa[i], gx);
    __builtin_amdgcn_sched_barrier(0);
    const u32 gold = gate_post(P, G_R, true, gate_inc(tag));
    STAMP(12);
    __builtin_amdgcn_sched_barrier(0);
#pragma unroll
    for (int i = 0; i < 9; i++) { gw = mfma16(wb[i], xa[i], gw); gs = mfma16(wb[i], wb[i], gs); }
    float sxx = (m & 2) ? ((m & 1) ? gx[3] : gx[2]) : ((m & 1) ? gx[1] : gx[0]);
    sxx = __uint_as_float(__builtin_amdgcn_ds_bpermute((m + (m & 12) * 4) * 4, __float_as_uint(sxx)));
    float sxw = m < 8 ? gw[0] : gw[1], sx = gw[2];
    sxw += dppf<0x128>(sxw);
    sx += dppf<0x128>(sx);
    sxx += dppf<0x128>(sxx);
    const float sw = __uint_as_float(__builtin_amdgcn_readlane(__float_as_uint(gs[0] + gs[1]), 2));
    const float mean = sx * (1.0f / D);
    const float var = fmaxf(fmaf(-mean, mean, sxx * (1.0f / D)), 0.f);
    const float mu = rbf(mean);
    const float sd = rbf(__builtin_amdgcn_sqrtf(rbf(rbf(var) + 1e-5f)));
    const float rs = __builtin_amdgcn_rcpf(sd);
    const u32 r = pk2(0.f, fmaf(-mu, sw, sxw) * rs);
    u32 key = ((r ^ (((int)r >> 31) | 0x80000000u)) & 0xFFFF0000u) | (u32)(2047 - (8 * C + (m & 7)));
    u32 msd = __float_as_uint(mu) | (__float_as_uint(sd) >> 16);
    asm volatile("" : "+v"(key), "+v"(msd));
    const v2u rec = {m < 8 ? key : msd, tag};
    const int ro = lane < 16 ? (m < 8 ? WS_R : WS_M) + (8 * C + (m & 7)) * 8 : OOB;
    lds_wait(sm, C_CLR, 1);
    __builtin_amdgcn_raw_buffer_store_b64(rec, P.rws, ro, 0, AUX_SC1);
    STAMP(2);
    if (lane < 8) { f2 st = {rs, -mu * rs}; *(LAS f2*)(sm + L_STAT + m * 8) = st; }
    {
      const int t = m & 7, t3 = (t * 11) >> 5;
      LAS char* xd = sm + L_U + t3 * UL_B + 512 + (t - 3 * t3) * (D * 2) + (m >> 3) * 64 + g4 * 16;
#pragma unroll
      for (int i = 0; i < 9; i++) *(LAS v4u*)(xd + i * 128) = xa[i];
    }
    lds_signal(sm, C_STAT);
    if (ROLE == 2) lds_signal(sm, C_PUB);
    gate_confirm(P, G_R, gold, gate_inc(tag));
    route(P, tag, epoch);
    if (ROLE != 1) __builtin_amdgcn_s_setprio(0);
  }
  if (ROLE == 1) {
    const int cw = wave - 6;
    const v4u s4 = *(const LAS v4u*)(sm + L_SEL + cw * 16);
    Row rr[4];
#pragma unroll
    for (int e = 0; e < 4; e++) {
      const int tok = 2047 - (int)(__builtin_amdgcn_readfirstlane(s4[e]) & 2047u);
      rr[e] = load_row<0>(rq, tok * (D * 2), lane);
    }
    const u32 ks = (lane & 2) ? ((lane & 1) ? s4.w : s4.z) : ((lane & 1) ? s4.y : s4.x);
    const u32 msd = load_msd(P, 2047 - (int)(ks & 2047u), tag);
    const float rsl = __builtin_amdgcn_rcpf(lo16(msd)), nml = -hi16(msd) * rsl;
#pragma unroll
    for (int e = 0; e < 4; e++) {
      const float rs = __uint_as_float(__builtin_amdgcn_readlane(__float_as_uint(rsl), e));
      const float nm = __uint_as_float(__builtin_amdgcn_readlane(__float_as_uint(nml), e));
      LAS char* dst = sm + L_XS + (cw * 4 + e) * XS_STRIDE;
      const Row& x = rr[e];
      v2u a = {pk2(fmaf(lo16(x.a0.x), rs, nm), fmaf(hi16(x.a0.x), rs, nm)), pk2(fmaf(lo16(x.a0.y), rs, nm), fmaf(hi16(x.a0.y), rs, nm))};
      v2u c = {pk2(fmaf(lo16(x.a1.x), rs, nm), fmaf(hi16(x.a1.x), rs, nm)), pk2(fmaf(lo16(x.a1.y), rs, nm), fmaf(hi16(x.a1.y), rs, nm))};
      *(LAS v2u*)(dst + lane * 8) = a;
      *(LAS v2u*)(dst + 512 + lane * 8) = c;
      *(LAS u16*)(dst + 1024 + lane * 2) = (u16)pk2(fmaf(lo16(x.a2), rs, nm), 0.f);
    }
    lds_signal(sm, C_XS);
    STAMP(6);
    ffn_act(P, wave);
  }
  if (wave == 6) {
    gate_wait(P, G_Y, 9u, gate_inc(tag));
    lds_signal(sm, C_GY);
    STAMP(7);
  } else if (ROLE == 0) {
    const int pi = lane & 7, ti = 4 * (pi & 1) + (pi & 2) + (pi >> 2);
    const u32 kx = ((LAS u32*)(sm + L_SEL))[ti];
    const int tok = 2047 - (int)(kx & 2047u);
    const v2u x2 = *(const GAS v2u*)((const GAS char*)P.q + (size_t)tok * (D * 2) + cg * 8);
    const v2u d2 = *(const GAS v2u*)((const GAS char*)P.dsk + cg * 8);
    const u32 msd = load_msd(P, tok, tag);
    const float rs = __builtin_amdgcn_rcpf(lo16(msd)), nm = -hi16(msd) * rs;
    const u32 rb = kx & 0xFFFF0000u;
    const float r = __uint_as_float((rb & 0x80000000u) ? (rb ^ 0x80000000u) : (~rb & 0xFFFF0000u));
    const float rsig = rbf(__builtin_amdgcn_rcpf(1.0f + __expf(-r)));
    STAMP(6);
    gate_wait(P, G_P, lane < 8 ? 5u : 4u, gate_inc(tag));
    STAMP(7);
    v4u g[9];
    const u32 tag12 = tag & 0xFFFu;
    u32 spin = 0;
    _Pragma("nounroll") for (; spin < SPIN_CAP; spin++) {
      asm volatile("" ::: "memory");
#pragma unroll
      for (int i = 0; i < 9; i++)
        g[i] = __builtin_amdgcn_raw_buffer_load_b128(P.rws, WS_P + (lane >> 3) * PART_B + cg * 128 + pi * 16, 8 * i * PART_B, AUX_SC1);
      u32 bad = 0;
#pragma unroll
      for (int i = 0; i < 9; i++)
#pragma unroll
        for (int e = 0; e < 4; e++) bad = __builtin_amdgcn_bitop3_b32(bad, g[i][e], tag12, 0xF6);
      if (__all((bad & 0xFFFu) == 0)) break;
      __builtin_amdgcn_s_sleep(4);
    }
    NOTE(25, spin + 1);
    STAMP(8);
    float sum[4];
#pragma unroll
    for (int e = 0; e < 4; e++) {
      float s = __uint_as_float(g[0][e] ^ tag12);
#pragma unroll
      for (int i = 1; i < 9; i++) s += __uint_as_float(g[i][e] ^ tag12);
      s += dppf<0x128>(s);
      sum[e] = xrow_sum(s);
    }
    lds_wait(sm, C_Y, 4);
    float o[4];
#pragma unroll
    for (int e = 0; e < 4; e++) {
      const float y = lo16(*(const LAS u16*)(sm + L_H + e * (HC_B + 64) + tok * 2));
      const float u = lo16(*(const LAS u16*)(sm + L_U + e * UL_B + (32 + (tok >> 3)) * 16 + (7 - (tok & 7)) * 2));
      const float x = (e & 1) ? hi16(x2[e >> 1]) : lo16(x2[e >> 1]);
      const float dk = (e & 1) ? hi16(d2[e >> 1]) : lo16(d2[e >> 1]);
      const float n = fmaf(x, rs, nm);
      o[e] = fmaf(n, fmaf(u, dk, y), x) + fmaf(rbf(sum[e]), rsig, n);
    }
    if (lane < 8) {
      v2u o2 = {pk2(o[0], o[1]), pk2(o[2], o[3])};
      *(GAS v2u*)((GAS char*)P.out + (size_t)tok * (D * 2) + cg * 8) = o2;
    }
  }
}

__global__ __launch_bounds__(NTHR, 2) void fused_kernel(
    const u16* __restrict__ q, const u16* __restrict__ k, const u16* __restrict__ v, const u16* __restrict__ shk,
    const u16* __restrict__ hk, const u16* __restrict__ dsk, const u16* __restrict__ wr, const u16* __restrict__ wg,
    const u16* __restrict__ wu, const u16* __restrict__ wd, u16* __restrict__ out, char* ws) {
  extern __shared__ __attribute__((aligned(16))) char smem_[];
  Ctx P;
  P.q = q; P.k = k; P.v = v; P.shk = shk; P.hk = hk; P.dsk = dsk; P.wr = wr; P.wg = wg; P.wu = wu; P.wd = wd;
  P.out = out; P.ws = ws;
  P.sm = (LAS char*)smem_;
  P.tid = threadIdx.x; P.lane = P.tid & 63;
  P.wave = __builtin_amdgcn_readfirstlane(P.tid >> 6);
  const int b = blockIdx.x;
  P.b = b;
  P.C = (b & 7) * 32 + (b >> 3);
  P.is_conv = b < NCONV;
  const bool is_moe = !P.is_conv && b < NCONV + NMOE;
  P.cg = (b & 7) * 18 + (b >> 3);
  P.mw = b - NCONV;
  P.rws = mkrsrc(ws, WS_END);
  const int lane = P.lane, wave = P.wave;
  (void)lane;
  STAMP(0); STAMPC(30);
  if (wave == 5) {
    ((LAS u32*)(P.sm + L_MISC))[lane] = 0u;
    ((LAS u32*)(P.sm + L_MISC))[64 + lane] = 0u;
  } else if (wave == 6) {
    const v4u z = {0u, 0u, 0u, 0u};
    *(LAS v4u*)(P.sm + L_U + (lane >> 4) * UL_B + (lane & 15) * 16) = z;
    *(LAS v4u*)(P.sm + L_U + (lane >> 4) * UL_B + 256 + (lane & 15) * 16) = z;
  }
  wg_barrier();
  STAMP(11);
  if (wave < 5) {
    if (is_moe) tile_wave<true>(P); else tile_wave<false>(P);
  } else {
    side_wave(P, P.is_conv ? 0 : (is_moe ? 1 : 2));
  }
  FLUSH();
}

}

extern "C" void kernel_launch(void* const* d_in, const int* in_sizes, int n_in, void* d_out, int out_size, void* d_ws,
                              size_t ws_size, hipStream_t stream) {
  (void)in_sizes; (void)n_in; (void)out_size; (void)ws_size;
  (void)hipFuncSetAttribute((const void*)fused_kernel, hipFuncAttributeMaxDynamicSharedMemorySize, L_TOTAL);
  fused_kernel<<<NWG, NTHR, L_TOTAL, stream>>>(
      (const u16*)d_in[0], (const u16*)d_in[1], (const u16*)d_in[2], (const u16*)d_in[3], (const u16*)d_in[4],
      (const u16*)d_in[5], (const u16*)d_in[7], (const u16*)d_in[8], (const u16*)d_in[9], (const u16*)d_in[10],
      (u16*)d_out, (char*)d_ws + (WS_AT_END ? ((ws_size - WS_END) & ~(size_t)0xFFFF) : 0));
}
```

```cpp
#include <hip/hip_runtime.h>
#include <hip/hip_bf16.h>

typedef unsigned int u32;
typedef unsigned long long u64;
typedef unsigned short u16;
typedef u32 v4u __attribute__((ext_vector_type(4)));
typedef u32 v2u __attribute__((ext_vector_type(2)));
typedef float f4 __attribute__((ext_vector_type(4)));
typedef float f2 __attribute__((ext_vector_type(2)));
typedef __bf16 bf8 __attribute__((ext_vector_type(8)));
typedef __bf16 bf2 __attribute__((ext_vector_type(2)));
#define LAS __attribute__((address_space(3)))
#define GAS __attribute__((address_space(1)))

namespace {

constexpr int S = 2048, D = 576, F = 4608;
constexpr int NWG = 256, NTHR = 512, NCONV = 144, NMOE = 72;
constexpr int AUX_SC1 = 16, AUX_NT = 0;
constexpr u32 CLR = 0xFFFFFFFFu, SAME = 0xFFFFFFFEu, MAXV = 0xFFFFFFFDu;
constexpr u32 SPIN_CAP = 1u << 16, GATE_CAP = 96;
constexpr u32 GMAGIC = 0x5AC3E900u;
constexpr int OOB = 0x7FFFFF00;
#define U_POST 0
#define WS_AT_END 0
#define H_DELAY 12
#define HELPERS 1

constexpr int WS_E = 0;
constexpr int WS_G = 4096;
constexpr int GATE_B = 256 * 256;
constexpr int WS_R = WS_G + 4 * GATE_B;
constexpr int WS_M = WS_R + S * 8;
constexpr int WS_U = WS_M + S * 8;
constexpr int TILE_B = D * 16;
constexpr int WS_P = WS_U + NWG * TILE_B;
constexpr int PART_B = NCONV * 128;
constexpr int WS_END = WS_P + NMOE * PART_B;
constexpr int WS_DBG = (WS_END + 4095) / 4096 * 4096;
enum { G_R = 0, G_U, G_Y, G_P };
#define ONES8 0x3F803F80u, 0x3F803F80u, 0x3F803F80u, 0x3F803F80u, 0x3F803F80u, 0x3F803F80u, 0x3F803F80u, 0x3F803F80u
#define ONES32 ONES8, ONES8, ONES8, ONES8
__device__ const u32 kOnes[288] __attribute__((aligned(128))) = {ONES32, ONES32, ONES32, ONES32, ONES32, ONES32, ONES32, ONES32, ONES32};

constexpr int HC_B = 2080 * 16;
constexpr int L_H = 0;
constexpr int UL_B = 290 * 16;
constexpr int L_U = L_H + 4 * HC_B;
constexpr int L_STG = L_U + 4 * UL_B;
constexpr int L_MISC = L_STG + TILE_B;
constexpr int L_CNT = L_MISC;
constexpr int L_CAND = L_CNT + 64;
constexpr int L_SEL = L_CAND + 288;
constexpr int L_REC = L_SEL + 32;
constexpr int L_STAT = L_REC + 64;
constexpr int L_END = L_STAT + 64;
constexpr int XS_STRIDE = 1184;
constexpr int L_XS = 0;
constexpr int L_RED = L_XS + 8 * XS_STRIDE;
constexpr int L_HB = L_RED + 6 * 8 * 128 * 4;
constexpr int HB_STRIDE = 160;
static_assert(L_END + 1024 <= 160 * 1024, "lds");
static_assert(L_HB + 8 * HB_STRIDE <= L_U, "lds");
static_assert(L_END - L_MISC == 512, "zero fill");

__device__ __forceinline__ constexpr int xrow_off(int t) { return L_U + (t / 3) * UL_B + 512 + (t % 3) * (D * 2); }
enum { C_CLR = 0, C_STAT, C_TRUST, C_GU, C_SEL, C_H, C_U, C_Y, C_XS, C_RED, C_HB, C_GY, C_PUB, C_TAG, C_QQ };

__device__ __forceinline__ float lo16(u32 x) { return __uint_as_float(x << 16); }
__device__ __forceinline__ float hi16(u32 x) { return __uint_as_float(x & 0xFFFF0000u); }
__device__ __forceinline__ u32 pk2(float a, float b) {
  bf2 p = {(__bf16)a, (__bf16)b};
  return __builtin_bit_cast(u32, p);
}
__device__ __forceinline__ float rbf(float a) { return lo16(pk2(a, a)); }
__device__ __forceinline__ float dot2(u32 a, u32 b, float c) {
  return __builtin_amdgcn_fdot2_f32_bf16(__builtin_bit_cast(bf2, a), __builtin_bit_cast(bf2, b), c, false);
}
template <int CTRL> __device__ __forceinline__ u32 dppu(u32 x) {
  return (u32)__builtin_amdgcn_mov_dpp((int)x, CTRL, 0xf, 0xf, true);
}
template <int CTRL> __device__ __forceinline__ float dppf(float x) { return __uint_as_float(dppu<CTRL>(__float_as_uint(x))); }
__device__ __forceinline__ float xrow_sum(float x) {
  auto s = __builtin_amdgcn_permlane16_swap(__float_as_uint(x), __float_as_uint(x), false, false);
  x = __uint_as_float(s[0]) + __uint_as_float(s[1]);
  auto t = __builtin_amdgcn_permlane32_swap(__float_as_uint(x), __float_as_uint(x), false, false);
  return __uint_as_float(t[0]) + __uint_as_float(t[1]);
}
__device__ __forceinline__ u32 umax(u32 a, u32 b) { return a > b ? a : b; }
__device__ __forceinline__ u32 umin(u32 a, u32 b) { return a < b ? a : b; }
__device__ __forceinline__ u32 umed3(u32 a, u32 b, u32 c) { return umax(umin(a, b), umin(umax(a, b), c)); }
__device__ __forceinline__ u32 xrow_umin(u32 x) {
  auto s = __builtin_amdgcn_permlane16_swap(x, x, false, false);
  x = umin(s[0], s[1]);
  auto t = __builtin_amdgcn_permlane32_swap(x, x, false, false);
  return umin(t[0], t[1]);
}
__device__ __forceinline__ u32 wave_umax(u32 x) {
  x = umax(x, dppu<0xB1>(x));
  x = umax(x, dppu<0x4E>(x));
  x = umax(x, dppu<0x141>(x));
  x = umax(x, dppu<0x140>(x));
  auto s = __builtin_amdgcn_permlane16_swap(x, x, false, false);
  x = umax(s[0], s[1]);
  auto t = __builtin_amdgcn_permlane32_swap(x, x, false, false);
  return umax(t[0], t[1]);
}
__device__ __forceinline__ void lds_signal(LAS char* sm, int c) {
  u64 keep;
  asm volatile("s_waitcnt lgkmcnt(0)\n\ts_mov_b64 %0, exec\n\ts_mov_b64 exec, 1\n\tds_add_u32 %1, %2\n\ts_mov_b64 exec, %0"
               : "=&s"(keep) : "v"((u32)(size_t)(sm + L_CNT + 4 * c)), "v"(1u) : "memory");
}
__device__ __forceinline__ void lds_wait(LAS char* sm, int c, u32 target) {
  asm volatile("" ::: "memory");
  while ((u32)__builtin_amdgcn_readfirstlane(
             __hip_atomic_load((LAS u32*)(sm + L_CNT) + c, __ATOMIC_RELAXED, __HIP_MEMORY_SCOPE_WORKGROUP)) < target)
    __builtin_amdgcn_s_sleep(1);
  asm volatile("" ::: "memory");
}
__device__ __forceinline__ void wg_barrier() { asm volatile("s_waitcnt lgkmcnt(0)\n\ts_barrier" ::: "memory"); }
__device__ __forceinline__ u32 umax3(u32 a, u32 b, u32 c) { return umax(umax(a, b), c); }
__device__ __forceinline__ u32 vmax4(v4u g, u32 acc) { return umax3(umax3(g.x, g.y, acc), g.z, g.w); }
__device__ __forceinline__ f4 mfma16(v4u a, v4u b, f4 c) {
  return __builtin_amdgcn_mfma_f32_16x16x32_bf16(__builtin_bit_cast(bf8, a), __builtin_bit_cast(bf8, b), c, 0, 0, 0);
}
__device__ __forceinline__ __amdgpu_buffer_rsrc_t mkrsrc(const void* p, int bytes) {
  return __builtin_amdgcn_make_buffer_rsrc((void*)p, 0, bytes, 0x00020000);
}

#define DBG_TS 1
#define STAMP(i) do { const u32 t_ = (u32)__builtin_amdgcn_s_memrealtime() | 1u; if (P.lane == 0) ((LAS u32*)(P.sm + L_END))[P.wave * 32 + (i)] = t_; } while (0)
#define STAMPC(i) do { const u32 t_ = (u32)__builtin_amdgcn_s_memtime(); if (P.lane == 0) ((LAS u32*)(P.sm + L_END))[P.wave * 32 + (i)] = t_; } while (0)
#define FLUSH() do { STAMP(29); if (P.wave == 0 && P.is_conv) { asm volatile("s_waitcnt lgkmcnt(0)" ::: "memory"); const u32 a_ = ((LAS u32*)(P.sm + L_END))[P.wave * 32 + 11], z_ = ((LAS u32*)(P.sm + L_END))[P.wave * 32], e_ = ((LAS u32*)(P.sm + L_END))[P.wave * 32 + 29]; const u32 d_ = __builtin_amdgcn_readfirstlane((a_ - z_) * 4), s_ = __builtin_amdgcn_readfirstlane(e_); while ((u32)__builtin_amdgcn_s_memrealtime() - s_ < d_ && d_ < 20000u) __builtin_amdgcn_s_sleep(1); } } while (0)
#define NOTE(i, v) do { if (P.lane == 0) ((LAS u32*)(P.sm + L_END))[P.wave * 32 + (i)] = (v); } while (0)
constexpr int L_TOTAL = L_END + 1024;

struct Ctx {
  const u16 *q, *k, *v, *shk, *hk, *dsk, *wr, *wg, *wu, *wd;
  u16* out;
  char* ws;
  __amdgpu_buffer_rsrc_t rws;
  LAS char* sm;
  int tid, lane, wave, b, C, cg, mw;
  bool is_conv;
};

__device__ __forceinline__ int gate_off(int gate, int rep, int shard) { return WS_G + gate * GATE_B + (rep * 16 + shard) * 256; }
__device__ __forceinline__ u32 gate_add(const Ctx& P, int off, int val) {
  return (u32)__builtin_amdgcn_raw_ptr_buffer_atomic_add_i32(val, P.rws, off, 0, 0);
}
__device__ __forceinline__ u32 gate_post(const Ctx& P, int gate, bool on, u32 inc) {
  return gate_add(P, (on && P.lane < 16) ? gate_off(gate, P.lane, P.b & 15) : OOB, (int)inc);
}
__device__ __forceinline__ void gate_confirm(const Ctx& P, int gate, u32 old, u32 inc) {
  bool bad = P.lane < 16;
  _Pragma("nounroll") for (u32 spin = 0; spin < GATE_CAP; spin++) {
    const u32 d = old - GMAGIC;
    bad = bad && ((d & 0xF0u) != 0u || d != (d & 0xFu) * inc);
    if (!__any(bad)) break;
    NOTE(26, spin + 1);
    __builtin_amdgcn_s_sleep(2);
    old = gate_add(P, bad ? gate_off(gate, P.lane, P.b & 15) : OOB, (int)inc);
  }
}
__device__ __forceinline__ void gate_wait(const Ctx& P, int gate, u32 expect, u32 inc) {
  const int off = P.lane < 16 ? gate_off(gate, P.b >> 4, P.lane) : OOB;
  const u32 want = P.lane < 16 ? GMAGIC + expect * inc : 0u;
  _Pragma("nounroll") for (u32 spin = 0; spin < GATE_CAP; spin++)
    if (__all(gate_add(P, off, 0) == want)) break;
}
__device__ __forceinline__ u32 gate_inc(u32 tag) { return 1u + (tag << 8); }

struct Row { v2u a0, a1; u32 a2; };
template <int AUX> __device__ __forceinline__ Row load_row(__amdgpu_buffer_rsrc_t r, int soff, int lane) {
  Row x;
  x.a0 = __builtin_amdgcn_raw_buffer_load_b64(r, lane * 8, soff, AUX);
  x.a1 = __builtin_amdgcn_raw_buffer_load_b64(r, lane * 8 + 512, soff, AUX);
  x.a2 = __builtin_amdgcn_raw_buffer_load_b16(r, lane * 2 + 1024, soff, AUX);
  return x;
}
__device__ __forceinline__ void hcopy_store(LAS char* dst, v4u lo, v4u hi, bool first) {
  u32 d[8], o[7];
#pragma unroll
  for (int e = 0; e < 4; e++) { d[e] = lo[e]; d[4 + e] = hi[e]; }
#pragma unroll
  for (int e = 0; e < 7; e++) o[e] = __builtin_amdgcn_alignbit(d[e + 1], d[e], 16);
  constexpr int BASE[8] = {0, 260, 519, 778, 1037, 1296, 1556, 1815};
#pragma unroll
  for (int e = 0; e < 4; e++) {
    v4u a = {d[e], d[e + 1], d[e + 2], d[e + 3]}, c = {o[e], o[e + 1], o[e + 2], o[e + 3]};
    if (e > 0 || first) *(LAS v4u*)(dst + BASE[2 * e] * 16) = a;
    *(LAS v4u*)(dst + BASE[2 * e + 1] * 16) = c;
  }
}
struct HCopy {
  v4u lo[8];
  __device__ __forceinline__ void load(const Ctx& P, int ch0) {
    const __amdgpu_buffer_rsrc_t rh = mkrsrc(P.hk + (size_t)(4 * P.cg + ch0) * S, 2 * S * 2);
#pragma unroll
    for (int r = 0; r < 8; r++) lo[r] = __builtin_amdgcn_raw_buffer_load_b128(rh, P.lane * 16 + (r & 3) * 1024, (r >> 2) * 4096, AUX_NT);
  }
  __device__ __forceinline__ void store(const Ctx& P, int ch0) {
    LAS char* dst = P.sm + L_H + ch0 * HC_B + (P.lane + 2) * 16;
#pragma unroll
    for (int r = 0; r < 8; r++) *(LAS v4u*)(dst + (r >> 2) * HC_B + (r & 3) * 1024) = lo[r];
    {
      const int c = P.lane & 1, m1 = (P.lane >> 1) & 1;
      const v4u z = {0u, 0u, 0u, 0u};
      v4u h0 = *(const LAS v4u*)(P.sm + L_H + (ch0 + c) * HC_B + 32);
      h0 = m1 ? h0 : z;
      if (P.lane < 4) hcopy_store(P.sm + L_H + (ch0 + c) * HC_B + m1 * 16, z, h0, true);
    }
    v4u hi[8];
#pragma unroll
    for (int r = 0; r < 8; r++) hi[r] = *(const LAS v4u*)(dst + (r >> 2) * HC_B + (r & 3) * 1024 + 16);
#pragma unroll
    for (int r = 0; r < 8; r++) hcopy_store(dst + (r >> 2) * HC_B + (r & 3) * 1024, lo[r], hi[r], false);
    lds_signal(P.sm, C_H);
  }
};

__device__ __forceinline__ void ffn_act(const Ctx& P, int tk) {
  LAS char* sm = P.sm;
  lds_wait(sm, C_RED, 6);
  float G = 0.f, U = 0.f;
#pragma unroll
  for (int w = 0; w < 6; w++) {
    G += *(const LAS float*)(sm + L_RED + ((w * 8 + tk) * 128 + P.lane) * 4);
    U += *(const LAS float*)(sm + L_RED + ((w * 8 + tk) * 128 + 64 + P.lane) * 4);
  }
  G = rbf(G);
  const float sg = rbf(G * __builtin_amdgcn_rcpf(1.0f + __expf(-G)));
  *(LAS u16*)(sm + L_HB + tk * HB_STRIDE + P.lane * 2) = (u16)pk2(sg * rbf(U), 0.f);
  lds_signal(sm, C_HB);
}

struct Ffn {
  v4u W1[3][8];
  v2u W2[2][2][8];
  __device__ __forceinline__ void load(const Ctx& P, int cu0) {
    const int n16 = P.lane & 15, g4 = P.lane >> 4;
    const GAS char* wp = (const GAS char*)(n16 < 8 ? P.wg : P.wu) + ((size_t)(96 * P.wave + 8 * g4) * F + 64 * P.mw + 8 * (n16 & 7)) * 2;
#pragma unroll
    for (int i = 0; i < 3; i++)
#pragma unroll
      for (int jj = 0; jj < 8; jj++) W1[i][jj] = *(const GAS v4u*)(wp + (size_t)(32 * i + jj) * (F * 2));
    __builtin_amdgcn_sched_barrier(0);
    const __amdgpu_buffer_rsrc_t rd = mkrsrc(P.wd + (size_t)64 * P.mw * D, 64 * D * 2);
    const int o2 = (8 * g4 * D + 64 * cu0 + 4 * n16) * 2;
#pragma unroll
    for (int ci = 0; ci < 2; ci++)
      if (ci == 0 || P.wave >= 3) {
#pragma unroll
        for (int ks = 0; ks < 2; ks++)
#pragma unroll
          for (int jj = 0; jj < 8; jj++)
            W2[ci][ks][jj] = __builtin_amdgcn_raw_buffer_load_b64(rd, o2 + (jj & 3) * (D * 2) + ci * 128, (32 * ks + (jj & 4)) * (D * 2), AUX_NT);
      }
  }
  __device__ __forceinline__ void run(const Ctx& P, int cu0) {
    LAS char* sm = P.sm;
    const int lane = P.lane, wave = P.wave, n16 = lane & 15, g4 = lane >> 4;
    v4u Bf[3][8];
#pragma unroll
    for (int i = 0; i < 3; i++)
#pragma unroll
      for (int e = 0; e < 8; e++) {
#pragma unroll
        for (int d = 0; d < 4; d++)
          Bf[i][e][d] = __builtin_amdgcn_perm(W1[i][2 * d + 1][e >> 1], W1[i][2 * d][e >> 1], (e & 1) ? 0x07060302u : 0x05040100u);
        asm volatile("" : "+v"(Bf[i][e]));
      }
    STAMP(4);
    lds_wait(sm, C_XS, 2);
    STAMP(5);
    const u32 tag = *(const LAS u32*)(sm + L_REC), tag12 = tag & 0xFFFu;
    f4 acc[8];
#pragma unroll
    for (int e = 0; e < 8; e++) acc[e] = (f4){0.f, 0.f, 0.f, 0.f};
#pragma unroll
    for (int i = 0; i < 3; i++) {
      const v4u A = *(const LAS v4u*)(sm + L_XS + (n16 & 7) * XS_STRIDE + (96 * wave + 32 * i + 8 * g4) * 2);
#pragma unroll
      for (int e = 0; e < 8; e++) acc[e] = mfma16(A, Bf[i][e], acc[e]);
    }
    {
      LAS char* dst = sm + L_RED + ((wave * 8 + 4 * (g4 & 1) + (g4 & 2)) * 128 + 8 * n16) * 4;
#pragma unroll
      for (int r = 0; r < 2; r++) {
        f4 a, c;
#pragma unroll
        for (int e = 0; e < 4; e++) { a[e] = g4 >= 2 ? acc[e][2 + r] : acc[e][r]; c[e] = g4 >= 2 ? acc[4 + e][2 + r] : acc[4 + e][r]; }
        *(LAS f4*)(dst + r * 512) = a;
        *(LAS f4*)(dst + r * 512 + 16) = c;
      }
    }
    lds_signal(sm, C_RED);
    v4u Bd[2][2][4];
#pragma unroll
    for (int ci = 0; ci < 2; ci++)
      if (ci == 0 || wave >= 3) {
#pragma unroll
        for (int ks = 0; ks < 2; ks++)
#pragma unroll
          for (int e = 0; e < 4; e++) {
#pragma unroll
            for (int d = 0; d < 4; d++)
              Bd[ci][ks][e][d] = __builtin_amdgcn_perm(W2[ci][ks][2 * d + 1][e >> 1], W2[ci][ks][2 * d][e >> 1], (e & 1) ? 0x07060302u : 0x05040100u);
            asm volatile("" : "+v"(Bd[ci][ks][e]));
          }
      }
    asm volatile("s_waitcnt vmcnt(0)" ::: "memory");
    (void)gate_post(P, G_P, wave == 5, gate_inc(tag));
    ffn_act(P, wave);
    STAMP(6);
    lds_wait(sm, C_HB, 8);
    const v4u A0 = *(const LAS v4u*)(sm + L_HB + (n16 & 7) * HB_STRIDE + g4 * 16);
    const v4u A1 = *(const LAS v4u*)(sm + L_HB + (n16 & 7) * HB_STRIDE + 64 + g4 * 16);
#pragma unroll
    for (int ci = 0; ci < 2; ci++) {
      if (ci == 1 && wave < 3) break;
      f4 ad[4];
#pragma unroll
      for (int e = 0; e < 4; e++) ad[e] = mfma16(A0, Bd[ci][0][e], (f4){0.f, 0.f, 0.f, 0.f});
#pragma unroll
      for (int e = 0; e < 4; e++) ad[e] = mfma16(A1, Bd[ci][1][e], ad[e]);
      v4u s0, s1;
#pragma unroll
      for (int e = 0; e < 4; e++) {
        const u32 x = ((__float_as_uint(g4 >= 2 ? ad[e][2] : ad[e][0]) + 0x800u) & 0xFFFFF000u) | tag12;
        const u32 y = ((__float_as_uint(g4 >= 2 ? ad[e][3] : ad[e][1]) + 0x800u) & 0xFFFFF000u) | tag12;
        s0[e] = (u32)__builtin_amdgcn_update_dpp((int)x, (int)y, 0x128, 0xf, 0xc, false);
        s1[e] = (u32)__builtin_amdgcn_update_dpp((int)y, (int)x, 0x128, 0xf, 0x3, false);
      }
      const int off = WS_P + P.mw * PART_B + (16 * (cu0 + ci) + (n16 & 7)) * 128 + (n16 >> 3) * 64 + g4 * 16;
      __builtin_amdgcn_raw_buffer_store_b128(s0, P.rws, off, 0, AUX_SC1);
      __builtin_amdgcn_raw_buffer_store_b128(s1, P.rws, off + 1024, 0, AUX_SC1);
    }
    STAMP(7);
  }
};

__device__ __forceinline__ void helper_prefetch(const Ctx& P) {
  const int g = P.b & 7, idx = (P.b - NCONV - NMOE) >> 3;
  v4u acc = {0u, 0u, 0u, 0u};
  v4u t[19];
#pragma unroll
  for (int i = 0; i < 19; i++) {
    const int u = (i * 5 + idx) * 7 + P.wave;
    const int sl = u / 72;
    const __amdgpu_buffer_rsrc_t rd = mkrsrc(P.wd, F * D * 2);
    t[i] = __builtin_amdgcn_raw_buffer_load_b128(rd, u < 648 ? P.lane * 16 : OOB, (sl * 8 + g) * (64 * D * 2) + (u - sl * 72) * 1024, AUX_NT);
  }
#pragma unroll
  for (int i = 0; i < 19; i++) acc |= t[i];
  asm volatile("" :: "v"(acc));
}

__device__ __forceinline__ void conv_phase(const Ctx& P) {
  LAS char* sm = P.sm;
  const int tid = P.tid, lane = P.lane, wave = P.wave, n16 = lane & 15, g4 = lane >> 4, gch = tid & 3;
  const int goff = WS_U + (tid >> 2) * TILE_B + (4 * P.cg + gch) * 16;
  const u32 inc = gate_inc(*(const LAS u32*)(sm + L_REC));
  if (wave == 0) {
    gate_wait(P, G_U, 16u, inc);
    lds_signal(sm, C_GU);
  } else {
    lds_wait(sm, C_GU, 1);
  }
  STAMP(3);
  lds_wait(sm, C_TRUST, 1);
  STAMP(4);
  {
    v4u g[4];
    u32 spin = 0;
    _Pragma("nounroll") for (; spin < SPIN_CAP; spin++) {
      asm volatile("" ::: "memory");
#pragma unroll
      for (int it = 0; it < 4; it++) g[it] = __builtin_amdgcn_raw_buffer_load_b128(P.rws, goff, it * 64 * TILE_B, AUX_SC1);
      u32 mx = 0;
#pragma unroll
      for (int it = 0; it < 4; it++) mx = vmax4(g[it], mx);
      if (__all(mx != CLR)) break;
      __builtin_amdgcn_s_sleep(4);
    }
    NOTE(24, spin + 1);
    asm volatile("s_waitcnt vmcnt(0)" ::: "memory");
    lds_wait(sm, C_QQ, 5);
#pragma unroll
    for (int it = 0; it < 4; it++) *(LAS v4u*)(sm + L_U + gch * UL_B + (32 + it * 64 + (tid >> 2)) * 16) = g[it];
  }
  lds_signal(sm, C_U);
  STAMP(5);
  lds_wait(sm, C_H, 2);
  lds_wait(sm, C_U, 4);
  STAMP(6);
  {
    const int p1 = n16 + 1, kap = p1 & 7;
    const int base16 = 258 * kap + ((0x98654320u >> (4 * kap)) & 15);
    const LAS char* pa = sm + L_H + wave * HC_B + (base16 + g4 + (p1 >> 3)) * 16;
    const LAS char* pb = sm + L_U + wave * UL_B + (32 + 2 * n16 + 1 - g4 - 28) * 16;
    f4 acc[8];
#pragma unroll
    for (int t = 0; t < 8; t++) acc[t] = (f4){0.f, 0.f, 0.f, 0.f};
    v4u A[2][8], B[2][8];
#pragma unroll
    for (int a = 0; a < 8; a++) {
      A[0][a] = *(const LAS v4u*)(pa + a * 512);
      B[0][a] = *(const LAS v4u*)(pb + a * 512 + 7 * 64);
    }
#pragma unroll
    for (int bb = 0; bb < 8; bb++) {
      __builtin_amdgcn_sched_barrier(0);
      if (bb < 7) {
#pragma unroll
        for (int a = 0; a < 8; a++) {
          A[(bb + 1) & 1][a] = *(const LAS v4u*)(pa + a * 512 + (bb + 1) * 64);
          B[(bb + 1) & 1][a] = *(const LAS v4u*)(pb + a * 512 + (6 - bb) * 64);
        }
      }
#pragma unroll
      for (int t = 0; t < 8; t++)
#pragma unroll
        for (int a = 0; a <= t; a++) acc[t] = mfma16(A[bb & 1][a], B[bb & 1][t - a], acc[t]);
      if (bb < 7) {
#pragma unroll
        for (int i = 0; i < 16; i++) {
          __builtin_amdgcn_sched_group_barrier(0x8, 2, 0);
          __builtin_amdgcn_sched_group_barrier(0x100, 1, 0);
        }
        __builtin_amdgcn_sched_group_barrier(0x8, 4, 0);
      }
    }
    __builtin_amdgcn_sched_barrier(0);
    (void)gate_post(P, G_Y, wave == 0, inc);
    LAS char* yb = sm + L_H + wave * (HC_B + 64) + n16 * 32 + g4 * 8;
#pragma unroll
    for (int t = 0; t < 8; t++) {
      v2u y2 = {umin(pk2(acc[t][0], acc[t][1]), MAXV), umin(pk2(acc[t][2], acc[t][3]), MAXV)};
      *(LAS v2u*)(yb + t * 512) = y2;
    }
  }
  lds_signal(sm, C_Y);
  STAMP(7);
  v4u u4[4];
#pragma unroll
  for (int it = 0; it < 4; it++) u4[it] = *(const LAS v4u*)(sm + L_U + gch * UL_B + (32 + it * 64 + (tid >> 2)) * 16);
  lds_wait(sm, C_Y, 4);
#pragma unroll
  for (int it = 0; it < 4; it++) {
    const v4u y4 = *(const LAS v4u*)(sm + L_H + gch * (HC_B + 64) + (it * 64 + (tid >> 2)) * 16);
    v4u w4;
#pragma unroll
    for (int e = 0; e < 4; e++) w4[e] = (y4[e] == u4[it][e]) ? SAME : y4[e];
    __builtin_amdgcn_raw_buffer_store_b128(w4, P.rws, goff, it * 64 * TILE_B, AUX_SC1);
  }
  STAMP(8);
}

template <bool MOE> __device__ __forceinline__ void tile_wave(const Ctx& P) {
  LAS char* sm = P.sm;
  const int tid = P.tid, lane = P.lane, wave = P.wave, C = P.C;
  const bool act = tid < 288;
  const int j4 = min(tid, 287) * 4;
  const __amdgpu_buffer_rsrc_t rk = mkrsrc(P.k, S * D * 2), rv = mkrsrc(P.v, S * D * 2);
  const int row0 = 8 * C * (D * 2);
  u32 kk[11], vv[8], qq[8];
#pragma unroll
  for (int i = 0; i < 3; i++) kk[i] = __builtin_amdgcn_raw_buffer_load_b32(rk, row0 + (i - 3) * (D * 2) + j4, 0, AUX_NT);
#pragma unroll
  for (int i = 0; i < 8; i++) {
    kk[3 + i] = __builtin_amdgcn_raw_buffer_load_b32(rk, j4 + (i & 3) * (D * 2), row0 + (i & 4) * (D * 2), AUX_NT);
    vv[i] = __builtin_amdgcn_raw_buffer_load_b32(rv, j4 + (i & 3) * (D * 2), row0 + (i & 4) * (D * 2), AUX_NT);
  }
  const v4u sk = *(const GAS v4u*)((const GAS char*)P.shk + j4 * 4);
  STAMP(10);
  lds_wait(sm, C_TAG, 1);
  const u32 uinc = gate_inc(*(const LAS u32*)(sm + L_REC));
  __builtin_amdgcn_sched_barrier(0);
  u32 gold;
  asm volatile("" :: "v"(sk) : "memory");
  gold = gate_post(P, G_U, wave == 4, uinc);
  STAMP(12);
  u32 up[2][4];
  {
    float w0[4] = {lo16(sk.x), hi16(sk.x), lo16(sk.y), hi16(sk.y)};
    float w1[4] = {lo16(sk.z), hi16(sk.z), lo16(sk.w), hi16(sk.w)};
    float u0[8], u1[8];
#pragma unroll
    for (int s = 0; s < 8; s++) {
      float a = lo16(kk[s]) * w0[3], c = hi16(kk[s]) * w1[3];
      a = fmaf(lo16(kk[s + 1]), w0[2], a); c = fmaf(hi16(kk[s + 1]), w1[2], c);
      a = fmaf(lo16(kk[s + 2]), w0[1], a); c = fmaf(hi16(kk[s + 2]), w1[1], c);
      a = fmaf(lo16(kk[s + 3]), w0[0], a); c = fmaf(hi16(kk[s + 3]), w1[0], c);
      u0[s] = a * lo16(vv[s]); u1[s] = c * hi16(vv[s]);
    }
#pragma unroll
    for (int i = 0; i < 4; i++) {
      up[0][i] = umin(pk2(u0[7 - 2 * i], u0[6 - 2 * i]), MAXV);
      up[1][i] = umin(pk2(u1[7 - 2 * i], u1[6 - 2 * i]), MAXV);
    }
    STAMP(1);
    LAS char* stg = sm + L_STG + wave * 2048;
    v4u p0 = {up[0][0], up[0][1], up[0][2], up[0][3]}, p1 = {up[1][0], up[1][1], up[1][2], up[1][3]};
    if (act) { *(LAS v4u*)(stg + lane * 32) = p0; *(LAS v4u*)(stg + lane * 32 + 16) = p1; }
    asm volatile("" ::: "memory");
    p0 = *(const LAS v4u*)(stg + lane * 16);
    p1 = *(const LAS v4u*)(stg + (wave < 4 ? 1024 : 0) + lane * 16);
    STAMP(13);
    lds_wait(sm, C_CLR, 1);
    const int so = WS_U + C * TILE_B + wave * 2048 + lane * 16;
    __builtin_amdgcn_raw_buffer_store_b128(p0, P.rws, so, 0, AUX_SC1);
    __builtin_amdgcn_raw_buffer_store_b128(p1, P.rws, wave < 4 ? so + 1024 : OOB, 0, AUX_SC1);
  }
  __builtin_amdgcn_sched_barrier(0);
  if (MOE) lds_signal(sm, C_PUB);
  STAMP(2);
  lds_wait(sm, C_STAT, 1);
#pragma unroll
  for (int i = 0; i < 8; i++) qq[i] = *(const LAS u32*)(sm + xrow_off(i) + j4);
  lds_signal(sm, C_QQ);
#pragma unroll
  for (int i = 0; i < 8; i++) asm volatile("" : "+v"(qq[i]));
  const u32 dd = *(const GAS u32*)((const GAS char*)P.dsk + j4);
  if (wave == 4) gate_confirm(P, G_U, gold, uinc);
  if (MOE) {
    Ffn ffn;
    const int cu0 = wave < 3 ? wave : 2 * wave - 3;
    lds_wait(sm, C_PUB, 5);
    ffn.load(P, cu0);
    STAMP(3);
    ffn.run(P, cu0);
  } else if (P.is_conv) {
    if (wave < 4) conv_phase(P);
  } else {
    lds_wait(sm, C_PUB, 1);
    helper_prefetch(P);
  }

  float xn[2][8], c0[2][8];
  {
    const float d0 = lo16(dd), d1 = hi16(dd);
#pragma unroll
    for (int s2 = 0; s2 < 4; s2++) {
      const f4 st = *(const LAS f4*)(sm + L_STAT + s2 * 16);
#pragma unroll
      for (int e = 0; e < 2; e++) {
        const int s = 2 * s2 + e;
        const float xa = lo16(qq[s]), xb = hi16(qq[s]);
        const float na = fmaf(xa, st[2 * e], st[2 * e + 1]), nb = fmaf(xb, st[2 * e], st[2 * e + 1]);
        const u32 pa = up[0][(7 - s) >> 1], pb = up[1][(7 - s) >> 1];
        const float ua = ((7 - s) & 1) ? hi16(pa) : lo16(pa), ub = ((7 - s) & 1) ? hi16(pb) : lo16(pb);
        xn[0][s] = na; xn[1][s] = nb;
        c0[0][s] = fmaf(na, fmaf(ua, d0, 1.0f), xa);
        c0[1][s] = fmaf(nb, fmaf(ub, d1, 1.0f), xb);
        asm volatile("" : "+v"(xn[0][s]), "+v"(xn[1][s]), "+v"(c0[0][s]), "+v"(c0[1][s]));
      }
    }
  }
  lds_wait(sm, C_SEL, 1);
  u32 skip = 0;
  {
    const v4u s0 = *(const LAS v4u*)(sm + L_SEL), s1 = *(const LAS v4u*)(sm + L_SEL + 16);
#pragma unroll
    for (int e = 0; e < 4; e++) {
      const u32 t0 = 2047u - (s0[e] & 2047u), t1 = 2047u - (s1[e] & 2047u);
      skip |= ((int)(t0 >> 3) == C ? 1u : 0u) << (t0 & 7);
      skip |= ((int)(t1 >> 3) == C ? 1u : 0u) << (t1 & 7);
    }
    if (!act) skip = 0xFF;
  }
  const __amdgpu_buffer_rsrc_t ro = mkrsrc(P.out, S * D * 2);
  int ooff[8];
#pragma unroll
  for (int s = 0; s < 8; s++) { ooff[s] = ((skip >> s) & 1) ? OOB : row0 + s * (D * 2) + j4; asm volatile("" : "+v"(ooff[s])); }
  const int uoff = WS_U + C * TILE_B + j4 * 8;
  if (!MOE && !(P.is_conv && wave < 4)) asm volatile("s_waitcnt vmcnt(0)" ::: "memory");
  lds_wait(sm, C_GY, 1);
  STAMP(9);
  v4u y0, y1;
  u32 spin = 0;
  _Pragma("nounroll") for (; spin < SPIN_CAP; spin++) {
    asm volatile("" ::: "memory");
    y0 = __builtin_amdgcn_raw_buffer_load_b128(P.rws, uoff, 0, AUX_SC1);
    y1 = __builtin_amdgcn_raw_buffer_load_b128(P.rws, uoff + 16, 0, AUX_SC1);
    bool ok = vmax4(y0, vmax4(y1, 0u)) != CLR;
#pragma unroll
    for (int e = 0; e < 4; e++) ok = ok & (y0[e] != up[0][e]) & (y1[e] != up[1][e]);
    if (__all(ok)) break;
    __builtin_amdgcn_s_sleep(4);
  }
  NOTE(25, spin + 1);
#pragma unroll
  for (int e = 0; e < 4; e++) {
    const u32 a = y0[e] == SAME ? up[0][e] : y0[e], c = y1[e] == SAME ? up[1][e] : y1[e];
    const u32 o0 = pk2(fmaf(xn[0][2 * e], lo16(a), c0[0][2 * e]), fmaf(xn[1][2 * e], lo16(c), c0[1][2 * e]));
    const u32 o1 = pk2(fmaf(xn[0][2 * e + 1], hi16(a), c0[0][2 * e + 1]), fmaf(xn[1][2 * e + 1], hi16(c), c0[1][2 * e + 1]));
    __builtin_amdgcn_raw_buffer_store_b32(o0, ro, ooff[2 * e], 0, 0);
    __builtin_amdgcn_raw_buffer_store_b32(o1, ro, ooff[2 * e + 1], 0, 0);
  }
}

__device__ __forceinline__ void route(const Ctx& P, u32 tag, u64 epoch) {
  LAS char* sm = P.sm;
  const int lane = P.lane, wave = P.wave;
  (void)wave;
  gate_wait(P, G_R, 16u, gate_inc(tag));
  STAMP(3);
  v4u g[16];
  u32 spin = 0;
  _Pragma("nounroll") for (; spin < SPIN_CAP; spin++) {
    asm volatile("" ::: "memory");
#pragma unroll
    for (int i = 0; i < 16; i++)
      g[i] = __builtin_amdgcn_raw_buffer_load_b128(P.rws, WS_R + lane * 16 + (i & 3) * 1024, (i >> 2) * 4096, AUX_SC1);
    u32 bad = 0;
#pragma unroll
    for (int i = 0; i < 16; i++) {
      bad = __builtin_amdgcn_bitop3_b32(bad, g[i].y, tag, 0xF6);
      bad = __builtin_amdgcn_bitop3_b32(bad, g[i].w, tag, 0xF6);
    }
    if (__all(bad == 0)) break;
    __builtin_amdgcn_s_sleep(4);
  }
  NOTE(24, spin + 1);
  lds_signal(sm, C_TRUST);
  STAMP(4);
  if (P.b == 0 && lane == 0) __hip_atomic_store((GAS u64*)(P.ws + WS_E), epoch, __ATOMIC_RELAXED, __HIP_MEMORY_SCOPE_AGENT);
  u32 k1 = umax(g[0].x, g[0].z), k2 = umin(g[0].x, g[0].z), k3 = 0;
#pragma unroll
  for (int i = 2; i < 32; i++) {
    const u32 x = (i & 1) ? g[i >> 1].z : g[i >> 1].x;
    k3 = umed3(k2, k3, x);
    k2 = umed3(k1, k2, x);
    k1 = umax(k1, x);
  }
  u32 tau = umax(k1, dppu<0xB1>(k1));
  tau = umax(tau, dppu<0x4E>(tau));
  tau = umax(tau, dppu<0x141>(tau));
  tau = umin(tau, dppu<0x140>(tau));
  tau = xrow_umin(tau);
  LAS u32* cand = (LAS u32*)(sm + L_CAND);
  const u64 m1 = __ballot(k1 >= tau), m2 = __ballot(k2 >= tau), m3 = __ballot(k3 >= tau);
  const u32 n1 = __builtin_popcountll(m1), n2 = n1 + __builtin_popcountll(m2), n = n2 + __builtin_popcountll(m3);
  bool slow = n > 64;
  if (!slow) {
    const u32 p1 = __builtin_amdgcn_mbcnt_hi((u32)(m1 >> 32), __builtin_amdgcn_mbcnt_lo((u32)m1, 0u));
    const u32 p2 = __builtin_amdgcn_mbcnt_hi((u32)(m2 >> 32), __builtin_amdgcn_mbcnt_lo((u32)m2, n1));
    const u32 p3 = __builtin_amdgcn_mbcnt_hi((u32)(m3 >> 32), __builtin_amdgcn_mbcnt_lo((u32)m3, n2));
    cand[k1 >= tau ? p1 : 64] = k1;
    cand[k2 >= tau ? p2 : 64] = k2;
    cand[k3 >= tau ? p3 : 64] = k3;
    asm volatile("" ::: "memory");
    const u32 mine = cand[lane];
    u32 rank = 0;
    _Pragma("nounroll") for (u32 jj = 0; jj < n; jj += 8) {
      const v4u c4 = *(const LAS v4u*)(cand + jj), d4 = *(const LAS v4u*)(cand + jj + 4);
      rank += (c4.x > mine) + (c4.y > mine) + (c4.z > mine) + (c4.w > mine);
      rank += (d4.x > mine) + (d4.y > mine) + (d4.z > mine) + (d4.w > mine);
    }
    const bool in = (u32)lane < n && rank < 8;
    if (in) ((LAS u32*)(sm + L_SEL))[rank] = mine;
    slow = __any(in && (u32)lane >= n2);
  }
  if (slow) {
    _Pragma("nounroll") for (int r = 0; r < 8; r++) {
      u32 mx = 0;
#pragma unroll
      for (int i = 0; i < 16; i++) mx = umax3(mx, g[i].x, g[i].z);
      mx = wave_umax(mx);
#pragma unroll
      for (int i = 0; i < 16; i++) { g[i].x = g[i].x == mx ? 0u : g[i].x; g[i].z = g[i].z == mx ? 0u : g[i].z; }
      if (lane == 0) ((LAS u32*)(sm + L_SEL))[r] = mx;
    }
  }
  lds_signal(sm, C_SEL);
  STAMP(5);
}

__device__ __forceinline__ u32 load_msd(const Ctx& P, int tok, u32 tag) {
  v2u g;
  _Pragma("nounroll") for (u32 spin = 0; spin < SPIN_CAP; spin++) {
    asm volatile("" ::: "memory");
    g = __builtin_amdgcn_raw_buffer_load_b64(P.rws, WS_M + tok * 8, 0, AUX_SC1);
    if (__all(g.y == tag)) break;
    __builtin_amdgcn_s_sleep(4);
  }
  return g.x;
}

__device__ __forceinline__ void side_wave(const Ctx& P, const int ROLE) {
  LAS char* sm = P.sm;
  const int lane = P.lane, wave = P.wave, C = P.C, cg = P.cg;
  const __amdgpu_buffer_rsrc_t rq = mkrsrc(P.q, S * D * 2);
  u32 tag;
  if (wave != 7) {
    if (wave == 5) {
      const v4u g4 = {GMAGIC, GMAGIC, GMAGIC, GMAGIC};
      __builtin_amdgcn_raw_buffer_store_b128(g4, P.rws, lane < 32 ? gate_off(lane >> 3, P.b >> 4, P.b & 15) + (lane & 7) * 16 : OOB, 0, AUX_SC1);
      const v4u c4 = {CLR, CLR, CLR, CLR};
#pragma unroll
      for (int i = 0; i < 9; i++) __builtin_amdgcn_raw_buffer_store_b128(c4, P.rws, WS_U + lane * 16 + (i & 3) * 1024, C * TILE_B + (i >> 2) * 4096, AUX_SC1);
      STAMP(10);
      asm volatile("s_waitcnt vmcnt(0)" ::: "memory");
      lds_signal(sm, C_CLR);
      STAMP(13);
    } else {
      lds_wait(sm, C_TAG, 1);
    }
    if (ROLE == 0) {
      HCopy hc;
      hc.load(P, 2 * (wave - 5));
      STAMP(12);
      hc.store(P, 2 * (wave - 5));
      STAMP(3);
    } else if (ROLE == 2) {
      lds_wait(sm, C_PUB, 1);
      helper_prefetch(P);
    }
    if (wave == 5) {
      if (ROLE == 1) {
        Ffn ffn;
        lds_wait(sm, C_STAT, 1);
        ffn.load(P, 7);
        STAMP(3);
        ffn.run(P, 7);
      }
      return;
    }
    tag = *(const LAS u32*)(sm + L_REC);
    if (ROLE == 1) { lds_wait(sm, C_SEL, 1); __builtin_amdgcn_s_setprio(3); }
  } else {
    __builtin_amdgcn_s_setprio(3);
    const int m = lane & 15, g4 = lane >> 4;
    u64 epoch = __hip_atomic_load((GAS u64*)(P.ws + WS_E), __ATOMIC_RELAXED, __HIP_MEMORY_SCOPE_AGENT);
    __builtin_amdgcn_sched_barrier(0);
    v4u xa[9], wb[9];
    const int xo = (m & 7) * (D * 2) + (m >> 3) * 64 + g4 * 16;
#pragma unroll
    for (int i = 0; i < 9; i++) xa[i] = __builtin_amdgcn_raw_buffer_load_b128(rq, xo + i * 128, 8 * C * (D * 2), AUX_NT);
    __builtin_amdgcn_sched_barrier(0);
    const size_t pw = (size_t)P.wr + m * 64, po = (size_t)kOnes;
    const GAS char* wp = (const GAS char*)(m < 2 ? pw : po) + g4 * 16;
#pragma unroll
    for (int i = 0; i < 9; i++) wb[i] = *(const GAS v4u*)(wp + i * 128);
    STAMP(10);
    __builtin_amdgcn_sched_barrier(0);
    asm volatile("" : "+v"(epoch) :: "memory");
    epoch += 1;
    tag = (u32)epoch;
    if (lane == 0) *(LAS u32*)(sm + L_REC) = tag;
    lds_signal(sm, C_TAG);
    STAMP(1);
    f4 gx = {0.f, 0.f, 0.f, 0.f}, gw = {0.f, 0.f, 0.f, 0.f}, gs = {0.f, 0.f, 0.f, 0.f};
#pragma unroll
    for (int i = 0; i < 9; i++) gx = mfma16(xa[i], xa[i], gx);
    __builtin_amdgcn_sched_barrier(0);
    const u32 gold = gate_post(P, G_R, true, gate_inc(tag));
    STAMP(12);
    __builtin_amdgcn_sched_barrier(0);
#pragma unroll
    for (int i = 0; i < 9; i++) { gw = mfma16(wb[i], xa[i], gw); gs = mfma16(wb[i], wb[i], gs); }
    float sxx = (m & 2) ? ((m & 1) ? gx[3] : gx[2]) : ((m & 1) ? gx[1] : gx[0]);
    sxx = __uint_as_float(__builtin_amdgcn_ds_bpermute((m + (m & 12) * 4) * 4, __float_as_uint(sxx)));
    float sxw = m < 8 ? gw[0] : gw[1], sx = gw[2];
    sxw += dppf<0x128>(sxw);
    sx += dppf<0x128>(sx);
    sxx += dppf<0x128>(sxx);
    const float sw = __uint_as_float(__builtin_amdgcn_readlane(__float_as_uint(gs[0] + gs[1]), 2));
    const float mean = sx * (1.0f / D);
    const float var = fmaxf(fmaf(-mean, mean, sxx * (1.0f / D)), 0.f);
    const float mu = rbf(mean);
    const float sd = rbf(__builtin_amdgcn_sqrtf(rbf(rbf(var) + 1e-5f)));
    const float rs = __builtin_amdgcn_rcpf(sd);
    const u32 r = pk2(0.f, fmaf(-mu, sw, sxw) * rs);
    u32 key = ((r ^ (((int)r >> 31) | 0x80000000u)) & 0xFFFF0000u) | (u32)(2047 - (8 * C + (m & 7)));
    u32 msd = __float_as_uint(mu) | (__float_as_uint(sd) >> 16);
    asm volatile("" : "+v"(key), "+v"(msd));
    const v2u rec = {m < 8 ? key : msd, tag};
    const int ro = lane < 16 ? (m < 8 ? WS_R : WS_M) + (8 * C + (m & 7)) * 8 : OOB;
    lds_wait(sm, C_CLR, 1);
    __builtin_amdgcn_raw_buffer_store_b64(rec, P.rws, ro, 0, AUX_SC1);
    STAMP(2);
    if (lane < 8) { f2 st = {rs, -mu * rs}; *(LAS f2*)(sm + L_STAT + m * 8) = st; }
    {
      const int t = m & 7, t3 = (t * 11) >> 5;
      LAS char* xd = sm + L_U + t3 * UL_B + 512 + (t - 3 * t3) * (D * 2) + (m >> 3) * 64 + g4 * 16;
#pragma unroll
      for (int i = 0; i < 9; i++) *(LAS v4u*)(xd + i * 128) = xa[i];
    }
    lds_signal(sm, C_STAT);
    if (ROLE == 2) lds_signal(sm, C_PUB);
    gate_confirm(P, G_R, gold, gate_inc(tag));
    route(P, tag, epoch);
    if (ROLE != 1) __builtin_amdgcn_s_setprio(0);
  }
  if (ROLE == 1) {
    const int cw = wave - 6;
    const v4u s4 = *(const LAS v4u*)(sm + L_SEL + cw * 16);
    Row rr[4];
#pragma unroll
    for (int e = 0; e < 4; e++) {
      const int tok = 2047 - (int)(__builtin_amdgcn_readfirstlane(s4[e]) & 2047u);
      rr[e] = load_row<0>(rq, tok * (D * 2), lane);
    }
    const u32 ks = (lane & 2) ? ((lane & 1) ? s4.w : s4.z) : ((lane & 1) ? s4.y : s4.x);
    const u32 msd = load_msd(P, 2047 - (int)(ks & 2047u), tag);
    const float rsl = __builtin_amdgcn_rcpf(lo16(msd)), nml = -hi16(msd) * rsl;
#pragma unroll
    for (int e = 0; e < 4; e++) {
      const float rs = __uint_as_float(__builtin_amdgcn_readlane(__float_as_uint(rsl), e));
      const float nm = __uint_as_float(__builtin_amdgcn_readlane(__float_as_uint(nml), e));
      LAS char* dst = sm + L_XS + (cw * 4 + e) * XS_STRIDE;
      const Row& x = rr[e];
      v2u a = {pk2(fmaf(lo16(x.a0.x), rs, nm), fmaf(hi16(x.a0.x), rs, nm)), pk2(fmaf(lo16(x.a0.y), rs, nm), fmaf(hi16(x.a0.y), rs, nm))};
      v2u c = {pk2(fmaf(lo16(x.a1.x), rs, nm), fmaf(hi16(x.a1.x), rs, nm)), pk2(fmaf(lo16(x.a1.y), rs, nm), fmaf(hi16(x.a1.y), rs, nm))};
      *(LAS v2u*)(dst + lane * 8) = a;
      *(LAS v2u*)(dst + 512 + lane * 8) = c;
      *(LAS u16*)(dst + 1024 + lane * 2) = (u16)pk2(fmaf(lo16(x.a2), rs, nm), 0.f);
    }
    lds_signal(sm, C_XS);
    STAMP(6);
    ffn_act(P, wave);
  }
  if (wave == 6) {
    gate_wait(P, G_Y, 9u, gate_inc(tag));
    lds_signal(sm, C_GY);
    STAMP(7);
  } else if (ROLE == 0) {
    const int pi = lane & 7, ti = 4 * (pi & 1) + (pi & 2) + (pi >> 2);
    const u32 kx = ((LAS u32*)(sm + L_SEL))[ti];
    const int tok = 2047 - (int)(kx & 2047u);
    const v2u x2 = *(const GAS v2u*)((const GAS char*)P.q + (size_t)tok * (D * 2) + cg * 8);
    const v2u d2 = *(const GAS v2u*)((const GAS char*)P.dsk + cg * 8);
    const u32 msd = load_msd(P, tok, tag);
    const float rs = __builtin_amdgcn_rcpf(lo16(msd)), nm = -hi16(msd) * rs;
    const u32 rb = kx & 0xFFFF0000u;
    const float r = __uint_as_float((rb & 0x80000000u) ? (rb ^ 0x80000000u) : (~rb & 0xFFFF0000u));
    const float rsig = rbf(__builtin_amdgcn_rcpf(1.0f + __expf(-r)));
    STAMP(6);
    gate_wait(P, G_P, lane < 8 ? 5u : 4u, gate_inc(tag));
    STAMP(7);
    v4u g[9];
    const u32 tag12 = tag & 0xFFFu;
    u32 spin = 0;
    _Pragma("nounroll") for (; spin < SPIN_CAP; spin++) {
      asm volatile("" ::: "memory");
#pragma unroll
      for (int i = 0; i < 9; i++)
        g[i] = __builtin_amdgcn_raw_buffer_load_b128(P.rws, WS_P + (lane >> 3) * PART_B + cg * 128 + pi * 16, 8 * i * PART_B, AUX_SC1);
      u32 bad = 0;
#pragma unroll
      for (int i = 0; i < 9; i++)
#pragma unroll
        for (int e = 0; e < 4; e++) bad = __builtin_amdgcn_bitop3_b32(bad, g[i][e], tag12, 0xF6);
      if (__all((bad & 0xFFFu) == 0)) break;
      __builtin_amdgcn_s_sleep(4);
    }
    NOTE(25, spin + 1);
    STAMP(8);
    float sum[4];
#pragma unroll
    for (int e = 0; e < 4; e++) {
      float s = __uint_as_float(g[0][e] ^ tag12);
#pragma unroll
      for (int i = 1; i < 9; i++) s += __uint_as_float(g[i][e] ^ tag12);
      s += dppf<0x128>(s);
      sum[e] = xrow_sum(s);
    }
    lds_wait(sm, C_Y, 4);
    float o[4];
#pragma unroll
    for (int e = 0; e < 4; e++) {
      const float y = lo16(*(const LAS u16*)(sm + L_H + e * (HC_B + 64) + tok * 2));
      const float u = lo16(*(const LAS u16*)(sm + L_U + e * UL_B + (32 + (tok >> 3)) * 16 + (7 - (tok & 7)) * 2));
      const float x = (e & 1) ? hi16(x2[e >> 1]) : lo16(x2[e >> 1]);
      const float dk = (e & 1) ? hi16(d2[e >> 1]) : lo16(d2[e >> 1]);
      const float n = fmaf(x, rs, nm);
      o[e] = fmaf(n, fmaf(u, dk, y), x) + fmaf(rbf(sum[e]), rsig, n);
    }
    if (lane < 8) {
      v2u o2 = {pk2(o[0], o[1]), pk2(o[2], o[3])};
      *(GAS v2u*)((GAS char*)P.out + (size_t)tok * (D * 2) + cg * 8) = o2;
    }
  }
}

__global__ __launch_bounds__(NTHR, 2) void fused_kernel(
    const u16* __restrict__ q, const u16* __restrict__ k, const u16* __restrict__ v, const u16* __restrict__ shk,
    const u16* __restrict__ hk, const u16* __restrict__ dsk, const u16* __restrict__ wr, const u16* __restrict__ wg,
    const u16* __restrict__ wu, const u16* __restrict__ wd, u16* __restrict__ out, char* ws) {
  extern __shared__ __attribute__((aligned(16))) char smem_[];
  Ctx P;
  P.q = q; P.k = k; P.v = v; P.shk = shk; P.hk = hk; P.dsk = dsk; P.wr = wr; P.wg = wg; P.wu = wu; P.wd = wd;
  P.out = out; P.ws = ws;
  P.sm = (LAS char*)smem_;
  P.tid = threadIdx.x; P.lane = P.tid & 63;
  P.wave = __builtin_amdgcn_readfirstlane(P.tid >> 6);
  const int b = blockIdx.x;
  P.b = b;
  P.C = (b & 7) * 32 + (b >> 3);
  P.is_conv = b < NCONV;
  const bool is_moe = !P.is_conv && b < NCONV + NMOE;
  P.cg = (b & 7) * 18 + (b >> 3);
  P.mw = b - NCONV;
  P.rws = mkrsrc(ws, WS_END);
  const int lane = P.lane, wave = P.wave;
  (void)lane;
  if (lane < 32) ((LAS u32*)(P.sm + L_END))[wave * 32 + lane] = 0;
  STAMP(0); STAMPC(30);
  if (wave == 5) {
    ((LAS u32*)(P.sm + L_MISC))[lane] = 0u;
    ((LAS u32*)(P.sm + L_MISC))[64 + lane] = 0u;
  } else if (wave == 6) {
    const v4u z = {0u, 0u, 0u, 0u};
    *(LAS v4u*)(P.sm + L_U + (lane >> 4) * UL_B + (lane & 15) * 16) = z;
    *(LAS v4u*)(P.sm + L_U + (lane >> 4) * UL_B + 256 + (lane & 15) * 16) = z;
  }
  wg_barrier();
  STAMP(11);
  if (wave < 5) {
    if (is_moe) tile_wave<true>(P); else tile_wave<false>(P);
  } else {
    side_wave(P, P.is_conv ? 0 : (is_moe ? 1 : 2));
  }
  FLUSH();
}

}

extern "C" void kernel_launch(void* const* d_in, const int* in_sizes, int n_in, void* d_out, int out_size, void* d_ws,
                              size_t ws_size, hipStream_t stream) {
  (void)in_sizes; (void)n_in; (void)out_size; (void)ws_size;
  (void)hipFuncSetAttribute((const void*)fused_kernel, hipFuncAttributeMaxDynamicSharedMemorySize, L_TOTAL);
  fused_kernel<<<NWG, NTHR, L_TOTAL, stream>>>(
      (const u16*)d_in[0], (const u16*)d_in[1], (const u16*)d_in[2], (const u16*)d_in[3], (const u16*)d_in[4],
      (const u16*)d_in[5], (const u16*)d_in[7], (const u16*)d_in[8], (const u16*)d_in[9], (const u16*)d_in[10],
      (u16*)d_out, (char*)d_ws + (WS_AT_END ? ((ws_size - WS_END) & ~(size_t)0xFFFF) : 0));
}
```

```cpp
#include <hip/hip_runtime.h>
#include <hip/hip_bf16.h>

typedef unsigned int u32;
typedef unsigned long long u64;
typedef unsigned short u16;
typedef u32 v4u __attribute__((ext_vector_type(4)));
typedef u32 v2u __attribute__((ext_vector_type(2)));
typedef float f4 __attribute__((ext_vector_type(4)));
typedef float f2 __attribute__((ext_vector_type(2)));
typedef __bf16 bf8 __attribute__((ext_vector_type(8)));
typedef __bf16 bf2 __attribute__((ext_vector_type(2)));
#define LAS __attribute__((address_space(3)))
#define GAS __attribute__((address_space(1)))

namespace {

constexpr int S = 2048, D = 576, F = 4608;
constexpr int NWG = 256, NTHR = 512, NCONV = 144, NMOE = 72;
constexpr int AUX_SC1 = 16;
constexpr u32 CLR = 0xFFFFFFFFu, SAME = 0xFFFFFFFEu, MAXV = 0xFFFFFFFDu;
constexpr u32 SPIN_CAP = 1u << 16, GATE_CAP = 96;
constexpr u32 GMAGIC = 0x5AC3E900u;
constexpr int OOB = 0x7FFFFF00;
#define U_POST 0
#define WS_AT_END 0
#define HELPERS 1
#define Y_POST 7
#define OUT_AUX 0
#define W2_GATE 2
#define TILE_DELAY 2
#define CODE_PF 40
#define USE_NT 0
constexpr int AUX_NT = USE_NT ? 2 : 0;

constexpr int WS_E = 0;
constexpr int WS_G = 4096;
constexpr int GATE_B = 256 * 256;
constexpr int WS_R = WS_G + 4 * GATE_B;
constexpr int WS_M = WS_R + S * 8;
constexpr int WS_U = WS_M + S * 8;
constexpr int TILE_B = D * 16;
constexpr int WS_P = WS_U + NWG * TILE_B;
constexpr int PART_B = NCONV * 128;
constexpr int WS_END = WS_P + NMOE * PART_B;
constexpr int WS_DBG = (WS_END + 4095) / 4096 * 4096;
enum { G_R = 0, G_U, G_Y, G_P };
#define ONES8 0x3F803F80u, 0x3F803F80u, 0x3F803F80u, 0x3F803F80u, 0x3F803F80u, 0x3F803F80u, 0x3F803F80u, 0x3F803F80u
#define ONES32 ONES8, ONES8, ONES8, ONES8
__device__ const u32 kOnes[288] __attribute__((aligned(128))) = {ONES32, ONES32, ONES32, ONES32, ONES32, ONES32, ONES32, ONES32, ONES32};

constexpr int HC_B = 2080 * 16;
constexpr int L_H = 0;
constexpr int UL_B = 290 * 16;
constexpr int L_U = L_H + 4 * HC_B;
constexpr int L_STG = L_U + 4 * UL_B;
constexpr int L_MISC = L_STG + TILE_B;
constexpr int L_CNT = L_MISC;
constexpr int L_CAND = L_CNT + 96;
constexpr int L_SEL = L_CAND + 288;
constexpr int L_REC = L_SEL + 32;
constexpr int L_STAT = L_REC + 32;
constexpr int Y1_B = 2048 + 64;
constexpr int L_END = L_STAT + 64;
constexpr int XS_STRIDE = 1184;
constexpr int L_XS = 0;
constexpr int L_RED = L_XS + 8 * XS_STRIDE;
constexpr int L_HB = L_RED + 6 * 8 * 128 * 4;
constexpr int HB_STRIDE = 160;
static_assert(L_END + 1024 <= 160 * 1024, "lds");
static_assert(L_HB + 8 * HB_STRIDE <= L_U, "lds");
static_assert(L_END - L_MISC == 512, "zero fill");

__device__ __forceinline__ constexpr int xrow_off(int t) { return L_U + (t / 3) * UL_B + 512 + (t % 3) * (D * 2); }
enum { C_CLR = 0, C_STAT, C_TRUST, C_GU, C_SEL, C_H, C_U, C_Y, C_XS, C_RED, C_HB, C_GY, C_PUB, C_TAG, C_QQ, C_U2, C_Y1, C_W1, C_DN };

__device__ __forceinline__ float lo16(u32 x) { return __uint_as_float(x << 16); }
__device__ __forceinline__ float hi16(u32 x) { return __uint_as_float(x & 0xFFFF0000u); }
__device__ __forceinline__ u32 pk2(float a, float b) {
  bf2 p = {(__bf16)a, (__bf16)b};
  return __builtin_bit_cast(u32, p);
}
__device__ __forceinline__ float rbf(float a) { return lo16(pk2(a, a)); }
__device__ __forceinline__ float dot2(u32 a, u32 b, float c) {
  return __builtin_amdgcn_fdot2_f32_bf16(__builtin_bit_cast(bf2, a), __builtin_bit_cast(bf2, b), c, false);
}
template <int CTRL> __device__ __forceinline__ u32 dppu(u32 x) {
  return (u32)__builtin_amdgcn_mov_dpp((int)x, CTRL, 0xf, 0xf, true);
}
template <int CTRL> __device__ __forceinline__ float dppf(float x) { return __uint_as_float(dppu<CTRL>(__float_as_uint(x))); }
__device__ __forceinline__ float xrow_sum(float x) {
  auto s = __builtin_amdgcn_permlane16_swap(__float_as_uint(x), __float_as_uint(x), false, false);
  x = __uint_as_float(s[0]) + __uint_as_float(s[1]);
  auto t = __builtin_amdgcn_permlane32_swap(__float_as_uint(x), __float_as_uint(x), false, false);
  return __uint_as_float(t[0]) + __uint_as_float(t[1]);
}
__device__ __forceinline__ u32 umax(u32 a, u32 b) { return a > b ? a : b; }
__device__ __forceinline__ u32 umin(u32 a, u32 b) { return a < b ? a : b; }
__device__ __forceinline__ u32 umed3(u32 a, u32 b, u32 c) { return umax(umin(a, b), umin(umax(a, b), c)); }
__device__ __forceinline__ u32 xrow_umin(u32 x) {
  auto s = __builtin_amdgcn_permlane16_swap(x, x, false, false);
  x = umin(s[0], s[1]);
  auto t = __builtin_amdgcn_permlane32_swap(x, x, false, false);
  return umin(t[0], t[1]);
}
__device__ __forceinline__ u32 wave_umax(u32 x) {
  x = umax(x, dppu<0xB1>(x));
  x = umax(x, dppu<0x4E>(x));
  x = umax(x, dppu<0x141>(x));
  x = umax(x, dppu<0x140>(x));
  auto s = __builtin_amdgcn_permlane16_swap(x, x, false, false);
  x = umax(s[0], s[1]);
  auto t = __builtin_amdgcn_permlane32_swap(x, x, false, false);
  return umax(t[0], t[1]);
}
__device__ __forceinline__ void lds_signal(LAS char* sm, int c) {
  u64 keep;
  asm volatile("s_waitcnt lgkmcnt(0)\n\ts_mov_b64 %0, exec\n\ts_mov_b64 exec, 1\n\tds_add_u32 %1, %2\n\ts_mov_b64 exec, %0"
               : "=&s"(keep) : "v"((u32)(size_t)(sm + L_CNT + 4 * c)), "v"(1u) : "memory");
}
template <int SLEEP = 0> __device__ __forceinline__ void lds_wait(LAS char* sm, int c, u32 target) {
  u32 v, sv;
  if (SLEEP == 0)
    asm volatile("1:\n\tds_read_b32 %0, %2\n\ts_waitcnt lgkmcnt(0)\n\tv_readfirstlane_b32 %1, %0\n\ts_cmp_lt_u32 %1, %3\n\ts_cbranch_scc1 1b"
                 : "=&v"(v), "=&s"(sv) : "v"((u32)(size_t)(sm + L_CNT + 4 * c)), "s"(target) : "memory", "scc");
  else
    asm volatile("1:\n\ts_sleep %4\n\tds_read_b32 %0, %2\n\ts_waitcnt lgkmcnt(0)\n\tv_readfirstlane_b32 %1, %0\n\ts_cmp_lt_u32 %1, %3\n\ts_cbranch_scc1 1b"
                 : "=&v"(v), "=&s"(sv) : "v"((u32)(size_t)(sm + L_CNT + 4 * c)), "s"(target), "n"(SLEEP) : "memory", "scc");
}
__device__ __forceinline__ bool lds_arrive(LAS char* sm, int c, u32 last) {
  u64 keep;
  u32 v, sv;
  asm volatile("s_mov_b64 %0, exec\n\ts_mov_b64 exec, 1\n\tds_add_rtn_u32 %1, %3, %4\n\ts_waitcnt lgkmcnt(0)\n\tv_readfirstlane_b32 %2, %1\n\ts_mov_b64 exec, %0"
               : "=&s"(keep), "=&v"(v), "=&s"(sv) : "v"((u32)(size_t)(sm + L_CNT + 4 * c)), "v"(1u) : "memory");
  return sv == last;
}
__device__ __forceinline__ void wg_barrier() { asm volatile("s_waitcnt lgkmcnt(0)\n\ts_barrier" ::: "memory"); }
__device__ __forceinline__ u32 umax3(u32 a, u32 b, u32 c) { return umax(umax(a, b), c); }
__device__ __forceinline__ u32 vmax4(v4u g, u32 acc) { return umax3(umax3(g.x, g.y, acc), g.z, g.w); }
__device__ __forceinline__ f4 mfma16(v4u a, v4u b, f4 c) {
  return __builtin_amdgcn_mfma_f32_16x16x32_bf16(__builtin_bit_cast(bf8, a), __builtin_bit_cast(bf8, b), c, 0, 0, 0);
}
__device__ __forceinline__ __amdgpu_buffer_rsrc_t mkrsrc(const void* p, int bytes) {
  return __builtin_amdgcn_make_buffer_rsrc((void*)p, 0, bytes, 0x00020000);
}

#define STAMP(i) do {} while (0)
#define STAMPC(i) do {} while (0)
#define FLUSH() do {} while (0)
#define NOTE(i, v) do {} while (0)
constexpr int L_TOTAL = L_END;

struct Ctx {
  const u16 *q, *k, *v, *shk, *hk, *dsk, *wr, *wg, *wu, *wd;
  u16* out;
  char* ws;
  const char* code;
  __amdgpu_buffer_rsrc_t rws;
  LAS char* sm;
  int tid, lane, wave, b, C, cg, mw;
  bool is_conv;
};

__device__ __forceinline__ int gate_off(int gate, int rep, int shard) { return WS_G + gate * GATE_B + (rep * 16 + shard) * 256; }
__device__ __forceinline__ u32 gate_add(const Ctx& P, int off, int val) {
  return (u32)__builtin_amdgcn_raw_ptr_buffer_atomic_add_i32(val, P.rws, off, 0, 0);
}
__device__ __forceinline__ u32 gate_post(const Ctx& P, int gate, bool on, u32 inc) {
  return gate_add(P, (on && P.lane < 16) ? gate_off(gate, P.lane, P.b & 15) : OOB, (int)inc);
}
__device__ __forceinline__ void gate_confirm(const Ctx& P, int gate, u32 old, u32 inc) {
  bool bad = P.lane < 16;
  _Pragma("nounroll") for (u32 spin = 0; spin < GATE_CAP; spin++) {
    const u32 d = old - GMAGIC;
    bad = bad && ((d & 0xF0u) != 0u || d != (d & 0xFu) * inc);
    if (__builtin_expect(!__any(bad), 1)) break;
    NOTE(26, spin + 1);
    __builtin_amdgcn_s_sleep(2);
    old = gate_add(P, bad ? gate_off(gate, P.lane, P.b & 15) : OOB, (int)inc);
  }
}
__device__ __forceinline__ void gate_wait(const Ctx& P, int gate, u32 expect, u32 inc) {
  const int off = P.lane < 16 ? gate_off(gate, P.b >> 4, P.lane) : OOB;
  const u32 want = P.lane < 16 ? GMAGIC + expect * inc : 0u;
  _Pragma("nounroll") for (u32 spin = 0; spin < GATE_CAP; spin++)
    if (__all(gate_add(P, off, 0) == want)) break;
}
__device__ __forceinline__ u32 gate_inc(u32 tag) { return 1u + (tag << 8); }

struct Row { v2u a0, a1; u32 a2; };
template <int AUX> __device__ __forceinline__ Row load_row(__amdgpu_buffer_rsrc_t r, int soff, int lane) {
  Row x;
  x.a0 = __builtin_amdgcn_raw_buffer_load_b64(r, lane * 8, soff, AUX);
  x.a1 = __builtin_amdgcn_raw_buffer_load_b64(r, lane * 8 + 512, soff, AUX);
  x.a2 = __builtin_amdgcn_raw_buffer_load_b16(r, lane * 2 + 1024, soff, AUX);
  return x;
}
__device__ __forceinline__ void hcopy_store(LAS char* dst, v4u lo, v4u hi, bool first) {
  u32 d[8], o[7];
#pragma unroll
  for (int e = 0; e < 4; e++) { d[e] = lo[e]; d[4 + e] = hi[e]; }
#pragma unroll
  for (int e = 0; e < 7; e++) o[e] = __builtin_amdgcn_alignbit(d[e + 1], d[e], 16);
  constexpr int BASE[8] = {0, 260, 519, 778, 1037, 1296, 1556, 1815};
#pragma unroll
  for (int e = 0; e < 4; e++) {
    v4u a = {d[e], d[e + 1], d[e + 2], d[e + 3]}, c = {o[e], o[e + 1], o[e + 2], o[e + 3]};
    if (e > 0 || first) *(LAS v4u*)(dst + BASE[2 * e] * 16) = a;
    *(LAS v4u*)(dst + BASE[2 * e + 1] * 16) = c;
  }
}
struct HCopy {
  v4u lo[8];
  __device__ __forceinline__ void load(const Ctx& P, int ch0) {
    const __amdgpu_buffer_rsrc_t rh = mkrsrc(P.hk + (size_t)(4 * P.cg + ch0) * S, 2 * S * 2);
#pragma unroll
    for (int r = 0; r < 8; r++) lo[r] = __builtin_amdgcn_raw_buffer_load_b128(rh, P.lane * 16 + (r & 3) * 1024, (r >> 2) * 4096, AUX_NT);
  }
  __device__ __forceinline__ void store(const Ctx& P, int ch0) {
    LAS char* dst = P.sm + L_H + ch0 * HC_B + (P.lane + 2) * 16;
#pragma unroll
    for (int r = 0; r < 8; r++) *(LAS v4u*)(dst + (r >> 2) * HC_B + (r & 3) * 1024) = lo[r];
    {
      const int c = P.lane & 1, m1 = (P.lane >> 1) & 1;
      const v4u z = {0u, 0u, 0u, 0u};
      v4u h0 = *(const LAS v4u*)(P.sm + L_H + (ch0 + c) * HC_B + 32);
      h0 = m1 ? h0 : z;
      if (P.lane < 4) hcopy_store(P.sm + L_H + (ch0 + c) * HC_B + m1 * 16, z, h0, true);
    }
    v4u hi[8];
#pragma unroll
    for (int r = 0; r < 8; r++) hi[r] = *(const LAS v4u*)(dst + (r >> 2) * HC_B + (r & 3) * 1024 + 16);
#pragma unroll
    for (int r = 0; r < 8; r++) hcopy_store(dst + (r >> 2) * HC_B + (r & 3) * 1024, lo[r], hi[r], false);
    lds_signal(P.sm, C_H);
  }
};

__device__ __forceinline__ void ffn_act(const Ctx& P, int tk) {
  LAS char* sm = P.sm;
  lds_wait(sm, C_RED, 6);
  float G = 0.f, U = 0.f;
#pragma unroll
  for (int w = 0; w < 6; w++) {
    G += *(const LAS float*)(sm + L_RED + ((w * 8 + tk) * 128 + P.lane) * 4);
    U += *(const LAS float*)(sm + L_RED + ((w * 8 + tk) * 128 + 64 + P.lane) * 4);
  }
  G = rbf(G);
  const float sg = rbf(G * __builtin_amdgcn_rcpf(1.0f + __expf(-G)));
  *(LAS u16*)(sm + L_HB + tk * HB_STRIDE + P.lane * 2) = (u16)pk2(sg * rbf(U), 0.f);
  lds_signal(sm, C_HB);
}

struct Ffn {
  template <class FN> __device__ __forceinline__ void run(const Ctx& P, int cu0, FN&& between) {
    LAS char* sm = P.sm;
    const int lane = P.lane, wave = P.wave, n16 = lane & 15, g4 = lane >> 4;
    v4u W1[3][8];
    v2u W2[2][2][8];
    const GAS char* wp = (const GAS char*)(n16 < 8 ? P.wg : P.wu) + ((size_t)(96 * wave + 8 * g4) * F + 64 * P.mw + 8 * (n16 & 7)) * 2;
#pragma unroll
    for (int i = 0; i < 3; i++)
#pragma unroll
      for (int jj = 0; jj < 8; jj++) {
        const GAS v4u* a = (const GAS v4u*)(wp + (size_t)(32 * i + jj) * (F * 2));
        W1[i][jj] = USE_NT ? __builtin_nontemporal_load(a) : *a;
      }
    __builtin_amdgcn_sched_barrier(0);
    STAMP(3);
    between();
    const __amdgpu_buffer_rsrc_t rd = mkrsrc(P.wd + (size_t)64 * P.mw * D, 64 * D * 2);
    const int o2 = (8 * g4 * D + 64 * cu0 + 4 * n16) * 2;
    auto load2 = [&]() {
      __builtin_amdgcn_sched_barrier(0);
#pragma unroll
      for (int ci = 0; ci < 2; ci++)
#pragma unroll
        for (int ks = 0; ks < 2; ks++)
#pragma unroll
          for (int jj = 0; jj < 8; jj++)
            W2[ci][ks][jj] = __builtin_amdgcn_raw_buffer_load_b64(rd, ((ci == 0 || wave >= 3) ? o2 : OOB) + (jj & 3) * (D * 2) + ci * 128,
                                                                  (32 * ks + (jj & 4)) * (D * 2), AUX_NT);
      __builtin_amdgcn_sched_barrier(0);
    };
    v4u Bf[3][8];
#pragma unroll
    for (int i = 0; i < 3; i++) {
#pragma unroll
      for (int e = 0; e < 8; e++) {
#pragma unroll
        for (int d = 0; d < 4; d++)
          Bf[i][e][d] = __builtin_amdgcn_perm(W1[i][2 * d + 1][e >> 1], W1[i][2 * d][e >> 1], (e & 1) ? 0x07060302u : 0x05040100u);
        asm volatile("" : "+v"(Bf[i][e]));
      }
      if (W2_GATE == 3 && i == 0) load2();
    }
    STAMP(17);
    load2();
    const u32 tag = *(const LAS u32*)(sm + L_REC), tag12 = tag & 0x7FFu, rnd = 0x800u - tag12;
    STAMP(4);
    lds_wait<1>(sm, C_XS, 2);
    STAMP(5);
    f4 acc[8];
#pragma unroll
    for (int e = 0; e < 8; e++) acc[e] = (f4){0.f, 0.f, 0.f, 0.f};
#pragma unroll
    for (int i = 0; i < 3; i++) {
      const v4u A = *(const LAS v4u*)(sm + L_XS + (n16 & 7) * XS_STRIDE + (96 * wave + 32 * i + 8 * g4) * 2);
#pragma unroll
      for (int e = 0; e < 8; e++) acc[e] = mfma16(A, Bf[i][e], acc[e]);
    }
    {
      LAS char* dst = sm + L_RED + ((wave * 8 + 4 * (g4 & 1) + (g4 & 2)) * 128 + 8 * n16) * 4;
#pragma unroll
      for (int r = 0; r < 2; r++) {
        f4 a, c;
#pragma unroll
        for (int e = 0; e < 4; e++) { a[e] = g4 >= 2 ? acc[e][2 + r] : acc[e][r]; c[e] = g4 >= 2 ? acc[4 + e][2 + r] : acc[4 + e][r]; }
        *(LAS f4*)(dst + r * 512) = a;
        *(LAS f4*)(dst + r * 512 + 16) = c;
      }
    }
    lds_signal(sm, C_RED);
    STAMP(14);
    ffn_act(P, wave);
    STAMP(15);
    __builtin_amdgcn_sched_barrier(0);
    v4u Bd[2][2][4];
#pragma unroll
    for (int ci = 0; ci < 2; ci++)
      {
#pragma unroll
        for (int ks = 0; ks < 2; ks++)
#pragma unroll
          for (int e = 0; e < 4; e++) {
#pragma unroll
            for (int d = 0; d < 4; d++)
              Bd[ci][ks][e][d] = __builtin_amdgcn_perm(W2[ci][ks][2 * d + 1][e >> 1], W2[ci][ks][2 * d][e >> 1], (e & 1) ? 0x07060302u : 0x05040100u);
            asm volatile("" : "+v"(Bd[ci][ks][e]));
          }
      }
    asm volatile("s_waitcnt vmcnt(0)" ::: "memory");
    (void)gate_post(P, G_P, lds_arrive(sm, C_DN, 5), gate_inc(tag));
    STAMP(6);
    lds_wait(sm, C_HB, 8);
    const v4u A0 = *(const LAS v4u*)(sm + L_HB + (n16 & 7) * HB_STRIDE + g4 * 16);
    const v4u A1 = *(const LAS v4u*)(sm + L_HB + (n16 & 7) * HB_STRIDE + 64 + g4 * 16);
#pragma unroll
    for (int ci = 0; ci < 2; ci++) {
      if (ci == 1 && wave < 3) break;
      f4 ad[4];
#pragma unroll
      for (int e = 0; e < 4; e++) ad[e] = mfma16(A0, Bd[ci][0][e], (f4){0.f, 0.f, 0.f, 0.f});
#pragma unroll
      for (int e = 0; e < 4; e++) ad[e] = mfma16(A1, Bd[ci][1][e], ad[e]);
      v4u s0, s1;
#pragma unroll
      for (int e = 0; e < 4; e++) {
        const u32 x = ((__float_as_uint(g4 >= 2 ? ad[e][2] : ad[e][0]) + rnd) & 0xFFFFF000u) | tag12;
        const u32 y = ((__float_as_uint(g4 >= 2 ? ad[e][3] : ad[e][1]) + rnd) & 0xFFFFF000u) | tag12;
        s0[e] = (u32)__builtin_amdgcn_update_dpp((int)x, (int)y, 0x128, 0xf, 0xc, false);
        s1[e] = (u32)__builtin_amdgcn_update_dpp((int)y, (int)x, 0x128, 0xf, 0x3, false);
      }
      const int off = WS_P + P.mw * PART_B + (16 * (cu0 + ci) + (n16 & 7)) * 128 + (n16 >> 3) * 64 + g4 * 16;
      __builtin_amdgcn_raw_buffer_store_b128(s0, P.rws, off, 0, AUX_SC1);
      __builtin_amdgcn_raw_buffer_store_b128(s1, P.rws, off + 1024, 0, AUX_SC1);
    }
    STAMP(7);
  }
};

__device__ __forceinline__ void helper_prefetch(const Ctx& P) {
  const int g = P.b & 7, idx = (P.b - NCONV - NMOE) >> 3;
  v4u acc = {0u, 0u, 0u, 0u};
  v4u t[19];
#pragma unroll
  for (int i = 0; i < 19; i++) {
    const int u = (i * 5 + idx) * 7 + P.wave;
    const __amdgpu_buffer_rsrc_t rd = mkrsrc(P.wd, F * D * 2);
    t[i] = __builtin_amdgcn_raw_buffer_load_b128(rd, u < 648 ? P.lane * 16 : OOB, g * (9 * 64 * D * 2) + u * 1024, AUX_NT);
  }
#pragma unroll
  for (int i = 0; i < 19; i++) acc |= t[i];
  asm volatile("" :: "v"(acc));
}

__device__ __forceinline__ void publish_y(const Ctx& P, const LAS char* yb, int ystride, int tile0, int t, int step, int n) {
  const int gch = t & 3;
  const int goff = WS_U + (tile0 + (t >> 2)) * TILE_B + (4 * P.cg + gch) * 16;
#pragma unroll
  for (int it = 0; it < n; it++) {
    const v4u y4 = *(const LAS v4u*)(yb + gch * ystride + (it * step + (t >> 2)) * 16);
    const v4u u4 = *(const LAS v4u*)(P.sm + L_U + gch * UL_B + (32 + tile0 + it * step + (t >> 2)) * 16);
    v4u w4;
#pragma unroll
    for (int e = 0; e < 4; e++) w4[e] = (y4[e] == u4[e]) ? SAME : y4[e];
    __builtin_amdgcn_raw_buffer_store_b128(w4, P.rws, goff, it * step * TILE_B, AUX_SC1);
  }
}

__device__ __forceinline__ void conv_phase(const Ctx& P, u32 inc) {
  LAS char* sm = P.sm;
  const int tid = P.tid, lane = P.lane, wave = P.wave, n16 = lane & 15, g4 = lane >> 4, gch = tid & 3;
  const int goff = WS_U + (tid >> 2) * TILE_B + (4 * P.cg + gch) * 16;
  LAS char* ud = sm + L_U + gch * UL_B + (32 + (tid >> 2)) * 16;
  const int p1 = n16 + 1, kap = p1 & 7;
  const int base16 = 258 * kap + ((0x98654320u >> (4 * kap)) & 15);
  const LAS char* pa = sm + L_H + wave * HC_B + (base16 + g4 + (p1 >> 3)) * 16;
  const LAS char* pb = sm + L_U + wave * UL_B + (32 + 2 * n16 + 1 - g4 - 28) * 16;
  if (wave == 0) {
    gate_wait(P, G_U, 16u, inc);
    lds_signal(sm, C_GU);
  } else {
    lds_wait(sm, C_GU, 1);
  }
  STAMP(3);
  lds_wait(sm, C_TRUST, 1);
  lds_wait(sm, C_QQ, 5);
  STAMP(4);
  v4u g[2];
#pragma unroll
  for (int it = 0; it < 2; it++) g[it] = __builtin_amdgcn_raw_buffer_load_b128(P.rws, goff, it * 64 * TILE_B, AUX_SC1);
  if (__builtin_expect(!__all(vmax4(g[0], vmax4(g[1], 0u)) != CLR), 0)) {
    _Pragma("nounroll") for (u32 spin = 0; spin < SPIN_CAP; spin++) {
      NOTE(24, spin + 1);
      __builtin_amdgcn_s_sleep(4);
      asm volatile("" ::: "memory");
      g[0] = __builtin_amdgcn_raw_buffer_load_b128(P.rws, goff, 0, AUX_SC1);
      g[1] = __builtin_amdgcn_raw_buffer_load_b128(P.rws, goff, 64 * TILE_B, AUX_SC1);
      if (__all(vmax4(g[0], vmax4(g[1], 0u)) != CLR)) break;
    }
  }
  *(LAS v4u*)ud = g[0];
  *(LAS v4u*)(ud + 1024) = g[1];
  asm volatile("s_waitcnt vmcnt(0)" ::: "memory");
  lds_signal(sm, C_U);
  STAMP(5);
  lds_wait(sm, C_H, 2);
  lds_wait(sm, C_U, 4);
  STAMP(6);
  f4 acc[8];
#pragma unroll
  for (int t = 0; t < 8; t++) acc[t] = (f4){0.f, 0.f, 0.f, 0.f};
  v4u A[2][8], B[2][4];
#pragma unroll
  for (int a = 0; a < 8; a++) {
    A[0][a] = *(const LAS v4u*)(pa + a * 512);
    if (a < 4) B[0][a] = *(const LAS v4u*)(pb + a * 512 + 7 * 64);
  }
#pragma unroll
  for (int bb = 0; bb < 8; bb++) {
    __builtin_amdgcn_sched_barrier(0);
    if (bb == 7) {
      lds_wait(sm, C_U2, 1);
      __builtin_amdgcn_sched_barrier(0);
    }
#pragma unroll
    for (int a = 0; a < 8; a++) {
      if (bb < 7) {
        A[(bb + 1) & 1][a] = *(const LAS v4u*)(pa + a * 512 + (bb + 1) * 64);
        if (a < 4) B[(bb + 1) & 1][a] = *(const LAS v4u*)(pb + a * 512 + (6 - bb) * 64);
      } else if (a < 4) {
        A[0][a] = *(const LAS v4u*)(pa + a * 512);
        B[0][a] = *(const LAS v4u*)(pb + (4 + a) * 512 + 7 * 64);
      }
    }
#pragma unroll
    for (int t = 0; t < 8; t++)
#pragma unroll
      for (int c = 0; c < 4; c++)
        if (c <= t) acc[t] = mfma16(A[bb & 1][t - c], B[bb & 1][c], acc[t]);
#pragma unroll
    for (int i = 0; i < (bb < 7 ? 12 : 8); i++) {
      __builtin_amdgcn_sched_group_barrier(0x8, 2, 0);
      __builtin_amdgcn_sched_group_barrier(0x100, 1, 0);
    }
    if (bb < 7) __builtin_amdgcn_sched_group_barrier(0x8, 2, 0);
    else __builtin_amdgcn_sched_group_barrier(0x8, 10, 0);
  }
#pragma unroll
  for (int bb = 0; bb < 8; bb++) {
    __builtin_amdgcn_sched_barrier(0);
    if (bb == 1) {
      LAS char* yb = sm + L_STG + wave * Y1_B + n16 * 32 + g4 * 8;
#pragma unroll
      for (int t = 0; t < 4; t++) {
        v2u y2 = {umin(pk2(acc[t][0], acc[t][1]), MAXV), umin(pk2(acc[t][2], acc[t][3]), MAXV)};
        *(LAS v2u*)(yb + t * 512) = y2;
      }
    }
    if (bb == 2) lds_signal(sm, C_Y1);
    if (bb == Y_POST) {
      (void)gate_post(P, G_Y, wave == 0, inc);
      __builtin_amdgcn_sched_barrier(0);
    }
    if (bb < 7) {
#pragma unroll
      for (int a = 0; a < 4; a++) {
        A[(bb + 1) & 1][a] = *(const LAS v4u*)(pa + a * 512 + (bb + 1) * 64);
        B[(bb + 1) & 1][a] = *(const LAS v4u*)(pb + (4 + a) * 512 + (6 - bb) * 64);
      }
    }
#pragma unroll
    for (int t = 0; t < 4; t++)
#pragma unroll
      for (int c = 0; c <= t; c++) acc[4 + t] = mfma16(A[bb & 1][t - c], B[bb & 1][c], acc[4 + t]);
    if (bb < 7) {
#pragma unroll
      for (int i = 0; i < 8; i++) {
        __builtin_amdgcn_sched_group_barrier(0x8, 1, 0);
        __builtin_amdgcn_sched_group_barrier(0x100, 1, 0);
      }
      __builtin_amdgcn_sched_group_barrier(0x8, 2, 0);
    }
  }
  __builtin_amdgcn_sched_barrier(0);
  if (Y_POST == 8) (void)gate_post(P, G_Y, wave == 0, inc);
  {
    LAS char* yb = sm + L_H + wave * (HC_B + 64) + n16 * 32 + g4 * 8;
#pragma unroll
    for (int t = 0; t < 4; t++) {
      v2u y2 = {umin(pk2(acc[4 + t][0], acc[4 + t][1]), MAXV), umin(pk2(acc[4 + t][2], acc[4 + t][3]), MAXV)};
      *(LAS v2u*)(yb + t * 512) = y2;
    }
  }
  lds_signal(sm, C_Y);
  STAMP(7);
  lds_wait(sm, C_Y, 4);
  publish_y(P, sm + L_H, HC_B + 64, 128, tid, 64, 2);
  STAMP(8);
}

struct Early {
  u32 kk[11], vv[8], dd;
  v4u sk;
  u64 epoch;
  u32 cx;
};

__device__ __forceinline__ void tile_wave(const Ctx& P, const Early& E, const bool MOE) {
  LAS char* sm = P.sm;
  const int tid = P.tid, lane = P.lane, wave = P.wave, C = P.C;
  const bool act = tid < 288;
  const int j4 = min(tid, 287) * 4;
  const int row0 = 8 * C * (D * 2);
  const u32 (&kk)[11] = E.kk;
  const u32 (&vv)[8] = E.vv;
  const v4u sk = E.sk;
  u32 qq[8];
  lds_wait(sm, C_TAG, 1);
  const u32 uinc = gate_inc(*(const LAS u32*)(sm + L_REC));
  __builtin_amdgcn_sched_barrier(0);
  u32 gold;
  asm volatile("" :: "v"(sk) : "memory");
  gold = gate_post(P, G_U, wave == 4, uinc);
  STAMP(12);
  u32 up[2][4];
  {
    float w0[4] = {lo16(sk.x), hi16(sk.x), lo16(sk.y), hi16(sk.y)};
    float w1[4] = {lo16(sk.z), hi16(sk.z), lo16(sk.w), hi16(sk.w)};
    float u0[8], u1[8];
#pragma unroll
    for (int s = 0; s < 8; s++) {
      float a = lo16(kk[s]) * w0[3], c = hi16(kk[s]) * w1[3];
      a = fmaf(lo16(kk[s + 1]), w0[2], a); c = fmaf(hi16(kk[s + 1]), w1[2], c);
      a = fmaf(lo16(kk[s + 2]), w0[1], a); c = fmaf(hi16(kk[s + 2]), w1[1], c);
      a = fmaf(lo16(kk[s + 3]), w0[0], a); c = fmaf(hi16(kk[s + 3]), w1[0], c);
      u0[s] = a * lo16(vv[s]); u1[s] = c * hi16(vv[s]);
    }
#pragma unroll
    for (int i = 0; i < 4; i++) {
      up[0][i] = umin(pk2(u0[7 - 2 * i], u0[6 - 2 * i]), MAXV);
      up[1][i] = umin(pk2(u1[7 - 2 * i], u1[6 - 2 * i]), MAXV);
    }
    STAMP(1);
    LAS char* stg = sm + L_STG + wave * 2048;
    v4u p0 = {up[0][0], up[0][1], up[0][2], up[0][3]}, p1 = {up[1][0], up[1][1], up[1][2], up[1][3]};
    if (act) { *(LAS v4u*)(stg + lane * 32) = p0; *(LAS v4u*)(stg + lane * 32 + 16) = p1; }
    asm volatile("" ::: "memory");
    p0 = *(const LAS v4u*)(stg + lane * 16);
    p1 = *(const LAS v4u*)(stg + (wave < 4 ? 1024 : 0) + lane * 16);
    STAMP(13);
    lds_wait(sm, C_CLR, 1);
    const int so = WS_U + C * TILE_B + wave * 2048 + lane * 16;
    __builtin_amdgcn_raw_buffer_store_b128(p0, P.rws, so, 0, AUX_SC1);
    __builtin_amdgcn_raw_buffer_store_b128(p1, P.rws, wave < 4 ? so + 1024 : OOB, 0, AUX_SC1);
  }
  __builtin_amdgcn_sched_barrier(0);
  if (MOE) lds_signal(sm, C_PUB);
  STAMP(2);
  lds_wait(sm, C_STAT, 1);
#pragma unroll
  for (int i = 0; i < 8; i++) qq[i] = *(const LAS u32*)(sm + xrow_off(i) + j4);
  lds_signal(sm, C_QQ);
  u32 dd = E.dd;
  asm volatile("" : "+v"(dd));
  if (wave == 4) gate_confirm(P, G_U, gold, uinc);
  float xn[2][8], c0[2][8];
  auto prepare = [&]()
  {
    const float d0 = lo16(dd), d1 = hi16(dd);
#pragma unroll
    for (int s2 = 0; s2 < 4; s2++) {
      const f4 st = *(const LAS f4*)(sm + L_STAT + s2 * 16);
#pragma unroll
      for (int e = 0; e < 2; e++) {
        const int s = 2 * s2 + e;
        const float xa = lo16(qq[s]), xb = hi16(qq[s]);
        const float na = fmaf(xa, st[2 * e], st[2 * e + 1]), nb = fmaf(xb, st[2 * e], st[2 * e + 1]);
        const u32 pa = up[0][(7 - s) >> 1], pb = up[1][(7 - s) >> 1];
        const float ua = ((7 - s) & 1) ? hi16(pa) : lo16(pa), ub = ((7 - s) & 1) ? hi16(pb) : lo16(pb);
        xn[0][s] = na; xn[1][s] = nb;
        c0[0][s] = fmaf(na, fmaf(ua, d0, 1.0f), xa);
        c0[1][s] = fmaf(nb, fmaf(ub, d1, 1.0f), xb);
        asm volatile("" : "+v"(xn[0][s]), "+v"(xn[1][s]), "+v"(c0[0][s]), "+v"(c0[1][s]));
      }
    }
  };
  if (MOE) {
    Ffn ffn;
    const int cu0 = wave < 3 ? wave : 2 * wave - 3;
    lds_wait(sm, C_PUB, 5);
    ffn.run(P, cu0, prepare);
  } else {
   prepare();
   if (P.is_conv) {
    if (wave < 4) {
      conv_phase(P, uinc);
    } else {
      lds_wait<1>(sm, C_GU, 1);
      lds_wait(sm, C_TRUST, 1);
      lds_wait(sm, C_QQ, 5);
      __builtin_amdgcn_s_sleep(2);
      const int goff = WS_U + (128 + (lane >> 2)) * TILE_B + (4 * P.cg + (lane & 3)) * 16;
      v4u g[8];
      _Pragma("nounroll") for (u32 spin = 0; spin < SPIN_CAP; spin++) {
        asm volatile("" ::: "memory");
#pragma unroll
        for (int it = 0; it < 8; it++) g[it] = __builtin_amdgcn_raw_buffer_load_b128(P.rws, goff, it * 16 * TILE_B, AUX_SC1);
        u32 mx = 0;
#pragma unroll
        for (int it = 0; it < 8; it++) mx = vmax4(g[it], mx);
        if (__builtin_expect(__all(mx != CLR), 1)) break;
        NOTE(27, spin + 1);
        __builtin_amdgcn_s_sleep(4);
      }
#pragma unroll
      for (int it = 0; it < 8; it++) *(LAS v4u*)(sm + L_U + (lane & 3) * UL_B + (32 + 128 + it * 16 + (lane >> 2)) * 16) = g[it];
      asm volatile("s_waitcnt vmcnt(0)" ::: "memory");
      lds_signal(sm, C_U2);
      STAMP(5);
      lds_wait<4>(sm, C_Y1, 4);
      publish_y(P, sm + L_STG, Y1_B, 0, lane, 32, 4);
    }
  } else {
    lds_wait(sm, C_PUB, 1);
    helper_prefetch(P);
    asm volatile("s_waitcnt vmcnt(0)" ::: "memory");
   }
  }
  lds_wait<1>(sm, C_SEL, 1);
  const u32 skip = __builtin_amdgcn_readfirstlane(*(const LAS u32*)(sm + L_REC + 4));
  const __amdgpu_buffer_rsrc_t ro = mkrsrc(P.out, S * D * 2);
  const int oo = act ? j4 : OOB;
  const int uoff = WS_U + C * TILE_B + j4 * 8;
  lds_wait<1>(sm, C_GY, 1);
  STAMP(9);
  v4u y0, y1;
  _Pragma("nounroll") for (u32 spin = 0; spin < SPIN_CAP; spin++) {
    asm volatile("" ::: "memory");
    y0 = __builtin_amdgcn_raw_buffer_load_b128(P.rws, uoff, 0, AUX_SC1);
    y1 = __builtin_amdgcn_raw_buffer_load_b128(P.rws, uoff + 16, 0, AUX_SC1);
    bool ok = vmax4(y0, vmax4(y1, 0u)) != CLR;
#pragma unroll
    for (int e = 0; e < 4; e++) ok = ok & (y0[e] != up[0][e]) & (y1[e] != up[1][e]);
    if (__builtin_expect(__all(ok), 1)) break;
    NOTE(25, spin + 1);
    __builtin_amdgcn_s_sleep(4);
  }
  STAMP(16);
#pragma unroll
  for (int e = 0; e < 4; e++) {
    const u32 a = y0[e] == SAME ? up[0][e] : y0[e], c = y1[e] == SAME ? up[1][e] : y1[e];
    const u32 o0 = pk2(fmaf(xn[0][2 * e], lo16(a), c0[0][2 * e]), fmaf(xn[1][2 * e], lo16(c), c0[1][2 * e]));
    const u32 o1 = pk2(fmaf(xn[0][2 * e + 1], hi16(a), c0[0][2 * e + 1]), fmaf(xn[1][2 * e + 1], hi16(c), c0[1][2 * e + 1]));
    const int so = row0 + (e >> 1) * 4 * (D * 2);
    if (!((skip >> (2 * e)) & 1)) __builtin_amdgcn_raw_buffer_store_b32(o0, ro, oo + ((2 * e) & 3) * (D * 2), so, OUT_AUX);
    if (!((skip >> (2 * e + 1)) & 1)) __builtin_amdgcn_raw_buffer_store_b32(o1, ro, oo + ((2 * e + 1) & 3) * (D * 2), so, OUT_AUX);
  }
}

__device__ __forceinline__ void route(const Ctx& P, u32 tag, u64 epoch) {
  LAS char* sm = P.sm;
  const int lane = P.lane, wave = P.wave;
  (void)wave;
  gate_wait(P, G_R, 16u, gate_inc(tag));
  STAMP(3);
  v4u g[16];
  u32 spin = 0;
  _Pragma("nounroll") for (; spin < SPIN_CAP; spin++) {
    asm volatile("" ::: "memory");
#pragma unroll
    for (int i = 0; i < 16; i++)
      g[i] = __builtin_amdgcn_raw_buffer_load_b128(P.rws, WS_R + lane * 16 + (i & 3) * 1024, (i >> 2) * 4096, AUX_SC1);
    u32 bad = 0;
#pragma unroll
    for (int i = 0; i < 16; i++) {
      bad = __builtin_amdgcn_bitop3_b32(bad, g[i].y, tag, 0xF6);
      bad = __builtin_amdgcn_bitop3_b32(bad, g[i].w, tag, 0xF6);
    }
    if (__builtin_expect(__all(bad == 0), 1)) break;
    NOTE(24, spin + 1);
    __builtin_amdgcn_s_sleep(4);
  }
  lds_signal(sm, C_TRUST);
  STAMP(4);
  if (P.b == 0 && lane == 0) __hip_atomic_store((GAS u64*)(P.ws + WS_E), epoch, __ATOMIC_RELAXED, __HIP_MEMORY_SCOPE_AGENT);
  u32 k1 = umax(g[0].x, g[0].z), k2 = umin(g[0].x, g[0].z), k3 = 0;
#pragma unroll
  for (int i = 2; i < 32; i++) {
    const u32 x = (i & 1) ? g[i >> 1].z : g[i >> 1].x;
    k3 = umed3(k2, k3, x);
    k2 = umed3(k1, k2, x);
    k1 = umax(k1, x);
  }
  u32 tau = umax(k1, dppu<0xB1>(k1));
  tau = umax(tau, dppu<0x4E>(tau));
  tau = umax(tau, dppu<0x141>(tau));
  tau = umin(tau, dppu<0x140>(tau));
  tau = xrow_umin(tau);
  LAS u32* cand = (LAS u32*)(sm + L_CAND);
  const u64 m1 = __ballot(k1 >= tau), m2 = __ballot(k2 >= tau), m3 = __ballot(k3 >= tau);
  const u32 n1 = __builtin_popcountll(m1), n2 = n1 + __builtin_popcountll(m2), n = n2 + __builtin_popcountll(m3);
  bool slow = n > 64;
  if (__builtin_expect(!slow, 1)) {
    const u32 p1 = __builtin_amdgcn_mbcnt_hi((u32)(m1 >> 32), __builtin_amdgcn_mbcnt_lo((u32)m1, 0u));
    const u32 p2 = __builtin_amdgcn_mbcnt_hi((u32)(m2 >> 32), __builtin_amdgcn_mbcnt_lo((u32)m2, n1));
    const u32 p3 = __builtin_amdgcn_mbcnt_hi((u32)(m3 >> 32), __builtin_amdgcn_mbcnt_lo((u32)m3, n2));
    cand[k1 >= tau ? p1 : 64] = k1;
    cand[k2 >= tau ? p2 : 64] = k2;
    cand[k3 >= tau ? p3 : 64] = k3;
    asm volatile("" ::: "memory");
    const u32 mine = cand[lane];
    u32 rank = 0;
    _Pragma("nounroll") for (u32 jj = 0; jj < n; jj += 8) {
      const v4u c4 = *(const LAS v4u*)(cand + jj), d4 = *(const LAS v4u*)(cand + jj + 4);
      rank += (c4.x > mine) + (c4.y > mine) + (c4.z > mine) + (c4.w > mine);
      rank += (d4.x > mine) + (d4.y > mine) + (d4.z > mine) + (d4.w > mine);
    }
    const bool in = (u32)lane < n && rank < 8;
    if (in) ((LAS u32*)(sm + L_SEL))[rank] = mine;
    slow = __any(in && (u32)lane >= n2);
  }
  if (__builtin_expect(slow, 0)) {
    _Pragma("nounroll") for (int r = 0; r < 8; r++) {
      u32 mx = 0;
#pragma unroll
      for (int i = 0; i < 16; i++) mx = umax3(mx, g[i].x, g[i].z);
      mx = wave_umax(mx);
#pragma unroll
      for (int i = 0; i < 16; i++) { g[i].x = g[i].x == mx ? 0u : g[i].x; g[i].z = g[i].z == mx ? 0u : g[i].z; }
      if (lane == 0) ((LAS u32*)(sm + L_SEL))[r] = mx;
    }
  }
  asm volatile("" ::: "memory");
  {
    const u32 tk = 2047u - (((LAS u32*)(sm + L_SEL))[lane & 7] & 2047u);
    u32 mk = (int)(tk >> 3) == P.C ? 1u << (tk & 7) : 0u;
    mk |= dppu<0xB1>(mk);
    mk |= dppu<0x4E>(mk);
    mk |= dppu<0x141>(mk);
    if (lane == 0) *(LAS u32*)(sm + L_REC + 4) = mk;
  }
  lds_signal(sm, C_SEL);
  STAMP(5);
}

__device__ __forceinline__ u32 load_msd(const Ctx& P, int tok, u32 tag) {
  v2u g;
  _Pragma("nounroll") for (u32 spin = 0; spin < SPIN_CAP; spin++) {
    asm volatile("" ::: "memory");
    g = __builtin_amdgcn_raw_buffer_load_b64(P.rws, WS_M + tok * 8, 0, AUX_SC1);
    if (__builtin_expect(__all(g.y == tag), 1)) break;
    __builtin_amdgcn_s_sleep(4);
  }
  return g.x;
}

__device__ __forceinline__ void routed_rows(const Ctx& P, u32 tag) {
  LAS char* sm = P.sm;
  const int lane = P.lane, wave = P.wave, C = P.C, cg = P.cg;
  (void)wave; (void)C; (void)cg;
  const __amdgpu_buffer_rsrc_t rq = mkrsrc(P.q, S * D * 2);
  {
    const int cw = wave - 6;
    const v4u s4 = *(const LAS v4u*)(sm + L_SEL + cw * 16);
    Row rr[4];
#pragma unroll
    for (int e = 0; e < 4; e++) {
      const int tok = 2047 - (int)(__builtin_amdgcn_readfirstlane(s4[e]) & 2047u);
      rr[e] = load_row<0>(rq, tok * (D * 2), lane);
    }
    const u32 ks = (lane & 2) ? ((lane & 1) ? s4.w : s4.z) : ((lane & 1) ? s4.y : s4.x);
    const u32 msd = load_msd(P, 2047 - (int)(ks & 2047u), tag);
    const float rsl = __builtin_amdgcn_rcpf(lo16(msd)), nml = -hi16(msd) * rsl;
#pragma unroll
    for (int e = 0; e < 4; e++) {
      const float rs = __uint_as_float(__builtin_amdgcn_readlane(__float_as_uint(rsl), e));
      const float nm = __uint_as_float(__builtin_amdgcn_readlane(__float_as_uint(nml), e));
      LAS char* dst = sm + L_XS + (cw * 4 + e) * XS_STRIDE;
      const Row& x = rr[e];
      v2u a = {pk2(fmaf(lo16(x.a0.x), rs, nm), fmaf(hi16(x.a0.x), rs, nm)), pk2(fmaf(lo16(x.a0.y), rs, nm), fmaf(hi16(x.a0.y), rs, nm))};
      v2u c = {pk2(fmaf(lo16(x.a1.x), rs, nm), fmaf(hi16(x.a1.x), rs, nm)), pk2(fmaf(lo16(x.a1.y), rs, nm), fmaf(hi16(x.a1.y), rs, nm))};
      *(LAS v2u*)(dst + lane * 8) = a;
      *(LAS v2u*)(dst + 512 + lane * 8) = c;
      *(LAS u16*)(dst + 1024 + lane * 2) = (u16)pk2(fmaf(lo16(x.a2), rs, nm), 0.f);
    }
    lds_signal(sm, C_XS);
    STAMP(6);
    ffn_act(P, wave);
  }
}

__device__ __forceinline__ void aux_wave(const Ctx& P, u32 cx, const int ROLE) {
  LAS char* sm = P.sm;
  const int lane = P.lane, wave = P.wave, C = P.C, cg = P.cg;
  (void)wave; (void)C; (void)cg;
  u32 tag;
    if (wave == 5) {
      asm volatile("s_waitcnt vmcnt(0)" ::: "memory");
      lds_signal(sm, C_CLR);
      STAMP(13);
    } else {
      if (CODE_PF) asm volatile("" :: "v"(cx));
      lds_wait(sm, C_TAG, 1);
    }
    if (ROLE == 0) {
      HCopy hc;
      hc.load(P, 2 * (wave - 5));
      STAMP(12);
      hc.store(P, 2 * (wave - 5));
      STAMP(3);
      if (wave == 5) {
        lds_wait<4>(sm, C_Y1, 4);
        publish_y(P, sm + L_STG + 256, Y1_B, 16, lane, 32, 4);
      }
    } else if (ROLE == 2) {
      lds_wait(sm, C_PUB, 1);
      helper_prefetch(P);
    }
    if (wave == 5) {
      if (ROLE == 1) {
        Ffn ffn;
        lds_wait(sm, C_STAT, 1);
        ffn.run(P, 7, []() {});
      }
      return;
    }
    tag = *(const LAS u32*)(sm + L_REC);
    if (ROLE == 1) { lds_wait<1>(sm, C_SEL, 1); __builtin_amdgcn_s_setprio(3); }
  if (ROLE == 1) routed_rows(P, tag);
  {
    gate_wait(P, G_Y, 9u, gate_inc(tag));
    lds_signal(sm, C_GY);
    STAMP(7);
  }
}

__device__ __forceinline__ void router_stats(const Ctx& P, Early& E, const int ROLE) {
  LAS char* sm = P.sm;
  const int lane = P.lane, C = P.C;
  u32 tag;
    __builtin_amdgcn_s_setprio(3);
    const int m = lane & 15, g4 = lane >> 4;
    u64 epoch = __hip_atomic_load((GAS u64*)(P.ws + WS_E), __ATOMIC_RELAXED, __HIP_MEMORY_SCOPE_AGENT);
    __builtin_amdgcn_sched_barrier(0);
    v4u xa[9], wb[9];
    const __amdgpu_buffer_rsrc_t rq = mkrsrc(P.q, S * D * 2);
    const int xo = (m & 7) * (D * 2) + (m >> 3) * 64 + g4 * 16;
#pragma unroll
    for (int i = 0; i < 9; i++) xa[i] = __builtin_amdgcn_raw_buffer_load_b128(rq, xo + i * 128, 8 * C * (D * 2), AUX_NT);
    __builtin_amdgcn_sched_barrier(0);
    const size_t pw = (size_t)P.wr + m * 64, po = (size_t)kOnes;
    const GAS char* wp = (const GAS char*)(m < 2 ? pw : po) + g4 * 16;
#pragma unroll
    for (int i = 0; i < 9; i++) wb[i] = *(const GAS v4u*)(wp + i * 128);
    STAMP(10);
    __builtin_amdgcn_sched_barrier(0);
    asm volatile("" : "+v"(epoch) :: "memory");
    epoch += 1;
    tag = (u32)epoch;
    if (lane == 0) *(LAS u32*)(sm + L_REC) = tag;
    lds_signal(sm, C_TAG);
    STAMP(1);
    f4 gx = {0.f, 0.f, 0.f, 0.f}, gw = {0.f, 0.f, 0.f, 0.f}, gs = {0.f, 0.f, 0.f, 0.f};
#pragma unroll
    for (int i = 0; i < 9; i++) gx = mfma16(xa[i], xa[i], gx);
    __builtin_amdgcn_sched_barrier(0);
    const u32 gold = gate_post(P, G_R, true, gate_inc(tag));
    STAMP(12);
    __builtin_amdgcn_sched_barrier(0);
#pragma unroll
    for (int i = 0; i < 9; i++) { gw = mfma16(wb[i], xa[i], gw); gs = mfma16(wb[i], wb[i], gs); }
    float sxx = (m & 2) ? ((m & 1) ? gx[3] : gx[2]) : ((m & 1) ? gx[1] : gx[0]);
    sxx = __uint_as_float(__builtin_amdgcn_ds_bpermute((m + (m & 12) * 4) * 4, __float_as_uint(sxx)));
    float sxw = m < 8 ? gw[0] : gw[1], sx = gw[2];
    sxw += dppf<0x128>(sxw);
    sx += dppf<0x128>(sx);
    sxx += dppf<0x128>(sxx);
    const float sw = __uint_as_float(__builtin_amdgcn_readlane(__float_as_uint(gs[0] + gs[1]), 2));
    const float mean = sx * (1.0f / D);
    const float var = fmaxf(fmaf(-mean, mean, sxx * (1.0f / D)), 0.f);
    const float mu = rbf(mean);
    const float sd = rbf(__builtin_amdgcn_sqrtf(rbf(rbf(var) + 1e-5f)));
    const float rs = __builtin_amdgcn_rcpf(sd);
    const u32 r = pk2(0.f, fmaf(-mu, sw, sxw) * rs);
    u32 key = ((r ^ (((int)r >> 31) | 0x80000000u)) & 0xFFFF0000u) | (u32)(2047 - (8 * C + (m & 7)));
    u32 msd = __float_as_uint(mu) | (__float_as_uint(sd) >> 16);
    asm volatile("" : "+v"(key), "+v"(msd));
    const v2u rec = {m < 8 ? key : msd, tag};
    const int ro = lane < 16 ? (m < 8 ? WS_R : WS_M) + (8 * C + (m & 7)) * 8 : OOB;
    lds_wait(sm, C_CLR, 1);
    __builtin_amdgcn_raw_buffer_store_b64(rec, P.rws, ro, 0, AUX_SC1);
    STAMP(2);
    if (lane < 8) { f2 st = {rs, -mu * rs}; *(LAS f2*)(sm + L_STAT + m * 8) = st; }
    {
      const int t = m & 7, t3 = (t * 11) >> 5;
      LAS char* xd = sm + L_U + t3 * UL_B + 512 + (t - 3 * t3) * (D * 2) + (m >> 3) * 64 + g4 * 16;
#pragma unroll
      for (int i = 0; i < 9; i++) *(LAS v4u*)(xd + i * 128) = xa[i];
    }
    lds_signal(sm, C_STAT);
    if (ROLE == 2) lds_signal(sm, C_PUB);
    E.epoch = epoch;
    E.cx = gold;
}

__device__ __forceinline__ void router_wave(const Ctx& P, const Early& E, const int ROLE) {
  LAS char* sm = P.sm;
  const int lane = P.lane, wave = P.wave, C = P.C, cg = P.cg;
  (void)wave; (void)C; (void)cg;
  u32 tag;
    const u64 epoch = E.epoch;
    tag = (u32)epoch;
    const u32 gold = E.cx;
    gate_confirm(P, G_R, gold, gate_inc(tag));
    route(P, tag, epoch);
    if (ROLE != 1) __builtin_amdgcn_s_setprio(0);
  if (ROLE == 1) routed_rows(P, tag);
  if (ROLE == 0) {
    const int pi = lane & 7, ti = 4 * (pi & 1) + (pi & 2) + (pi >> 2);
    const u32 kx = ((LAS u32*)(sm + L_SEL))[ti];
    const int tok = 2047 - (int)(kx & 2047u);
    const v2u x2 = *(const GAS v2u*)((const GAS char*)P.q + (size_t)tok * (D * 2) + cg * 8);
    const v2u d2 = *(const GAS v2u*)((const GAS char*)P.dsk + cg * 8);
    const u32 msd = load_msd(P, tok, tag);
    const float rs = __builtin_amdgcn_rcpf(lo16(msd)), nm = -hi16(msd) * rs;
    const u32 rb = kx & 0xFFFF0000u;
    const float r = __uint_as_float((rb & 0x80000000u) ? (rb ^ 0x80000000u) : (~rb & 0xFFFF0000u));
    const float rsig = rbf(__builtin_amdgcn_rcpf(1.0f + __expf(-r)));
    STAMP(6);
    lds_wait<2>(sm, C_Y, 4);
    float o[4];
#pragma unroll
    for (int e = 0; e < 4; e++) {
      const float y = lo16(*(const LAS u16*)(sm + (tok < 1024 ? L_STG + e * Y1_B : L_H + e * (HC_B + 64) - 2048) + tok * 2));
      const float u = lo16(*(const LAS u16*)(sm + L_U + e * UL_B + (32 + (tok >> 3)) * 16 + (7 - (tok & 7)) * 2));
      const float x = (e & 1) ? hi16(x2[e >> 1]) : lo16(x2[e >> 1]);
      const float dk = (e & 1) ? hi16(d2[e >> 1]) : lo16(d2[e >> 1]);
      const float n = fmaf(x, rs, nm);
      o[e] = fmaf(n, fmaf(u, dk, y), x) + n;
      asm volatile("" : "+v"(o[e]));
    }
    gate_wait(P, G_P, lane < 8 ? 5u : 4u, gate_inc(tag));
    STAMP(7);
    v4u g[9];
    const u32 tag12 = tag & 0x7FFu;
    _Pragma("nounroll") for (u32 spin = 0; spin < SPIN_CAP; spin++) {
      asm volatile("" ::: "memory");
#pragma unroll
      for (int i = 0; i < 9; i++)
        g[i] = __builtin_amdgcn_raw_buffer_load_b128(P.rws, WS_P + (lane >> 3) * PART_B + cg * 128 + pi * 16, 8 * i * PART_B, AUX_SC1);
      u32 bad = 0;
#pragma unroll
      for (int i = 0; i < 9; i++)
#pragma unroll
        for (int e = 0; e < 4; e++) bad = __builtin_amdgcn_bitop3_b32(bad, g[i][e], tag12, 0xF6);
      if (__builtin_expect(__all((bad & 0xFFFu) == 0), 1)) break;
      NOTE(25, spin + 1);
      __builtin_amdgcn_s_sleep(4);
    }
    STAMP(8);
    f4 s4 = __builtin_bit_cast(f4, g[0]);
#pragma unroll
    for (int i = 1; i < 9; i++) s4 += __builtin_bit_cast(f4, g[i]);
#pragma unroll
    for (int e = 0; e < 4; e++) {
      float sv = s4[e];
      sv += dppf<0x128>(sv);
      o[e] = fmaf(rbf(xrow_sum(sv)), rsig, o[e]);
    }
    if (lane < 8) {
      v2u o2 = {pk2(o[0], o[1]), pk2(o[2], o[3])};
      *(GAS v2u*)((GAS char*)P.out + (size_t)tok * (D * 2) + cg * 8) = o2;
    }
  }
}

__global__ __launch_bounds__(NTHR, 2) void fused_kernel(
    const u16* __restrict__ q, const u16* __restrict__ k, const u16* __restrict__ v, const u16* __restrict__ shk,
    const u16* __restrict__ hk, const u16* __restrict__ dsk, const u16* __restrict__ wr, const u16* __restrict__ wg,
    const u16* __restrict__ wu, const u16* __restrict__ wd, u16* __restrict__ out, char* ws) {
  extern __shared__ __attribute__((aligned(16))) char smem_[];
  Ctx P;
  P.q = q; P.k = k; P.v = v; P.shk = shk; P.hk = hk; P.dsk = dsk; P.wr = wr; P.wg = wg; P.wu = wu; P.wd = wd;
  P.out = out; P.ws = ws;
  u64 pc;
  asm volatile("s_getpc_b64 %0" : "=s"(pc));
  P.code = (const char*)(pc & ~(u64)127);
  P.sm = (LAS char*)smem_;
  P.tid = threadIdx.x; P.lane = P.tid & 63;
  P.wave = __builtin_amdgcn_readfirstlane(P.tid >> 6);
  const int b = blockIdx.x;
  P.b = b;
  P.C = (b & 7) * 32 + (b >> 3);
  P.is_conv = b < NCONV;
  const bool is_moe = !P.is_conv && b < NCONV + NMOE;
  P.cg = (b & 7) * 18 + (b >> 3);
  P.mw = (b & 7) * 9 + ((b - NCONV) >> 3);
  P.rws = mkrsrc(ws, WS_END);
  const int lane = P.lane, wave = P.wave;
  (void)lane;
  STAMP(0); STAMPC(30);
  Early E;
  if (wave == 6) {
    constexpr int NL = (CODE_PF * 8 + 31) / 32;
    if (CODE_PF) E.cx = *(const GAS u32*)(P.code + min(((b >> 3) * NL + min(lane, NL - 1)) * 128, CODE_PF * 1024 - 128));
    const v4u z = {0u, 0u, 0u, 0u};
    *(LAS v4u*)(P.sm + L_U + (lane >> 4) * UL_B + (lane & 15) * 16) = z;
    *(LAS v4u*)(P.sm + L_U + (lane >> 4) * UL_B + 256 + (lane & 15) * 16) = z;
    ((LAS u32*)(P.sm + L_MISC))[lane] = 0u;
    ((LAS u32*)(P.sm + L_MISC))[64 + lane] = 0u;
    asm volatile("s_waitcnt lgkmcnt(0)" ::: "memory");
  }
  asm volatile("s_barrier" ::: "memory");
  asm volatile("" :: "s"(q), "s"(k), "s"(v), "s"(shk), "s"(hk), "s"(dsk), "s"(wr), "s"(wg), "s"(wu), "s"(wd), "s"(out), "s"(ws));
  STAMP(11);
  const int row0 = 8 * P.C * (D * 2);
  asm volatile("" : "+s"(P.wave));
  if (P.wave == 5) {
    const v4u g4 = {GMAGIC, GMAGIC, GMAGIC, GMAGIC};
    __builtin_amdgcn_raw_buffer_store_b128(g4, P.rws, lane < 32 ? gate_off(lane >> 3, b >> 4, b & 15) + (lane & 7) * 16 : OOB, 0, AUX_SC1);
    const v4u c4 = {CLR, CLR, CLR, CLR};
#pragma unroll
    for (int i = 0; i < 9; i++) __builtin_amdgcn_raw_buffer_store_b128(c4, P.rws, WS_U + lane * 16 + (i & 3) * 1024, P.C * TILE_B + (i >> 2) * 4096, AUX_SC1);
  }
  asm volatile("" : "+s"(P.wave));
  if (P.wave < 5) {
    const int j4 = min(P.tid, 287) * 4;
    const __amdgpu_buffer_rsrc_t rk = mkrsrc(P.k, S * D * 2), rv = mkrsrc(P.v, S * D * 2);
    if (TILE_DELAY) __builtin_amdgcn_s_sleep(TILE_DELAY);
    __builtin_amdgcn_sched_barrier(0);
#pragma unroll
    for (int i = 0; i < 3; i++) E.kk[i] = __builtin_amdgcn_raw_buffer_load_b32(rk, row0 + (i - 3) * (D * 2) + j4, 0, AUX_NT);
#pragma unroll
    for (int i = 0; i < 8; i++) {
      E.kk[3 + i] = __builtin_amdgcn_raw_buffer_load_b32(rk, j4 + (i & 3) * (D * 2), row0 + (i & 4) * (D * 2), AUX_NT);
      E.vv[i] = __builtin_amdgcn_raw_buffer_load_b32(rv, j4 + (i & 3) * (D * 2), row0 + (i & 4) * (D * 2), AUX_NT);
    }
    E.sk = *(const GAS v4u*)((const GAS char*)P.shk + j4 * 4);
    E.dd = *(const GAS u32*)((const GAS char*)P.dsk + j4);
  }
  const int role = P.is_conv ? 0 : (is_moe ? 1 : 2);
  asm volatile("" : "+s"(P.wave));
  STAMP(10);
  if (P.wave == 7) router_stats(P, E, role);
  asm volatile("" : "+s"(P.wave));
  if (P.wave != 7) {
    if (P.wave >= 5) aux_wave(P, E.cx, role);
    else tile_wave(P, E, is_moe);
  } else {
    router_wave(P, E, role);
  }
  FLUSH();
}

}

extern "C" void kernel_launch(void* const* d_in, const int* in_sizes, int n_in, void* d_out, int out_size, void* d_ws,
                              size_t ws_size, hipStream_t stream) {
  (void)in_sizes; (void)n_in; (void)out_size; (void)ws_size;
  (void)hipFuncSetAttribute((const void*)fused_kernel, hipFuncAttributeMaxDynamicSharedMemorySize, L_TOTAL);
  fused_kernel<<<NWG, NTHR, L_TOTAL, stream>>>(
      (const u16*)d_in[0], (const u16*)d_in[1], (const u16*)d_in[2], (const u16*)d_in[3], (const u16*)d_in[4],
      (const u16*)d_in[5], (const u16*)d_in[7], (const u16*)d_in[8], (const u16*)d_in[9], (const u16*)d_in[10],
      (u16*)d_out, (char*)d_ws + (WS_AT_END ? ((ws_size - WS_END) & ~(size_t)0xFFFF) : 0));
}
```

```cpp
#include <hip/hip_runtime.h>
#include <hip/hip_bf16.h>

typedef unsigned int u32;
typedef unsigned long long u64;
typedef unsigned short u16;
typedef u32 v4u __attribute__((ext_vector_type(4)));
typedef u32 v2u __attribute__((ext_vector_type(2)));
typedef float f4 __attribute__((ext_vector_type(4)));
typedef float f2 __attribute__((ext_vector_type(2)));
typedef __bf16 bf8 __attribute__((ext_vector_type(8)));
typedef __bf16 bf2 __attribute__((ext_vector_type(2)));
#define LAS __attribute__((address_space(3)))
#define GAS __attribute__((address_space(1)))

namespace {

constexpr int S = 2048, D = 576, F = 4608;
constexpr int NWG = 256, NTHR = 512, NCONV = 144, NMOE = 72;
constexpr int AUX_SC1 = 16;
constexpr u32 CLR = 0xFFFFFFFFu, SAME = 0xFFFFFFFEu, MAXV = 0xFFFFFFFDu;
constexpr u32 SPIN_CAP = 1u << 16, GATE_CAP = 96;
constexpr u32 GMAGIC = 0x5AC3E900u;
constexpr int OOB = 0x7FFFFF00;
#define U_POST 0
#define WS_AT_END 0
#define HELPERS 1
#define Y_POST 7
#define OUT_AUX 0
#define W2_GATE 2
#define TILE_DELAY 2
#define CODE_PF 40
#define USE_NT 0
constexpr int AUX_NT = USE_NT ? 2 : 0;

constexpr int WS_E = 0;
constexpr int WS_G = 4096;
constexpr int GATE_B = 256 * 256;
constexpr int WS_R = WS_G + 4 * GATE_B;
constexpr int WS_M = WS_R + S * 8;
constexpr int WS_U = WS_M + S * 8;
constexpr int TILE_B = D * 16;
constexpr int WS_P = WS_U + NWG * TILE_B;
constexpr int PART_B = NCONV * 128;
constexpr int WS_END = WS_P + NMOE * PART_B;
constexpr int WS_DBG = (WS_END + 4095) / 4096 * 4096;
enum { G_R = 0, G_U, G_Y, G_P };
#define ONES8 0x3F803F80u, 0x3F803F80u, 0x3F803F80u, 0x3F803F80u, 0x3F803F80u, 0x3F803F80u, 0x3F803F80u, 0x3F803F80u
#define ONES32 ONES8, ONES8, ONES8, ONES8
__device__ const u32 kOnes[288] __attribute__((aligned(128))) = {ONES32, ONES32, ONES32, ONES32, ONES32, ONES32, ONES32, ONES32, ONES32};

constexpr int HC_B = 2080 * 16;
constexpr int L_H = 0;
constexpr int UL_B = 290 * 16;
constexpr int L_U = L_H + 4 * HC_B;
constexpr int L_STG = L_U + 4 * UL_B;
constexpr int L_MISC = L_STG + TILE_B;
constexpr int L_CNT = L_MISC;
constexpr int L_CAND = L_CNT + 96;
constexpr int L_SEL = L_CAND + 288;
constexpr int L_REC = L_SEL + 32;
constexpr int L_STAT = L_REC + 32;
constexpr int Y1_B = 2048 + 64;
constexpr int L_END = L_STAT + 64;
constexpr int XS_STRIDE = 1184;
constexpr int L_XS = 0;
constexpr int L_RED = L_XS + 8 * XS_STRIDE;
constexpr int L_HB = L_RED + 6 * 8 * 128 * 4;
constexpr int HB_STRIDE = 160;
static_assert(L_END + 1024 <= 160 * 1024, "lds");
static_assert(L_HB + 8 * HB_STRIDE <= L_U, "lds");
static_assert(L_END - L_MISC == 512, "zero fill");

__device__ __forceinline__ constexpr int xrow_off(int t) { return L_U + (t / 3) * UL_B + 512 + (t % 3) * (D * 2); }
enum { C_CLR = 0, C_STAT, C_TRUST, C_GU, C_SEL, C_H, C_U, C_Y, C_XS, C_RED, C_HB, C_GY, C_PUB, C_TAG, C_QQ, C_U2, C_Y1, C_W1, C_DN };

__device__ __forceinline__ float lo16(u32 x) { return __uint_as_float(x << 16); }
__device__ __forceinline__ float hi16(u32 x) { return __uint_as_float(x & 0xFFFF0000u); }
__device__ __forceinline__ u32 pk2(float a, float b) {
  bf2 p = {(__bf16)a, (__bf16)b};
  return __builtin_bit_cast(u32, p);
}
__device__ __forceinline__ float rbf(float a) { return lo16(pk2(a, a)); }
__device__ __forceinline__ float dot2(u32 a, u32 b, float c) {
  return __builtin_amdgcn_fdot2_f32_bf16(__builtin_bit_cast(bf2, a), __builtin_bit_cast(bf2, b), c, false);
}
template <int CTRL> __device__ __forceinline__ u32 dppu(u32 x) {
  return (u32)__builtin_amdgcn_mov_dpp((int)x, CTRL, 0xf, 0xf, true);
}
template <int CTRL> __device__ __forceinline__ float dppf(float x) { return __uint_as_float(dppu<CTRL>(__float_as_uint(x))); }
__device__ __forceinline__ float xrow_sum(float x) {
  auto s = __builtin_amdgcn_permlane16_swap(__float_as_uint(x), __float_as_uint(x), false, false);
  x = __uint_as_float(s[0]) + __uint_as_float(s[1]);
  auto t = __builtin_amdgcn_permlane32_swap(__float_as_uint(x), __float_as_uint(x), false, false);
  return __uint_as_float(t[0]) + __uint_as_float(t[1]);
}
__device__ __forceinline__ u32 umax(u32 a, u32 b) { return a > b ? a : b; }
__device__ __forceinline__ u32 umin(u32 a, u32 b) { return a < b ? a : b; }
__device__ __forceinline__ u32 umed3(u32 a, u32 b, u32 c) { return umax(umin(a, b), umin(umax(a, b), c)); }
__device__ __forceinline__ u32 xrow_umin(u32 x) {
  auto s = __builtin_amdgcn_permlane16_swap(x, x, false, false);
  x = umin(s[0], s[1]);
  auto t = __builtin_amdgcn_permlane32_swap(x, x, false, false);
  return umin(t[0], t[1]);
}
__device__ __forceinline__ u32 wave_umax(u32 x) {
  x = umax(x, dppu<0xB1>(x));
  x = umax(x, dppu<0x4E>(x));
  x = umax(x, dppu<0x141>(x));
  x = umax(x, dppu<0x140>(x));
  auto s = __builtin_amdgcn_permlane16_swap(x, x, false, false);
  x = umax(s[0], s[1]);
  auto t = __builtin_amdgcn_permlane32_swap(x, x, false, false);
  return umax(t[0], t[1]);
}
__device__ __forceinline__ void lds_signal(LAS char* sm, int c) {
  u64 keep;
  asm volatile("s_waitcnt lgkmcnt(0)\n\ts_mov_b64 %0, exec\n\ts_mov_b64 exec, 1\n\tds_add_u32 %1, %2\n\ts_mov_b64 exec, %0"
               : "=&s"(keep) : "v"((u32)(size_t)(sm + L_CNT + 4 * c)), "v"(1u) : "memory");
}
template <int SLEEP = 0> __device__ __forceinline__ void lds_wait(LAS char* sm, int c, u32 target) {
  u32 v, sv;
  if (SLEEP == 0)
    asm volatile("1:\n\tds_read_b32 %0, %2\n\ts_waitcnt lgkmcnt(0)\n\tv_readfirstlane_b32 %1, %0\n\ts_cmp_lt_u32 %1, %3\n\ts_cbranch_scc1 1b"
                 : "=&v"(v), "=&s"(sv) : "v"((u32)(size_t)(sm + L_CNT + 4 * c)), "s"(target) : "memory", "scc");
  else
    asm volatile("1:\n\ts_sleep %4\n\tds_read_b32 %0, %2\n\ts_waitcnt lgkmcnt(0)\n\tv_readfirstlane_b32 %1, %0\n\ts_cmp_lt_u32 %1, %3\n\ts_cbranch_scc1 1b"
                 : "=&v"(v), "=&s"(sv) : "v"((u32)(size_t)(sm + L_CNT + 4 * c)), "s"(target), "n"(SLEEP) : "memory", "scc");
}
__device__ __forceinline__ bool lds_arrive(LAS char* sm, int c, u32 last) {
  u64 keep;
  u32 v, sv;
  asm volatile("s_mov_b64 %0, exec\n\ts_mov_b64 exec, 1\n\tds_add_rtn_u32 %1, %3, %4\n\ts_waitcnt lgkmcnt(0)\n\tv_readfirstlane_b32 %2, %1\n\ts_mov_b64 exec, %0"
               : "=&s"(keep), "=&v"(v), "=&s"(sv) : "v"((u32)(size_t)(sm + L_CNT + 4 * c)), "v"(1u) : "memory");
  return sv == last;
}
__device__ __forceinline__ void wg_barrier() { asm volatile("s_waitcnt lgkmcnt(0)\n\ts_barrier" ::: "memory"); }
__device__ __forceinline__ u32 umax3(u32 a, u32 b, u32 c) { return umax(umax(a, b), c); }
__device__ __forceinline__ u32 vmax4(v4u g, u32 acc) { return umax3(umax3(g.x, g.y, acc), g.z, g.w); }
__device__ __forceinline__ f4 mfma16(v4u a, v4u b, f4 c) {
  return __builtin_amdgcn_mfma_f32_16x16x32_bf16(__builtin_bit_cast(bf8, a), __builtin_bit_cast(bf8, b), c, 0, 0, 0);
}
__device__ __forceinline__ __amdgpu_buffer_rsrc_t mkrsrc(const void* p, int bytes) {
  return __builtin_amdgcn_make_buffer_rsrc((void*)p, 0, bytes, 0x00020000);
}

#define DBG_TS 1
#define STAMP(i) do { const u32 t_ = (u32)__builtin_amdgcn_s_memrealtime() | 1u; if (P.lane == 0) ((LAS u32*)(P.sm + L_END))[P.wave * 32 + (i)] = t_; } while (0)
#define STAMPC(i) do { const u32 t_ = (u32)__builtin_amdgcn_s_memtime(); if (P.lane == 0) ((LAS u32*)(P.sm + L_END))[P.wave * 32 + (i)] = t_; } while (0)
#define FLUSH() do { STAMP(29); if (P.wave == 7 && P.is_conv) { asm volatile("s_waitcnt lgkmcnt(0)" ::: "memory"); const u32 a_ = ((LAS u32*)(P.sm + L_END))[P.wave * 32 + 29], z_ = ((LAS u32*)(P.sm + L_END))[P.wave * 32], e_ = ((LAS u32*)(P.sm + L_END))[P.wave * 32 + 29]; const u32 d_ = __builtin_amdgcn_readfirstlane((a_ - z_) * 0), s_ = __builtin_amdgcn_readfirstlane(e_); while ((u32)__builtin_amdgcn_s_memrealtime() - s_ < d_ && d_ < 20000u) __builtin_amdgcn_s_sleep(1); } } while (0)
#define NOTE(i, v) do { if (P.lane == 0) ((LAS u32*)(P.sm + L_END))[P.wave * 32 + (i)] = (v); } while (0)
constexpr int L_TOTAL = L_END + 1024;

struct Ctx {
  const u16 *q, *k, *v, *shk, *hk, *dsk, *wr, *wg, *wu, *wd;
  u16* out;
  char* ws;
  const char* code;
  __amdgpu_buffer_rsrc_t rws;
  LAS char* sm;
  int tid, lane, wave, b, C, cg, mw;
  bool is_conv;
};

__device__ __forceinline__ int gate_off(int gate, int rep, int shard) { return WS_G + gate * GATE_B + (rep * 16 + shard) * 256; }
__device__ __forceinline__ u32 gate_add(const Ctx& P, int off, int val) {
  return (u32)__builtin_amdgcn_raw_ptr_buffer_atomic_add_i32(val, P.rws, off, 0, 0);
}
__device__ __forceinline__ u32 gate_post(const Ctx& P, int gate, bool on, u32 inc) {
  return gate_add(P, (on && P.lane < 16) ? gate_off(gate, P.lane, P.b & 15) : OOB, (int)inc);
}
__device__ __forceinline__ void gate_confirm(const Ctx& P, int gate, u32 old, u32 inc) {
  bool bad = P.lane < 16;
  _Pragma("nounroll") for (u32 spin = 0; spin < GATE_CAP; spin++) {
    const u32 d = old - GMAGIC;
    bad = bad && ((d & 0xF0u) != 0u || d != (d & 0xFu) * inc);
    if (__builtin_expect(!__any(bad), 1)) break;
    NOTE(26, spin + 1);
    __builtin_amdgcn_s_sleep(2);
    old = gate_add(P, bad ? gate_off(gate, P.lane, P.b & 15) : OOB, (int)inc);
  }
}
__device__ __forceinline__ void gate_wait(const Ctx& P, int gate, u32 expect, u32 inc) {
  const int off = P.lane < 16 ? gate_off(gate, P.b >> 4, P.lane) : OOB;
  const u32 want = P.lane < 16 ? GMAGIC + expect * inc : 0u;
  _Pragma("nounroll") for (u32 spin = 0; spin < GATE_CAP; spin++)
    if (__all(gate_add(P, off, 0) == want)) break;
}
__device__ __forceinline__ u32 gate_inc(u32 tag) { return 1u + (tag << 8); }

struct Row { v2u a0, a1; u32 a2; };
template <int AUX> __device__ __forceinline__ Row load_row(__amdgpu_buffer_rsrc_t r, int soff, int lane) {
  Row x;
  x.a0 = __builtin_amdgcn_raw_buffer_load_b64(r, lane * 8, soff, AUX);
  x.a1 = __builtin_amdgcn_raw_buffer_load_b64(r, lane * 8 + 512, soff, AUX);
  x.a2 = __builtin_amdgcn_raw_buffer_load_b16(r, lane * 2 + 1024, soff, AUX);
  return x;
}
__device__ __forceinline__ void hcopy_store(LAS char* dst, v4u lo, v4u hi, bool first) {
  u32 d[8], o[7];
#pragma unroll
  for (int e = 0; e < 4; e++) { d[e] = lo[e]; d[4 + e] = hi[e]; }
#pragma unroll
  for (int e = 0; e < 7; e++) o[e] = __builtin_amdgcn_alignbit(d[e + 1], d[e], 16);
  constexpr int BASE[8] = {0, 260, 519, 778, 1037, 1296, 1556, 1815};
#pragma unroll
  for (int e = 0; e < 4; e++) {
    v4u a = {d[e], d[e + 1], d[e + 2], d[e + 3]}, c = {o[e], o[e + 1], o[e + 2], o[e + 3]};
    if (e > 0 || first) *(LAS v4u*)(dst + BASE[2 * e] * 16) = a;
    *(LAS v4u*)(dst + BASE[2 * e + 1] * 16) = c;
  }
}
struct HCopy {
  v4u lo[8];
  __device__ __forceinline__ void load(const Ctx& P, int ch0) {
    const __amdgpu_buffer_rsrc_t rh = mkrsrc(P.hk + (size_t)(4 * P.cg + ch0) * S, 2 * S * 2);
#pragma unroll
    for (int r = 0; r < 8; r++) lo[r] = __builtin_amdgcn_raw_buffer_load_b128(rh, P.lane * 16 + (r & 3) * 1024, (r >> 2) * 4096, AUX_NT);
  }
  __device__ __forceinline__ void store(const Ctx& P, int ch0) {
    LAS char* dst = P.sm + L_H + ch0 * HC_B + (P.lane + 2) * 16;
#pragma unroll
    for (int r = 0; r < 8; r++) *(LAS v4u*)(dst + (r >> 2) * HC_B + (r & 3) * 1024) = lo[r];
    {
      const int c = P.lane & 1, m1 = (P.lane >> 1) & 1;
      const v4u z = {0u, 0u, 0u, 0u};
      v4u h0 = *(const LAS v4u*)(P.sm + L_H + (ch0 + c) * HC_B + 32);
      h0 = m1 ? h0 : z;
      if (P.lane < 4) hcopy_store(P.sm + L_H + (ch0 + c) * HC_B + m1 * 16, z, h0, true);
    }
    v4u hi[8];
#pragma unroll
    for (int r = 0; r < 8; r++) hi[r] = *(const LAS v4u*)(dst + (r >> 2) * HC_B + (r & 3) * 1024 + 16);
#pragma unroll
    for (int r = 0; r < 8; r++) hcopy_store(dst + (r >> 2) * HC_B + (r & 3) * 1024, lo[r], hi[r], false);
    lds_signal(P.sm, C_H);
  }
};

__device__ __forceinline__ void ffn_act(const Ctx& P, int tk) {
  LAS char* sm = P.sm;
  lds_wait(sm, C_RED, 6);
  float G = 0.f, U = 0.f;
#pragma unroll
  for (int w = 0; w < 6; w++) {
    G += *(const LAS float*)(sm + L_RED + ((w * 8 + tk) * 128 + P.lane) * 4);
    U += *(const LAS float*)(sm + L_RED + ((w * 8 + tk) * 128 + 64 + P.lane) * 4);
  }
  G = rbf(G);
  const float sg = rbf(G * __builtin_amdgcn_rcpf(1.0f + __expf(-G)));
  *(LAS u16*)(sm + L_HB + tk * HB_STRIDE + P.lane * 2) = (u16)pk2(sg * rbf(U), 0.f);
  lds_signal(sm, C_HB);
}

struct Ffn {
  template <class FN> __device__ __forceinline__ void run(const Ctx& P, int cu0, FN&& between) {
    LAS char* sm = P.sm;
    const int lane = P.lane, wave = P.wave, n16 = lane & 15, g4 = lane >> 4;
    v4u W1[3][8];
    v2u W2[2][2][8];
    const GAS char* wp = (const GAS char*)(n16 < 8 ? P.wg : P.wu) + ((size_t)(96 * wave + 8 * g4) * F + 64 * P.mw + 8 * (n16 & 7)) * 2;
#pragma unroll
    for (int i = 0; i < 3; i++)
#pragma unroll
      for (int jj = 0; jj < 8; jj++) {
        const GAS v4u* a = (const GAS v4u*)(wp + (size_t)(32 * i + jj) * (F * 2));
        W1[i][jj] = USE_NT ? __builtin_nontemporal_load(a) : *a;
      }
    __builtin_amdgcn_sched_barrier(0);
    STAMP(3);
    between();
    const __amdgpu_buffer_rsrc_t rd = mkrsrc(P.wd + (size_t)64 * P.mw * D, 64 * D * 2);
    const int o2 = (8 * g4 * D + 64 * cu0 + 4 * n16) * 2;
    auto load2 = [&]() {
      __builtin_amdgcn_sched_barrier(0);
#pragma unroll
      for (int ci = 0; ci < 2; ci++)
#pragma unroll
        for (int ks = 0; ks < 2; ks++)
#pragma unroll
          for (int jj = 0; jj < 8; jj++)
            W2[ci][ks][jj] = __builtin_amdgcn_raw_buffer_load_b64(rd, ((ci == 0 || wave >= 3) ? o2 : OOB) + (jj & 3) * (D * 2) + ci * 128,
                                                                  (32 * ks + (jj & 4)) * (D * 2), AUX_NT);
      __builtin_amdgcn_sched_barrier(0);
    };
    v4u Bf[3][8];
#pragma unroll
    for (int i = 0; i < 3; i++) {
#pragma unroll
      for (int e = 0; e < 8; e++) {
#pragma unroll
        for (int d = 0; d < 4; d++)
          Bf[i][e][d] = __builtin_amdgcn_perm(W1[i][2 * d + 1][e >> 1], W1[i][2 * d][e >> 1], (e & 1) ? 0x07060302u : 0x05040100u);
        asm volatile("" : "+v"(Bf[i][e]));
      }
      if (W2_GATE == 3 && i == 0) load2();
    }
    STAMP(17);
    load2();
    const u32 tag = *(const LAS u32*)(sm + L_REC), tag12 = tag & 0x7FFu, rnd = 0x800u - tag12;
    STAMP(4);
    lds_wait<1>(sm, C_XS, 2);
    STAMP(5);
    f4 acc[8];
#pragma unroll
    for (int e = 0; e < 8; e++) acc[e] = (f4){0.f, 0.f, 0.f, 0.f};
#pragma unroll
    for (int i = 0; i < 3; i++) {
      const v4u A = *(const LAS v4u*)(sm + L_XS + (n16 & 7) * XS_STRIDE + (96 * wave + 32 * i + 8 * g4) * 2);
#pragma unroll
      for (int e = 0; e < 8; e++) acc[e] = mfma16(A, Bf[i][e], acc[e]);
    }
    {
      LAS char* dst = sm + L_RED + ((wave * 8 + 4 * (g4 & 1) + (g4 & 2)) * 128 + 8 * n16) * 4;
#pragma unroll
      for (int r = 0; r < 2; r++) {
        f4 a, c;
#pragma unroll
        for (int e = 0; e < 4; e++) { a[e] = g4 >= 2 ? acc[e][2 + r] : acc[e][r]; c[e] = g4 >= 2 ? acc[4 + e][2 + r] : acc[4 + e][r]; }
        *(LAS f4*)(dst + r * 512) = a;
        *(LAS f4*)(dst + r * 512 + 16) = c;
      }
    }
    lds_signal(sm, C_RED);
    STAMP(14);
    ffn_act(P, wave);
    STAMP(15);
    __builtin_amdgcn_sched_barrier(0);
    v4u Bd[2][2][4];
#pragma unroll
    for (int ci = 0; ci < 2; ci++)
      {
#pragma unroll
        for (int ks = 0; ks < 2; ks++)
#pragma unroll
          for (int e = 0; e < 4; e++) {
#pragma unroll
            for (int d = 0; d < 4; d++)
              Bd[ci][ks][e][d] = __builtin_amdgcn_perm(W2[ci][ks][2 * d + 1][e >> 1], W2[ci][ks][2 * d][e >> 1], (e & 1) ? 0x07060302u : 0x05040100u);
            asm volatile("" : "+v"(Bd[ci][ks][e]));
          }
      }
    asm volatile("s_waitcnt vmcnt(0)" ::: "memory");
    (void)gate_post(P, G_P, lds_arrive(sm, C_DN, 5), gate_inc(tag));
    STAMP(6);
    lds_wait(sm, C_HB, 8);
    const v4u A0 = *(const LAS v4u*)(sm + L_HB + (n16 & 7) * HB_STRIDE + g4 * 16);
    const v4u A1 = *(const LAS v4u*)(sm + L_HB + (n16 & 7) * HB_STRIDE + 64 + g4 * 16);
#pragma unroll
    for (int ci = 0; ci < 2; ci++) {
      if (ci == 1 && wave < 3) break;
      f4 ad[4];
#pragma unroll
      for (int e = 0; e < 4; e++) ad[e] = mfma16(A0, Bd[ci][0][e], (f4){0.f, 0.f, 0.f, 0.f});
#pragma unroll
      for (int e = 0; e < 4; e++) ad[e] = mfma16(A1, Bd[ci][1][e], ad[e]);
      v4u s0, s1;
#pragma unroll
      for (int e = 0; e < 4; e++) {
        const u32 x = ((__float_as_uint(g4 >= 2 ? ad[e][2] : ad[e][0]) + rnd) & 0xFFFFF000u) | tag12;
        const u32 y = ((__float_as_uint(g4 >= 2 ? ad[e][3] : ad[e][1]) + rnd) & 0xFFFFF000u) | tag12;
        s0[e] = (u32)__builtin_amdgcn_update_dpp((int)x, (int)y, 0x128, 0xf, 0xc, false);
        s1[e] = (u32)__builtin_amdgcn_update_dpp((int)y, (int)x, 0x128, 0xf, 0x3, false);
      }
      const int off = WS_P + P.mw * PART_B + (16 * (cu0 + ci) + (n16 & 7)) * 128 + (n16 >> 3) * 64 + g4 * 16;
      __builtin_amdgcn_raw_buffer_store_b128(s0, P.rws, off, 0, AUX_SC1);
      __builtin_amdgcn_raw_buffer_store_b128(s1, P.rws, off + 1024, 0, AUX_SC1);
    }
    STAMP(7);
  }
};

__device__ __forceinline__ void helper_prefetch(const Ctx& P) {
  const int g = P.b & 7, idx = (P.b - NCONV - NMOE) >> 3;
  v4u acc = {0u, 0u, 0u, 0u};
  v4u t[19];
#pragma unroll
  for (int i = 0; i < 19; i++) {
    const int u = (i * 5 + idx) * 7 + P.wave;
    const __amdgpu_buffer_rsrc_t rd = mkrsrc(P.wd, F * D * 2);
    t[i] = __builtin_amdgcn_raw_buffer_load_b128(rd, u < 648 ? P.lane * 16 : OOB, g * (9 * 64 * D * 2) + u * 1024, AUX_NT);
  }
#pragma unroll
  for (int i = 0; i < 19; i++) acc |= t[i];
  asm volatile("" :: "v"(acc));
}

__device__ __forceinline__ void publish_y(const Ctx& P, const LAS char* yb, int ystride, int tile0, int t, int step, int n) {
  const int gch = t & 3;
  const int goff = WS_U + (tile0 + (t >> 2)) * TILE_B + (4 * P.cg + gch) * 16;
#pragma unroll
  for (int it = 0; it < n; it++) {
    const v4u y4 = *(const LAS v4u*)(yb + gch * ystride + (it * step + (t >> 2)) * 16);
    const v4u u4 = *(const LAS v4u*)(P.sm + L_U + gch * UL_B + (32 + tile0 + it * step + (t >> 2)) * 16);
    v4u w4;
#pragma unroll
    for (int e = 0; e < 4; e++) w4[e] = (y4[e] == u4[e]) ? SAME : y4[e];
    __builtin_amdgcn_raw_buffer_store_b128(w4, P.rws, goff, it * step * TILE_B, AUX_SC1);
  }
}

__device__ __forceinline__ void conv_phase(const Ctx& P, u32 inc) {
  LAS char* sm = P.sm;
  const int tid = P.tid, lane = P.lane, wave = P.wave, n16 = lane & 15, g4 = lane >> 4, gch = tid & 3;
  const int goff = WS_U + (tid >> 2) * TILE_B + (4 * P.cg + gch) * 16;
  LAS char* ud = sm + L_U + gch * UL_B + (32 + (tid >> 2)) * 16;
  const int p1 = n16 + 1, kap = p1 & 7;
  const int base16 = 258 * kap + ((0x98654320u >> (4 * kap)) & 15);
  const LAS char* pa = sm + L_H + wave * HC_B + (base16 + g4 + (p1 >> 3)) * 16;
  const LAS char* pb = sm + L_U + wave * UL_B + (32 + 2 * n16 + 1 - g4 - 28) * 16;
  if (wave == 0) {
    gate_wait(P, G_U, 16u, inc);
    lds_signal(sm, C_GU);
  } else {
    lds_wait(sm, C_GU, 1);
  }
  STAMP(3);
  lds_wait(sm, C_TRUST, 1);
  lds_wait(sm, C_QQ, 5);
  STAMP(4);
  v4u g[2];
#pragma unroll
  for (int it = 0; it < 2; it++) g[it] = __builtin_amdgcn_raw_buffer_load_b128(P.rws, goff, it * 64 * TILE_B, AUX_SC1);
  if (__builtin_expect(!__all(vmax4(g[0], vmax4(g[1], 0u)) != CLR), 0)) {
    _Pragma("nounroll") for (u32 spin = 0; spin < SPIN_CAP; spin++) {
      NOTE(24, spin + 1);
      __builtin_amdgcn_s_sleep(4);
      asm volatile("" ::: "memory");
      g[0] = __builtin_amdgcn_raw_buffer_load_b128(P.rws, goff, 0, AUX_SC1);
      g[1] = __builtin_amdgcn_raw_buffer_load_b128(P.rws, goff, 64 * TILE_B, AUX_SC1);
      if (__all(vmax4(g[0], vmax4(g[1], 0u)) != CLR)) break;
    }
  }
  *(LAS v4u*)ud = g[0];
  *(LAS v4u*)(ud + 1024) = g[1];
  asm volatile("s_waitcnt vmcnt(0)" ::: "memory");
  lds_signal(sm, C_U);
  STAMP(5);
  lds_wait(sm, C_H, 2);
  lds_wait(sm, C_U, 4);
  STAMP(6);
  f4 acc[8];
#pragma unroll
  for (int t = 0; t < 8; t++) acc[t] = (f4){0.f, 0.f, 0.f, 0.f};
  v4u A[2][8], B[2][4];
#pragma unroll
  for (int a = 0; a < 8; a++) {
    A[0][a] = *(const LAS v4u*)(pa + a * 512);
    if (a < 4) B[0][a] = *(const LAS v4u*)(pb + a * 512 + 7 * 64);
  }
#pragma unroll
  for (int bb = 0; bb < 8; bb++) {
    __builtin_amdgcn_sched_barrier(0);
    if (bb == 7) {
      lds_wait(sm, C_U2, 1);
      __builtin_amdgcn_sched_barrier(0);
    }
#pragma unroll
    for (int a = 0; a < 8; a++) {
      if (bb < 7) {
        A[(bb + 1) & 1][a] = *(const LAS v4u*)(pa + a * 512 + (bb + 1) * 64);
        if (a < 4) B[(bb + 1) & 1][a] = *(const LAS v4u*)(pb + a * 512 + (6 - bb) * 64);
      } else if (a < 4) {
        A[0][a] = *(const LAS v4u*)(pa + a * 512);
        B[0][a] = *(const LAS v4u*)(pb + (4 + a) * 512 + 7 * 64);
      }
    }
#pragma unroll
    for (int t = 0; t < 8; t++)
#pragma unroll
      for (int c = 0; c < 4; c++)
        if (c <= t) acc[t] = mfma16(A[bb & 1][t - c], B[bb & 1][c], acc[t]);
#pragma unroll
    for (int i = 0; i < (bb < 7 ? 12 : 8); i++) {
      __builtin_amdgcn_sched_group_barrier(0x8, 2, 0);
      __builtin_amdgcn_sched_group_barrier(0x100, 1, 0);
    }
    if (bb < 7) __builtin_amdgcn_sched_group_barrier(0x8, 2, 0);
    else __builtin_amdgcn_sched_group_barrier(0x8, 10, 0);
  }
#pragma unroll
  for (int bb = 0; bb < 8; bb++) {
    __builtin_amdgcn_sched_barrier(0);
    if (bb == 1) {
      LAS char* yb = sm + L_STG + wave * Y1_B + n16 * 32 + g4 * 8;
#pragma unroll
      for (int t = 0; t < 4; t++) {
        v2u y2 = {umin(pk2(acc[t][0], acc[t][1]), MAXV), umin(pk2(acc[t][2], acc[t][3]), MAXV)};
        *(LAS v2u*)(yb + t * 512) = y2;
      }
    }
    if (bb == 2) lds_signal(sm, C_Y1);
    if (bb == Y_POST) {
      (void)gate_post(P, G_Y, wave == 0, inc);
      __builtin_amdgcn_sched_barrier(0);
    }
    if (bb < 7) {
#pragma unroll
      for (int a = 0; a < 4; a++) {
        A[(bb + 1) & 1][a] = *(const LAS v4u*)(pa + a * 512 + (bb + 1) * 64);
        B[(bb + 1) & 1][a] = *(const LAS v4u*)(pb + (4 + a) * 512 + (6 - bb) * 64);
      }
    }
#pragma unroll
    for (int t = 0; t < 4; t++)
#pragma unroll
      for (int c = 0; c <= t; c++) acc[4 + t] = mfma16(A[bb & 1][t - c], B[bb & 1][c], acc[4 + t]);
    if (bb < 7) {
#pragma unroll
      for (int i = 0; i < 8; i++) {
        __builtin_amdgcn_sched_group_barrier(0x8, 1, 0);
        __builtin_amdgcn_sched_group_barrier(0x100, 1, 0);
      }
      __builtin_amdgcn_sched_group_barrier(0x8, 2, 0);
    }
  }
  __builtin_amdgcn_sched_barrier(0);
  if (Y_POST == 8) (void)gate_post(P, G_Y, wave == 0, inc);
  {
    LAS char* yb = sm + L_H + wave * (HC_B + 64) + n16 * 32 + g4 * 8;
#pragma unroll
    for (int t = 0; t < 4; t++) {
      v2u y2 = {umin(pk2(acc[4 + t][0], acc[4 + t][1]), MAXV), umin(pk2(acc[4 + t][2], acc[4 + t][3]), MAXV)};
      *(LAS v2u*)(yb + t * 512) = y2;
    }
  }
  lds_signal(sm, C_Y);
  STAMP(7);
  lds_wait(sm, C_Y, 4);
  publish_y(P, sm + L_H, HC_B + 64, 128, tid, 64, 2);
  STAMP(8);
}

struct Early {
  u32 kk[11], vv[8], dd;
  v4u sk;
  u64 epoch;
  u32 cx;
};

__device__ __forceinline__ void tile_wave(const Ctx& P, const Early& E, const bool MOE) {
  LAS char* sm = P.sm;
  const int tid = P.tid, lane = P.lane, wave = P.wave, C = P.C;
  const bool act = tid < 288;
  const int j4 = min(tid, 287) * 4;
  const int row0 = 8 * C * (D * 2);
  const u32 (&kk)[11] = E.kk;
  const u32 (&vv)[8] = E.vv;
  const v4u sk = E.sk;
  u32 qq[8];
  lds_wait(sm, C_TAG, 1);
  const u32 uinc = gate_inc(*(const LAS u32*)(sm + L_REC));
  __builtin_amdgcn_sched_barrier(0);
  u32 gold;
  asm volatile("" :: "v"(sk) : "memory");
  gold = gate_post(P, G_U, wave == 4, uinc);
  STAMP(12);
  u32 up[2][4];
  {
    float w0[4] = {lo16(sk.x), hi16(sk.x), lo16(sk.y), hi16(sk.y)};
    float w1[4] = {lo16(sk.z), hi16(sk.z), lo16(sk.w), hi16(sk.w)};
    float u0[8], u1[8];
#pragma unroll
    for (int s = 0; s < 8; s++) {
      float a = lo16(kk[s]) * w0[3], c = hi16(kk[s]) * w1[3];
      a = fmaf(lo16(kk[s + 1]), w0[2], a); c = fmaf(hi16(kk[s + 1]), w1[2], c);
      a = fmaf(lo16(kk[s + 2]), w0[1], a); c = fmaf(hi16(kk[s + 2]), w1[1], c);
      a = fmaf(lo16(kk[s + 3]), w0[0], a); c = fmaf(hi16(kk[s + 3]), w1[0], c);
      u0[s] = a * lo16(vv[s]); u1[s] = c * hi16(vv[s]);
    }
#pragma unroll
    for (int i = 0; i < 4; i++) {
      up[0][i] = umin(pk2(u0[7 - 2 * i], u0[6 - 2 * i]), MAXV);
      up[1][i] = umin(pk2(u1[7 - 2 * i], u1[6 - 2 * i]), MAXV);
    }
    STAMP(1);
    LAS char* stg = sm + L_STG + wave * 2048;
    v4u p0 = {up[0][0], up[0][1], up[0][2], up[0][3]}, p1 = {up[1][0], up[1][1], up[1][2], up[1][3]};
    if (act) { *(LAS v4u*)(stg + lane * 32) = p0; *(LAS v4u*)(stg + lane * 32 + 16) = p1; }
    asm volatile("" ::: "memory");
    p0 = *(const LAS v4u*)(stg + lane * 16);
    p1 = *(const LAS v4u*)(stg + (wave < 4 ? 1024 : 0) + lane * 16);
    STAMP(13);
    lds_wait(sm, C_CLR, 1);
    const int so = WS_U + C * TILE_B + wave * 2048 + lane * 16;
    __builtin_amdgcn_raw_buffer_store_b128(p0, P.rws, so, 0, AUX_SC1);
    __builtin_amdgcn_raw_buffer_store_b128(p1, P.rws, wave < 4 ? so + 1024 : OOB, 0, AUX_SC1);
  }
  __builtin_amdgcn_sched_barrier(0);
  if (MOE) lds_signal(sm, C_PUB);
  STAMP(2);
  lds_wait(sm, C_STAT, 1);
#pragma unroll
  for (int i = 0; i < 8; i++) qq[i] = *(const LAS u32*)(sm + xrow_off(i) + j4);
  lds_signal(sm, C_QQ);
  u32 dd = E.dd;
  asm volatile("" : "+v"(dd));
  if (wave == 4) gate_confirm(P, G_U, gold, uinc);
  float xn[2][8], c0[2][8];
  auto prepare = [&]()
  {
    const float d0 = lo16(dd), d1 = hi16(dd);
#pragma unroll
    for (int s2 = 0; s2 < 4; s2++) {
      const f4 st = *(const LAS f4*)(sm + L_STAT + s2 * 16);
#pragma unroll
      for (int e = 0; e < 2; e++) {
        const int s = 2 * s2 + e;
        const float xa = lo16(qq[s]), xb = hi16(qq[s]);
        const float na = fmaf(xa, st[2 * e], st[2 * e + 1]), nb = fmaf(xb, st[2 * e], st[2 * e + 1]);
        const u32 pa = up[0][(7 - s) >> 1], pb = up[1][(7 - s) >> 1];
        const float ua = ((7 - s) & 1) ? hi16(pa) : lo16(pa), ub = ((7 - s) & 1) ? hi16(pb) : lo16(pb);
        xn[0][s] = na; xn[1][s] = nb;
        c0[0][s] = fmaf(na, fmaf(ua, d0, 1.0f), xa);
        c0[1][s] = fmaf(nb, fmaf(ub, d1, 1.0f), xb);
        asm volatile("" : "+v"(xn[0][s]), "+v"(xn[1][s]), "+v"(c0[0][s]), "+v"(c0[1][s]));
      }
    }
  };
  if (MOE) {
    Ffn ffn;
    const int cu0 = wave < 3 ? wave : 2 * wave - 3;
    lds_wait(sm, C_PUB, 5);
    ffn.run(P, cu0, prepare);
  } else {
   prepare();
   if (P.is_conv) {
    if (wave < 4) {
      conv_phase(P, uinc);
    } else {
      lds_wait<1>(sm, C_GU, 1);
      lds_wait(sm, C_TRUST, 1);
      lds_wait(sm, C_QQ, 5);
      __builtin_amdgcn_s_sleep(2);
      const int goff = WS_U + (128 + (lane >> 2)) * TILE_B + (4 * P.cg + (lane & 3)) * 16;
      v4u g[8];
      _Pragma("nounroll") for (u32 spin = 0; spin < SPIN_CAP; spin++) {
        asm volatile("" ::: "memory");
#pragma unroll
        for (int it = 0; it < 8; it++) g[it] = __builtin_amdgcn_raw_buffer_load_b128(P.rws, goff, it * 16 * TILE_B, AUX_SC1);
        u32 mx = 0;
#pragma unroll
        for (int it = 0; it < 8; it++) mx = vmax4(g[it], mx);
        if (__builtin_expect(__all(mx != CLR), 1)) break;
        NOTE(27, spin + 1);
        __builtin_amdgcn_s_sleep(4);
      }
#pragma unroll
      for (int it = 0; it < 8; it++) *(LAS v4u*)(sm + L_U + (lane & 3) * UL_B + (32 + 128 + it * 16 + (lane >> 2)) * 16) = g[it];
      asm volatile("s_waitcnt vmcnt(0)" ::: "memory");
      lds_signal(sm, C_U2);
      STAMP(5);
      lds_wait<4>(sm, C_Y1, 4);
      publish_y(P, sm + L_STG, Y1_B, 0, lane, 32, 4);
    }
  } else {
    lds_wait(sm, C_PUB, 1);
    helper_prefetch(P);
    asm volatile("s_waitcnt vmcnt(0)" ::: "memory");
   }
  }
  lds_wait<1>(sm, C_SEL, 1);
  const u32 skip = __builtin_amdgcn_readfirstlane(*(const LAS u32*)(sm + L_REC + 4));
  const __amdgpu_buffer_rsrc_t ro = mkrsrc(P.out, S * D * 2);
  const int oo = act ? j4 : OOB;
  const int uoff = WS_U + C * TILE_B + j4 * 8;
  lds_wait<1>(sm, C_GY, 1);
  STAMP(9);
  v4u y0, y1;
  _Pragma("nounroll") for (u32 spin = 0; spin < SPIN_CAP; spin++) {
    asm volatile("" ::: "memory");
    y0 = __builtin_amdgcn_raw_buffer_load_b128(P.rws, uoff, 0, AUX_SC1);
    y1 = __builtin_amdgcn_raw_buffer_load_b128(P.rws, uoff + 16, 0, AUX_SC1);
    bool ok = vmax4(y0, vmax4(y1, 0u)) != CLR;
#pragma unroll
    for (int e = 0; e < 4; e++) ok = ok & (y0[e] != up[0][e]) & (y1[e] != up[1][e]);
    if (__builtin_expect(__all(ok), 1)) break;
    NOTE(25, spin + 1);
    __builtin_amdgcn_s_sleep(4);
  }
  STAMP(16);
#pragma unroll
  for (int e = 0; e < 4; e++) {
    const u32 a = y0[e] == SAME ? up[0][e] : y0[e], c = y1[e] == SAME ? up[1][e] : y1[e];
    const u32 o0 = pk2(fmaf(xn[0][2 * e], lo16(a), c0[0][2 * e]), fmaf(xn[1][2 * e], lo16(c), c0[1][2 * e]));
    const u32 o1 = pk2(fmaf(xn[0][2 * e + 1], hi16(a), c0[0][2 * e + 1]), fmaf(xn[1][2 * e + 1], hi16(c), c0[1][2 * e + 1]));
    const int so = row0 + (e >> 1) * 4 * (D * 2);
    if (!((skip >> (2 * e)) & 1)) __builtin_amdgcn_raw_buffer_store_b32(o0, ro, oo + ((2 * e) & 3) * (D * 2), so, OUT_AUX);
    if (!((skip >> (2 * e + 1)) & 1)) __builtin_amdgcn_raw_buffer_store_b32(o1, ro, oo + ((2 * e + 1) & 3) * (D * 2), so, OUT_AUX);
  }
}

__device__ __forceinline__ void route(const Ctx& P, u32 tag, u64 epoch) {
  LAS char* sm = P.sm;
  const int lane = P.lane, wave = P.wave;
  (void)wave;
  gate_wait(P, G_R, 16u, gate_inc(tag));
  STAMP(3);
  v4u g[16];
  u32 spin = 0;
  _Pragma("nounroll") for (; spin < SPIN_CAP; spin++) {
    asm volatile("" ::: "memory");
#pragma unroll
    for (int i = 0; i < 16; i++)
      g[i] = __builtin_amdgcn_raw_buffer_load_b128(P.rws, WS_R + lane * 16 + (i & 3) * 1024, (i >> 2) * 4096, AUX_SC1);
    u32 bad = 0;
#pragma unroll
    for (int i = 0; i < 16; i++) {
      bad = __builtin_amdgcn_bitop3_b32(bad, g[i].y, tag, 0xF6);
      bad = __builtin_amdgcn_bitop3_b32(bad, g[i].w, tag, 0xF6);
    }
    if (__builtin_expect(__all(bad == 0), 1)) break;
    NOTE(24, spin + 1);
    __builtin_amdgcn_s_sleep(4);
  }
  lds_signal(sm, C_TRUST);
  STAMP(4);
  if (P.b == 0 && lane == 0) __hip_atomic_store((GAS u64*)(P.ws + WS_E), epoch, __ATOMIC_RELAXED, __HIP_MEMORY_SCOPE_AGENT);
  u32 k1 = umax(g[0].x, g[0].z), k2 = umin(g[0].x, g[0].z), k3 = 0;
#pragma unroll
  for (int i = 2; i < 32; i++) {
    const u32 x = (i & 1) ? g[i >> 1].z : g[i >> 1].x;
    k3 = umed3(k2, k3, x);
    k2 = umed3(k1, k2, x);
    k1 = umax(k1, x);
  }
  u32 tau = umax(k1, dppu<0xB1>(k1));
  tau = umax(tau, dppu<0x4E>(tau));
  tau = umax(tau, dppu<0x141>(tau));
  tau = umin(tau, dppu<0x140>(tau));
  tau = xrow_umin(tau);
  LAS u32* cand = (LAS u32*)(sm + L_CAND);
  const u64 m1 = __ballot(k1 >= tau), m2 = __ballot(k2 >= tau), m3 = __ballot(k3 >= tau);
  const u32 n1 = __builtin_popcountll(m1), n2 = n1 + __builtin_popcountll(m2), n = n2 + __builtin_popcountll(m3);
  bool slow = n > 64;
  if (__builtin_expect(!slow, 1)) {
    const u32 p1 = __builtin_amdgcn_mbcnt_hi((u32)(m1 >> 32), __builtin_amdgcn_mbcnt_lo((u32)m1, 0u));
    const u32 p2 = __builtin_amdgcn_mbcnt_hi((u32)(m2 >> 32), __builtin_amdgcn_mbcnt_lo((u32)m2, n1));
    const u32 p3 = __builtin_amdgcn_mbcnt_hi((u32)(m3 >> 32), __builtin_amdgcn_mbcnt_lo((u32)m3, n2));
    cand[k1 >= tau ? p1 : 64] = k1;
    cand[k2 >= tau ? p2 : 64] = k2;
    cand[k3 >= tau ? p3 : 64] = k3;
    asm volatile("" ::: "memory");
    const u32 mine = cand[lane];
    u32 rank = 0;
    _Pragma("nounroll") for (u32 jj = 0; jj < n; jj += 8) {
      const v4u c4 = *(const LAS v4u*)(cand + jj), d4 = *(const LAS v4u*)(cand + jj + 4);
      rank += (c4.x > mine) + (c4.y > mine) + (c4.z > mine) + (c4.w > mine);
      rank += (d4.x > mine) + (d4.y > mine) + (d4.z > mine) + (d4.w > mine);
    }
    const bool in = (u32)lane < n && rank < 8;
    if (in) ((LAS u32*)(sm + L_SEL))[rank] = mine;
    slow = __any(in && (u32)lane >= n2);
  }
  if (__builtin_expect(slow, 0)) {
    _Pragma("nounroll") for (int r = 0; r < 8; r++) {
      u32 mx = 0;
#pragma unroll
      for (int i = 0; i < 16; i++) mx = umax3(mx, g[i].x, g[i].z);
      mx = wave_umax(mx);
#pragma unroll
      for (int i = 0; i < 16; i++) { g[i].x = g[i].x == mx ? 0u : g[i].x; g[i].z = g[i].z == mx ? 0u : g[i].z; }
      if (lane == 0) ((LAS u32*)(sm + L_SEL))[r] = mx;
    }
  }
  asm volatile("" ::: "memory");
  {
    const u32 tk = 2047u - (((LAS u32*)(sm + L_SEL))[lane & 7] & 2047u);
    u32 mk = (int)(tk >> 3) == P.C ? 1u << (tk & 7) : 0u;
    mk |= dppu<0xB1>(mk);
    mk |= dppu<0x4E>(mk);
    mk |= dppu<0x141>(mk);
    if (lane == 0) *(LAS u32*)(sm + L_REC + 4) = mk;
  }
  lds_signal(sm, C_SEL);
  STAMP(5);
}

__device__ __forceinline__ u32 load_msd(const Ctx& P, int tok, u32 tag) {
  v2u g;
  _Pragma("nounroll") for (u32 spin = 0; spin < SPIN_CAP; spin++) {
    asm volatile("" ::: "memory");
    g = __builtin_amdgcn_raw_buffer_load_b64(P.rws, WS_M + tok * 8, 0, AUX_SC1);
    if (__builtin_expect(__all(g.y == tag), 1)) break;
    __builtin_amdgcn_s_sleep(4);
  }
  return g.x;
}

__device__ __forceinline__ void routed_rows(const Ctx& P, u32 tag) {
  LAS char* sm = P.sm;
  const int lane = P.lane, wave = P.wave, C = P.C, cg = P.cg;
  (void)wave; (void)C; (void)cg;
  const __amdgpu_buffer_rsrc_t rq = mkrsrc(P.q, S * D * 2);
  {
    const int cw = wave - 6;
    const v4u s4 = *(const LAS v4u*)(sm + L_SEL + cw * 16);
    Row rr[4];
#pragma unroll
    for (int e = 0; e < 4; e++) {
      const int tok = 2047 - (int)(__builtin_amdgcn_readfirstlane(s4[e]) & 2047u);
      rr[e] = load_row<0>(rq, tok * (D * 2), lane);
    }
    const u32 ks = (lane & 2) ? ((lane & 1) ? s4.w : s4.z) : ((lane & 1) ? s4.y : s4.x);
    const u32 msd = load_msd(P, 2047 - (int)(ks & 2047u), tag);
    const float rsl = __builtin_amdgcn_rcpf(lo16(msd)), nml = -hi16(msd) * rsl;
#pragma unroll
    for (int e = 0; e < 4; e++) {
      const float rs = __uint_as_float(__builtin_amdgcn_readlane(__float_as_uint(rsl), e));
      const float nm = __uint_as_float(__builtin_amdgcn_readlane(__float_as_uint(nml), e));
      LAS char* dst = sm + L_XS + (cw * 4 + e) * XS_STRIDE;
      const Row& x = rr[e];
      v2u a = {pk2(fmaf(lo16(x.a0.x), rs, nm), fmaf(hi16(x.a0.x), rs, nm)), pk2(fmaf(lo16(x.a0.y), rs, nm), fmaf(hi16(x.a0.y), rs, nm))};
      v2u c = {pk2(fmaf(lo16(x.a1.x), rs, nm), fmaf(hi16(x.a1.x), rs, nm)), pk2(fmaf(lo16(x.a1.y), rs, nm), fmaf(hi16(x.a1.y), rs, nm))};
      *(LAS v2u*)(dst + lane * 8) = a;
      *(LAS v2u*)(dst + 512 + lane * 8) = c;
      *(LAS u16*)(dst + 1024 + lane * 2) = (u16)pk2(fmaf(lo16(x.a2), rs, nm), 0.f);
    }
    lds_signal(sm, C_XS);
    STAMP(6);
    ffn_act(P, wave);
  }
}

__device__ __forceinline__ void aux_wave(const Ctx& P, u32 cx, const int ROLE) {
  LAS char* sm = P.sm;
  const int lane = P.lane, wave = P.wave, C = P.C, cg = P.cg;
  (void)wave; (void)C; (void)cg;
  u32 tag;
    if (wave == 5) {
      asm volatile("s_waitcnt vmcnt(0)" ::: "memory");
      lds_signal(sm, C_CLR);
      STAMP(13);
    } else {
      if (CODE_PF) asm volatile("" :: "v"(cx));
      lds_wait(sm, C_TAG, 1);
    }
    if (ROLE == 0) {
      HCopy hc;
      hc.load(P, 2 * (wave - 5));
      STAMP(12);
      hc.store(P, 2 * (wave - 5));
      STAMP(3);
      if (wave == 5) {
        lds_wait<4>(sm, C_Y1, 4);
        publish_y(P, sm + L_STG + 256, Y1_B, 16, lane, 32, 4);
      }
    } else if (ROLE == 2) {
      lds_wait(sm, C_PUB, 1);
      helper_prefetch(P);
    }
    if (wave == 5) {
      if (ROLE == 1) {
        Ffn ffn;
        lds_wait(sm, C_STAT, 1);
        ffn.run(P, 7, []() {});
      }
      return;
    }
    tag = *(const LAS u32*)(sm + L_REC);
    if (ROLE == 1) { lds_wait<1>(sm, C_SEL, 1); __builtin_amdgcn_s_setprio(3); }
  if (ROLE == 1) routed_rows(P, tag);
  {
    gate_wait(P, G_Y, 9u, gate_inc(tag));
    lds_signal(sm, C_GY);
    STAMP(7);
  }
}

__device__ __forceinline__ void router_stats(const Ctx& P, Early& E, const int ROLE) {
  LAS char* sm = P.sm;
  const int lane = P.lane, C = P.C;
  u32 tag;
    __builtin_amdgcn_s_setprio(3);
    const int m = lane & 15, g4 = lane >> 4;
    u64 epoch = __hip_atomic_load((GAS u64*)(P.ws + WS_E), __ATOMIC_RELAXED, __HIP_MEMORY_SCOPE_AGENT);
    __builtin_amdgcn_sched_barrier(0);
    v4u xa[9], wb[9];
    const __amdgpu_buffer_rsrc_t rq = mkrsrc(P.q, S * D * 2);
    const int xo = (m & 7) * (D * 2) + (m >> 3) * 64 + g4 * 16;
#pragma unroll
    for (int i = 0; i < 9; i++) xa[i] = __builtin_amdgcn_raw_buffer_load_b128(rq, xo + i * 128, 8 * C * (D * 2), AUX_NT);
    __builtin_amdgcn_sched_barrier(0);
    const size_t pw = (size_t)P.wr + m * 64, po = (size_t)kOnes;
    const GAS char* wp = (const GAS char*)(m < 2 ? pw : po) + g4 * 16;
#pragma unroll
    for (int i = 0; i < 9; i++) wb[i] = *(const GAS v4u*)(wp + i * 128);
    STAMP(10);
    __builtin_amdgcn_sched_barrier(0);
    asm volatile("" : "+v"(epoch) :: "memory");
    epoch += 1;
    tag = (u32)epoch;
    if (lane == 0) *(LAS u32*)(sm + L_REC) = tag;
    lds_signal(sm, C_TAG);
    STAMP(1);
    f4 gx = {0.f, 0.f, 0.f, 0.f}, gw = {0.f, 0.f, 0.f, 0.f}, gs = {0.f, 0.f, 0.f, 0.f};
#pragma unroll
    for (int i = 0; i < 9; i++) gx = mfma16(xa[i], xa[i], gx);
    __builtin_amdgcn_sched_barrier(0);
    const u32 gold = gate_post(P, G_R, true, gate_inc(tag));
    STAMP(12);
    __builtin_amdgcn_sched_barrier(0);
#pragma unroll
    for (int i = 0; i < 9; i++) { gw = mfma16(wb[i], xa[i], gw); gs = mfma16(wb[i], wb[i], gs); }
    float sxx = (m & 2) ? ((m & 1) ? gx[3] : gx[2]) : ((m & 1) ? gx[1] : gx[0]);
    sxx = __uint_as_float(__builtin_amdgcn_ds_bpermute((m + (m & 12) * 4) * 4, __float_as_uint(sxx)));
    float sxw = m < 8 ? gw[0] : gw[1], sx = gw[2];
    sxw += dppf<0x128>(sxw);
    sx += dppf<0x128>(sx);
    sxx += dppf<0x128>(sxx);
    const float sw = __uint_as_float(__builtin_amdgcn_readlane(__float_as_uint(gs[0] + gs[1]), 2));
    const float mean = sx * (1.0f / D);
    const float var = fmaxf(fmaf(-mean, mean, sxx * (1.0f / D)), 0.f);
    const float mu = rbf(mean);
    const float sd = rbf(__builtin_amdgcn_sqrtf(rbf(rbf(var) + 1e-5f)));
    const float rs = __builtin_amdgcn_rcpf(sd);
    const u32 r = pk2(0.f, fmaf(-mu, sw, sxw) * rs);
    u32 key = ((r ^ (((int)r >> 31) | 0x80000000u)) & 0xFFFF0000u) | (u32)(2047 - (8 * C + (m & 7)));
    u32 msd = __float_as_uint(mu) | (__float_as_uint(sd) >> 16);
    asm volatile("" : "+v"(key), "+v"(msd));
    const v2u rec = {m < 8 ? key : msd, tag};
    const int ro = lane < 16 ? (m < 8 ? WS_R : WS_M) + (8 * C + (m & 7)) * 8 : OOB;
    lds_wait(sm, C_CLR, 1);
    __builtin_amdgcn_raw_buffer_store_b64(rec, P.rws, ro, 0, AUX_SC1);
    STAMP(2);
    if (lane < 8) { f2 st = {rs, -mu * rs}; *(LAS f2*)(sm + L_STAT + m * 8) = st; }
    {
      const int t = m & 7, t3 = (t * 11) >> 5;
      LAS char* xd = sm + L_U + t3 * UL_B + 512 + (t - 3 * t3) * (D * 2) + (m >> 3) * 64 + g4 * 16;
#pragma unroll
      for (int i = 0; i < 9; i++) *(LAS v4u*)(xd + i * 128) = xa[i];
    }
    lds_signal(sm, C_STAT);
    if (ROLE == 2) lds_signal(sm, C_PUB);
    E.epoch = epoch;
    E.cx = gold;
}

__device__ __forceinline__ void router_wave(const Ctx& P, const Early& E, const int ROLE) {
  LAS char* sm = P.sm;
  const int lane = P.lane, wave = P.wave, C = P.C, cg = P.cg;
  (void)wave; (void)C; (void)cg;
  u32 tag;
    const u64 epoch = E.epoch;
    tag = (u32)epoch;
    const u32 gold = E.cx;
    gate_confirm(P, G_R, gold, gate_inc(tag));
    route(P, tag, epoch);
    if (ROLE != 1) __builtin_amdgcn_s_setprio(0);
  if (ROLE == 1) routed_rows(P, tag);
  if (ROLE == 0) {
    const int pi = lane & 7, ti = 4 * (pi & 1) + (pi & 2) + (pi >> 2);
    const u32 kx = ((LAS u32*)(sm + L_SEL))[ti];
    const int tok = 2047 - (int)(kx & 2047u);
    const v2u x2 = *(const GAS v2u*)((const GAS char*)P.q + (size_t)tok * (D * 2) + cg * 8);
    const v2u d2 = *(const GAS v2u*)((const GAS char*)P.dsk + cg * 8);
    const u32 msd = load_msd(P, tok, tag);
    const float rs = __builtin_amdgcn_rcpf(lo16(msd)), nm = -hi16(msd) * rs;
    const u32 rb = kx & 0xFFFF0000u;
    const float r = __uint_as_float((rb & 0x80000000u) ? (rb ^ 0x80000000u) : (~rb & 0xFFFF0000u));
    const float rsig = rbf(__builtin_amdgcn_rcpf(1.0f + __expf(-r)));
    STAMP(6);
    lds_wait<2>(sm, C_Y, 4);
    float o[4];
#pragma unroll
    for (int e = 0; e < 4; e++) {
      const float y = lo16(*(const LAS u16*)(sm + (tok < 1024 ? L_STG + e * Y1_B : L_H + e * (HC_B + 64) - 2048) + tok * 2));
      const float u = lo16(*(const LAS u16*)(sm + L_U + e * UL_B + (32 + (tok >> 3)) * 16 + (7 - (tok & 7)) * 2));
      const float x = (e & 1) ? hi16(x2[e >> 1]) : lo16(x2[e >> 1]);
      const float dk = (e & 1) ? hi16(d2[e >> 1]) : lo16(d2[e >> 1]);
      const float n = fmaf(x, rs, nm);
      o[e] = fmaf(n, fmaf(u, dk, y), x) + n;
      asm volatile("" : "+v"(o[e]));
    }
    gate_wait(P, G_P, lane < 8 ? 5u : 4u, gate_inc(tag));
    STAMP(7);
    v4u g[9];
    const u32 tag12 = tag & 0x7FFu;
    _Pragma("nounroll") for (u32 spin = 0; spin < SPIN_CAP; spin++) {
      asm volatile("" ::: "memory");
#pragma unroll
      for (int i = 0; i < 9; i++)
        g[i] = __builtin_amdgcn_raw_buffer_load_b128(P.rws, WS_P + (lane >> 3) * PART_B + cg * 128 + pi * 16, 8 * i * PART_B, AUX_SC1);
      u32 bad = 0;
#pragma unroll
      for (int i = 0; i < 9; i++)
#pragma unroll
        for (int e = 0; e < 4; e++) bad = __builtin_amdgcn_bitop3_b32(bad, g[i][e], tag12, 0xF6);
      if (__builtin_expect(__all((bad & 0xFFFu) == 0), 1)) break;
      NOTE(25, spin + 1);
      __builtin_amdgcn_s_sleep(4);
    }
    STAMP(8);
    f4 s4 = __builtin_bit_cast(f4, g[0]);
#pragma unroll
    for (int i = 1; i < 9; i++) s4 += __builtin_bit_cast(f4, g[i]);
#pragma unroll
    for (int e = 0; e < 4; e++) {
      float sv = s4[e];
      sv += dppf<0x128>(sv);
      o[e] = fmaf(rbf(xrow_sum(sv)), rsig, o[e]);
    }
    if (lane < 8) {
      v2u o2 = {pk2(o[0], o[1]), pk2(o[2], o[3])};
      *(GAS v2u*)((GAS char*)P.out + (size_t)tok * (D * 2) + cg * 8) = o2;
    }
  }
}

__global__ __launch_bounds__(NTHR, 2) void fused_kernel(
    const u16* __restrict__ q, const u16* __restrict__ k, const u16* __restrict__ v, const u16* __restrict__ shk,
    const u16* __restrict__ hk, const u16* __restrict__ dsk, const u16* __restrict__ wr, const u16* __restrict__ wg,
    const u16* __restrict__ wu, const u16* __restrict__ wd, u16* __restrict__ out, char* ws) {
  extern __shared__ __attribute__((aligned(16))) char smem_[];
  Ctx P;
  P.q = q; P.k = k; P.v = v; P.shk = shk; P.hk = hk; P.dsk = dsk; P.wr = wr; P.wg = wg; P.wu = wu; P.wd = wd;
  P.out = out; P.ws = ws;
  u64 pc;
  asm volatile("s_getpc_b64 %0" : "=s"(pc));
  P.code = (const char*)(pc & ~(u64)127);
  P.sm = (LAS char*)smem_;
  P.tid = threadIdx.x; P.lane = P.tid & 63;
  P.wave = __builtin_amdgcn_readfirstlane(P.tid >> 6);
  const int b = blockIdx.x;
  P.b = b;
  P.C = (b & 7) * 32 + (b >> 3);
  P.is_conv = b < NCONV;
  const bool is_moe = !P.is_conv && b < NCONV + NMOE;
  P.cg = (b & 7) * 18 + (b >> 3);
  P.mw = (b & 7) * 9 + ((b - NCONV) >> 3);
  P.rws = mkrsrc(ws, WS_END);
  const int lane = P.lane, wave = P.wave;
  (void)lane;
  if (lane < 32) ((LAS u32*)(P.sm + L_END))[wave * 32 + lane] = 0;
  STAMP(0); STAMPC(30);
  Early E;
  if (wave == 6) {
    constexpr int NL = (CODE_PF * 8 + 31) / 32;
    if (CODE_PF) E.cx = *(const GAS u32*)(P.code + min(((b >> 3) * NL + min(lane, NL - 1)) * 128, CODE_PF * 1024 - 128));
    const v4u z = {0u, 0u, 0u, 0u};
    *(LAS v4u*)(P.sm + L_U + (lane >> 4) * UL_B + (lane & 15) * 16) = z;
    *(LAS v4u*)(P.sm + L_U + (lane >> 4) * UL_B + 256 + (lane & 15) * 16) = z;
    ((LAS u32*)(P.sm + L_MISC))[lane] = 0u;
    ((LAS u32*)(P.sm + L_MISC))[64 + lane] = 0u;
    asm volatile("s_waitcnt lgkmcnt(0)" ::: "memory");
  }
  asm volatile("s_barrier" ::: "memory");
  asm volatile("" :: "s"(q), "s"(k), "s"(v), "s"(shk), "s"(hk), "s"(dsk), "s"(wr), "s"(wg), "s"(wu), "s"(wd), "s"(out), "s"(ws));
  STAMP(11);
  const int row0 = 8 * P.C * (D * 2);
  asm volatile("" : "+s"(P.wave));
  if (P.wave == 5) {
    const v4u g4 = {GMAGIC, GMAGIC, GMAGIC, GMAGIC};
    __builtin_amdgcn_raw_buffer_store_b128(g4, P.rws, lane < 32 ? gate_off(lane >> 3, b >> 4, b & 15) + (lane & 7) * 16 : OOB, 0, AUX_SC1);
    const v4u c4 = {CLR, CLR, CLR, CLR};
#pragma unroll
    for (int i = 0; i < 9; i++) __builtin_amdgcn_raw_buffer_store_b128(c4, P.rws, WS_U + lane * 16 + (i & 3) * 1024, P.C * TILE_B + (i >> 2) * 4096, AUX_SC1);
  }
  asm volatile("" : "+s"(P.wave));
  if (P.wave < 5) {
    const int j4 = min(P.tid, 287) * 4;
    const __amdgpu_buffer_rsrc_t rk = mkrsrc(P.k, S * D * 2), rv = mkrsrc(P.v, S * D * 2);
    if (TILE_DELAY) __builtin_amdgcn_s_sleep(TILE_DELAY);
    __builtin_amdgcn_sched_barrier(0);
#pragma unroll
    for (int i = 0; i < 3; i++) E.kk[i] = __builtin_amdgcn_raw_buffer_load_b32(rk, row0 + (i - 3) * (D * 2) + j4, 0, AUX_NT);
#pragma unroll
    for (int i = 0; i < 8; i++) {
      E.kk[3 + i] = __builtin_amdgcn_raw_buffer_load_b32(rk, j4 + (i & 3) * (D * 2), row0 + (i & 4) * (D * 2), AUX_NT);
      E.vv[i] = __builtin_amdgcn_raw_buffer_load_b32(rv, j4 + (i & 3) * (D * 2), row0 + (i & 4) * (D * 2), AUX_NT);
    }
    E.sk = *(const GAS v4u*)((const GAS char*)P.shk + j4 * 4);
    E.dd = *(const GAS u32*)((const GAS char*)P.dsk + j4);
  }
  const int role = P.is_conv ? 0 : (is_moe ? 1 : 2);
  asm volatile("" : "+s"(P.wave));
  STAMP(10);
  if (P.wave == 7) router_stats(P, E, role);
  asm volatile("" : "+s"(P.wave));
  if (P.wave != 7) {
    if (P.wave >= 5) aux_wave(P, E.cx, role);
    else tile_wave(P, E, is_moe);
  } else {
    router_wave(P, E, role);
  }
  FLUSH();
}

}

extern "C" void kernel_launch(void* const* d_in, const int* in_sizes, int n_in, void* d_out, int out_size, void* d_ws,
                              size_t ws_size, hipStream_t stream) {
  (void)in_sizes; (void)n_in; (void)out_size; (void)ws_size;
  (void)hipFuncSetAttribute((const void*)fused_kernel, hipFuncAttributeMaxDynamicSharedMemorySize, L_TOTAL);
  fused_kernel<<<NWG, NTHR, L_TOTAL, stream>>>(
      (const u16*)d_in[0], (const u16*)d_in[1], (const u16*)d_in[2], (const u16*)d_in[3], (const u16*)d_in[4],
      (const u16*)d_in[5], (const u16*)d_in[7], (const u16*)d_in[8], (const u16*)d_in[9], (const u16*)d_in[10],
      (u16*)d_out, (char*)d_ws + (WS_AT_END ? ((ws_size - WS_END) & ~(size_t)0xFFFF) : 0));
}
```

```cpp
#include <hip/hip_runtime.h>
#include <hip/hip_bf16.h>

typedef unsigned int u32;
typedef unsigned long long u64;
typedef unsigned short u16;
typedef u32 v4u __attribute__((ext_vector_type(4)));
typedef u32 v2u __attribute__((ext_vector_type(2)));
typedef float f4 __attribute__((ext_vector_type(4)));
typedef float f2 __attribute__((ext_vector_type(2)));
typedef __bf16 bf8 __attribute__((ext_vector_type(8)));
typedef __bf16 bf2 __attribute__((ext_vector_type(2)));
#define LAS __attribute__((address_space(3)))
#define GAS __attribute__((address_space(1)))

namespace {

constexpr int S = 2048, D = 576, F = 4608;
constexpr int NWG = 256, NTHR = 512, NCONV = 144, NMOE = 72;
constexpr int AUX_SC1 = 16;
constexpr u32 SAME = 0xFFFFFFFFu, MAXV = 0xFFFFFFFEu;
constexpr u32 SPIN_CAP = 1u << 16, GATE_CAP = 96;
constexpr u32 GMAGIC = 0x5AC3E900u;
constexpr int OOB = 0x7FFFFF00;
#define U_POST 2
#define WS_AT_END 0
#define HELPERS 1
#define Y_POST 7
#define OUT_AUX 16
#define W2_GATE 2
#define PF_R0 192
#define STAGED 1
#define H_DELAY 40
#define PF_CE 0
#define PF_WHEN 3
#define W1_DEPTH 0
#define DRAIN_FIRST 1
#define U_DELAY 0
#define PF_FIRST 1
#define TILE_DELAY 2
#define CODE_PF 44
#define USE_NT 0
constexpr int AUX_NT = USE_NT ? 2 : 0;

constexpr int WS_E = 0;
constexpr int WS_G = 4096;
constexpr int GATE_B = 256 * 256;
constexpr int WS_R = WS_G + 5 * GATE_B;
constexpr int WS_M = WS_R + S * 8;
constexpr int WS_U = WS_M + S * 8;
constexpr int TILE_B = D * 16;
constexpr int WS_P = WS_U + NWG * TILE_B;
constexpr int PART_B = NCONV * 128;
constexpr int WS_END = WS_P + NMOE * PART_B;
constexpr int WS_DBG = (WS_END + 4095) / 4096 * 4096;
enum { G_R = 0, G_U, G_Y, G_P, G_V, NGATE };
#define ONES8 0x3F803F80u, 0x3F803F80u, 0x3F803F80u, 0x3F803F80u, 0x3F803F80u, 0x3F803F80u, 0x3F803F80u, 0x3F803F80u
#define ONES32 ONES8, ONES8, ONES8, ONES8
__device__ const u32 kOnes[288] __attribute__((aligned(128))) = {ONES32, ONES32, ONES32, ONES32, ONES32, ONES32, ONES32, ONES32, ONES32};

constexpr int HC_B = 2080 * 16;
constexpr int L_H = 0;
constexpr int UL_B = 290 * 16;
constexpr int L_U = L_H + 4 * HC_B;
constexpr int L_STG = L_U + 4 * UL_B;
constexpr int L_MISC = L_STG + TILE_B;
constexpr int L_CNT = L_MISC;
constexpr int L_CAND = L_CNT + 96;
constexpr int L_SEL = L_CAND + 288;
constexpr int L_REC = L_SEL + 32;
constexpr int L_STAT = L_REC + 32;
constexpr int Y1_B = 2048 + 64;
constexpr int L_END = L_STAT + 64;
constexpr int XS_STRIDE = 1184;
constexpr int L_XS = 0;
constexpr int L_RED = L_XS + 8 * XS_STRIDE;
constexpr int L_HB = L_RED + 6 * 8 * 128 * 4;
constexpr int HB_STRIDE = 160;
static_assert(L_END + 1024 <= 160 * 1024, "lds");
static_assert(L_HB + 8 * HB_STRIDE <= L_U, "lds");
static_assert(L_END - L_MISC == 512, "zero fill");

__device__ __forceinline__ constexpr int xrow_off(int t) { return L_U + (t / 3) * UL_B + 512 + (t % 3) * (D * 2); }
enum { C_DRAIN = 0, C_STAT, C_TRUST, C_GU, C_SEL, C_H, C_U, C_Y, C_XS, C_RED, C_HB, C_GY, C_PUB, C_TAG, C_QQ, C_U2, C_Y1, C_W1, C_DN, C_H0, C_VER, C_REDO, C_OK };

__device__ __forceinline__ float lo16(u32 x) { return __uint_as_float(x << 16); }
__device__ __forceinline__ float hi16(u32 x) { return __uint_as_float(x & 0xFFFF0000u); }
__device__ __forceinline__ u32 pk2(float a, float b) {
  bf2 p = {(__bf16)a, (__bf16)b};
  return __builtin_bit_cast(u32, p);
}
__device__ __forceinline__ float rbf(float a) { return lo16(pk2(a, a)); }
__device__ __forceinline__ float dot2(u32 a, u32 b, float c) {
  return __builtin_amdgcn_fdot2_f32_bf16(__builtin_bit_cast(bf2, a), __builtin_bit_cast(bf2, b), c, false);
}
template <int CTRL> __device__ __forceinline__ u32 dppu(u32 x) {
  return (u32)__builtin_amdgcn_mov_dpp((int)x, CTRL, 0xf, 0xf, true);
}
template <int CTRL> __device__ __forceinline__ float dppf(float x) { return __uint_as_float(dppu<CTRL>(__float_as_uint(x))); }
__device__ __forceinline__ float xrow_sum(float x) {
  auto s = __builtin_amdgcn_permlane16_swap(__float_as_uint(x), __float_as_uint(x), false, false);
  x = __uint_as_float(s[0]) + __uint_as_float(s[1]);
  auto t = __builtin_amdgcn_permlane32_swap(__float_as_uint(x), __float_as_uint(x), false, false);
  return __uint_as_float(t[0]) + __uint_as_float(t[1]);
}
__device__ __forceinline__ u32 umax(u32 a, u32 b) { return a > b ? a : b; }
__device__ __forceinline__ u32 umin(u32 a, u32 b) { return a < b ? a : b; }
__device__ __forceinline__ u32 umed3(u32 a, u32 b, u32 c) { return umax(umin(a, b), umin(umax(a, b), c)); }
__device__ __forceinline__ u32 xrow_umin(u32 x) {
  auto s = __builtin_amdgcn_permlane16_swap(x, x, false, false);
  x = umin(s[0], s[1]);
  auto t = __builtin_amdgcn_permlane32_swap(x, x, false, false);
  return umin(t[0], t[1]);
}
__device__ __forceinline__ u32 wave_umax(u32 x) {
  x = umax(x, dppu<0xB1>(x));
  x = umax(x, dppu<0x4E>(x));
  x = umax(x, dppu<0x141>(x));
  x = umax(x, dppu<0x140>(x));
  auto s = __builtin_amdgcn_permlane16_swap(x, x, false, false);
  x = umax(s[0], s[1]);
  auto t = __builtin_amdgcn_permlane32_swap(x, x, false, false);
  return umax(t[0], t[1]);
}
__device__ __forceinline__ void lds_signal(LAS char* sm, int c) {
  u64 keep;
  asm volatile("s_waitcnt lgkmcnt(0)\n\ts_mov_b64 %0, exec\n\ts_mov_b64 exec, 1\n\tds_add_u32 %1, %2\n\ts_mov_b64 exec, %0"
               : "=&s"(keep) : "v"((u32)(size_t)(sm + L_CNT + 4 * c)), "v"(1u) : "memory");
}
template <int SLEEP = 0> __device__ __forceinline__ void lds_wait(LAS char* sm, int c, u32 target) {
  u32 v, sv;
  if (SLEEP == 0)
    asm volatile("1:\n\tds_read_b32 %0, %2\n\ts_waitcnt lgkmcnt(0)\n\tv_readfirstlane_b32 %1, %0\n\ts_cmp_lt_u32 %1, %3\n\ts_cbranch_scc1 1b"
                 : "=&v"(v), "=&s"(sv) : "v"((u32)(size_t)(sm + L_CNT + 4 * c)), "s"(target) : "memory", "scc");
  else
    asm volatile("1:\n\ts_sleep %4\n\tds_read_b32 %0, %2\n\ts_waitcnt lgkmcnt(0)\n\tv_readfirstlane_b32 %1, %0\n\ts_cmp_lt_u32 %1, %3\n\ts_cbranch_scc1 1b"
                 : "=&v"(v), "=&s"(sv) : "v"((u32)(size_t)(sm + L_CNT + 4 * c)), "s"(target), "n"(SLEEP) : "memory", "scc");
}
__device__ __forceinline__ bool lds_arrive(LAS char* sm, int c, u32 last) {
  u64 keep;
  u32 v, sv;
  asm volatile("s_mov_b64 %0, exec\n\ts_mov_b64 exec, 1\n\tds_add_rtn_u32 %1, %3, %4\n\ts_waitcnt lgkmcnt(0)\n\tv_readfirstlane_b32 %2, %1\n\ts_mov_b64 exec, %0"
               : "=&s"(keep), "=&v"(v), "=&s"(sv) : "v"((u32)(size_t)(sm + L_CNT + 4 * c)), "v"(1u) : "memory");
  return sv == last;
}
__device__ __forceinline__ void wg_barrier() { asm volatile("s_waitcnt lgkmcnt(0)\n\ts_barrier" ::: "memory"); }
__device__ __forceinline__ u32 umax3(u32 a, u32 b, u32 c) { return umax(umax(a, b), c); }
__device__ __forceinline__ u32 vmax4(v4u g, u32 acc) { return umax3(umax3(g.x, g.y, acc), g.z, g.w); }
__device__ __forceinline__ f4 mfma16(v4u a, v4u b, f4 c) {
  return __builtin_amdgcn_mfma_f32_16x16x32_bf16(__builtin_bit_cast(bf8, a), __builtin_bit_cast(bf8, b), c, 0, 0, 0);
}
__device__ __forceinline__ __amdgpu_buffer_rsrc_t mkrsrc(const void* p, int bytes) {
  return __builtin_amdgcn_make_buffer_rsrc((void*)p, 0, bytes, 0x00020000);
}

#define STAMP(i) do {} while (0)
#define STAMPC(i) do {} while (0)
#define FLUSH() do {} while (0)
#define NOTE(i, v) do {} while (0)
constexpr int L_TOTAL = L_END;

struct Ctx {
  const u16 *q, *k, *v, *shk, *hk, *dsk, *wr, *wg, *wu, *wd;
  u16* out;
  char* ws;
  const char* code;
  __amdgpu_buffer_rsrc_t rws;
  LAS char* sm;
  int tid, lane, wave, b, C, cg, mw;
  bool is_conv;
};

__device__ __forceinline__ int gate_off(int gate, int rep, int shard) { return WS_G + gate * GATE_B + (rep * 16 + shard) * 256; }
__device__ __forceinline__ u32 gate_add(const Ctx& P, int off, int val) {
  return (u32)__builtin_amdgcn_raw_ptr_buffer_atomic_add_i32(val, P.rws, off, 0, 0);
}
__device__ __forceinline__ u32 gate_post(const Ctx& P, int gate, bool on, u32 inc) {
  return gate_add(P, (on && P.lane < 16) ? gate_off(gate, P.lane, P.b & 15) : OOB, (int)inc);
}
__device__ __forceinline__ void gate_confirm(const Ctx& P, int gate, u32 old, u32 inc) {
  bool bad = P.lane < 16;
  _Pragma("nounroll") for (u32 spin = 0; spin < SPIN_CAP; spin++) {
    const u32 d = old - GMAGIC;
    bad = bad && ((d & 0xF0u) != 0u || d != (d & 0xFu) * inc);
    if (__builtin_expect(!__any(bad), 1)) break;
    NOTE(26, spin + 1);
    __builtin_amdgcn_s_sleep(2);
    old = gate_add(P, bad ? gate_off(gate, P.lane, P.b & 15) : OOB, (int)inc);
  }
}
__device__ __forceinline__ void gate_wait(const Ctx& P, int gate, u32 expect, u32 inc, u32 cap = GATE_CAP) {
  const int off = P.lane < 16 ? gate_off(gate, P.b >> 4, P.lane) : OOB;
  const u32 want = P.lane < 16 ? GMAGIC + expect * inc : 0u;
  _Pragma("nounroll") for (u32 spin = 0; spin < cap; spin++)
    if (__all(gate_add(P, off, 0) == want)) break;
}
__device__ __forceinline__ u32 gate_inc(u32 tag) { return 1u + (tag << 8); }
__device__ __forceinline__ void post_u_complete(const Ctx& P, u32 inc) {
  lds_wait<1>(P.sm, C_DRAIN, 5);
  gate_confirm(P, G_V, gate_post(P, G_V, true, inc), inc);
}

struct Row { v2u a0, a1; u32 a2; };
template <int AUX> __device__ __forceinline__ Row load_row(__amdgpu_buffer_rsrc_t r, int soff, int lane) {
  Row x;
  x.a0 = __builtin_amdgcn_raw_buffer_load_b64(r, lane * 8, soff, AUX);
  x.a1 = __builtin_amdgcn_raw_buffer_load_b64(r, lane * 8 + 512, soff, AUX);
  x.a2 = __builtin_amdgcn_raw_buffer_load_b16(r, lane * 2 + 1024, soff, AUX);
  return x;
}
__device__ __forceinline__ void hcopy_store(LAS char* dst, v4u lo, v4u hi, bool first) {
  u32 d[8], o[7];
#pragma unroll
  for (int e = 0; e < 4; e++) { d[e] = lo[e]; d[4 + e] = hi[e]; }
#pragma unroll
  for (int e = 0; e < 7; e++) o[e] = __builtin_amdgcn_alignbit(d[e + 1], d[e], 16);
  constexpr int BASE[8] = {0, 260, 519, 778, 1037, 1296, 1556, 1815};
#pragma unroll
  for (int e = 0; e < 4; e++) {
    v4u a = {d[e], d[e + 1], d[e + 2], d[e + 3]}, c = {o[e], o[e + 1], o[e + 2], o[e + 3]};
    if (e > 0 || first) *(LAS v4u*)(dst + BASE[2 * e] * 16) = a;
    *(LAS v4u*)(dst + BASE[2 * e + 1] * 16) = c;
  }
}
__device__ __forceinline__ void hcopy(const Ctx& P, int ch0) {
  const __amdgpu_buffer_rsrc_t rh = mkrsrc(P.hk + (size_t)(4 * P.cg + ch0) * S, 2 * S * 2);
  v4u lo[8], hi[8];
#pragma unroll
  for (int r = 0; r < 8; r++) lo[r] = __builtin_amdgcn_raw_buffer_load_b128(rh, P.lane * 16 + (r & 3) * 1024, (r >> 2) * 4096, AUX_NT);
  STAMP(12);
  LAS char* dst = P.sm + L_H + ch0 * HC_B + (P.lane + 2) * 16;
#pragma unroll
  for (int r = 0; r < 8; r++) *(LAS v4u*)(dst + (r >> 2) * HC_B + (r & 3) * 1024) = lo[r];
  asm volatile("" ::: "memory");
  STAMP(1);
#pragma unroll
  for (int r = 0; r < 8; r++) hi[r] = *(const LAS v4u*)(dst + (r >> 2) * HC_B + (r & 3) * 1024 + 16);
  {
    const int c = P.lane & 1, m1 = (P.lane >> 1) & 1;
    const v4u z = {0u, 0u, 0u, 0u};
    v4u h0 = *(const LAS v4u*)(P.sm + L_H + (ch0 + c) * HC_B + 32);
    h0 = m1 ? h0 : z;
    if (P.lane < 4) hcopy_store(P.sm + L_H + (ch0 + c) * HC_B + m1 * 16, z, h0, true);
  }
#pragma unroll
  for (int r = 0; r < 8; r++) hcopy_store(dst + (r >> 2) * HC_B + (r & 3) * 1024, lo[r], hi[r], false);
  lds_signal(P.sm, C_H);
}

__device__ __forceinline__ void ffn_gate_up(const Ctx& P) {
  LAS char* sm = P.sm;
  const int lane = P.lane, wave = P.wave, n16 = lane & 15, g4 = lane >> 4;
  v4u W1[3][8];
  const GAS char* wp = (const GAS char*)(n16 < 8 ? P.wg : P.wu) + ((size_t)(96 * wave + 8 * g4) * F + 64 * P.mw + 8 * (n16 & 7)) * 2;
  if (wave >= PF_FIRST) lds_wait<1>(sm, wave < 2 ? C_PUB : C_W1, wave < 2 ? 1 : 2);
  __builtin_amdgcn_sched_barrier(0);
#pragma unroll
  for (int i = 0; i < 3; i++)
#pragma unroll
    for (int jj = 0; jj < 8; jj++) {
      const GAS v4u* a = (const GAS v4u*)(wp + (size_t)(32 * i + jj) * (F * 2));
      W1[i][jj] = USE_NT ? __builtin_nontemporal_load(a) : *a;
    }
  __builtin_amdgcn_sched_barrier(0);
  lds_signal(sm, C_H0);
  STAMP(3);
  v4u Bf[3][8];
#pragma unroll
  for (int i = 0; i < 3; i++)
#pragma unroll
    for (int e = 0; e < 8; e++) {
#pragma unroll
      for (int d = 0; d < 4; d++)
        Bf[i][e][d] = __builtin_amdgcn_perm(W1[i][2 * d + 1][e >> 1], W1[i][2 * d][e >> 1], (e & 1) ? 0x07060302u : 0x05040100u);
      asm volatile("" : "+v"(Bf[i][e]));
    }
  STAMP(17);
  lds_wait<1>(sm, C_XS, 2);
  STAMP(5);
  f4 acc[8];
#pragma unroll
  for (int e = 0; e < 8; e++) acc[e] = (f4){0.f, 0.f, 0.f, 0.f};
#pragma unroll
  for (int i = 0; i < 3; i++) {
    const v4u A = *(const LAS v4u*)(sm + L_XS + (n16 & 7) * XS_STRIDE + (96 * wave + 32 * i + 8 * g4) * 2);
#pragma unroll
    for (int e = 0; e < 8; e++) acc[e] = mfma16(A, Bf[i][e], acc[e]);
  }
  {
    LAS char* dst = sm + L_RED + ((wave * 8 + 4 * (g4 & 1) + (g4 & 2)) * 128 + 8 * n16) * 4;
#pragma unroll
    for (int r = 0; r < 2; r++) {
      f4 a, c;
#pragma unroll
      for (int e = 0; e < 4; e++) { a[e] = g4 >= 2 ? acc[e][2 + r] : acc[e][r]; c[e] = g4 >= 2 ? acc[4 + e][2 + r] : acc[4 + e][r]; }
      *(LAS f4*)(dst + r * 512) = a;
      *(LAS f4*)(dst + r * 512 + 16) = c;
    }
  }
  lds_signal(sm, C_RED);
  STAMP(18);
  if (wave >= 2) {
    const int tk = 2 * (wave - 2) + (lane >> 5);
    const LAS char* src = sm + L_RED + (tk * 128 + 2 * (lane & 31)) * 4;
    lds_wait(sm, C_RED, 6);
    f2 G = {0.f, 0.f}, U = {0.f, 0.f};
#pragma unroll
    for (int w = 0; w < 6; w++) {
      G += *(const LAS f2*)(src + w * 4096);
      U += *(const LAS f2*)(src + w * 4096 + 256);
    }
    const u32 gb = pk2(G.x, G.y), ub = pk2(U.x, U.y);
    const float g0 = lo16(gb), g1 = hi16(gb);
    const u32 sg = pk2(g0 * __builtin_amdgcn_rcpf(1.0f + __expf(-g0)), g1 * __builtin_amdgcn_rcpf(1.0f + __expf(-g1)));
    *(LAS u32*)(sm + L_HB + tk * HB_STRIDE + (lane & 31) * 4) = pk2(lo16(sg) * lo16(ub), hi16(sg) * hi16(ub));
    lds_signal(sm, C_HB);
    STAMP(15);
  }
}

__device__ __forceinline__ void ffn_down(const Ctx& P, int cu0) {
  LAS char* sm = P.sm;
  const int lane = P.lane, wave = P.wave, n16 = lane & 15, g4 = lane >> 4;
  const __amdgpu_buffer_rsrc_t rd = mkrsrc(P.wd + (size_t)64 * P.mw * D, 64 * D * 2);
  const int o2 = (8 * g4 * D + 64 * cu0 + 4 * n16) * 2;
  const u32 tag = *(const LAS u32*)(sm + L_REC), tag12 = tag & 0xFFFu;
  v2u W2[3][2][8];
  lds_wait<1>(sm, C_H0, 6);
  __builtin_amdgcn_sched_barrier(0);
#pragma unroll
  for (int ci = 0; ci < 3; ci++)
#pragma unroll
    for (int ks = 0; ks < 2; ks++)
#pragma unroll
      for (int jj = 0; jj < 8; jj++)
        W2[ci][ks][jj] = __builtin_amdgcn_raw_buffer_load_b64(rd, ((ci < 2 || wave == 6) ? o2 : OOB) + (jj & 3) * (D * 2) + ci * 128,
                                                              (32 * ks + (jj & 4)) * (D * 2), AUX_NT);
  __builtin_amdgcn_sched_barrier(0);
  STAMP(4);
  v4u Bd[3][2][4];
#pragma unroll
  for (int ci = 0; ci < 3; ci++)
#pragma unroll
    for (int ks = 0; ks < 2; ks++)
#pragma unroll
      for (int e = 0; e < 4; e++) {
#pragma unroll
        for (int d = 0; d < 4; d++)
          Bd[ci][ks][e][d] = __builtin_amdgcn_perm(W2[ci][ks][2 * d + 1][e >> 1], W2[ci][ks][2 * d][e >> 1], (e & 1) ? 0x07060302u : 0x05040100u);
        asm volatile("" : "+v"(Bd[ci][ks][e]));
      }
  STAMP(19);
  lds_wait(sm, C_HB, 4);
  (void)gate_post(P, G_P, lds_arrive(sm, C_DN, 3), gate_inc(tag));
  STAMP(20);
  const v4u A0 = *(const LAS v4u*)(sm + L_HB + (n16 & 7) * HB_STRIDE + g4 * 16);
  const v4u A1 = *(const LAS v4u*)(sm + L_HB + (n16 & 7) * HB_STRIDE + 64 + g4 * 16);
#pragma unroll
  for (int ci = 0; ci < 3; ci++) {
    if (ci == 2 && wave != 6) break;
    f4 ad[4];
#pragma unroll
    for (int e = 0; e < 4; e++) ad[e] = mfma16(A0, Bd[ci][0][e], (f4){0.f, 0.f, 0.f, 0.f});
#pragma unroll
    for (int e = 0; e < 4; e++) ad[e] = mfma16(A1, Bd[ci][1][e], ad[e]);
    v4u s0, s1;
#pragma unroll
    for (int e = 0; e < 4; e++) {
      const u32 x = ((__float_as_uint(g4 >= 2 ? ad[e][2] : ad[e][0]) + 0x800u) & 0xFFFFF000u) | tag12;
      const u32 y = ((__float_as_uint(g4 >= 2 ? ad[e][3] : ad[e][1]) + 0x800u) & 0xFFFFF000u) | tag12;
      s0[e] = (u32)__builtin_amdgcn_update_dpp((int)x, (int)y, 0x128, 0xf, 0xc, false);
      s1[e] = (u32)__builtin_amdgcn_update_dpp((int)y, (int)x, 0x128, 0xf, 0x3, false);
    }
    const int off = WS_P + P.mw * PART_B + (16 * (cu0 + ci) + (n16 & 7)) * 128 + (n16 >> 3) * 64 + g4 * 16;
    __builtin_amdgcn_raw_buffer_store_b128(s0, P.rws, off, 0, AUX_SC1);
    __builtin_amdgcn_raw_buffer_store_b128(s1, P.rws, off + 1024, 0, AUX_SC1);
  }
  STAMP(21);
}

__device__ __forceinline__ u32 touch_w1(const Ctx& P, int mat, int blk) {
  const int r7 = (P.lane * 57) >> 9, c9 = P.lane - 9 * r7;
  return __builtin_amdgcn_raw_buffer_load_b32(mkrsrc(__builtin_amdgcn_readfirstlane(mat) ? P.wu : P.wg, D * F * 2), P.lane < 63 ? r7 * (F * 2) + c9 * 128 : OOB,
                                              (PF_R0 + 7 * blk) * (F * 2) + (P.b & 7) * 1152, AUX_NT);
}
__device__ __forceinline__ u32 touch_w2(const Ctx& P, int unit) {
  return __builtin_amdgcn_raw_buffer_load_b32(mkrsrc(P.wd, D * F * 2), unit < 81 ? P.lane * 128 : OOB,
                                              (P.b & 7) * (9 * 64 * D * 2) + unit * 8192, AUX_NT);
}

__device__ __forceinline__ void publish_y(const Ctx& P, const LAS char* yb, int ystride, int tile0, int t, int step, int n) {
  const int gch = t & 3;
  const int goff = WS_U + (tile0 + (t >> 2)) * TILE_B + (4 * P.cg + gch) * 16;
#pragma unroll
  for (int it = 0; it < n; it++) {
    const v4u y4 = *(const LAS v4u*)(yb + gch * ystride + (it * step + (t >> 2)) * 16);
    const v4u u4 = *(const LAS v4u*)(P.sm + L_U + gch * UL_B + (32 + tile0 + it * step + (t >> 2)) * 16);
    v4u w4;
#pragma unroll
    for (int e = 0; e < 4; e++) w4[e] = (y4[e] == u4[e]) ? SAME : y4[e];
    __builtin_amdgcn_raw_buffer_store_b128(w4, P.rws, goff, it * step * TILE_B, AUX_SC1);
  }
}

__device__ __forceinline__ void gather_half(const Ctx& P, int tile0, v4u (&g)[8]) {
  const int goff = WS_U + (tile0 + (P.lane >> 2)) * TILE_B + (4 * P.cg + (P.lane & 3)) * 16;
#pragma unroll
  for (int it = 0; it < 8; it++) g[it] = __builtin_amdgcn_raw_buffer_load_b128(P.rws, goff, it * 16 * TILE_B, AUX_SC1);
}

__device__ __forceinline__ void verify_half(const Ctx& P, int tile0) {
  LAS char* sm = P.sm;
  lds_wait<1>(sm, C_TRUST, 1);
  v4u g[8];
  gather_half(P, tile0, g);
  LAS char* ud = sm + L_U + (P.lane & 3) * UL_B + (32 + tile0 + (P.lane >> 2)) * 16;
  u32 df = 0;
#pragma unroll
  for (int it = 0; it < 8; it++) {
    const v4u x = *(const LAS v4u*)(ud + it * 256) ^ g[it];
    df |= x.x | x.y;
    df |= x.z | x.w;
  }
  if (__builtin_expect(__any(df != 0), 0)) {
#pragma unroll
    for (int it = 0; it < 8; it++) *(LAS v4u*)(ud + it * 256) = g[it];
    lds_signal(sm, C_REDO);
  }
  lds_signal(sm, C_OK);
}

__device__ __forceinline__ void conv_phase(const Ctx& P, u32 inc) {
  LAS char* sm = P.sm;
  const int tid = P.tid, lane = P.lane, wave = P.wave, n16 = lane & 15, g4 = lane >> 4, gch = tid & 3;
  const int goff = WS_U + (tid >> 2) * TILE_B + (4 * P.cg + gch) * 16;
  LAS char* ud = sm + L_U + gch * UL_B + (32 + (tid >> 2)) * 16;
  const int p1 = n16 + 1, kap = p1 & 7;
  const int base16 = 258 * kap + ((0x98654320u >> (4 * kap)) & 15);
  const LAS char* pa = sm + L_H + wave * HC_B + (base16 + g4 + (p1 >> 3)) * 16;
  const LAS char* pb = sm + L_U + wave * UL_B + (32 + 2 * n16 + 1 - g4 - 28) * 16;
  lds_wait<1>(sm, C_QQ, 5);
  lds_wait<1>(sm, C_GU, 1);
  STAMP(3);
  if (U_DELAY) __builtin_amdgcn_s_sleep(U_DELAY);
  STAMP(4);
  v4u g[2];
#pragma unroll
  for (int it = 0; it < 2; it++) g[it] = __builtin_amdgcn_raw_buffer_load_b128(P.rws, goff, it * 64 * TILE_B, AUX_SC1);
  *(LAS v4u*)ud = g[0];
  *(LAS v4u*)(ud + 1024) = g[1];
  lds_signal(sm, C_U);
  if (!DRAIN_FIRST) {
    asm volatile("s_waitcnt vmcnt(0)" ::: "memory");
    lds_signal(sm, C_DRAIN);
  }
  STAMP(5);
  lds_wait(sm, C_H, 2);
  lds_wait(sm, C_U, 4);
  STAMP(6);
  f4 acc[8];
  v4u A[2][8], B[2][4];
  _Pragma("nounroll") for (int pass = 0; pass < 2; pass++) {
#pragma unroll
  for (int t = 0; t < 8; t++) acc[t] = (f4){0.f, 0.f, 0.f, 0.f};
#pragma unroll
  for (int a = 0; a < 8; a++) {
    A[0][a] = *(const LAS v4u*)(pa + a * 512);
    if (a < 4) B[0][a] = *(const LAS v4u*)(pb + a * 512 + 7 * 64);
  }
#pragma unroll
  for (int bb = 0; bb < 8; bb++) {
    __builtin_amdgcn_sched_barrier(0);
    if (bb == 7) {
      lds_wait(sm, C_U2, 1);
      __builtin_amdgcn_sched_barrier(0);
    }
#pragma unroll
    for (int a = 0; a < 8; a++) {
      if (bb < 7) {
        A[(bb + 1) & 1][a] = *(const LAS v4u*)(pa + a * 512 + (bb + 1) * 64);
        if (a < 4) B[(bb + 1) & 1][a] = *(const LAS v4u*)(pb + a * 512 + (6 - bb) * 64);
      } else if (a < 4) {
        A[0][a] = *(const LAS v4u*)(pa + a * 512);
        B[0][a] = *(const LAS v4u*)(pb + (4 + a) * 512 + 7 * 64);
      }
    }
#pragma unroll
    for (int t = 0; t < 8; t++)
#pragma unroll
      for (int c = 0; c < 4; c++)
        if (c <= t) acc[t] = mfma16(A[bb & 1][t - c], B[bb & 1][c], acc[t]);
#pragma unroll
    for (int i = 0; i < (bb < 7 ? 12 : 8); i++) {
      __builtin_amdgcn_sched_group_barrier(0x8, 2, 0);
      __builtin_amdgcn_sched_group_barrier(0x100, 1, 0);
    }
    if (bb < 7) __builtin_amdgcn_sched_group_barrier(0x8, 2, 0);
    else __builtin_amdgcn_sched_group_barrier(0x8, 10, 0);
  }
#pragma unroll
  for (int bb = 0; bb < 8; bb++) {
    __builtin_amdgcn_sched_barrier(0);
    if (bb == 1) {
      LAS char* yb = sm + L_STG + wave * Y1_B + n16 * 32 + g4 * 8;
#pragma unroll
      for (int t = 0; t < 4; t++) {
        v2u y2 = {umin(pk2(acc[t][0], acc[t][1]), MAXV), umin(pk2(acc[t][2], acc[t][3]), MAXV)};
        *(LAS v2u*)(yb + t * 512) = y2;
      }
    }
    if (bb == 2) lds_signal(sm, C_Y1);
    if (bb == Y_POST) {
      (void)gate_post(P, G_Y, wave == 0 && pass == 0, inc);
      __builtin_amdgcn_sched_barrier(0);
    }
    if (bb < 7) {
#pragma unroll
      for (int a = 0; a < 4; a++) {
        A[(bb + 1) & 1][a] = *(const LAS v4u*)(pa + a * 512 + (bb + 1) * 64);
        B[(bb + 1) & 1][a] = *(const LAS v4u*)(pb + (4 + a) * 512 + (6 - bb) * 64);
      }
    }
#pragma unroll
    for (int t = 0; t < 4; t++)
#pragma unroll
      for (int c = 0; c <= t; c++) acc[4 + t] = mfma16(A[bb & 1][t - c], B[bb & 1][c], acc[4 + t]);
    if (bb < 7) {
#pragma unroll
      for (int i = 0; i < 8; i++) {
        __builtin_amdgcn_sched_group_barrier(0x8, 1, 0);
        __builtin_amdgcn_sched_group_barrier(0x100, 1, 0);
      }
      __builtin_amdgcn_sched_group_barrier(0x8, 2, 0);
    }
  }
  __builtin_amdgcn_sched_barrier(0);
    lds_wait(sm, C_OK, 2);
    if (__builtin_expect(__builtin_amdgcn_readfirstlane(((const LAS u32*)(sm + L_CNT))[C_REDO]) == 0u, 1)) break;
    NOTE(26, 7);
  }
  if (Y_POST == 8) (void)gate_post(P, G_Y, wave == 0, inc);
  {
    LAS char* yb = sm + L_H + wave * (HC_B + 64) + n16 * 32 + g4 * 8;
#pragma unroll
    for (int t = 0; t < 4; t++) {
      v2u y2 = {umin(pk2(acc[4 + t][0], acc[4 + t][1]), MAXV), umin(pk2(acc[4 + t][2], acc[4 + t][3]), MAXV)};
      *(LAS v2u*)(yb + t * 512) = y2;
    }
  }
  lds_signal(sm, C_Y);
  STAMP(7);
  lds_wait(sm, C_Y, 4);
  publish_y(P, sm + L_H, HC_B + 64, 128, tid, 64, 2);
  STAMP(8);
}

struct EarlyX { v4u xa[9], wb[9]; };
struct Early {
  u32 kk[11], vv[8], dd;
  v4u sk;
  u64 epoch;
  u32 cx;
};

__device__ __forceinline__ void tile_wave(const Ctx& P, const Early& E, const bool MOE) {
  LAS char* sm = P.sm;
  const int tid = P.tid, lane = P.lane, wave = P.wave, C = P.C;
  const bool act = tid < 288;
  const int j4 = min(tid, 287) * 4;
  const int row0 = 8 * C * (D * 2);
  const u32 (&kk)[11] = E.kk;
  const u32 (&vv)[8] = E.vv;
  const v4u sk = E.sk;
  u32 qq[8];
  lds_wait<1>(sm, C_TAG, 1);
  const u32 uinc = gate_inc(*(const LAS u32*)(sm + L_REC));
  __builtin_amdgcn_sched_barrier(0);
  u32 gold;
  u32 up[2][4];
  {
    float w0[4] = {lo16(sk.x), hi16(sk.x), lo16(sk.y), hi16(sk.y)};
    float w1[4] = {lo16(sk.z), hi16(sk.z), lo16(sk.w), hi16(sk.w)};
    float u0[8], u1[8];
#pragma unroll
    for (int s = 0; s < 8; s++) {
      float a = lo16(kk[s]) * w0[3], c = hi16(kk[s]) * w1[3];
      a = fmaf(lo16(kk[s + 1]), w0[2], a); c = fmaf(hi16(kk[s + 1]), w1[2], c);
      a = fmaf(lo16(kk[s + 2]), w0[1], a); c = fmaf(hi16(kk[s + 2]), w1[1], c);
      a = fmaf(lo16(kk[s + 3]), w0[0], a); c = fmaf(hi16(kk[s + 3]), w1[0], c);
      u0[s] = a * lo16(vv[s]); u1[s] = c * hi16(vv[s]);
    }
#pragma unroll
    for (int i = 0; i < 4; i++) {
      up[0][i] = umin(pk2(u0[7 - 2 * i], u0[6 - 2 * i]), MAXV);
      up[1][i] = umin(pk2(u1[7 - 2 * i], u1[6 - 2 * i]), MAXV);
    }
    STAMP(1);
    LAS char* stg = sm + L_STG + wave * 2048;
    v4u p0 = {up[0][0], up[0][1], up[0][2], up[0][3]}, p1 = {up[1][0], up[1][1], up[1][2], up[1][3]};
    if (act) { *(LAS v4u*)(stg + lane * 32) = p0; *(LAS v4u*)(stg + lane * 32 + 16) = p1; }
    asm volatile("" ::: "memory");
    p0 = *(const LAS v4u*)(stg + lane * 16);
    p1 = *(const LAS v4u*)(stg + (wave < 4 ? 1024 : 0) + lane * 16);
    STAMP(13);
    const int so = WS_U + C * TILE_B + wave * 2048 + lane * 16;
    __builtin_amdgcn_raw_buffer_store_b128(p0, P.rws, so, 0, AUX_SC1);
    __builtin_amdgcn_raw_buffer_store_b128(p1, P.rws, wave < 4 ? so + 1024 : OOB, 0, AUX_SC1);
  }
  __builtin_amdgcn_sched_barrier(0);
  const bool poster = lds_arrive(sm, C_VER, 4);
  gold = gate_post(P, G_U, poster, uinc);
  STAMP(2);
  lds_wait(sm, C_STAT, 1);
#pragma unroll
  for (int i = 0; i < 8; i++) qq[i] = *(const LAS u32*)(sm + xrow_off(i) + j4);
  lds_signal(sm, C_QQ);
  u32 dd = E.dd;
  asm volatile("" : "+v"(dd));
  if (poster) gate_confirm(P, G_U, gold, uinc);
  float xn[2][8], c0[2][8];
  auto prepare = [&]()
  {
    const float d0 = lo16(dd), d1 = hi16(dd);
#pragma unroll
    for (int s2 = 0; s2 < 4; s2++) {
      const f4 st = *(const LAS f4*)(sm + L_STAT + s2 * 16);
#pragma unroll
      for (int e = 0; e < 2; e++) {
        const int s = 2 * s2 + e;
        const float xa = lo16(qq[s]), xb = hi16(qq[s]);
        const float na = fmaf(xa, st[2 * e], st[2 * e + 1]), nb = fmaf(xb, st[2 * e], st[2 * e + 1]);
        const u32 pa = up[0][(7 - s) >> 1], pb = up[1][(7 - s) >> 1];
        const float ua = ((7 - s) & 1) ? hi16(pa) : lo16(pa), ub = ((7 - s) & 1) ? hi16(pb) : lo16(pb);
        xn[0][s] = na; xn[1][s] = nb;
        c0[0][s] = fmaf(na, fmaf(ua, d0, 1.0f), xa);
        c0[1][s] = fmaf(nb, fmaf(ub, d1, 1.0f), xb);
        asm volatile("" : "+v"(xn[0][s]), "+v"(xn[1][s]), "+v"(c0[0][s]), "+v"(c0[1][s]));
      }
    }
  };
  prepare();
  if (DRAIN_FIRST || !(P.is_conv && wave < 4)) {
    asm volatile("s_waitcnt vmcnt(0)" ::: "memory");
    lds_signal(sm, C_DRAIN);
  }
  STAMP(14);
  if (MOE) {
    ffn_gate_up(P);
    if (wave < 2) ffn_down(P, 2 * wave);
  } else {
   if (P.is_conv) {
    if (wave < 4) {
      conv_phase(P, uinc);
    } else {
      lds_wait<1>(sm, C_QQ, 5);
      lds_wait<1>(sm, C_GU, 1);
      __builtin_amdgcn_s_sleep(3 + U_DELAY);
      v4u g[8];
      gather_half(P, 128, g);
#pragma unroll
      for (int it = 0; it < 8; it++) *(LAS v4u*)(sm + L_U + (lane & 3) * UL_B + (32 + 128 + it * 16 + (lane >> 2)) * 16) = g[it];
      lds_signal(sm, C_U2);
      STAMP(5);
      verify_half(P, 128);
      STAMP(6);
      lds_wait<2>(sm, C_Y1, 4);
      lds_wait<1>(sm, C_OK, 2);
      lds_wait<1>(sm, C_Y1, __builtin_amdgcn_readfirstlane(((const LAS u32*)(sm + L_CNT))[C_REDO]) ? 8 : 4);
      publish_y(P, sm + L_STG, Y1_B, 0, lane, 32, 4);
    }
  } else {
    STAMP(5);
   }
  }
  lds_wait<1>(sm, C_SEL, 1);
  const u32 skip = __builtin_amdgcn_readfirstlane(*(const LAS u32*)(sm + L_REC + 4));
  const __amdgpu_buffer_rsrc_t ro = mkrsrc(P.out, S * D * 2);
  const int oo = act ? j4 : OOB;
  const int uoff = WS_U + C * TILE_B + j4 * 8;
  lds_wait<1>(sm, C_GY, 1);
  STAMP(9);
  v4u y0, y1;
  _Pragma("nounroll") for (u32 spin = 0; spin < SPIN_CAP; spin++) {
    asm volatile("" ::: "memory");
    y0 = __builtin_amdgcn_raw_buffer_load_b128(P.rws, uoff, 0, AUX_SC1);
    y1 = __builtin_amdgcn_raw_buffer_load_b128(P.rws, uoff + 16, 0, AUX_SC1);
    bool ok = true;
#pragma unroll
    for (int e = 0; e < 4; e++) ok = ok & (y0[e] != up[0][e]) & (y1[e] != up[1][e]);
    if (__builtin_expect(__all(ok), 1)) break;
    NOTE(25, spin + 1);
    __builtin_amdgcn_s_sleep(4);
  }
  STAMP(16);
#pragma unroll
  for (int e = 0; e < 4; e++) {
    const u32 a = y0[e] == SAME ? up[0][e] : y0[e], c = y1[e] == SAME ? up[1][e] : y1[e];
    const u32 o0 = pk2(fmaf(xn[0][2 * e], lo16(a), c0[0][2 * e]), fmaf(xn[1][2 * e], lo16(c), c0[1][2 * e]));
    const u32 o1 = pk2(fmaf(xn[0][2 * e + 1], hi16(a), c0[0][2 * e + 1]), fmaf(xn[1][2 * e + 1], hi16(c), c0[1][2 * e + 1]));
    const int so = row0 + (e >> 1) * 4 * (D * 2);
    if (!((skip >> (2 * e)) & 1)) __builtin_amdgcn_raw_buffer_store_b32(o0, ro, oo + ((2 * e) & 3) * (D * 2), so, OUT_AUX);
    if (!((skip >> (2 * e + 1)) & 1)) __builtin_amdgcn_raw_buffer_store_b32(o1, ro, oo + ((2 * e + 1) & 3) * (D * 2), so, OUT_AUX);
  }
}

__device__ __forceinline__ void route(const Ctx& P, u32 tag, u64 epoch, const int ROLE) {
  LAS char* sm = P.sm;
  const int lane = P.lane, wave = P.wave;
  (void)wave;
  v4u g[16];
  if (ROLE == 2) __builtin_amdgcn_s_setprio(0);
  if (ROLE == 0) {
    if (HELPERS != 0 && PF_CE != 0) {
      lds_wait<1>(sm, C_VER, PF_WHEN);
      const int u0 = 50 + (P.b >> 3);
      u32 t[PF_CE ? PF_CE : 1];
#pragma unroll
      for (int i = 0; i < PF_CE; i++) t[i] = touch_w1(P, (u0 + 18 * i) & 1, (u0 + 18 * i) >> 1);
#pragma unroll
      for (int i = 0; i < PF_CE; i++) asm volatile("" :: "v"(t[i]));
      STAMP(18);
    }
    gate_wait(P, G_U, 16u, gate_inc(tag));
    lds_signal(sm, C_GU);
    STAMP(15);
    post_u_complete(P, gate_inc(tag));
    STAMP(13);
    gate_wait(P, G_V, 16u, gate_inc(tag), SPIN_CAP);
    lds_signal(sm, C_TRUST);
    __builtin_amdgcn_s_setprio(0);
  } else {
    gate_wait(P, ROLE == 1 ? G_R : G_U, 16u, gate_inc(tag));
  }
  STAMP(3);
  _Pragma("nounroll") for (u32 spin = 0; spin < SPIN_CAP; spin++) {
    asm volatile("" ::: "memory");
#pragma unroll
    for (int i = 0; i < 16; i++)
      g[i] = __builtin_amdgcn_raw_buffer_load_b128(P.rws, WS_R + lane * 16 + (i & 3) * 1024, (i >> 2) * 4096, AUX_SC1);
    if (spin == 0) lds_signal(sm, C_PUB);
    u32 bad = 0;
#pragma unroll
    for (int i = 0; i < 16; i++) {
      bad = __builtin_amdgcn_bitop3_b32(bad, g[i].y, tag, 0xF6);
      bad = __builtin_amdgcn_bitop3_b32(bad, g[i].w, tag, 0xF6);
    }
    if (__builtin_expect(__all(bad == 0), 1)) break;
    NOTE(24, spin + 1);
    __builtin_amdgcn_s_sleep(4);
  }
  STAMP(4);
  if (P.b == 0 && lane == 0) __hip_atomic_store((GAS u64*)(P.ws + WS_E), epoch, __ATOMIC_RELAXED, __HIP_MEMORY_SCOPE_AGENT);
  u32 k1 = umax(g[0].x, g[0].z), k2 = umin(g[0].x, g[0].z), k3 = 0;
#pragma unroll
  for (int i = 2; i < 32; i++) {
    const u32 x = (i & 1) ? g[i >> 1].z : g[i >> 1].x;
    k3 = umed3(k2, k3, x);
    k2 = umed3(k1, k2, x);
    k1 = umax(k1, x);
  }
  u32 tau = umax(k1, dppu<0xB1>(k1));
  tau = umax(tau, dppu<0x4E>(tau));
  tau = umax(tau, dppu<0x141>(tau));
  tau = umin(tau, dppu<0x140>(tau));
  tau = xrow_umin(tau);
  LAS u32* cand = (LAS u32*)(sm + L_CAND);
  const u64 m1 = __ballot(k1 >= tau), m2 = __ballot(k2 >= tau), m3 = __ballot(k3 >= tau);
  const u32 n1 = __builtin_popcountll(m1), n2 = n1 + __builtin_popcountll(m2), n = n2 + __builtin_popcountll(m3);
  bool slow = n > 64;
  if (__builtin_expect(!slow, 1)) {
    const u32 p1 = __builtin_amdgcn_mbcnt_hi((u32)(m1 >> 32), __builtin_amdgcn_mbcnt_lo((u32)m1, 0u));
    const u32 p2 = __builtin_amdgcn_mbcnt_hi((u32)(m2 >> 32), __builtin_amdgcn_mbcnt_lo((u32)m2, n1));
    const u32 p3 = __builtin_amdgcn_mbcnt_hi((u32)(m3 >> 32), __builtin_amdgcn_mbcnt_lo((u32)m3, n2));
    cand[k1 >= tau ? p1 : 64] = k1;
    cand[k2 >= tau ? p2 : 64] = k2;
    cand[k3 >= tau ? p3 : 64] = k3;
    asm volatile("" ::: "memory");
    const u32 mine = cand[lane];
    u32 rank = 0;
    _Pragma("nounroll") for (u32 jj = 0; jj < n; jj += 8) {
      const v4u c4 = *(const LAS v4u*)(cand + jj), d4 = *(const LAS v4u*)(cand + jj + 4);
      rank += (c4.x > mine) + (c4.y > mine) + (c4.z > mine) + (c4.w > mine);
      rank += (d4.x > mine) + (d4.y > mine) + (d4.z > mine) + (d4.w > mine);
    }
    const bool in = (u32)lane < n && rank < 8;
    if (in) ((LAS u32*)(sm + L_SEL))[rank] = mine;
    slow = __any(in && (u32)lane >= n2);
  }
  if (__builtin_expect(slow, 0)) {
    _Pragma("nounroll") for (int r = 0; r < 8; r++) {
      u32 mx = 0;
#pragma unroll
      for (int i = 0; i < 16; i++) mx = umax3(mx, g[i].x, g[i].z);
      mx = wave_umax(mx);
#pragma unroll
      for (int i = 0; i < 16; i++) { g[i].x = g[i].x == mx ? 0u : g[i].x; g[i].z = g[i].z == mx ? 0u : g[i].z; }
      if (lane == 0) ((LAS u32*)(sm + L_SEL))[r] = mx;
    }
  }
  asm volatile("" ::: "memory");
  {
    const u32 tk = 2047u - (((LAS u32*)(sm + L_SEL))[lane & 7] & 2047u);
    u32 mk = (int)(tk >> 3) == P.C ? 1u << (tk & 7) : 0u;
    mk |= dppu<0xB1>(mk);
    mk |= dppu<0x4E>(mk);
    mk |= dppu<0x141>(mk);
    if (lane == 0) *(LAS u32*)(sm + L_REC + 4) = mk;
  }
  lds_signal(sm, C_SEL);
  STAMP(5);
}

__device__ __forceinline__ u32 load_msd(const Ctx& P, int tok, u32 tag) {
  v2u g;
  _Pragma("nounroll") for (u32 spin = 0; spin < SPIN_CAP; spin++) {
    asm volatile("" ::: "memory");
    g = __builtin_amdgcn_raw_buffer_load_b64(P.rws, WS_M + tok * 8, 0, AUX_SC1);
    if (__builtin_expect(__all(g.y == tag), 1)) break;
    __builtin_amdgcn_s_sleep(4);
  }
  return g.x;
}

__device__ __forceinline__ void routed_rows(const Ctx& P, u32 tag) {
  LAS char* sm = P.sm;
  const int lane = P.lane, wave = P.wave, C = P.C, cg = P.cg;
  (void)wave; (void)C; (void)cg;
  const __amdgpu_buffer_rsrc_t rq = mkrsrc(P.q, S * D * 2);
  {
    const int cw = wave - 6;
    const v4u s4 = *(const LAS v4u*)(sm + L_SEL + cw * 16);
    Row rr[4];
#pragma unroll
    for (int e = 0; e < 4; e++) {
      const int tok = 2047 - (int)(__builtin_amdgcn_readfirstlane(s4[e]) & 2047u);
      rr[e] = load_row<0>(rq, tok * (D * 2), lane);
    }
    const u32 ks = (lane & 2) ? ((lane & 1) ? s4.w : s4.z) : ((lane & 1) ? s4.y : s4.x);
    const int mtok = 2047 - (int)(ks & 2047u);
    v2u mg = __builtin_amdgcn_raw_buffer_load_b64(P.rws, WS_M + mtok * 8, 0, AUX_SC1);
    __builtin_amdgcn_sched_barrier(0);
    lds_signal(sm, C_W1);
    __builtin_amdgcn_sched_barrier(0);
    u32 msd = mg.x;
    if (__builtin_expect(!__all(mg.y == tag), 0)) msd = load_msd(P, mtok, tag);
    STAMP(15);
    const float rsl = __builtin_amdgcn_rcpf(lo16(msd)), nml = -hi16(msd) * rsl;
#pragma unroll
    for (int e = 0; e < 4; e++) {
      const float rs = __uint_as_float(__builtin_amdgcn_readlane(__float_as_uint(rsl), e));
      const float nm = __uint_as_float(__builtin_amdgcn_readlane(__float_as_uint(nml), e));
      LAS char* dst = sm + L_XS + (cw * 4 + e) * XS_STRIDE;
      const Row& x = rr[e];
      v2u a = {pk2(fmaf(lo16(x.a0.x), rs, nm), fmaf(hi16(x.a0.x), rs, nm)), pk2(fmaf(lo16(x.a0.y), rs, nm), fmaf(hi16(x.a0.y), rs, nm))};
      v2u c = {pk2(fmaf(lo16(x.a1.x), rs, nm), fmaf(hi16(x.a1.x), rs, nm)), pk2(fmaf(lo16(x.a1.y), rs, nm), fmaf(hi16(x.a1.y), rs, nm))};
      *(LAS v2u*)(dst + lane * 8) = a;
      *(LAS v2u*)(dst + 512 + lane * 8) = c;
      *(LAS u16*)(dst + 1024 + lane * 2) = (u16)pk2(fmaf(lo16(x.a2), rs, nm), 0.f);
    }
    lds_signal(sm, C_XS);
    STAMP(6);
    __builtin_amdgcn_s_setprio(0);
    ffn_down(P, wave == 6 ? 4 : 7);
  }
}

__device__ __forceinline__ void aux_wave(const Ctx& P, u32 cx, const int ROLE) {
  LAS char* sm = P.sm;
  const int lane = P.lane, wave = P.wave, C = P.C, cg = P.cg;
  (void)wave; (void)C; (void)cg;
  u32 tag;
    if (CODE_PF && wave == 6) asm volatile("" :: "v"(cx));
    if (ROLE != 0 && wave == 5) {
      __builtin_amdgcn_s_setprio(3);
      lds_wait<1>(sm, C_TAG, 1);
      post_u_complete(P, gate_inc(*(const LAS u32*)(sm + L_REC)));
      __builtin_amdgcn_s_setprio(0);
      STAMP(13);
    }
    if (ROLE == 2 && HELPERS && wave == 6) {
      lds_wait<1>(sm, C_VER, 5);
      const int idx = (P.b - NCONV - NMOE) >> 3;
      u32 ta = touch_w1(P, 0, idx), tb = touch_w1(P, 1, idx);
#pragma unroll
      for (int i = 1; i < 5; i++) {
        const u32 tc = touch_w1(P, 0, i * 5 + idx), td = touch_w1(P, 1, i * 5 + idx);
        asm volatile("" :: "v"(ta), "v"(tb));
        ta = tc; tb = td;
      }
      asm volatile("" :: "v"(ta), "v"(tb));
      STAMP(5);
    }
    if (ROLE == 0) {
      __builtin_amdgcn_s_sleep(H_DELAY);
      hcopy(P, 2 * (wave - 5));
      STAMP(3);
      if (wave == 5) {
        lds_wait<1>(sm, C_U, 4);
        verify_half(P, 0);
        STAMP(6);
        lds_wait<2>(sm, C_Y1, 4);
        lds_wait<1>(sm, C_OK, 2);
        lds_wait<1>(sm, C_Y1, __builtin_amdgcn_readfirstlane(((const LAS u32*)(sm + L_CNT))[C_REDO]) ? 8 : 4);
        publish_y(P, sm + L_STG + 256, Y1_B, 16, lane, 32, 4);
      } else if (HELPERS) {
        lds_wait<1>(sm, C_U2, 1);
        u32 t[9];
#pragma unroll
        for (int i = 0; i < 4; i++) { const int u = 50 + i * 18 + (P.b >> 3); t[5 + i] = touch_w1(P, u & 1, u >> 1); }
#pragma unroll
        for (int i = 0; i < 5; i++) t[i] = touch_w2(P, i * 18 + (P.b >> 3));
        asm volatile("" :: "v"(t[5]), "v"(t[6]), "v"(t[7]), "v"(t[8]));
        STAMP(5);
        asm volatile("" :: "v"(t[0]), "v"(t[1]), "v"(t[2]), "v"(t[3]), "v"(t[4]));
        STAMP(6);
      }
    }
    if (wave == 5) {
      if (ROLE == 1) {
        ffn_gate_up(P);
        gate_wait(P, G_Y, 9u, gate_inc(*(const LAS u32*)(sm + L_REC)));
        lds_signal(sm, C_GY);
      }
      return;
    }
    lds_wait<1>(sm, C_TAG, 1);
    tag = *(const LAS u32*)(sm + L_REC);
    if (ROLE == 1) { lds_wait<1>(sm, C_SEL, 1); __builtin_amdgcn_s_setprio(3); }
  if (ROLE == 1) {
    routed_rows(P, tag);
  } else {
    gate_wait(P, G_Y, 9u, gate_inc(tag));
    lds_signal(sm, C_GY);
    STAMP(7);
  }
}

__device__ __forceinline__ void router_stats(const Ctx& P, Early& E, const EarlyX& X, const int ROLE) {
  LAS char* sm = P.sm;
  const int lane = P.lane, C = P.C;
  u32 tag;
    const int m = lane & 15, g4 = lane >> 4;
    u64 epoch = E.epoch;
    const v4u (&xa)[9] = X.xa;
    const v4u (&wb)[9] = X.wb;
    asm volatile("" : "+v"(epoch) :: "memory");
    epoch += 1;
    tag = (u32)epoch;
    if (lane == 0) *(LAS u32*)(sm + L_REC) = tag;
    lds_signal(sm, C_TAG);
    STAMP(1);
    f4 gx = {0.f, 0.f, 0.f, 0.f}, gw = {0.f, 0.f, 0.f, 0.f}, gs = {0.f, 0.f, 0.f, 0.f};
#pragma unroll
    for (int i = 0; i < 9; i++) gx = mfma16(xa[i], xa[i], gx);
    __builtin_amdgcn_sched_barrier(0);
    const u32 gold = gate_post(P, G_R, true, gate_inc(tag));
    STAMP(12);
    __builtin_amdgcn_sched_barrier(0);
#pragma unroll
    for (int i = 0; i < 9; i++) { gw = mfma16(wb[i], xa[i], gw); gs = mfma16(wb[i], wb[i], gs); }
    float sxx = (m & 2) ? ((m & 1) ? gx[3] : gx[2]) : ((m & 1) ? gx[1] : gx[0]);
    sxx = __uint_as_float(__builtin_amdgcn_ds_bpermute((m + (m & 12) * 4) * 4, __float_as_uint(sxx)));
    float sxw = m < 8 ? gw[0] : gw[1], sx = gw[2];
    sxw += dppf<0x128>(sxw);
    sx += dppf<0x128>(sx);
    sxx += dppf<0x128>(sxx);
    const float sw = __uint_as_float(__builtin_amdgcn_readlane(__float_as_uint(gs[0] + gs[1]), 2));
    const float mean = sx * (1.0f / D);
    const float var = fmaxf(fmaf(-mean, mean, sxx * (1.0f / D)), 0.f);
    const float mu = rbf(mean);
    const float sd = rbf(__builtin_amdgcn_sqrtf(rbf(rbf(var) + 1e-5f)));
    const float rs = __builtin_amdgcn_rcpf(sd);
    const u32 r = pk2(0.f, fmaf(-mu, sw, sxw) * rs);
    u32 key = ((r ^ (((int)r >> 31) | 0x80000000u)) & 0xFFFF0000u) | (u32)(2047 - (8 * C + (m & 7)));
    u32 msd = __float_as_uint(mu) | (__float_as_uint(sd) >> 16);
    asm volatile("" : "+v"(key), "+v"(msd));
    const v2u rec = {m < 8 ? key : msd, tag};
    const int ro = lane < 16 ? (m < 8 ? WS_R : WS_M) + (8 * C + (m & 7)) * 8 : OOB;
    __builtin_amdgcn_raw_buffer_store_b64(rec, P.rws, ro, 0, AUX_SC1);
    STAMP(2);
    if (lane < 8) { f2 st = {rs, -mu * rs}; *(LAS f2*)(sm + L_STAT + m * 8) = st; }
    {
      const int t = m & 7, t3 = (t * 11) >> 5;
      LAS char* xd = sm + L_U + t3 * UL_B + 512 + (t - 3 * t3) * (D * 2) + (m >> 3) * 64 + g4 * 16;
#pragma unroll
      for (int i = 0; i < 9; i++) *(LAS v4u*)(xd + i * 128) = xa[i];
    }
    lds_signal(sm, C_STAT);
    E.epoch = epoch;
    E.cx = gold;
}

__device__ __forceinline__ void router_wave(const Ctx& P, const Early& E, const int ROLE) {
  LAS char* sm = P.sm;
  const int lane = P.lane, wave = P.wave, C = P.C, cg = P.cg;
  (void)wave; (void)C; (void)cg;
  u32 tag;
    const u64 epoch = E.epoch;
    tag = (u32)epoch;
    const u32 gold = E.cx;
    gate_confirm(P, G_R, gold, gate_inc(tag));
    route(P, tag, epoch, ROLE);
  if (ROLE == 1) routed_rows(P, tag);
  if (ROLE == 0) {
    const int pi = lane & 7, ti = 4 * (pi & 1) + (pi & 2) + (pi >> 2);
    const u32 kx = ((LAS u32*)(sm + L_SEL))[ti];
    const int tok = 2047 - (int)(kx & 2047u);
    const v2u x2 = *(const GAS v2u*)((const GAS char*)P.q + (size_t)tok * (D * 2) + cg * 8);
    const v2u d2 = *(const GAS v2u*)((const GAS char*)P.dsk + cg * 8);
    const u32 msd = load_msd(P, tok, tag);
    const float rs = __builtin_amdgcn_rcpf(lo16(msd)), nm = -hi16(msd) * rs;
    const u32 rb = kx & 0xFFFF0000u;
    const float r = __uint_as_float((rb & 0x80000000u) ? (rb ^ 0x80000000u) : (~rb & 0xFFFF0000u));
    const float rsig = rbf(__builtin_amdgcn_rcpf(1.0f + __expf(-r)));
    STAMP(6);
    lds_wait<2>(sm, C_Y, 4);
    float o[4];
#pragma unroll
    for (int e = 0; e < 4; e++) {
      const float y = lo16(*(const LAS u16*)(sm + (tok < 1024 ? L_STG + e * Y1_B : L_H + e * (HC_B + 64) - 2048) + tok * 2));
      const float u = lo16(*(const LAS u16*)(sm + L_U + e * UL_B + (32 + (tok >> 3)) * 16 + (7 - (tok & 7)) * 2));
      const float x = (e & 1) ? hi16(x2[e >> 1]) : lo16(x2[e >> 1]);
      const float dk = (e & 1) ? hi16(d2[e >> 1]) : lo16(d2[e >> 1]);
      const float n = fmaf(x, rs, nm);
      o[e] = fmaf(n, fmaf(u, dk, y), x) + n;
      asm volatile("" : "+v"(o[e]));
    }
    gate_wait(P, G_P, lane < 8 ? 5u : 4u, gate_inc(tag));
    STAMP(7);
    v4u g[9];
    const u32 tag12 = tag & 0xFFFu;
    _Pragma("nounroll") for (u32 spin = 0; spin < SPIN_CAP; spin++) {
      asm volatile("" ::: "memory");
#pragma unroll
      for (int i = 0; i < 9; i++)
        g[i] = __builtin_amdgcn_raw_buffer_load_b128(P.rws, WS_P + (lane >> 3) * PART_B + cg * 128 + pi * 16, 8 * i * PART_B, AUX_SC1);
      u32 bad = 0;
#pragma unroll
      for (int i = 0; i < 9; i++) {
        g[i] ^= tag12;
        bad |= g[i].x | g[i].y;
        bad |= g[i].z | g[i].w;
      }
      if (__builtin_expect(__all((bad & 0xFFFu) == 0), 1)) break;
      NOTE(25, spin + 1);
      __builtin_amdgcn_s_sleep(4);
    }
    STAMP(8);
    f4 s4 = __builtin_bit_cast(f4, g[0]);
#pragma unroll
    for (int i = 1; i < 9; i++) s4 += __builtin_bit_cast(f4, g[i]);
#pragma unroll
    for (int e = 0; e < 4; e++) {
      float sv = s4[e];
      sv += dppf<0x128>(sv);
      o[e] = fmaf(rbf(xrow_sum(sv)), rsig, o[e]);
    }
    if (lane < 8) {
      v2u o2 = {pk2(o[0], o[1]), pk2(o[2], o[3])};
      *(GAS v2u*)((GAS char*)P.out + (size_t)tok * (D * 2) + cg * 8) = o2;
    }
  }
}

__global__ __launch_bounds__(NTHR, 2) void fused_kernel(
    const u16* __restrict__ q, const u16* __restrict__ k, const u16* __restrict__ v, const u16* __restrict__ shk,
    const u16* __restrict__ hk, const u16* __restrict__ dsk, const u16* __restrict__ wr, const u16* __restrict__ wg,
    const u16* __restrict__ wu, const u16* __restrict__ wd, u16* __restrict__ out, char* ws) {
  extern __shared__ __attribute__((aligned(16))) char smem_[];
  Ctx P;
  P.q = q; P.k = k; P.v = v; P.shk = shk; P.hk = hk; P.dsk = dsk; P.wr = wr; P.wg = wg; P.wu = wu; P.wd = wd;
  P.out = out; P.ws = ws;
  u64 pc;
  asm volatile("s_getpc_b64 %0" : "=s"(pc));
  P.code = (const char*)(pc & ~(u64)127);
  P.sm = (LAS char*)smem_;
  P.tid = threadIdx.x; P.lane = P.tid & 63;
  P.wave = __builtin_amdgcn_readfirstlane(P.tid >> 6);
  const int b = blockIdx.x;
  P.b = b;
  P.C = (b & 7) * 32 + (b >> 3);
  P.is_conv = b < NCONV;
  const bool is_moe = !P.is_conv && b < NCONV + NMOE;
  P.cg = (b & 7) * 18 + (b >> 3);
  P.mw = (b & 7) * 9 + ((b - NCONV) >> 3);
  P.rws = mkrsrc(ws, WS_END);
  const int lane = P.lane, wave = P.wave;
  (void)lane;
  STAMP(0); STAMPC(30);
  Early E;
  if (wave == 6) {
    constexpr int NL = (CODE_PF * 8 + 31) / 32;
    if (CODE_PF) E.cx = *(const GAS u32*)(P.code + min(((b >> 3) * NL + min(lane, NL - 1)) * 128, CODE_PF * 1024 - 128));
    const v4u z = {0u, 0u, 0u, 0u};
    *(LAS v4u*)(P.sm + L_U + (lane >> 4) * UL_B + (lane & 15) * 16) = z;
    *(LAS v4u*)(P.sm + L_U + (lane >> 4) * UL_B + 256 + (lane & 15) * 16) = z;
    ((LAS u32*)(P.sm + L_MISC))[lane] = 0u;
    ((LAS u32*)(P.sm + L_MISC))[64 + lane] = 0u;
    asm volatile("s_waitcnt lgkmcnt(0)" ::: "memory");
  }
  asm volatile("s_barrier" ::: "memory");
  asm volatile("" :: "s"(q), "s"(k), "s"(v), "s"(shk), "s"(hk), "s"(dsk), "s"(wr), "s"(wg), "s"(wu), "s"(wd), "s"(out), "s"(ws));
  STAMP(11);
  const int row0 = 8 * P.C * (D * 2);
  EarlyX X;
  if (P.wave == 7) {
    __builtin_amdgcn_s_setprio(3);
    const int m = lane & 15, g4 = lane >> 4;
    E.epoch = __hip_atomic_load((GAS u64*)(P.ws + WS_E), __ATOMIC_RELAXED, __HIP_MEMORY_SCOPE_AGENT);
    __builtin_amdgcn_sched_barrier(0);
    const __amdgpu_buffer_rsrc_t rq = mkrsrc(P.q, S * D * 2);
    const int xo = (m & 7) * (D * 2) + (m >> 3) * 64 + g4 * 16;
#pragma unroll
    for (int i = 0; i < 9; i++) X.xa[i] = __builtin_amdgcn_raw_buffer_load_b128(rq, xo + i * 128, row0, AUX_NT);
    __builtin_amdgcn_sched_barrier(0);
    const size_t pw = (size_t)P.wr + m * 64, po = (size_t)kOnes;
    const GAS char* wp = (const GAS char*)(m < 2 ? pw : po) + g4 * 16;
#pragma unroll
    for (int i = 0; i < 9; i++) X.wb[i] = *(const GAS v4u*)(wp + i * 128);
  }
  asm volatile("" : "+s"(P.wave));
  if (P.wave == 5) {
    const v4u g4 = {GMAGIC, GMAGIC, GMAGIC, GMAGIC};
    __builtin_amdgcn_raw_buffer_store_b128(g4, P.rws, lane < 8 * NGATE ? gate_off(lane >> 3, b >> 4, b & 15) + (lane & 7) * 16 : OOB, 0, AUX_SC1);
  }
  asm volatile("" : "+s"(P.wave));
  if (P.wave < 5) {
    const int j4 = min(P.tid, 287) * 4;
    const __amdgpu_buffer_rsrc_t rk = mkrsrc(P.k, S * D * 2), rv = mkrsrc(P.v, S * D * 2);
    if (TILE_DELAY) __builtin_amdgcn_s_sleep(TILE_DELAY);
    __builtin_amdgcn_sched_barrier(0);
#pragma unroll
    for (int i = 0; i < 3; i++) E.kk[i] = __builtin_amdgcn_raw_buffer_load_b32(rk, row0 + (i - 3) * (D * 2) + j4, 0, AUX_NT);
#pragma unroll
    for (int i = 0; i < 8; i++) {
      E.kk[3 + i] = __builtin_amdgcn_raw_buffer_load_b32(rk, j4 + (i & 3) * (D * 2), row0 + (i & 4) * (D * 2), AUX_NT);
      E.vv[i] = __builtin_amdgcn_raw_buffer_load_b32(rv, j4 + (i & 3) * (D * 2), row0 + (i & 4) * (D * 2), AUX_NT);
    }
    E.sk = *(const GAS v4u*)((const GAS char*)P.shk + j4 * 4);
    E.dd = *(const GAS u32*)((const GAS char*)P.dsk + j4);
  }
  const int role = P.is_conv ? 0 : (is_moe ? 1 : 2);
  asm volatile("" : "+s"(P.wave));
  STAMP(10);
  if (P.wave == 7) router_stats(P, E, X, role);
  asm volatile("" : "+s"(P.wave));
  if (P.wave != 7) {
    if (P.wave >= 5) aux_wave(P, E.cx, role);
    else tile_wave(P, E, is_moe);
  } else {
    router_wave(P, E, role);
  }
  FLUSH();
}

}

extern "C" void kernel_launch(void* const* d_in, const int* in_sizes, int n_in, void* d_out, int out_size, void* d_ws,
                              size_t ws_size, hipStream_t stream) {
  (void)in_sizes; (void)n_in; (void)out_size; (void)ws_size;
  (void)hipFuncSetAttribute((const void*)fused_kernel, hipFuncAttributeMaxDynamicSharedMemorySize, L_TOTAL);
  fused_kernel<<<NWG, NTHR, L_TOTAL, stream>>>(
      (const u16*)d_in[0], (const u16*)d_in[1], (const u16*)d_in[2], (const u16*)d_in[3], (const u16*)d_in[4],
      (const u16*)d_in[5], (const u16*)d_in[7], (const u16*)d_in[8], (const u16*)d_in[9], (const u16*)d_in[10],
      (u16*)d_out, (char*)d_ws + (WS_AT_END ? ((ws_size - WS_END) & ~(size_t)0xFFFF) : 0));
}
```

```cpp
#include <hip/hip_runtime.h>
#include <hip/hip_bf16.h>

typedef unsigned int u32;
typedef unsigned long long u64;
typedef unsigned short u16;
typedef u32 v4u __attribute__((ext_vector_type(4)));
typedef u32 v2u __attribute__((ext_vector_type(2)));
typedef float f4 __attribute__((ext_vector_type(4)));
typedef float f2 __attribute__((ext_vector_type(2)));
typedef __bf16 bf8 __attribute__((ext_vector_type(8)));
typedef __bf16 bf2 __attribute__((ext_vector_type(2)));
#define LAS __attribute__((address_space(3)))
#define GAS __attribute__((address_space(1)))

namespace {

constexpr int S = 2048, D = 576, F = 4608;
constexpr int NWG = 256, NTHR = 512, NCONV = 144, NMOE = 72;
constexpr int AUX_SC1 = 16;
constexpr u32 SAME = 0xFFFFFFFFu, MAXV = 0xFFFFFFFEu;
constexpr u32 SPIN_CAP = 1u << 16, GATE_CAP = 96;
constexpr u32 GMAGIC = 0x5AC3E900u;
constexpr int OOB = 0x7FFFFF00;
#define U_POST 2
#define WS_AT_END 0
#define HELPERS 1
#define Y_POST 8
#define OUT_AUX 16
#define W2_GATE 2
#define PF_R0 192
#define STAGED 1
#define H_DELAY 30
#define PF_CE 2
#define PF_WHEN 0
#define W1_DEPTH 0
#define DRAIN_FIRST 1
#define U_DELAY 0
#define PF_FIRST 1
#define SK_POS 1
#define ROUTER_DELAY 0
#define W2_EARLY 0
#define TILE_DELAY 4
#define CODE_PF 40
#define USE_NT 0
constexpr int AUX_NT = USE_NT ? 2 : 0;

constexpr int WS_E = 0;
constexpr int WS_G = 4096;
constexpr int GATE_B = 256 * 256;
constexpr int WS_R = WS_G + 5 * GATE_B;
constexpr int WS_M = WS_R + S * 8;
constexpr int WS_U = WS_M + S * 8;
constexpr int TILE_B = D * 16;
constexpr int WS_P = WS_U + NWG * TILE_B;
constexpr int PART_B = NCONV * 128;
constexpr int WS_END = WS_P + NMOE * PART_B;
constexpr int WS_DBG = 4  __H2__ ;
static_assert(WS_END <= WS_DBG, "ws");
enum { G_R = 0, G_U, G_Y, G_P, G_V, NGATE };
#define ONES8 0x3F803F80u, 0x3F803F80u, 0x3F803F80u, 0x3F803F80u, 0x3F803F80u, 0x3F803F80u, 0x3F803F80u, 0x3F803F80u
#define ONES32 ONES8, ONES8, ONES8, ONES8
__device__ const u32 kOnes[288] __attribute__((aligned(128))) = {ONES32, ONES32, ONES32, ONES32, ONES32, ONES32, ONES32, ONES32, ONES32};

constexpr int HC_B = 2080 * 16;
constexpr int L_H = 0;
constexpr int UL_B = 290 * 16;
constexpr int L_U = L_H + 4 * HC_B;
constexpr int L_STG = L_U + 4 * UL_B;
constexpr int L_MISC = L_STG + TILE_B;
constexpr int L_CNT = L_MISC;
constexpr int L_CAND = L_CNT + 96;
constexpr int L_SEL = L_CAND + 288;
constexpr int L_REC = L_SEL + 32;
constexpr int L_STAT = L_REC + 32;
constexpr int Y1_B = 2048 + 64;
constexpr int L_END = L_STAT + 64;
constexpr int XS_STRIDE = 1184;
constexpr int L_XS = 0;
constexpr int L_RED = L_XS + 8 * XS_STRIDE;
constexpr int L_HB = L_RED + 6 * 8 * 128 * 4;
constexpr int HB_STRIDE = 160;
static_assert(L_END + 1024 <= 160 * 1024, "lds");
static_assert(L_HB + 8 * HB_STRIDE <= L_U, "lds");
static_assert(L_END - L_MISC == 512, "zero fill");

__device__ __forceinline__ constexpr int xrow_off(int t) { return L_U + (t / 3) * UL_B + 512 + (t % 3) * (D * 2); }
enum { C_DRAIN = 0, C_STAT, C_TRUST, C_GU, C_SEL, C_H, C_U, C_Y, C_XS, C_RED, C_HB, C_GY, C_PUB, C_TAG, C_QQ, C_U2, C_Y1, C_W1, C_DN, C_H0, C_VER, C_REDO, C_OK };

__device__ __forceinline__ float lo16(u32 x) { return __uint_as_float(x << 16); }
__device__ __forceinline__ float hi16(u32 x) { return __uint_as_float(x & 0xFFFF0000u); }
__device__ __forceinline__ u32 pk2(float a, float b) {
  bf2 p = {(__bf16)a, (__bf16)b};
  return __builtin_bit_cast(u32, p);
}
__device__ __forceinline__ float rbf(float a) { return lo16(pk2(a, a)); }
__device__ __forceinline__ float dot2(u32 a, u32 b, float c) {
  return __builtin_amdgcn_fdot2_f32_bf16(__builtin_bit_cast(bf2, a), __builtin_bit_cast(bf2, b), c, false);
}
template <int CTRL> __device__ __forceinline__ u32 dppu(u32 x) {
  return (u32)__builtin_amdgcn_mov_dpp((int)x, CTRL, 0xf, 0xf, true);
}
template <int CTRL> __device__ __forceinline__ float dppf(float x) { return __uint_as_float(dppu<CTRL>(__float_as_uint(x))); }
__device__ __forceinline__ float xrow_sum(float x) {
  auto s = __builtin_amdgcn_permlane16_swap(__float_as_uint(x), __float_as_uint(x), false, false);
  x = __uint_as_float(s[0]) + __uint_as_float(s[1]);
  auto t = __builtin_amdgcn_permlane32_swap(__float_as_uint(x), __float_as_uint(x), false, false);
  return __uint_as_float(t[0]) + __uint_as_float(t[1]);
}
__device__ __forceinline__ u32 umax(u32 a, u32 b) { return a > b ? a : b; }
__device__ __forceinline__ u32 umin(u32 a, u32 b) { return a < b ? a : b; }
__device__ __forceinline__ u32 umed3(u32 a, u32 b, u32 c) { return umax(umin(a, b), umin(umax(a, b), c)); }
__device__ __forceinline__ u32 xrow_umin(u32 x) {
  auto s = __builtin_amdgcn_permlane16_swap(x, x, false, false);
  x = umin(s[0], s[1]);
  auto t = __builtin_amdgcn_permlane32_swap(x, x, false, false);
  return umin(t[0], t[1]);
}
__device__ __forceinline__ u32 wave_umax(u32 x) {
  x = umax(x, dppu<0xB1>(x));
  x = umax(x, dppu<0x4E>(x));
  x = umax(x, dppu<0x141>(x));
  x = umax(x, dppu<0x140>(x));
  auto s = __builtin_amdgcn_permlane16_swap(x, x, false, false);
  x = umax(s[0], s[1]);
  auto t = __builtin_amdgcn_permlane32_swap(x, x, false, false);
  return umax(t[0], t[1]);
}
__device__ __forceinline__ void lds_signal(LAS char* sm, int c) {
  u64 keep;
  asm volatile("s_waitcnt lgkmcnt(0)\n\ts_mov_b64 %0, exec\n\ts_mov_b64 exec, 1\n\tds_add_u32 %1, %2\n\ts_mov_b64 exec, %0"
               : "=&s"(keep) : "v"((u32)(size_t)(sm + L_CNT + 4 * c)), "v"(1u) : "memory");
}
template <int SLEEP = 0> __device__ __forceinline__ void lds_wait(LAS char* sm, int c, u32 target) {
  u32 v, sv;
  if (SLEEP == 0)
    asm volatile("1:\n\tds_read_b32 %0, %2\n\ts_waitcnt lgkmcnt(0)\n\tv_readfirstlane_b32 %1, %0\n\ts_cmp_lt_u32 %1, %3\n\ts_cbranch_scc1 1b"
                 : "=&v"(v), "=&s"(sv) : "v"((u32)(size_t)(sm + L_CNT + 4 * c)), "s"(target) : "memory", "scc");
  else
    asm volatile("1:\n\ts_sleep %4\n\tds_read_b32 %0, %2\n\ts_waitcnt lgkmcnt(0)\n\tv_readfirstlane_b32 %1, %0\n\ts_cmp_lt_u32 %1, %3\n\ts_cbranch_scc1 1b"
                 : "=&v"(v), "=&s"(sv) : "v"((u32)(size_t)(sm + L_CNT + 4 * c)), "s"(target), "n"(SLEEP) : "memory", "scc");
}
__device__ __forceinline__ bool lds_arrive(LAS char* sm, int c, u32 last) {
  u64 keep;
  u32 v, sv;
  asm volatile("s_mov_b64 %0, exec\n\ts_mov_b64 exec, 1\n\tds_add_rtn_u32 %1, %3, %4\n\ts_waitcnt lgkmcnt(0)\n\tv_readfirstlane_b32 %2, %1\n\ts_mov_b64 exec, %0"
               : "=&s"(keep), "=&v"(v), "=&s"(sv) : "v"((u32)(size_t)(sm + L_CNT + 4 * c)), "v"(1u) : "memory");
  return sv == last;
}
__device__ __forceinline__ void wg_barrier() { asm volatile("s_waitcnt lgkmcnt(0)\n\ts_barrier" ::: "memory"); }
__device__ __forceinline__ u32 umax3(u32 a, u32 b, u32 c) { return umax(umax(a, b), c); }
__device__ __forceinline__ u32 vmax4(v4u g, u32 acc) { return umax3(umax3(g.x, g.y, acc), g.z, g.w); }
__device__ __forceinline__ f4 mfma16(v4u a, v4u b, f4 c) {
  return __builtin_amdgcn_mfma_f32_16x16x32_bf16(__builtin_bit_cast(bf8, a), __builtin_bit_cast(bf8, b), c, 0, 0, 0);
}
__device__ __forceinline__ __amdgpu_buffer_rsrc_t mkrsrc(const void* p, int bytes) {
  return __builtin_amdgcn_make_buffer_rsrc((void*)p, 0, bytes, 0x00020000);
}

#define STAMP(i) do {} while (0)
#define STAMPC(i) do {} while (0)
#define FLUSH() do {} while (0)
#define NOTE(i, v) do {} while (0)
constexpr int L_TOTAL = L_END;

struct Ctx {
  const u16 *q, *k, *v, *shk, *hk, *dsk, *wr, *wg, *wu, *wd;
  u16* out;
  char* ws;
  const char* code;
  __amdgpu_buffer_rsrc_t rws;
  LAS char* sm;
  int tid, lane, wave, b, C, cg, mw;
  bool is_conv;
  u32 t0;
};

__device__ __forceinline__ int gate_off(int gate, int rep, int shard) { return WS_G + gate * GATE_B + (rep * 16 + shard) * 256; }
__device__ __forceinline__ u32 gate_add(const Ctx& P, int off, int val) {
  return (u32)__builtin_amdgcn_raw_ptr_buffer_atomic_add_i32(val, P.rws, off, 0, 0);
}
__device__ __forceinline__ u32 gate_post(const Ctx& P, int gate, bool on, u32 inc) {
  return gate_add(P, (on && P.lane < 16) ? gate_off(gate, P.lane, P.b & 15) : OOB, (int)inc);
}
__device__ __forceinline__ void gate_confirm(const Ctx& P, int gate, u32 old, u32 inc) {
  bool bad = P.lane < 16;
  _Pragma("nounroll") for (u32 spin = 0; spin < SPIN_CAP; spin++) {
    const u32 d = old - GMAGIC;
    bad = bad && ((d & 0xF0u) != 0u || d != (d & 0xFu) * inc);
    if (__builtin_expect(!__any(bad), 1)) break;
    NOTE(26, spin + 1);
    __builtin_amdgcn_s_sleep(2);
    old = gate_add(P, bad ? gate_off(gate, P.lane, P.b & 15) : OOB, (int)inc);
  }
}
__device__ __forceinline__ void gate_wait(const Ctx& P, int gate, u32 expect, u32 inc, u32 cap = GATE_CAP) {
  const int off = P.lane < 16 ? gate_off(gate, P.b >> 4, P.lane) : OOB;
  const u32 want = P.lane < 16 ? GMAGIC + expect * inc : 0u;
  _Pragma("nounroll") for (u32 spin = 0; spin < cap; spin++)
    if (__all(gate_add(P, off, 0) == want)) break;
}
__device__ __forceinline__ u32 gate_inc(u32 tag) { return 1u + (tag << 8); }
__device__ __forceinline__ void post_u_complete(const Ctx& P, u32 inc) {
  lds_wait<1>(P.sm, C_DRAIN, 5);
  gate_confirm(P, G_V, gate_post(P, G_V, true, inc), inc);
}

struct Row { v2u a0, a1; u32 a2; };
template <int AUX> __device__ __forceinline__ Row load_row(__amdgpu_buffer_rsrc_t r, int soff, int lane) {
  Row x;
  x.a0 = __builtin_amdgcn_raw_buffer_load_b64(r, lane * 8, soff, AUX);
  x.a1 = __builtin_amdgcn_raw_buffer_load_b64(r, lane * 8 + 512, soff, AUX);
  x.a2 = __builtin_amdgcn_raw_buffer_load_b16(r, lane * 2 + 1024, soff, AUX);
  return x;
}
__device__ __forceinline__ void hcopy_store(LAS char* dst, v4u lo, v4u hi, bool first) {
  u32 d[8], o[7];
#pragma unroll
  for (int e = 0; e < 4; e++) { d[e] = lo[e]; d[4 + e] = hi[e]; }
#pragma unroll
  for (int e = 0; e < 7; e++) o[e] = __builtin_amdgcn_alignbit(d[e + 1], d[e], 16);
  constexpr int BASE[8] = {0, 260, 519, 778, 1037, 1296, 1556, 1815};
#pragma unroll
  for (int e = 0; e < 4; e++) {
    v4u a = {d[e], d[e + 1], d[e + 2], d[e + 3]}, c = {o[e], o[e + 1], o[e + 2], o[e + 3]};
    if (e > 0 || first) *(LAS v4u*)(dst + BASE[2 * e] * 16) = a;
    *(LAS v4u*)(dst + BASE[2 * e + 1] * 16) = c;
  }
}
__device__ __forceinline__ u32 touch_w1(const Ctx& P, int mat, int blk);
__device__ __forceinline__ u32 hcopy_first(const Ctx& P, int ch0) {
  const __amdgpu_buffer_rsrc_t rh = mkrsrc(P.hk + (size_t)(4 * P.cg + ch0) * S, 2 * S * 2);
  v4u lo[8];
#pragma unroll
  for (int r = 0; r < 8; r++) lo[r] = __builtin_amdgcn_raw_buffer_load_b128(rh, P.lane * 16 + (r & 3) * 1024, (r >> 2) * 4096, AUX_NT);
  __builtin_amdgcn_sched_barrier(0);
  STAMP(12);
  LAS char* dst = P.sm + L_H + ch0 * HC_B + (P.lane + 2) * 16;
#pragma unroll
  for (int r = 0; r < 8; r++) *(LAS v4u*)(dst + (r >> 2) * HC_B + (r & 3) * 1024) = lo[r];
  lds_signal(P.sm, C_H0);
  u32 t = 0, t2 = 0;
  if (HELPERS != 0 && PF_WHEN == 0 && PF_CE >= 1) t = touch_w1(P, ch0 >> 1, 25 + (P.b >> 3));
  if (HELPERS != 0 && PF_WHEN == 0 && PF_CE >= 2) t2 = touch_w1(P, ch0 >> 1, 43 + (P.b >> 3));
  __builtin_amdgcn_sched_barrier(0);
  STAMP(1);
  {
    const int c = P.lane & 1, m1 = (P.lane >> 1) & 1;
    const v4u z = {0u, 0u, 0u, 0u};
    v4u h0 = *(const LAS v4u*)(P.sm + L_H + (ch0 + c) * HC_B + 32);
    h0 = m1 ? h0 : z;
    if (P.lane < 4) hcopy_store(P.sm + L_H + (ch0 + c) * HC_B + m1 * 16, z, h0, true);
  }
  return t | t2;
}
template <int NR> __device__ __forceinline__ void hcopy_rest(const Ctx& P, int r0) {
  v4u lo[NR], hi[NR];
  LAS char* dst[NR];
#pragma unroll
  for (int r = 0; r < NR; r++) {
    dst[r] = P.sm + L_H + ((r0 + r) >> 2) * HC_B + ((r0 + r) & 3) * 1024 + (P.lane + 2) * 16;
    lo[r] = *(const LAS v4u*)dst[r];
    hi[r] = *(const LAS v4u*)(dst[r] + 16);
  }
#pragma unroll
  for (int r = 0; r < NR; r++) hcopy_store(dst[r], lo[r], hi[r], false);
  lds_signal(P.sm, C_H);
}

template <class FN> __device__ __forceinline__ void ffn_gate_up(const Ctx& P, FN&& between) {
  LAS char* sm = P.sm;
  const int lane = P.lane, wave = P.wave, n16 = lane & 15, g4 = lane >> 4;
  v4u W1[3][8];
  const GAS char* wp = (const GAS char*)(n16 < 8 ? P.wg : P.wu) + ((size_t)(96 * wave + 8 * g4) * F + 64 * P.mw + 8 * (n16 & 7)) * 2;
  if (wave >= PF_FIRST) lds_wait<1>(sm, wave < 2 ? C_PUB : C_W1, wave < 2 ? 1 : 2);
  __builtin_amdgcn_sched_barrier(0);
#pragma unroll
  for (int i = 0; i < 3; i++)
#pragma unroll
    for (int jj = 0; jj < 8; jj++) {
      const GAS v4u* a = (const GAS v4u*)(wp + (size_t)(32 * i + jj) * (F * 2));
      W1[i][jj] = USE_NT ? __builtin_nontemporal_load(a) : *a;
    }
  __builtin_amdgcn_sched_barrier(0);
  lds_signal(sm, C_H0);
  STAMP(3);
  v4u Bf[3][8];
#pragma unroll
  for (int i = 0; i < 3; i++)
#pragma unroll
    for (int e = 0; e < 8; e++) {
#pragma unroll
      for (int d = 0; d < 4; d++)
        Bf[i][e][d] = __builtin_amdgcn_perm(W1[i][2 * d + 1][e >> 1], W1[i][2 * d][e >> 1], (e & 1) ? 0x07060302u : 0x05040100u);
      asm volatile("" : "+v"(Bf[i][e]));
    }
  STAMP(17);
  lds_wait<1>(sm, C_XS, 2);
  STAMP(5);
  f4 acc[8];
#pragma unroll
  for (int e = 0; e < 8; e++) acc[e] = (f4){0.f, 0.f, 0.f, 0.f};
#pragma unroll
  for (int i = 0; i < 3; i++) {
    const v4u A = *(const LAS v4u*)(sm + L_XS + (n16 & 7) * XS_STRIDE + (96 * wave + 32 * i + 8 * g4) * 2);
#pragma unroll
    for (int e = 0; e < 8; e++) acc[e] = mfma16(A, Bf[i][e], acc[e]);
  }
  {
    LAS char* dst = sm + L_RED + ((wave * 8 + 4 * (g4 & 1) + (g4 & 2)) * 128 + 8 * n16) * 4;
#pragma unroll
    for (int r = 0; r < 2; r++) {
      f4 a, c;
#pragma unroll
      for (int e = 0; e < 4; e++) { a[e] = g4 >= 2 ? acc[e][2 + r] : acc[e][r]; c[e] = g4 >= 2 ? acc[4 + e][2 + r] : acc[4 + e][r]; }
      *(LAS f4*)(dst + r * 512) = a;
      *(LAS f4*)(dst + r * 512 + 16) = c;
    }
  }
  lds_signal(sm, C_RED);
  STAMP(18);
  between();
  if (wave >= 2) {
    const int tk = 2 * (wave - 2) + (lane >> 5);
    const LAS char* src = sm + L_RED + (tk * 128 + 2 * (lane & 31)) * 4;
    lds_wait(sm, C_RED, 6);
    f2 G = {0.f, 0.f}, U = {0.f, 0.f};
#pragma unroll
    for (int w = 0; w < 6; w++) {
      G += *(const LAS f2*)(src + w * 4096);
      U += *(const LAS f2*)(src + w * 4096 + 256);
    }
    const u32 gb = pk2(G.x, G.y), ub = pk2(U.x, U.y);
    const float g0 = lo16(gb), g1 = hi16(gb);
    const u32 sg = pk2(g0 * __builtin_amdgcn_rcpf(1.0f + __expf(-g0)), g1 * __builtin_amdgcn_rcpf(1.0f + __expf(-g1)));
    *(LAS u32*)(sm + L_HB + tk * HB_STRIDE + (lane & 31) * 4) = pk2(lo16(sg) * lo16(ub), hi16(sg) * hi16(ub));
    lds_signal(sm, C_HB);
    STAMP(15);
  }
}

struct Down {
  v4u W2[2][8];
  __device__ __forceinline__ void load(const Ctx& P, int cu) {
    const int n16 = P.lane & 15, g4 = P.lane >> 4;
    const __amdgpu_buffer_rsrc_t rd = mkrsrc(P.wd + (size_t)64 * P.mw * D, 64 * D * 2);
    const int o2 = (cu == 4 && n16 >= 8) ? OOB : (8 * g4 * D + 128 * cu + 8 * n16) * 2;
    lds_wait<1>(P.sm, C_H0, 6);
    __builtin_amdgcn_sched_barrier(0);
#pragma unroll
    for (int ks = 0; ks < 2; ks++)
#pragma unroll
      for (int jj = 0; jj < 8; jj++)
        W2[ks][jj] = __builtin_amdgcn_raw_buffer_load_b128(rd, o2 + (jj & 3) * (D * 2), (32 * ks + (jj & 4)) * (D * 2), AUX_NT);
    __builtin_amdgcn_sched_barrier(0);
    STAMP(4);
  }
  __device__ __forceinline__ void run(const Ctx& P, int cu) {
    LAS char* sm = P.sm;
    const int n16 = P.lane & 15, g4 = P.lane >> 4;
    const u32 tag = *(const LAS u32*)(sm + L_REC), tag12 = tag & 0xFFFu;
    __builtin_amdgcn_sched_barrier(0);
    v4u Bd[2][8];
#pragma unroll
    for (int ks = 0; ks < 2; ks++)
#pragma unroll
      for (int e = 0; e < 8; e++) {
#pragma unroll
        for (int d = 0; d < 4; d++)
          Bd[ks][e][d] = __builtin_amdgcn_perm(W2[ks][2 * d + 1][e >> 1], W2[ks][2 * d][e >> 1], (e & 1) ? 0x07060302u : 0x05040100u);
        asm volatile("" : "+v"(Bd[ks][e]));
      }
    const int off = WS_P + P.mw * PART_B + (32 * cu + 2 * (n16 & 7)) * 128 + (n16 >> 3) * 64 + g4 * 16;
    STAMP(19);
    lds_wait(sm, C_HB, 4);
    const v4u A0 = *(const LAS v4u*)(sm + L_HB + (n16 & 7) * HB_STRIDE + g4 * 16);
    const v4u A1 = *(const LAS v4u*)(sm + L_HB + (n16 & 7) * HB_STRIDE + 64 + g4 * 16);
    if (lds_arrive(sm, C_DN, 4)) (void)gate_post(P, G_P, true, gate_inc(tag));
    STAMP(20);
    f4 ad[8];
#pragma unroll
    for (int e = 0; e < 8; e++) ad[e] = mfma16(A0, Bd[0][e], (f4){0.f, 0.f, 0.f, 0.f});
#pragma unroll
    for (int e = 0; e < 8; e++) ad[e] = mfma16(A1, Bd[1][e], ad[e]);
#pragma unroll
    for (int hh = 0; hh < 2; hh++) {
      v4u s0, s1;
#pragma unroll
      for (int e = 0; e < 4; e++) {
        const f4 a = ad[4 * hh + e];
        const u32 x = ((__float_as_uint(g4 >= 2 ? a[2] : a[0]) + 0x800u) & 0xFFFFF000u) | tag12;
        const u32 y = ((__float_as_uint(g4 >= 2 ? a[3] : a[1]) + 0x800u) & 0xFFFFF000u) | tag12;
        s0[e] = (u32)__builtin_amdgcn_update_dpp((int)x, (int)y, 0x128, 0xf, 0xc, false);
        s1[e] = (u32)__builtin_amdgcn_update_dpp((int)y, (int)x, 0x128, 0xf, 0x3, false);
      }
      __builtin_amdgcn_raw_buffer_store_b128(s0, P.rws, off + hh * 128, 0, AUX_SC1);
      if (cu != 4) __builtin_amdgcn_raw_buffer_store_b128(s1, P.rws, off + hh * 128 + 2048, 0, AUX_SC1);
    }
    STAMP(21);
  }
};

__device__ __forceinline__ u32 touch_w1(const Ctx& P, int mat, int blk) {
  const int r7 = (P.lane * 57) >> 9, c9 = P.lane - 9 * r7;
  return __builtin_amdgcn_raw_buffer_load_b32(mkrsrc(__builtin_amdgcn_readfirstlane(mat) ? P.wu : P.wg, D * F * 2), P.lane < 63 ? r7 * (F * 2) + c9 * 128 : OOB,
                                              (PF_R0 + 7 * blk) * (F * 2) + (P.b & 7) * 1152, AUX_NT);
}
__device__ __forceinline__ u32 touch_w2(const Ctx& P, int unit) {
  return __builtin_amdgcn_raw_buffer_load_b32(mkrsrc(P.wd, D * F * 2), unit < 81 ? P.lane * 128 : OOB,
                                              (P.b & 7) * (9 * 64 * D * 2) + unit * 8192, AUX_NT);
}

__device__ __forceinline__ void publish_y(const Ctx& P, const LAS char* yb, int ystride, int tile0, int t, int step, int n) {
  const int gch = t & 3;
  const int goff = WS_U + (tile0 + (t >> 2)) * TILE_B + (4 * P.cg + gch) * 16;
#pragma unroll
  for (int it = 0; it < n; it++) {
    const v4u y4 = *(const LAS v4u*)(yb + gch * ystride + (it * step + (t >> 2)) * 16);
    const v4u u4 = *(const LAS v4u*)(P.sm + L_U + gch * UL_B + (32 + tile0 + it * step + (t >> 2)) * 16);
    v4u w4;
#pragma unroll
    for (int e = 0; e < 4; e++) w4[e] = (y4[e] == u4[e]) ? SAME : y4[e];
    __builtin_amdgcn_raw_buffer_store_b128(w4, P.rws, goff, it * step * TILE_B, AUX_SC1);
  }
}

__device__ __forceinline__ void gather_half(const Ctx& P, int tile0, v4u (&g)[8]) {
  const int goff = WS_U + (tile0 + (P.lane >> 2)) * TILE_B + (4 * P.cg + (P.lane & 3)) * 16;
#pragma unroll
  for (int it = 0; it < 8; it++) g[it] = __builtin_amdgcn_raw_buffer_load_b128(P.rws, goff, it * 16 * TILE_B, AUX_SC1);
}

__device__ __forceinline__ void verify_half(const Ctx& P, int tile0) {
  LAS char* sm = P.sm;
  lds_wait<1>(sm, C_TRUST, 1);
  v4u g[8];
  gather_half(P, tile0, g);
  LAS char* ud = sm + L_U + (P.lane & 3) * UL_B + (32 + tile0 + (P.lane >> 2)) * 16;
  u32 df = 0;
#pragma unroll
  for (int it = 0; it < 8; it++) {
    const v4u x = *(const LAS v4u*)(ud + it * 256) ^ g[it];
    df |= x.x | x.y;
    df |= x.z | x.w;
  }
  if (__builtin_expect(__any(df != 0), 0)) {
#pragma unroll
    for (int it = 0; it < 8; it++) *(LAS v4u*)(ud + it * 256) = g[it];
    lds_signal(sm, C_REDO);
  }
  lds_signal(sm, C_OK);
}

__device__ __forceinline__ void conv_phase(const Ctx& P, u32 inc) {
  LAS char* sm = P.sm;
  const int tid = P.tid, lane = P.lane, wave = P.wave, n16 = lane & 15, g4 = lane >> 4, gch = tid & 3;
  const int goff = WS_U + (tid >> 2) * TILE_B + (4 * P.cg + gch) * 16;
  LAS char* ud = sm + L_U + gch * UL_B + (32 + (tid >> 2)) * 16;
  const int p1 = n16 + 1, kap = p1 & 7;
  const int base16 = 258 * kap + ((0x98654320u >> (4 * kap)) & 15);
  const LAS char* pa = sm + L_H + wave * HC_B + (base16 + g4 + (p1 >> 3)) * 16;
  const LAS char* pb = sm + L_U + wave * UL_B + (32 + 2 * n16 + 1 - g4 - 28) * 16;
  lds_wait<1>(sm, C_H0, 2);
  hcopy_rest<3>(P, 3 * wave);
  STAMP(17);
  asm volatile("s_waitcnt vmcnt(0)" ::: "memory");
  lds_signal(sm, C_DRAIN);
  lds_wait<1>(sm, C_QQ, 5);
  lds_wait<1>(sm, C_GU, 1);
  __builtin_amdgcn_s_setprio(2);
  STAMP(3);
  if (U_DELAY) __builtin_amdgcn_s_sleep(U_DELAY);
  STAMP(4);
  v4u g[2];
#pragma unroll
  for (int it = 0; it < 2; it++) g[it] = __builtin_amdgcn_raw_buffer_load_b128(P.rws, goff, it * 64 * TILE_B, AUX_SC1);
  *(LAS v4u*)ud = g[0];
  *(LAS v4u*)(ud + 1024) = g[1];
  lds_signal(sm, C_U);
  STAMP(5);
  lds_wait(sm, C_H, 6);
  lds_wait(sm, C_U, 4);
  STAMP(6);
  f4 acc[8];
  v4u A[2][8], B[2][4];
  _Pragma("nounroll") for (int pass = 0; pass < 2; pass++) {
#pragma unroll
  for (int t = 0; t < 8; t++) acc[t] = (f4){0.f, 0.f, 0.f, 0.f};
#pragma unroll
  for (int a = 0; a < 8; a++) {
    A[0][a] = *(const LAS v4u*)(pa + a * 512);
    if (a < 4) B[0][a] = *(const LAS v4u*)(pb + a * 512 + 7 * 64);
  }
#pragma unroll
  for (int bb = 0; bb < 8; bb++) {
    __builtin_amdgcn_sched_barrier(0);
    if (bb == 7) {
      lds_wait(sm, C_U2, 1);
      __builtin_amdgcn_sched_barrier(0);
    }
#pragma unroll
    for (int a = 0; a < 8; a++) {
      if (bb < 7) {
        A[(bb + 1) & 1][a] = *(const LAS v4u*)(pa + a * 512 + (bb + 1) * 64);
        if (a < 4) B[(bb + 1) & 1][a] = *(const LAS v4u*)(pb + a * 512 + (6 - bb) * 64);
      } else if (a < 4) {
        A[0][a] = *(const LAS v4u*)(pa + a * 512);
        B[0][a] = *(const LAS v4u*)(pb + (4 + a) * 512 + 7 * 64);
      }
    }
#pragma unroll
    for (int t = 0; t < 8; t++)
#pragma unroll
      for (int c = 0; c < 4; c++)
        if (c <= t) acc[t] = mfma16(A[bb & 1][t - c], B[bb & 1][c], acc[t]);
#pragma unroll
    for (int i = 0; i < (bb < 7 ? 12 : 8); i++) {
      __builtin_amdgcn_sched_group_barrier(0x8, 2, 0);
      __builtin_amdgcn_sched_group_barrier(0x100, 1, 0);
    }
    if (bb < 7) __builtin_amdgcn_sched_group_barrier(0x8, 2, 0);
    else __builtin_amdgcn_sched_group_barrier(0x8, 10, 0);
  }
#pragma unroll
  for (int bb = 0; bb < 8; bb++) {
    __builtin_amdgcn_sched_barrier(0);
    if (bb == 1) {
      LAS char* yb = sm + L_STG + wave * Y1_B + n16 * 32 + g4 * 8;
#pragma unroll
      for (int t = 0; t < 4; t++) {
        v2u y2 = {umin(pk2(acc[t][0], acc[t][1]), MAXV), umin(pk2(acc[t][2], acc[t][3]), MAXV)};
        *(LAS v2u*)(yb + t * 512) = y2;
      }
    }
    if (bb == 2) lds_signal(sm, C_Y1);
    if (bb == Y_POST) {
      (void)gate_post(P, G_Y, wave == 0 && pass == 0, inc);
      __builtin_amdgcn_sched_barrier(0);
    }
    if (bb < 7) {
#pragma unroll
      for (int a = 0; a < 4; a++) {
        A[(bb + 1) & 1][a] = *(const LAS v4u*)(pa + a * 512 + (bb + 1) * 64);
        B[(bb + 1) & 1][a] = *(const LAS v4u*)(pb + (4 + a) * 512 + (6 - bb) * 64);
      }
    }
#pragma unroll
    for (int t = 0; t < 4; t++)
#pragma unroll
      for (int c = 0; c <= t; c++) acc[4 + t] = mfma16(A[bb & 1][t - c], B[bb & 1][c], acc[4 + t]);
    if (bb < 7) {
#pragma unroll
      for (int i = 0; i < 8; i++) {
        __builtin_amdgcn_sched_group_barrier(0x8, 1, 0);
        __builtin_amdgcn_sched_group_barrier(0x100, 1, 0);
      }
      __builtin_amdgcn_sched_group_barrier(0x8, 2, 0);
    }
  }
  __builtin_amdgcn_sched_barrier(0);
    lds_wait(sm, C_OK, 2);
    if (__builtin_expect(__builtin_amdgcn_readfirstlane(((const LAS u32*)(sm + L_CNT))[C_REDO]) == 0u, 1)) break;
    NOTE(26, 7);
  }
  if (Y_POST == 8 && wave == 0) (void)gate_post(P, G_Y, true, inc);
  {
    LAS char* yb = sm + L_H + wave * (HC_B + 64) + n16 * 32 + g4 * 8;
#pragma unroll
    for (int t = 0; t < 4; t++) {
      v2u y2 = {umin(pk2(acc[4 + t][0], acc[4 + t][1]), MAXV), umin(pk2(acc[4 + t][2], acc[4 + t][3]), MAXV)};
      *(LAS v2u*)(yb + t * 512) = y2;
    }
  }
  lds_signal(sm, C_Y);
  STAMP(7);
  lds_wait(sm, C_Y, 4);
  publish_y(P, sm + L_H, HC_B + 64, 128, tid, 64, 2);
  STAMP(8);
}

struct EarlyX { v4u xa[9], wb[9]; };
struct Early {
  u32 kk[11], vv[8], dd;
  v4u sk;
  u64 epoch;
  u32 cx;
};

__device__ __forceinline__ void tile_wave(const Ctx& P, const Early& E, const bool MOE) {
  LAS char* sm = P.sm;
  const int tid = P.tid, lane = P.lane, wave = P.wave, C = P.C;
  const bool act = tid < 288;
  const int j4 = min(tid, 287) * 4;
  const int row0 = 8 * C * (D * 2);
  const u32 (&kk)[11] = E.kk;
  const u32 (&vv)[8] = E.vv;
  const v4u sk = E.sk;
  u32 qq[8];
  u32 gold, uinc;
  bool poster;
  u32 up[2][4];
  {
    f2 w[4], kf[11], ks[8];
    w[0] = (f2){lo16(sk.x), lo16(sk.z)}; w[1] = (f2){hi16(sk.x), hi16(sk.z)};
    w[2] = (f2){lo16(sk.y), lo16(sk.w)}; w[3] = (f2){hi16(sk.y), hi16(sk.w)};
#pragma unroll
    for (int i = 0; i < 11; i++) kf[i] = (f2){lo16(kk[i]), hi16(kk[i])};
#pragma unroll
    for (int t = 0; t < 8; t++) {
      ks[t] = __builtin_elementwise_fma(kf[t + 3], w[0], __builtin_elementwise_fma(kf[t + 2], w[1], __builtin_elementwise_fma(kf[t + 1], w[2], kf[t] * w[3])));
      asm volatile("" : "+v"(ks[t]));
    }
    __builtin_amdgcn_sched_barrier(0);
    lds_wait(sm, C_TAG, 1);
    uinc = gate_inc(*(const LAS u32*)(sm + L_REC));
    STAMP(12);
#pragma unroll
    for (int t = 0; t < 8; t++) ks[t] *= (f2){lo16(vv[t]), hi16(vv[t])};
#pragma unroll
    for (int i = 0; i < 4; i++) {
      up[0][i] = umin(pk2(ks[7 - 2 * i].x, ks[6 - 2 * i].x), MAXV);
      up[1][i] = umin(pk2(ks[7 - 2 * i].y, ks[6 - 2 * i].y), MAXV);
    }
    STAMP(1);
    LAS char* stg = sm + L_STG + wave * 2048;
    v4u p0 = {up[0][0], up[0][1], up[0][2], up[0][3]}, p1 = {up[1][0], up[1][1], up[1][2], up[1][3]};
    if (act) { *(LAS v4u*)(stg + lane * 32) = p0; *(LAS v4u*)(stg + lane * 32 + 16) = p1; }
    asm volatile("" ::: "memory");
    p0 = *(const LAS v4u*)(stg + lane * 16);
    p1 = *(const LAS v4u*)(stg + (wave < 4 ? 1024 : 0) + lane * 16);
    STAMP(13);
    const int so = WS_U + C * TILE_B + wave * 2048 + lane * 16;
    __builtin_amdgcn_raw_buffer_store_b128(p0, P.rws, so, 0, AUX_SC1);
    if (wave < 4) __builtin_amdgcn_raw_buffer_store_b128(p1, P.rws, so + 1024, 0, AUX_SC1);
  }
  __builtin_amdgcn_sched_barrier(0);
  poster = lds_arrive(sm, C_VER, 4);
  if (poster) gold = gate_post(P, G_U, true, uinc);
  STAMP(2);
  lds_wait(sm, C_STAT, 1);
#pragma unroll
  for (int i = 0; i < 8; i++) qq[i] = *(const LAS u32*)(sm + xrow_off(i) + j4);
  lds_signal(sm, C_QQ);
  u32 dd = E.dd;
  asm volatile("" : "+v"(dd));
  float xn[2][8], c0[2][8];
  auto prepare = [&]()
  {
    const float d0 = lo16(dd), d1 = hi16(dd);
#pragma unroll
    for (int s2 = 0; s2 < 4; s2++) {
      const f4 st = *(const LAS f4*)(sm + L_STAT + s2 * 16);
#pragma unroll
      for (int e = 0; e < 2; e++) {
        const int s = 2 * s2 + e;
        const float xa = lo16(qq[s]), xb = hi16(qq[s]);
        const float na = fmaf(xa, st[2 * e], st[2 * e + 1]), nb = fmaf(xb, st[2 * e], st[2 * e + 1]);
        const u32 pa = up[0][(7 - s) >> 1], pb = up[1][(7 - s) >> 1];
        const float ua = ((7 - s) & 1) ? hi16(pa) : lo16(pa), ub = ((7 - s) & 1) ? hi16(pb) : lo16(pb);
        xn[0][s] = na; xn[1][s] = nb;
        c0[0][s] = fmaf(na, fmaf(ua, d0, 1.0f), xa);
        c0[1][s] = fmaf(nb, fmaf(ub, d1, 1.0f), xb);
        asm volatile("" : "+v"(xn[0][s]), "+v"(xn[1][s]), "+v"(c0[0][s]), "+v"(c0[1][s]));
      }
    }
  };
  prepare();
  if (poster) gate_confirm(P, G_U, gold, uinc);
  if (!(P.is_conv && wave < 4)) {
    asm volatile("s_waitcnt vmcnt(0)" ::: "memory");
    lds_signal(sm, C_DRAIN);
  }
  STAMP(14);
  if (MOE) {
    ffn_gate_up(P, []() {});
    if (wave < 2) { Down dn; dn.load(P, wave); dn.run(P, wave); }
  } else {
   if (P.is_conv) {
    if (wave < 4) {
      conv_phase(P, uinc);
    } else {
      lds_wait<1>(sm, C_QQ, 5);
      lds_wait<1>(sm, C_GU, 1);
      __builtin_amdgcn_s_sleep(3 + U_DELAY);
      v4u g[8];
      gather_half(P, 128, g);
#pragma unroll
      for (int it = 0; it < 8; it++) *(LAS v4u*)(sm + L_U + (lane & 3) * UL_B + (32 + 128 + it * 16 + (lane >> 2)) * 16) = g[it];
      lds_signal(sm, C_U2);
      STAMP(5);
      gate_wait(P, G_V, 16u, uinc, SPIN_CAP);
      lds_signal(sm, C_TRUST);
      STAMP(4);
      verify_half(P, 128);
      STAMP(6);
      lds_wait<2>(sm, C_Y1, 4);
      lds_wait<1>(sm, C_OK, 2);
      lds_wait<1>(sm, C_Y1, __builtin_amdgcn_readfirstlane(((const LAS u32*)(sm + L_CNT))[C_REDO]) ? 8 : 4);
      publish_y(P, sm + L_STG, Y1_B, 0, lane, 32, 4);
    }
  } else {
    STAMP(5);
   }
  }
  lds_wait<1>(sm, C_SEL, 1);
  const u32 skip = __builtin_amdgcn_readfirstlane(*(const LAS u32*)(sm + L_REC + 4));
  const __amdgpu_buffer_rsrc_t ro = mkrsrc(P.out, S * D * 2);
  const int oo = act ? j4 : OOB;
  const int uoff = WS_U + C * TILE_B + j4 * 8;
  lds_wait<1>(sm, C_GY, 1);
  STAMP(9);
  v4u y0, y1;
  _Pragma("nounroll") for (u32 spin = 0; spin < SPIN_CAP; spin++) {
    asm volatile("" ::: "memory");
    y0 = __builtin_amdgcn_raw_buffer_load_b128(P.rws, uoff, 0, AUX_SC1);
    y1 = __builtin_amdgcn_raw_buffer_load_b128(P.rws, uoff + 16, 0, AUX_SC1);
    bool ok = true;
#pragma unroll
    for (int e = 0; e < 4; e++) ok = ok & (y0[e] != up[0][e]) & (y1[e] != up[1][e]);
    if (__builtin_expect(__all(ok), 1)) break;
    NOTE(25, spin + 1);
    __builtin_amdgcn_s_sleep(4);
  }
  STAMP(16);
#pragma unroll
  for (int e = 0; e < 4; e++) {
    const u32 a = y0[e] == SAME ? up[0][e] : y0[e], c = y1[e] == SAME ? up[1][e] : y1[e];
    const u32 o0 = pk2(fmaf(xn[0][2 * e], lo16(a), c0[0][2 * e]), fmaf(xn[1][2 * e], lo16(c), c0[1][2 * e]));
    const u32 o1 = pk2(fmaf(xn[0][2 * e + 1], hi16(a), c0[0][2 * e + 1]), fmaf(xn[1][2 * e + 1], hi16(c), c0[1][2 * e + 1]));
    const int so = row0 + (e >> 1) * 4 * (D * 2);
    if (!((skip >> (2 * e)) & 1)) __builtin_amdgcn_raw_buffer_store_b32(o0, ro, oo + ((2 * e) & 3) * (D * 2), so, OUT_AUX);
    if (!((skip >> (2 * e + 1)) & 1)) __builtin_amdgcn_raw_buffer_store_b32(o1, ro, oo + ((2 * e + 1) & 3) * (D * 2), so, OUT_AUX);
  }
}

__device__ __forceinline__ void route(const Ctx& P, u32 tag, u64 epoch, const int ROLE) {
  LAS char* sm = P.sm;
  const int lane = P.lane, wave = P.wave;
  (void)wave;
  v4u g[16];
  if (ROLE == 2) __builtin_amdgcn_s_setprio(0);
  if (ROLE == 0) {
    if (HELPERS != 0 && PF_WHEN != 0 && PF_CE != 0) {
      lds_wait<1>(sm, C_VER, PF_WHEN);
      u32 t[4] = {0u, 0u, 0u, 0u};
#pragma unroll
      for (int i = 0; i < 2 * PF_CE; i++) t[i] = touch_w1(P, i & 1, 25 + 18 * (i >> 1) + (P.b >> 3));
      asm volatile("" :: "v"(t[0]), "v"(t[1]), "v"(t[2]), "v"(t[3]));
      STAMP(18);
    }
    gate_wait(P, G_U, 16u, gate_inc(tag));
    lds_signal(sm, C_GU);
    STAMP(15);
    __builtin_amdgcn_s_setprio(0);
    post_u_complete(P, gate_inc(tag));
    STAMP(13);
    lds_wait<1>(sm, C_U, 4);
  } else {
    gate_wait(P, ROLE == 1 ? G_R : G_U, 16u, gate_inc(tag));
  }
  STAMP(3);
  _Pragma("nounroll") for (u32 spin = 0; spin < SPIN_CAP; spin++) {
    asm volatile("" ::: "memory");
#pragma unroll
    for (int i = 0; i < 16; i++)
      g[i] = __builtin_amdgcn_raw_buffer_load_b128(P.rws, WS_R + lane * 16 + (i & 3) * 1024, (i >> 2) * 4096, AUX_SC1);
    if (spin == 0) lds_signal(sm, C_PUB);
    u32 bad = 0;
#pragma unroll
    for (int i = 0; i < 16; i++) {
      bad = __builtin_amdgcn_bitop3_b32(bad, g[i].y, tag, 0xF6);
      bad = __builtin_amdgcn_bitop3_b32(bad, g[i].w, tag, 0xF6);
    }
    if (__builtin_expect(__all(bad == 0), 1)) break;
    NOTE(24, spin + 1);
    __builtin_amdgcn_s_sleep(4);
  }
  STAMP(4);
  if (P.b == 0 && lane == 0) __hip_atomic_store((GAS u64*)(P.ws + WS_E), epoch, __ATOMIC_RELAXED, __HIP_MEMORY_SCOPE_AGENT);
  u32 k1 = umax(g[0].x, g[0].z), k2 = umin(g[0].x, g[0].z), k3 = 0;
#pragma unroll
  for (int i = 2; i < 32; i++) {
    const u32 x = (i & 1) ? g[i >> 1].z : g[i >> 1].x;
    k3 = umed3(k2, k3, x);
    k2 = umed3(k1, k2, x);
    k1 = umax(k1, x);
  }
  u32 tau = umax(k1, dppu<0xB1>(k1));
  tau = umax(tau, dppu<0x4E>(tau));
  tau = umax(tau, dppu<0x141>(tau));
  tau = umin(tau, dppu<0x140>(tau));
  tau = xrow_umin(tau);
  LAS u32* cand = (LAS u32*)(sm + L_CAND);
  const u64 m1 = __ballot(k1 >= tau), m2 = __ballot(k2 >= tau), m3 = __ballot(k3 >= tau);
  const u32 n1 = __builtin_popcountll(m1), n2 = n1 + __builtin_popcountll(m2), n = n2 + __builtin_popcountll(m3);
  bool slow = n > 64;
  if (__builtin_expect(!slow, 1)) {
    const u32 p1 = __builtin_amdgcn_mbcnt_hi((u32)(m1 >> 32), __builtin_amdgcn_mbcnt_lo((u32)m1, 0u));
    const u32 p2 = __builtin_amdgcn_mbcnt_hi((u32)(m2 >> 32), __builtin_amdgcn_mbcnt_lo((u32)m2, n1));
    const u32 p3 = __builtin_amdgcn_mbcnt_hi((u32)(m3 >> 32), __builtin_amdgcn_mbcnt_lo((u32)m3, n2));
    cand[k1 >= tau ? p1 : 64] = k1;
    cand[k2 >= tau ? p2 : 64] = k2;
    cand[k3 >= tau ? p3 : 64] = k3;
    asm volatile("" ::: "memory");
    const u32 mine = cand[lane];
    u32 rank = 0;
    _Pragma("nounroll") for (u32 jj = 0; jj < n; jj += 8) {
      const v4u c4 = *(const LAS v4u*)(cand + jj), d4 = *(const LAS v4u*)(cand + jj + 4);
      rank += (c4.x > mine) + (c4.y > mine) + (c4.z > mine) + (c4.w > mine);
      rank += (d4.x > mine) + (d4.y > mine) + (d4.z > mine) + (d4.w > mine);
    }
    const bool in = (u32)lane < n && rank < 8;
    if (in) ((LAS u32*)(sm + L_SEL))[rank] = mine;
    slow = __any(in && (u32)lane >= n2);
  }
  if (__builtin_expect(slow, 0)) {
    _Pragma("nounroll") for (int r = 0; r < 8; r++) {
      u32 mx = 0;
#pragma unroll
      for (int i = 0; i < 16; i++) mx = umax3(mx, g[i].x, g[i].z);
      mx = wave_umax(mx);
#pragma unroll
      for (int i = 0; i < 16; i++) { g[i].x = g[i].x == mx ? 0u : g[i].x; g[i].z = g[i].z == mx ? 0u : g[i].z; }
      if (lane == 0) ((LAS u32*)(sm + L_SEL))[r] = mx;
    }
  }
  asm volatile("" ::: "memory");
  {
    const u32 tk = 2047u - (((LAS u32*)(sm + L_SEL))[lane & 7] & 2047u);
    u32 mk = (int)(tk >> 3) == P.C ? 1u << (tk & 7) : 0u;
    mk |= dppu<0xB1>(mk);
    mk |= dppu<0x4E>(mk);
    mk |= dppu<0x141>(mk);
    if (lane == 0) *(LAS u32*)(sm + L_REC + 4) = mk;
  }
  lds_signal(sm, C_SEL);
  STAMP(5);
}

__device__ __forceinline__ u32 load_msd(const Ctx& P, int tok, u32 tag) {
  v2u g;
  _Pragma("nounroll") for (u32 spin = 0; spin < SPIN_CAP; spin++) {
    asm volatile("" ::: "memory");
    g = __builtin_amdgcn_raw_buffer_load_b64(P.rws, WS_M + tok * 8, 0, AUX_SC1);
    if (__builtin_expect(__all(g.y == tag), 1)) break;
    __builtin_amdgcn_s_sleep(4);
  }
  return g.x;
}

__device__ __forceinline__ void routed_rows(const Ctx& P) {
  LAS char* sm = P.sm;
  const int lane = P.lane, wave = P.wave, C = P.C, cg = P.cg;
  (void)wave; (void)C; (void)cg;
  const __amdgpu_buffer_rsrc_t rq = mkrsrc(P.q, S * D * 2);
  {
    const int cw = wave - 6;
    const u32 tag = *(const LAS u32*)(sm + L_REC);
    const v4u s4 = *(const LAS v4u*)(sm + L_SEL + cw * 16);
    Row rr[4];
#pragma unroll
    for (int e = 0; e < 4; e++) {
      const int tok = 2047 - (int)(__builtin_amdgcn_readfirstlane(s4[e]) & 2047u);
      rr[e] = load_row<0>(rq, tok * (D * 2), lane);
    }
    const u32 ks = (lane & 2) ? ((lane & 1) ? s4.w : s4.z) : ((lane & 1) ? s4.y : s4.x);
    const int mtok = 2047 - (int)(ks & 2047u);
    v2u mg = __builtin_amdgcn_raw_buffer_load_b64(P.rws, WS_M + mtok * 8, 0, AUX_SC1);
    __builtin_amdgcn_sched_barrier(0);
    lds_signal(sm, C_W1);
    __builtin_amdgcn_sched_barrier(0);
    u32 msd = mg.x;
    if (__builtin_expect(!__all(mg.y == tag), 0)) msd = load_msd(P, mtok, tag);
    STAMP(15);
    const float rsl = __builtin_amdgcn_rcpf(lo16(msd)), nml = -hi16(msd) * rsl;
#pragma unroll
    for (int e = 0; e < 4; e++) {
      const float rs = __uint_as_float(__builtin_amdgcn_readlane(__float_as_uint(rsl), e));
      const float nm = __uint_as_float(__builtin_amdgcn_readlane(__float_as_uint(nml), e));
      LAS char* dst = sm + L_XS + (cw * 4 + e) * XS_STRIDE;
      const Row& x = rr[e];
      v2u a = {pk2(fmaf(lo16(x.a0.x), rs, nm), fmaf(hi16(x.a0.x), rs, nm)), pk2(fmaf(lo16(x.a0.y), rs, nm), fmaf(hi16(x.a0.y), rs, nm))};
      v2u c = {pk2(fmaf(lo16(x.a1.x), rs, nm), fmaf(hi16(x.a1.x), rs, nm)), pk2(fmaf(lo16(x.a1.y), rs, nm), fmaf(hi16(x.a1.y), rs, nm))};
      *(LAS v2u*)(dst + lane * 8) = a;
      *(LAS v2u*)(dst + 512 + lane * 8) = c;
      *(LAS u16*)(dst + 1024 + lane * 2) = (u16)pk2(fmaf(lo16(x.a2), rs, nm), 0.f);
    }
    lds_signal(sm, C_XS);
    STAMP(6);
    __builtin_amdgcn_s_setprio(0);
    Down dn;
    dn.load(P, wave - 4);
    dn.run(P, wave - 4);
  }
}

__device__ __forceinline__ void aux_wave(const Ctx& P, const int ROLE) {
  LAS char* sm = P.sm;
  const int lane = P.lane, wave = P.wave, C = P.C, cg = P.cg;
  (void)wave; (void)C; (void)cg;
  u32 tag;
    if (CODE_PF && wave == 6) asm volatile("s_waitcnt vmcnt(0)" ::: "v255");
    if (ROLE != 0 && wave == 5) {
      __builtin_amdgcn_s_setprio(3);
      lds_wait<1>(sm, C_TAG, 1);
      post_u_complete(P, gate_inc(*(const LAS u32*)(sm + L_REC)));
      __builtin_amdgcn_s_setprio(0);
      STAMP(13);
    }
    if (ROLE == 2 && HELPERS && wave == 6) {
      lds_wait<1>(sm, C_VER, 5);
      const int idx = (P.b - NCONV - NMOE) >> 3;
      u32 ta = touch_w1(P, 0, idx), tb = touch_w1(P, 1, idx);
#pragma unroll
      for (int i = 1; i < 5; i++) {
        const u32 tc = touch_w1(P, 0, i * 5 + idx), td = touch_w1(P, 1, i * 5 + idx);
        asm volatile("" :: "v"(ta), "v"(tb));
        ta = tc; tb = td;
      }
      asm volatile("" :: "v"(ta), "v"(tb));
      STAMP(5);
    }
    if (ROLE == 0) {
      __builtin_amdgcn_s_sleep(H_DELAY);
      const u32 tt = hcopy_first(P, 2 * (wave - 5));
      lds_wait(sm, C_H0, 2);
      hcopy_rest<2>(P, 12 + 2 * (wave - 5));
      asm volatile("" :: "v"(tt));
      STAMP(3);
      if (wave == 5) {
        lds_wait<1>(sm, C_U, 4);
        verify_half(P, 0);
        STAMP(6);
        lds_wait<2>(sm, C_Y1, 4);
        lds_wait<1>(sm, C_OK, 2);
        lds_wait<1>(sm, C_Y1, __builtin_amdgcn_readfirstlane(((const LAS u32*)(sm + L_CNT))[C_REDO]) ? 8 : 4);
        publish_y(P, sm + L_STG + 256, Y1_B, 16, lane, 32, 4);
      } else if (HELPERS) {
        lds_wait<1>(sm, C_U2, 1);
        u32 t[9];
#pragma unroll
        for (int i = 0; i < 4; i++) {
          const int u = 50 + 36 * PF_CE + i * 18 + (P.b >> 3);
          t[5 + i] = (i >= 4 - 2 * PF_CE) ? 0u : touch_w1(P, u & 1, u >> 1);
        }
#pragma unroll
        for (int i = 0; i < 5; i++) t[i] = touch_w2(P, i * 18 + (P.b >> 3));
        asm volatile("" :: "v"(t[5]), "v"(t[6]), "v"(t[7]), "v"(t[8]));
        STAMP(5);
        asm volatile("" :: "v"(t[0]), "v"(t[1]), "v"(t[2]), "v"(t[3]), "v"(t[4]));
        STAMP(6);
      }
    }
    if (wave == 5) {
      if (ROLE == 1) {
        Down dn;
        ffn_gate_up(P, [&]() { dn.load(P, 4); });
        dn.run(P, 4);
        gate_wait(P, G_Y, 9u, gate_inc(*(const LAS u32*)(sm + L_REC)));
        lds_signal(sm, C_GY);
      }
      return;
    }
    lds_wait<1>(sm, C_TAG, 1);
    tag = *(const LAS u32*)(sm + L_REC);
    if (ROLE == 1) { lds_wait<1>(sm, C_SEL, 1); __builtin_amdgcn_s_setprio(3); }
  if (ROLE != 1) {
    gate_wait(P, G_Y, 9u, gate_inc(tag));
    lds_signal(sm, C_GY);
    STAMP(7);
  }
}

__device__ __forceinline__ void router_stats(const Ctx& P, Early& E, const EarlyX& X, const int ROLE) {
  LAS char* sm = P.sm;
  const int lane = P.lane, C = P.C;
  u32 tag;
    const int m = lane & 15, g4 = lane >> 4;
    u64 epoch = E.epoch;
    const v4u (&xa)[9] = X.xa;
    const v4u (&wb)[9] = X.wb;
    asm volatile("" : "+v"(epoch) :: "memory");
    epoch += 1;
    tag = (u32)epoch;
    if (lane == 0) *(LAS u32*)(sm + L_REC) = tag;
    lds_signal(sm, C_TAG);
    STAMP(1);
    f4 gx = {0.f, 0.f, 0.f, 0.f}, gw = {0.f, 0.f, 0.f, 0.f}, gs = {0.f, 0.f, 0.f, 0.f};
#pragma unroll
    for (int i = 0; i < 9; i++) gx = mfma16(xa[i], xa[i], gx);
    __builtin_amdgcn_sched_barrier(0);
    const u32 gold = gate_post(P, G_R, true, gate_inc(tag));
    STAMP(12);
    __builtin_amdgcn_sched_barrier(0);
#pragma unroll
    for (int i = 0; i < 9; i++) { gw = mfma16(wb[i], xa[i], gw); gs = mfma16(wb[i], wb[i], gs); }
    float sxx = (m & 2) ? ((m & 1) ? gx[3] : gx[2]) : ((m & 1) ? gx[1] : gx[0]);
    sxx = __uint_as_float(__builtin_amdgcn_ds_bpermute((m + (m & 12) * 4) * 4, __float_as_uint(sxx)));
    float sxw = m < 8 ? gw[0] : gw[1], sx = gw[2];
    sxw += dppf<0x128>(sxw);
    sx += dppf<0x128>(sx);
    sxx += dppf<0x128>(sxx);
    const float sw = __uint_as_float(__builtin_amdgcn_readlane(__float_as_uint(gs[0] + gs[1]), 2));
    const float mean = sx * (1.0f / D);
    const float var = fmaxf(fmaf(-mean, mean, sxx * (1.0f / D)), 0.f);
    const float mu = rbf(mean);
    const float sd = rbf(__builtin_amdgcn_sqrtf(rbf(rbf(var) + 1e-5f)));
    const float rs = __builtin_amdgcn_rcpf(sd);
    const u32 r = pk2(0.f, fmaf(-mu, sw, sxw) * rs);
    u32 key = ((r ^ (((int)r >> 31) | 0x80000000u)) & 0xFFFF0000u) | (u32)(2047 - (8 * C + (m & 7)));
    u32 msd = __float_as_uint(mu) | (__float_as_uint(sd) >> 16);
    asm volatile("" : "+v"(key), "+v"(msd));
    const v2u rec = {m < 8 ? key : msd, tag};
    const int ro = lane < 16 ? (m < 8 ? WS_R : WS_M) + (8 * C + (m & 7)) * 8 : OOB;
    __builtin_amdgcn_raw_buffer_store_b64(rec, P.rws, ro, 0, AUX_SC1);
    STAMP(2);
    if (lane < 8) { f2 st = {rs, -mu * rs}; *(LAS f2*)(sm + L_STAT + m * 8) = st; }
    {
      const int t = m & 7, t3 = (t * 11) >> 5;
      LAS char* xd = sm + L_U + t3 * UL_B + 512 + (t - 3 * t3) * (D * 2) + (m >> 3) * 64 + g4 * 16;
#pragma unroll
      for (int i = 0; i < 9; i++) *(LAS v4u*)(xd + i * 128) = xa[i];
    }
    lds_signal(sm, C_STAT);
    E.epoch = epoch;
    E.cx = gold;
}

__device__ __forceinline__ void router_wave(const Ctx& P, const Early& E, const int ROLE) {
  LAS char* sm = P.sm;
  const int lane = P.lane, wave = P.wave, C = P.C, cg = P.cg;
  (void)wave; (void)C; (void)cg;
  u32 tag;
    const u64 epoch = E.epoch;
    tag = (u32)epoch;
    const u32 gold = E.cx;
    gate_confirm(P, G_R, gold, gate_inc(tag));
    route(P, tag, epoch, ROLE);
  if (ROLE == 0) {
    const int pi = lane & 7, ti = 4 * (pi & 1) + (pi & 2) + (pi >> 2);
    const u32 kx = ((LAS u32*)(sm + L_SEL))[ti];
    const int tok = 2047 - (int)(kx & 2047u);
    const v2u x2 = *(const GAS v2u*)((const GAS char*)P.q + (size_t)tok * (D * 2) + cg * 8);
    const v2u d2 = *(const GAS v2u*)((const GAS char*)P.dsk + cg * 8);
    const u32 msd = load_msd(P, tok, tag);
    const float rs = __builtin_amdgcn_rcpf(lo16(msd)), nm = -hi16(msd) * rs;
    const u32 rb = kx & 0xFFFF0000u;
    const float r = __uint_as_float((rb & 0x80000000u) ? (rb ^ 0x80000000u) : (~rb & 0xFFFF0000u));
    const float rsig = rbf(__builtin_amdgcn_rcpf(1.0f + __expf(-r)));
    STAMP(6);
    lds_wait<2>(sm, C_Y, 4);
    float o[4];
#pragma unroll
    for (int e = 0; e < 4; e++) {
      const float y = lo16(*(const LAS u16*)(sm + (tok < 1024 ? L_STG + e * Y1_B : L_H + e * (HC_B + 64) - 2048) + tok * 2));
      const float u = lo16(*(const LAS u16*)(sm + L_U + e * UL_B + (32 + (tok >> 3)) * 16 + (7 - (tok & 7)) * 2));
      const float x = (e & 1) ? hi16(x2[e >> 1]) : lo16(x2[e >> 1]);
      const float dk = (e & 1) ? hi16(d2[e >> 1]) : lo16(d2[e >> 1]);
      const float n = fmaf(x, rs, nm);
      o[e] = fmaf(n, fmaf(u, dk, y), x) + n;
      asm volatile("" : "+v"(o[e]));
    }
    gate_wait(P, G_P, lane < 8 ? 5u : 4u, gate_inc(tag));
    STAMP(7);
    v4u g[9];
    const u32 tag12 = tag & 0xFFFu;
    _Pragma("nounroll") for (u32 spin = 0; spin < SPIN_CAP; spin++) {
      asm volatile("" ::: "memory");
#pragma unroll
      for (int i = 0; i < 9; i++)
        g[i] = __builtin_amdgcn_raw_buffer_load_b128(P.rws, WS_P + (lane >> 3) * PART_B + cg * 128 + pi * 16, 8 * i * PART_B, AUX_SC1);
      u32 bad = 0;
#pragma unroll
      for (int i = 0; i < 9; i++) {
        g[i] ^= tag12;
        bad |= g[i].x | g[i].y;
        bad |= g[i].z | g[i].w;
      }
      if (__builtin_expect(__all((bad & 0xFFFu) == 0), 1)) break;
      NOTE(25, spin + 1);
      __builtin_amdgcn_s_sleep(4);
    }
    STAMP(8);
    f4 s4 = __builtin_bit_cast(f4, g[0]);
#pragma unroll
    for (int i = 1; i < 9; i++) s4 += __builtin_bit_cast(f4, g[i]);
#pragma unroll
    for (int e = 0; e < 4; e++) {
      float sv = s4[e];
      sv += dppf<0x128>(sv);
      o[e] = fmaf(rbf(xrow_sum(sv)), rsig, o[e]);
    }
    if (lane < 8) {
      v2u o2 = {pk2(o[0], o[1]), pk2(o[2], o[3])};
      __builtin_amdgcn_raw_buffer_store_b64(o2, mkrsrc(P.out, S * D * 2), tok * (D * 2) + cg * 8, 0, OUT_AUX);
    }
  }
}

__global__ __launch_bounds__(NTHR, 2) void fused_kernel(
    const u16* __restrict__ q, const u16* __restrict__ k, const u16* __restrict__ v, const u16* __restrict__ shk,
    const u16* __restrict__ hk, const u16* __restrict__ dsk, const u16* __restrict__ wr, const u16* __restrict__ wg,
    const u16* __restrict__ wu, const u16* __restrict__ wd, u16* __restrict__ out, char* ws) {
  extern __shared__ __attribute__((aligned(16))) char smem_[];
  Ctx P;
  P.q = q; P.k = k; P.v = v; P.shk = shk; P.hk = hk; P.dsk = dsk; P.wr = wr; P.wg = wg; P.wu = wu; P.wd = wd;
  P.out = out; P.ws = ws;
  u64 pc;
  asm volatile("s_getpc_b64 %0" : "=s"(pc));
  P.code = (const char*)(pc & ~(u64)127);
  P.sm = (LAS char*)smem_;
  P.tid = threadIdx.x; P.lane = P.tid & 63;
  P.wave = __builtin_amdgcn_readfirstlane(P.tid >> 6);
  const int b = blockIdx.x;
  P.b = b;
  P.C = (b & 7) * 32 + (b >> 3);
  P.is_conv = b < NCONV;
  const bool is_moe = !P.is_conv && b < NCONV + NMOE;
  P.cg = (b & 7) * 18 + (b >> 3);
  P.mw = (b & 7) * 9 + ((b - NCONV) >> 3);
  P.rws = mkrsrc(ws, WS_END);
  const int lane = P.lane, wave = P.wave;
  (void)lane;
  STAMP(0); STAMPC(30);
  Early E;
  if (wave == 6) {
    constexpr int NL = (CODE_PF * 8 + 31) / 32;
    if (CODE_PF)
      asm volatile("global_load_dword v255, %0, off" :: "v"(P.code + min(((b >> 3) * NL + min(lane, NL - 1)) * 128, CODE_PF * 1024 - 128)) : "v255");
    const v4u z = {0u, 0u, 0u, 0u};
    *(LAS v4u*)(P.sm + L_U + (lane >> 4) * UL_B + (lane & 15) * 16) = z;
    *(LAS v4u*)(P.sm + L_U + (lane >> 4) * UL_B + 256 + (lane & 15) * 16) = z;
    ((LAS u32*)(P.sm + L_MISC))[lane] = 0u;
    ((LAS u32*)(P.sm + L_MISC))[64 + lane] = 0u;
    asm volatile("s_waitcnt lgkmcnt(0)" ::: "memory");
  }
  asm volatile("s_barrier" ::: "memory");
  asm volatile("" :: "s"(q), "s"(k), "s"(v), "s"(shk), "s"(hk), "s"(dsk), "s"(wr), "s"(wg), "s"(wu), "s"(wd), "s"(out), "s"(ws));
  STAMP(11);
  const int row0 = 8 * P.C * (D * 2);
  EarlyX X;
  if (P.wave == 7) {
    __builtin_amdgcn_s_setprio(3);
    const int m = lane & 15, g4 = lane >> 4;
    E.epoch = __hip_atomic_load((GAS u64*)(P.ws + WS_E), __ATOMIC_RELAXED, __HIP_MEMORY_SCOPE_AGENT);
    if (ROUTER_DELAY) __builtin_amdgcn_s_sleep(ROUTER_DELAY);
    __builtin_amdgcn_sched_barrier(0);
    const __amdgpu_buffer_rsrc_t rq = mkrsrc(P.q, S * D * 2);
    const int xo = (m & 7) * (D * 2) + (m >> 3) * 64 + g4 * 16;
#pragma unroll
    for (int i = 0; i < 9; i++) X.xa[i] = __builtin_amdgcn_raw_buffer_load_b128(rq, xo + i * 128, row0, AUX_NT);
    __builtin_amdgcn_sched_barrier(0);
    const size_t pw = (size_t)P.wr + m * 64, po = (size_t)kOnes;
    const GAS char* wp = (const GAS char*)(m < 2 ? pw : po) + g4 * 16;
#pragma unroll
    for (int i = 0; i < 9; i++) X.wb[i] = *(const GAS v4u*)(wp + i * 128);
  }
  asm volatile("" : "+s"(P.wave));
  if (P.wave == 5) {
    const v4u g4 = {GMAGIC, GMAGIC, GMAGIC, GMAGIC};
    __builtin_amdgcn_raw_buffer_store_b128(g4, P.rws, lane < 8 * NGATE ? gate_off(lane >> 3, b >> 4, b & 15) + (lane & 7) * 16 : OOB, 0, AUX_SC1);
  }
  asm volatile("" : "+s"(P.wave));
  if (P.wave < 5) {
    const int j4 = min(P.tid, 287) * 4;
    const __amdgpu_buffer_rsrc_t rk = mkrsrc(P.k, S * D * 2), rv = mkrsrc(P.v, S * D * 2);
    if (TILE_DELAY) __builtin_amdgcn_s_sleep(TILE_DELAY);
    __builtin_amdgcn_sched_barrier(0);
#pragma unroll
    for (int i = 0; i < 3; i++) E.kk[i] = __builtin_amdgcn_raw_buffer_load_b32(rk, row0 + (i - 3) * (D * 2) + j4, 0, AUX_NT);
#pragma unroll
    for (int i = 0; i < 8; i++) E.kk[3 + i] = __builtin_amdgcn_raw_buffer_load_b32(rk, j4 + (i & 3) * (D * 2), row0 + (i & 4) * (D * 2), AUX_NT);
    __builtin_amdgcn_sched_barrier(0);
    if (SK_POS == 1) E.sk = *(const GAS v4u*)((const GAS char*)P.shk + j4 * 4);
    __builtin_amdgcn_sched_barrier(0);
#pragma unroll
    for (int i = 0; i < 8; i++) E.vv[i] = __builtin_amdgcn_raw_buffer_load_b32(rv, j4 + (i & 3) * (D * 2), row0 + (i & 4) * (D * 2), AUX_NT);
    __builtin_amdgcn_sched_barrier(0);
    if (SK_POS == 2) E.sk = *(const GAS v4u*)((const GAS char*)P.shk + j4 * 4);
    E.dd = *(const GAS u32*)((const GAS char*)P.dsk + j4);
  }
  const int role = P.is_conv ? 0 : (is_moe ? 1 : 2);
  asm volatile("" : "+s"(P.wave));
  STAMP(10);
  if (P.wave == 7) router_stats(P, E, X, role);
  asm volatile("" : "+s"(P.wave));
  if (P.wave != 7) {
    if (P.wave >= 5) aux_wave(P, role);
    else tile_wave(P, E, is_moe);
  } else {
    router_wave(P, E, role);
  }
  asm volatile("" : "+s"(P.wave));
  if (is_moe && P.wave >= 6) routed_rows(P);
  FLUSH();
}

}

extern "C" void kernel_launch(void* const* d_in, const int* in_sizes, int n_in, void* d_out, int out_size, void* d_ws,
                              size_t ws_size, hipStream_t stream) {
  (void)in_sizes; (void)n_in; (void)out_size; (void)ws_size;
  (void)hipFuncSetAttribute((const void*)fused_kernel, hipFuncAttributeMaxDynamicSharedMemorySize, L_TOTAL);
  fused_kernel<<<NWG, NTHR, L_TOTAL, stream>>>(
      (const u16*)d_in[0], (const u16*)d_in[1], (const u16*)d_in[2], (const u16*)d_in[3], (const u16*)d_in[4],
      (const u16*)d_in[5], (const u16*)d_in[7], (const u16*)d_in[8], (const u16*)d_in[9], (const u16*)d_in[10],
      (u16*)d_out, (char*)d_ws + (WS_AT_END ? ((ws_size - WS_END) & ~(size_t)0xFFFF) : 0));
}
```

```cpp
#include <hip/hip_runtime.h>
#include <hip/hip_bf16.h>

typedef unsigned int u32;
typedef unsigned long long u64;
typedef unsigned short u16;
typedef u32 v4u __attribute__((ext_vector_type(4)));
typedef u32 v2u __attribute__((ext_vector_type(2)));
typedef float f4 __attribute__((ext_vector_type(4)));
typedef float f2 __attribute__((ext_vector_type(2)));
typedef __bf16 bf8 __attribute__((ext_vector_type(8)));
typedef __bf16 bf2 __attribute__((ext_vector_type(2)));
#define LAS __attribute__((address_space(3)))
#define GAS __attribute__((address_space(1)))

namespace {

constexpr int S = 2048, D = 576, F = 4608;
constexpr int NWG = 256, NTHR = 512, NCONV = 144, NMOE = 72;
constexpr int AUX_SC1 = 16;
constexpr u32 SAME = 0xFFFFFFFFu, MAXV = 0xFFFFFFFEu;
constexpr u32 SPIN_CAP = 1u << 16, GATE_CAP = 96;
constexpr u32 GMAGIC = 0x5AC3E900u;
constexpr int OOB = 0x7FFFFF00;
#define U_POST 2
#define WS_AT_END 0
#define HELPERS 1
#define Y_POST 8
#define OUT_AUX 16
#define W2_GATE 2
#define PF_R0 192
#define STAGED 1
#define H_DELAY 30
#define PF_CE 2
#define PF_WHEN 0
#define W1_DEPTH 0
#define DRAIN_FIRST 1
#define U_DELAY 0
#define PF_FIRST 1
#define SK_POS 1
#define ROUTER_DELAY 0
#define W2_EARLY 0
#define TILE_DELAY 4
#define CODE_PF 40
#define USE_NT 0
constexpr int AUX_NT = USE_NT ? 2 : 0;

constexpr int WS_E = 0;
constexpr int WS_G = 4096;
constexpr int GATE_B = 256 * 256;
constexpr int WS_R = WS_G + 5 * GATE_B;
constexpr int WS_M = WS_R + S * 8;
constexpr int WS_U = WS_M + S * 8;
constexpr int TILE_B = D * 16;
constexpr int WS_P = WS_U + NWG * TILE_B;
constexpr int PART_B = NCONV * 128;
constexpr int WS_END = WS_P + NMOE * PART_B;
constexpr int WS_DBG = 4  __H2__ ;
static_assert(WS_END <= WS_DBG, "ws");
enum { G_R = 0, G_U, G_Y, G_P, G_V, NGATE };
#define ONES8 0x3F803F80u, 0x3F803F80u, 0x3F803F80u, 0x3F803F80u, 0x3F803F80u, 0x3F803F80u, 0x3F803F80u, 0x3F803F80u
#define ONES32 ONES8, ONES8, ONES8, ONES8
__device__ const u32 kOnes[288] __attribute__((aligned(128))) = {ONES32, ONES32, ONES32, ONES32, ONES32, ONES32, ONES32, ONES32, ONES32};

constexpr int HC_B = 2080 * 16;
constexpr int L_H = 0;
constexpr int UL_B = 290 * 16;
constexpr int L_U = L_H + 4 * HC_B;
constexpr int L_STG = L_U + 4 * UL_B;
constexpr int L_MISC = L_STG + TILE_B;
constexpr int L_CNT = L_MISC;
constexpr int L_CAND = L_CNT + 96;
constexpr int L_SEL = L_CAND + 288;
constexpr int L_REC = L_SEL + 32;
constexpr int L_STAT = L_REC + 32;
constexpr int Y1_B = 2048 + 64;
constexpr int L_END = L_STAT + 64;
constexpr int XS_STRIDE = 1184;
constexpr int L_XS = 0;
constexpr int L_RED = L_XS + 8 * XS_STRIDE;
constexpr int L_HB = L_RED + 6 * 8 * 128 * 4;
constexpr int HB_STRIDE = 160;
static_assert(L_END + 1024 <= 160 * 1024, "lds");
static_assert(L_HB + 8 * HB_STRIDE <= L_U, "lds");
static_assert(L_END - L_MISC == 512, "zero fill");

__device__ __forceinline__ constexpr int xrow_off(int t) { return L_U + (t / 3) * UL_B + 512 + (t % 3) * (D * 2); }
enum { C_DRAIN = 0, C_STAT, C_TRUST, C_GU, C_SEL, C_H, C_U, C_Y, C_XS, C_RED, C_HB, C_GY, C_PUB, C_TAG, C_QQ, C_U2, C_Y1, C_W1, C_DN, C_H0, C_VER, C_REDO, C_OK };

__device__ __forceinline__ float lo16(u32 x) { return __uint_as_float(x << 16); }
__device__ __forceinline__ float hi16(u32 x) { return __uint_as_float(x & 0xFFFF0000u); }
__device__ __forceinline__ u32 pk2(float a, float b) {
  bf2 p = {(__bf16)a, (__bf16)b};
  return __builtin_bit_cast(u32, p);
}
__device__ __forceinline__ float rbf(float a) { return lo16(pk2(a, a)); }
__device__ __forceinline__ float dot2(u32 a, u32 b, float c) {
  return __builtin_amdgcn_fdot2_f32_bf16(__builtin_bit_cast(bf2, a), __builtin_bit_cast(bf2, b), c, false);
}
template <int CTRL> __device__ __forceinline__ u32 dppu(u32 x) {
  return (u32)__builtin_amdgcn_mov_dpp((int)x, CTRL, 0xf, 0xf, true);
}
template <int CTRL> __device__ __forceinline__ float dppf(float x) { return __uint_as_float(dppu<CTRL>(__float_as_uint(x))); }
__device__ __forceinline__ float xrow_sum(float x) {
  auto s = __builtin_amdgcn_permlane16_swap(__float_as_uint(x), __float_as_uint(x), false, false);
  x = __uint_as_float(s[0]) + __uint_as_float(s[1]);
  auto t = __builtin_amdgcn_permlane32_swap(__float_as_uint(x), __float_as_uint(x), false, false);
  return __uint_as_float(t[0]) + __uint_as_float(t[1]);
}
__device__ __forceinline__ u32 umax(u32 a, u32 b) { return a > b ? a : b; }
__device__ __forceinline__ u32 umin(u32 a, u32 b) { return a < b ? a : b; }
__device__ __forceinline__ u32 umed3(u32 a, u32 b, u32 c) { return umax(umin(a, b), umin(umax(a, b), c)); }
__device__ __forceinline__ u32 xrow_umin(u32 x) {
  auto s = __builtin_amdgcn_permlane16_swap(x, x, false, false);
  x = umin(s[0], s[1]);
  auto t = __builtin_amdgcn_permlane32_swap(x, x, false, false);
  return umin(t[0], t[1]);
}
__device__ __forceinline__ u32 wave_umax(u32 x) {
  x = umax(x, dppu<0xB1>(x));
  x = umax(x, dppu<0x4E>(x));
  x = umax(x, dppu<0x141>(x));
  x = umax(x, dppu<0x140>(x));
  auto s = __builtin_amdgcn_permlane16_swap(x, x, false, false);
  x = umax(s[0], s[1]);
  auto t = __builtin_amdgcn_permlane32_swap(x, x, false, false);
  return umax(t[0], t[1]);
}
__device__ __forceinline__ void lds_signal(LAS char* sm, int c) {
  u64 keep;
  asm volatile("s_waitcnt lgkmcnt(0)\n\ts_mov_b64 %0, exec\n\ts_mov_b64 exec, 1\n\tds_add_u32 %1, %2\n\ts_mov_b64 exec, %0"
               : "=&s"(keep) : "v"((u32)(size_t)(sm + L_CNT + 4 * c)), "v"(1u) : "memory");
}
template <int SLEEP = 0> __device__ __forceinline__ void lds_wait(LAS char* sm, int c, u32 target) {
  u32 v, sv;
  if (SLEEP == 0)
    asm volatile("1:\n\tds_read_b32 %0, %2\n\ts_waitcnt lgkmcnt(0)\n\tv_readfirstlane_b32 %1, %0\n\ts_cmp_lt_u32 %1, %3\n\ts_cbranch_scc1 1b"
                 : "=&v"(v), "=&s"(sv) : "v"((u32)(size_t)(sm + L_CNT + 4 * c)), "s"(target) : "memory", "scc");
  else
    asm volatile("1:\n\ts_sleep %4\n\tds_read_b32 %0, %2\n\ts_waitcnt lgkmcnt(0)\n\tv_readfirstlane_b32 %1, %0\n\ts_cmp_lt_u32 %1, %3\n\ts_cbranch_scc1 1b"
                 : "=&v"(v), "=&s"(sv) : "v"((u32)(size_t)(sm + L_CNT + 4 * c)), "s"(target), "n"(SLEEP) : "memory", "scc");
}
__device__ __forceinline__ bool lds_arrive(LAS char* sm, int c, u32 last) {
  u64 keep;
  u32 v, sv;
  asm volatile("s_mov_b64 %0, exec\n\ts_mov_b64 exec, 1\n\tds_add_rtn_u32 %1, %3, %4\n\ts_waitcnt lgkmcnt(0)\n\tv_readfirstlane_b32 %2, %1\n\ts_mov_b64 exec, %0"
               : "=&s"(keep), "=&v"(v), "=&s"(sv) : "v"((u32)(size_t)(sm + L_CNT + 4 * c)), "v"(1u) : "memory");
  return sv == last;
}
__device__ __forceinline__ void wg_barrier() { asm volatile("s_waitcnt lgkmcnt(0)\n\ts_barrier" ::: "memory"); }
__device__ __forceinline__ u32 umax3(u32 a, u32 b, u32 c) { return umax(umax(a, b), c); }
__device__ __forceinline__ u32 vmax4(v4u g, u32 acc) { return umax3(umax3(g.x, g.y, acc), g.z, g.w); }
__device__ __forceinline__ f4 mfma16(v4u a, v4u b, f4 c) {
  return __builtin_amdgcn_mfma_f32_16x16x32_bf16(__builtin_bit_cast(bf8, a), __builtin_bit_cast(bf8, b), c, 0, 0, 0);
}
__device__ __forceinline__ __amdgpu_buffer_rsrc_t mkrsrc(const void* p, int bytes) {
  return __builtin_amdgcn_make_buffer_rsrc((void*)p, 0, bytes, 0x00020000);
}

#define DBG_TS 1
#define STAMP(i) do { P.ts[i] = (u32)__builtin_amdgcn_s_memrealtime() | 1u; } while (0)
#define STAMPC(i) do { P.ts[i] = (u32)__builtin_amdgcn_s_memtime(); } while (0)
#define FLUSH() do { STAMP(29); if (P.wave == 0 && P.is_conv) { const u32 d_ = (P.ts[2] - P.ts[0]) * 3; while ((u32)__builtin_amdgcn_s_memrealtime() - P.ts[29] < d_ && d_ < 20000u) __builtin_amdgcn_s_sleep(1); } } while (0)
#define NOTE(i, v) do { P.ts[i] = (v); } while (0)
constexpr int L_TOTAL = L_END;

struct Ctx {
  const u16 *q, *k, *v, *shk, *hk, *dsk, *wr, *wg, *wu, *wd;
  u16* out;
  char* ws;
  const char* code;
  __amdgpu_buffer_rsrc_t rws;
  LAS char* sm;
  int tid, lane, wave, b, C, cg, mw;
  bool is_conv;
  u32 t0;
  mutable u32 ts[32];
};

__device__ __forceinline__ int gate_off(int gate, int rep, int shard) { return WS_G + gate * GATE_B + (rep * 16 + shard) * 256; }
__device__ __forceinline__ u32 gate_add(const Ctx& P, int off, int val) {
  return (u32)__builtin_amdgcn_raw_ptr_buffer_atomic_add_i32(val, P.rws, off, 0, 0);
}
__device__ __forceinline__ u32 gate_post(const Ctx& P, int gate, bool on, u32 inc) {
  return gate_add(P, (on && P.lane < 16) ? gate_off(gate, P.lane, P.b & 15) : OOB, (int)inc);
}
__device__ __forceinline__ void gate_confirm(const Ctx& P, int gate, u32 old, u32 inc) {
  bool bad = P.lane < 16;
  _Pragma("nounroll") for (u32 spin = 0; spin < SPIN_CAP; spin++) {
    const u32 d = old - GMAGIC;
    bad = bad && ((d & 0xF0u) != 0u || d != (d & 0xFu) * inc);
    if (__builtin_expect(!__any(bad), 1)) break;
    NOTE(26, spin + 1);
    __builtin_amdgcn_s_sleep(2);
    old = gate_add(P, bad ? gate_off(gate, P.lane, P.b & 15) : OOB, (int)inc);
  }
}
__device__ __forceinline__ void gate_wait(const Ctx& P, int gate, u32 expect, u32 inc, u32 cap = GATE_CAP) {
  const int off = P.lane < 16 ? gate_off(gate, P.b >> 4, P.lane) : OOB;
  const u32 want = P.lane < 16 ? GMAGIC + expect * inc : 0u;
  _Pragma("nounroll") for (u32 spin = 0; spin < cap; spin++)
    if (__all(gate_add(P, off, 0) == want)) break;
}
__device__ __forceinline__ u32 gate_inc(u32 tag) { return 1u + (tag << 8); }
__device__ __forceinline__ void post_u_complete(const Ctx& P, u32 inc) {
  lds_wait<1>(P.sm, C_DRAIN, 5);
  gate_confirm(P, G_V, gate_post(P, G_V, true, inc), inc);
}

struct Row { v2u a0, a1; u32 a2; };
template <int AUX> __device__ __forceinline__ Row load_row(__amdgpu_buffer_rsrc_t r, int soff, int lane) {
  Row x;
  x.a0 = __builtin_amdgcn_raw_buffer_load_b64(r, lane * 8, soff, AUX);
  x.a1 = __builtin_amdgcn_raw_buffer_load_b64(r, lane * 8 + 512, soff, AUX);
  x.a2 = __builtin_amdgcn_raw_buffer_load_b16(r, lane * 2 + 1024, soff, AUX);
  return x;
}
__device__ __forceinline__ void hcopy_store(LAS char* dst, v4u lo, v4u hi, bool first) {
  u32 d[8], o[7];
#pragma unroll
  for (int e = 0; e < 4; e++) { d[e] = lo[e]; d[4 + e] = hi[e]; }
#pragma unroll
  for (int e = 0; e < 7; e++) o[e] = __builtin_amdgcn_alignbit(d[e + 1], d[e], 16);
  constexpr int BASE[8] = {0, 260, 519, 778, 1037, 1296, 1556, 1815};
#pragma unroll
  for (int e = 0; e < 4; e++) {
    v4u a = {d[e], d[e + 1], d[e + 2], d[e + 3]}, c = {o[e], o[e + 1], o[e + 2], o[e + 3]};
    if (e > 0 || first) *(LAS v4u*)(dst + BASE[2 * e] * 16) = a;
    *(LAS v4u*)(dst + BASE[2 * e + 1] * 16) = c;
  }
}
__device__ __forceinline__ u32 touch_w1(const Ctx& P, int mat, int blk);
__device__ __forceinline__ u32 hcopy_first(const Ctx& P, int ch0) {
  const __amdgpu_buffer_rsrc_t rh = mkrsrc(P.hk + (size_t)(4 * P.cg + ch0) * S, 2 * S * 2);
  v4u lo[8];
#pragma unroll
  for (int r = 0; r < 8; r++) lo[r] = __builtin_amdgcn_raw_buffer_load_b128(rh, P.lane * 16 + (r & 3) * 1024, (r >> 2) * 4096, AUX_NT);
  __builtin_amdgcn_sched_barrier(0);
  STAMP(12);
  LAS char* dst = P.sm + L_H + ch0 * HC_B + (P.lane + 2) * 16;
#pragma unroll
  for (int r = 0; r < 8; r++) *(LAS v4u*)(dst + (r >> 2) * HC_B + (r & 3) * 1024) = lo[r];
  lds_signal(P.sm, C_H0);
  u32 t = 0, t2 = 0;
  if (HELPERS != 0 && PF_WHEN == 0 && PF_CE >= 1) t = touch_w1(P, ch0 >> 1, 25 + (P.b >> 3));
  if (HELPERS != 0 && PF_WHEN == 0 && PF_CE >= 2) t2 = touch_w1(P, ch0 >> 1, 43 + (P.b >> 3));
  __builtin_amdgcn_sched_barrier(0);
  STAMP(1);
  {
    const int c = P.lane & 1, m1 = (P.lane >> 1) & 1;
    const v4u z = {0u, 0u, 0u, 0u};
    v4u h0 = *(const LAS v4u*)(P.sm + L_H + (ch0 + c) * HC_B + 32);
    h0 = m1 ? h0 : z;
    if (P.lane < 4) hcopy_store(P.sm + L_H + (ch0 + c) * HC_B + m1 * 16, z, h0, true);
  }
  return t | t2;
}
template <int NR> __device__ __forceinline__ void hcopy_rest(const Ctx& P, int r0) {
  v4u lo[NR], hi[NR];
  LAS char* dst[NR];
#pragma unroll
  for (int r = 0; r < NR; r++) {
    dst[r] = P.sm + L_H + ((r0 + r) >> 2) * HC_B + ((r0 + r) & 3) * 1024 + (P.lane + 2) * 16;
    lo[r] = *(const LAS v4u*)dst[r];
    hi[r] = *(const LAS v4u*)(dst[r] + 16);
  }
#pragma unroll
  for (int r = 0; r < NR; r++) hcopy_store(dst[r], lo[r], hi[r], false);
  lds_signal(P.sm, C_H);
}

template <class FN> __device__ __forceinline__ void ffn_gate_up(const Ctx& P, FN&& between) {
  LAS char* sm = P.sm;
  const int lane = P.lane, wave = P.wave, n16 = lane & 15, g4 = lane >> 4;
  v4u W1[3][8];
  const GAS char* wp = (const GAS char*)(n16 < 8 ? P.wg : P.wu) + ((size_t)(96 * wave + 8 * g4) * F + 64 * P.mw + 8 * (n16 & 7)) * 2;
  if (wave >= PF_FIRST) lds_wait<1>(sm, wave < 2 ? C_PUB : C_W1, wave < 2 ? 1 : 2);
  __builtin_amdgcn_sched_barrier(0);
#pragma unroll
  for (int i = 0; i < 3; i++)
#pragma unroll
    for (int jj = 0; jj < 8; jj++) {
      const GAS v4u* a = (const GAS v4u*)(wp + (size_t)(32 * i + jj) * (F * 2));
      W1[i][jj] = USE_NT ? __builtin_nontemporal_load(a) : *a;
    }
  __builtin_amdgcn_sched_barrier(0);
  lds_signal(sm, C_H0);
  STAMP(3);
  v4u Bf[3][8];
#pragma unroll
  for (int i = 0; i < 3; i++)
#pragma unroll
    for (int e = 0; e < 8; e++) {
#pragma unroll
      for (int d = 0; d < 4; d++)
        Bf[i][e][d] = __builtin_amdgcn_perm(W1[i][2 * d + 1][e >> 1], W1[i][2 * d][e >> 1], (e & 1) ? 0x07060302u : 0x05040100u);
      asm volatile("" : "+v"(Bf[i][e]));
    }
  STAMP(17);
  lds_wait<1>(sm, C_XS, 2);
  STAMP(5);
  f4 acc[8];
#pragma unroll
  for (int e = 0; e < 8; e++) acc[e] = (f4){0.f, 0.f, 0.f, 0.f};
#pragma unroll
  for (int i = 0; i < 3; i++) {
    const v4u A = *(const LAS v4u*)(sm + L_XS + (n16 & 7) * XS_STRIDE + (96 * wave + 32 * i + 8 * g4) * 2);
#pragma unroll
    for (int e = 0; e < 8; e++) acc[e] = mfma16(A, Bf[i][e], acc[e]);
  }
  {
    LAS char* dst = sm + L_RED + ((wave * 8 + 4 * (g4 & 1) + (g4 & 2)) * 128 + 8 * n16) * 4;
#pragma unroll
    for (int r = 0; r < 2; r++) {
      f4 a, c;
#pragma unroll
      for (int e = 0; e < 4; e++) { a[e] = g4 >= 2 ? acc[e][2 + r] : acc[e][r]; c[e] = g4 >= 2 ? acc[4 + e][2 + r] : acc[4 + e][r]; }
      *(LAS f4*)(dst + r * 512) = a;
      *(LAS f4*)(dst + r * 512 + 16) = c;
    }
  }
  lds_signal(sm, C_RED);
  STAMP(18);
  between();
  if (wave >= 2) {
    const int tk = 2 * (wave - 2) + (lane >> 5);
    const LAS char* src = sm + L_RED + (tk * 128 + 2 * (lane & 31)) * 4;
    lds_wait(sm, C_RED, 6);
    f2 G = {0.f, 0.f}, U = {0.f, 0.f};
#pragma unroll
    for (int w = 0; w < 6; w++) {
      G += *(const LAS f2*)(src + w * 4096);
      U += *(const LAS f2*)(src + w * 4096 + 256);
    }
    const u32 gb = pk2(G.x, G.y), ub = pk2(U.x, U.y);
    const float g0 = lo16(gb), g1 = hi16(gb);
    const u32 sg = pk2(g0 * __builtin_amdgcn_rcpf(1.0f + __expf(-g0)), g1 * __builtin_amdgcn_rcpf(1.0f + __expf(-g1)));
    *(LAS u32*)(sm + L_HB + tk * HB_STRIDE + (lane & 31) * 4) = pk2(lo16(sg) * lo16(ub), hi16(sg) * hi16(ub));
    lds_signal(sm, C_HB);
    STAMP(15);
  }
}

struct Down {
  v4u W2[2][8];
  __device__ __forceinline__ void load(const Ctx& P, int cu) {
    const int n16 = P.lane & 15, g4 = P.lane >> 4;
    const __amdgpu_buffer_rsrc_t rd = mkrsrc(P.wd + (size_t)64 * P.mw * D, 64 * D * 2);
    const int o2 = (cu == 4 && n16 >= 8) ? OOB : (8 * g4 * D + 128 * cu + 8 * n16) * 2;
    lds_wait<1>(P.sm, C_H0, 6);
    __builtin_amdgcn_sched_barrier(0);
#pragma unroll
    for (int ks = 0; ks < 2; ks++)
#pragma unroll
      for (int jj = 0; jj < 8; jj++)
        W2[ks][jj] = __builtin_amdgcn_raw_buffer_load_b128(rd, o2 + (jj & 3) * (D * 2), (32 * ks + (jj & 4)) * (D * 2), AUX_NT);
    __builtin_amdgcn_sched_barrier(0);
    STAMP(4);
  }
  __device__ __forceinline__ void run(const Ctx& P, int cu) {
    LAS char* sm = P.sm;
    const int n16 = P.lane & 15, g4 = P.lane >> 4;
    const u32 tag = *(const LAS u32*)(sm + L_REC), tag12 = tag & 0xFFFu;
    __builtin_amdgcn_sched_barrier(0);
    v4u Bd[2][8];
#pragma unroll
    for (int ks = 0; ks < 2; ks++)
#pragma unroll
      for (int e = 0; e < 8; e++) {
#pragma unroll
        for (int d = 0; d < 4; d++)
          Bd[ks][e][d] = __builtin_amdgcn_perm(W2[ks][2 * d + 1][e >> 1], W2[ks][2 * d][e >> 1], (e & 1) ? 0x07060302u : 0x05040100u);
        asm volatile("" : "+v"(Bd[ks][e]));
      }
    const int off = WS_P + P.mw * PART_B + (32 * cu + 2 * (n16 & 7)) * 128 + (n16 >> 3) * 64 + g4 * 16;
    STAMP(19);
    lds_wait(sm, C_HB, 4);
    const v4u A0 = *(const LAS v4u*)(sm + L_HB + (n16 & 7) * HB_STRIDE + g4 * 16);
    const v4u A1 = *(const LAS v4u*)(sm + L_HB + (n16 & 7) * HB_STRIDE + 64 + g4 * 16);
    if (lds_arrive(sm, C_DN, 4)) (void)gate_post(P, G_P, true, gate_inc(tag));
    STAMP(20);
    f4 ad[8];
#pragma unroll
    for (int e = 0; e < 8; e++) ad[e] = mfma16(A0, Bd[0][e], (f4){0.f, 0.f, 0.f, 0.f});
#pragma unroll
    for (int e = 0; e < 8; e++) ad[e] = mfma16(A1, Bd[1][e], ad[e]);
#pragma unroll
    for (int hh = 0; hh < 2; hh++) {
      v4u s0, s1;
#pragma unroll
      for (int e = 0; e < 4; e++) {
        const f4 a = ad[4 * hh + e];
        const u32 x = ((__float_as_uint(g4 >= 2 ? a[2] : a[0]) + 0x800u) & 0xFFFFF000u) | tag12;
        const u32 y = ((__float_as_uint(g4 >= 2 ? a[3] : a[1]) + 0x800u) & 0xFFFFF000u) | tag12;
        s0[e] = (u32)__builtin_amdgcn_update_dpp((int)x, (int)y, 0x128, 0xf, 0xc, false);
        s1[e] = (u32)__builtin_amdgcn_update_dpp((int)y, (int)x, 0x128, 0xf, 0x3, false);
      }
      __builtin_amdgcn_raw_buffer_store_b128(s0, P.rws, off + hh * 128, 0, AUX_SC1);
      if (cu != 4) __builtin_amdgcn_raw_buffer_store_b128(s1, P.rws, off + hh * 128 + 2048, 0, AUX_SC1);
    }
    STAMP(21);
  }
};

__device__ __forceinline__ u32 touch_w1(const Ctx& P, int mat, int blk) {
  const int r7 = (P.lane * 57) >> 9, c9 = P.lane - 9 * r7;
  return __builtin_amdgcn_raw_buffer_load_b32(mkrsrc(__builtin_amdgcn_readfirstlane(mat) ? P.wu : P.wg, D * F * 2), P.lane < 63 ? r7 * (F * 2) + c9 * 128 : OOB,
                                              (PF_R0 + 7 * blk) * (F * 2) + (P.b & 7) * 1152, AUX_NT);
}
__device__ __forceinline__ u32 touch_w2(const Ctx& P, int unit) {
  return __builtin_amdgcn_raw_buffer_load_b32(mkrsrc(P.wd, D * F * 2), unit < 81 ? P.lane * 128 : OOB,
                                              (P.b & 7) * (9 * 64 * D * 2) + unit * 8192, AUX_NT);
}

__device__ __forceinline__ void publish_y(const Ctx& P, const LAS char* yb, int ystride, int tile0, int t, int step, int n) {
  const int gch = t & 3;
  const int goff = WS_U + (tile0 + (t >> 2)) * TILE_B + (4 * P.cg + gch) * 16;
#pragma unroll
  for (int it = 0; it < n; it++) {
    const v4u y4 = *(const LAS v4u*)(yb + gch * ystride + (it * step + (t >> 2)) * 16);
    const v4u u4 = *(const LAS v4u*)(P.sm + L_U + gch * UL_B + (32 + tile0 + it * step + (t >> 2)) * 16);
    v4u w4;
#pragma unroll
    for (int e = 0; e < 4; e++) w4[e] = (y4[e] == u4[e]) ? SAME : y4[e];
    __builtin_amdgcn_raw_buffer_store_b128(w4, P.rws, goff, it * step * TILE_B, AUX_SC1);
  }
}

__device__ __forceinline__ void gather_half(const Ctx& P, int tile0, v4u (&g)[8]) {
  const int goff = WS_U + (tile0 + (P.lane >> 2)) * TILE_B + (4 * P.cg + (P.lane & 3)) * 16;
#pragma unroll
  for (int it = 0; it < 8; it++) g[it] = __builtin_amdgcn_raw_buffer_load_b128(P.rws, goff, it * 16 * TILE_B, AUX_SC1);
}

__device__ __forceinline__ void verify_half(const Ctx& P, int tile0) {
  LAS char* sm = P.sm;
  lds_wait<1>(sm, C_TRUST, 1);
  v4u g[8];
  gather_half(P, tile0, g);
  LAS char* ud = sm + L_U + (P.lane & 3) * UL_B + (32 + tile0 + (P.lane >> 2)) * 16;
  u32 df = 0;
#pragma unroll
  for (int it = 0; it < 8; it++) {
    const v4u x = *(const LAS v4u*)(ud + it * 256) ^ g[it];
    df |= x.x | x.y;
    df |= x.z | x.w;
  }
  if (__builtin_expect(__any(df != 0), 0)) {
#pragma unroll
    for (int it = 0; it < 8; it++) *(LAS v4u*)(ud + it * 256) = g[it];
    lds_signal(sm, C_REDO);
  }
  lds_signal(sm, C_OK);
}

__device__ __forceinline__ void conv_phase(const Ctx& P, u32 inc) {
  LAS char* sm = P.sm;
  const int tid = P.tid, lane = P.lane, wave = P.wave, n16 = lane & 15, g4 = lane >> 4, gch = tid & 3;
  const int goff = WS_U + (tid >> 2) * TILE_B + (4 * P.cg + gch) * 16;
  LAS char* ud = sm + L_U + gch * UL_B + (32 + (tid >> 2)) * 16;
  const int p1 = n16 + 1, kap = p1 & 7;
  const int base16 = 258 * kap + ((0x98654320u >> (4 * kap)) & 15);
  const LAS char* pa = sm + L_H + wave * HC_B + (base16 + g4 + (p1 >> 3)) * 16;
  const LAS char* pb = sm + L_U + wave * UL_B + (32 + 2 * n16 + 1 - g4 - 28) * 16;
  lds_wait<1>(sm, C_H0, 2);
  hcopy_rest<3>(P, 3 * wave);
  STAMP(17);
  asm volatile("s_waitcnt vmcnt(0)" ::: "memory");
  lds_signal(sm, C_DRAIN);
  lds_wait<1>(sm, C_QQ, 5);
  lds_wait<1>(sm, C_GU, 1);
  __builtin_amdgcn_s_setprio(2);
  STAMP(3);
  if (U_DELAY) __builtin_amdgcn_s_sleep(U_DELAY);
  STAMP(4);
  v4u g[2];
#pragma unroll
  for (int it = 0; it < 2; it++) g[it] = __builtin_amdgcn_raw_buffer_load_b128(P.rws, goff, it * 64 * TILE_B, AUX_SC1);
  *(LAS v4u*)ud = g[0];
  *(LAS v4u*)(ud + 1024) = g[1];
  lds_signal(sm, C_U);
  STAMP(5);
  lds_wait(sm, C_H, 6);
  lds_wait(sm, C_U, 4);
  STAMP(6);
  f4 acc[8];
  v4u A[2][8], B[2][4];
  _Pragma("nounroll") for (int pass = 0; pass < 2; pass++) {
#pragma unroll
  for (int t = 0; t < 8; t++) acc[t] = (f4){0.f, 0.f, 0.f, 0.f};
#pragma unroll
  for (int a = 0; a < 8; a++) {
    A[0][a] = *(const LAS v4u*)(pa + a * 512);
    if (a < 4) B[0][a] = *(const LAS v4u*)(pb + a * 512 + 7 * 64);
  }
#pragma unroll
  for (int bb = 0; bb < 8; bb++) {
    __builtin_amdgcn_sched_barrier(0);
    if (bb == 7) {
      lds_wait(sm, C_U2, 1);
      __builtin_amdgcn_sched_barrier(0);
    }
#pragma unroll
    for (int a = 0; a < 8; a++) {
      if (bb < 7) {
        A[(bb + 1) & 1][a] = *(const LAS v4u*)(pa + a * 512 + (bb + 1) * 64);
        if (a < 4) B[(bb + 1) & 1][a] = *(const LAS v4u*)(pb + a * 512 + (6 - bb) * 64);
      } else if (a < 4) {
        A[0][a] = *(const LAS v4u*)(pa + a * 512);
        B[0][a] = *(const LAS v4u*)(pb + (4 + a) * 512 + 7 * 64);
      }
    }
#pragma unroll
    for (int t = 0; t < 8; t++)
#pragma unroll
      for (int c = 0; c < 4; c++)
        if (c <= t) acc[t] = mfma16(A[bb & 1][t - c], B[bb & 1][c], acc[t]);
#pragma unroll
    for (int i = 0; i < (bb < 7 ? 12 : 8); i++) {
      __builtin_amdgcn_sched_group_barrier(0x8, 2, 0);
      __builtin_amdgcn_sched_group_barrier(0x100, 1, 0);
    }
    if (bb < 7) __builtin_amdgcn_sched_group_barrier(0x8, 2, 0);
    else __builtin_amdgcn_sched_group_barrier(0x8, 10, 0);
  }
#pragma unroll
  for (int bb = 0; bb < 8; bb++) {
    __builtin_amdgcn_sched_barrier(0);
    if (bb == 1) {
      LAS char* yb = sm + L_STG + wave * Y1_B + n16 * 32 + g4 * 8;
#pragma unroll
      for (int t = 0; t < 4; t++) {
        v2u y2 = {umin(pk2(acc[t][0], acc[t][1]), MAXV), umin(pk2(acc[t][2], acc[t][3]), MAXV)};
        *(LAS v2u*)(yb + t * 512) = y2;
      }
    }
    if (bb == 2) lds_signal(sm, C_Y1);
    if (bb == Y_POST) {
      (void)gate_post(P, G_Y, wave == 0 && pass == 0, inc);
      __builtin_amdgcn_sched_barrier(0);
    }
    if (bb < 7) {
#pragma unroll
      for (int a = 0; a < 4; a++) {
        A[(bb + 1) & 1][a] = *(const LAS v4u*)(pa + a * 512 + (bb + 1) * 64);
        B[(bb + 1) & 1][a] = *(const LAS v4u*)(pb + (4 + a) * 512 + (6 - bb) * 64);
      }
    }
#pragma unroll
    for (int t = 0; t < 4; t++)
#pragma unroll
      for (int c = 0; c <= t; c++) acc[4 + t] = mfma16(A[bb & 1][t - c], B[bb & 1][c], acc[4 + t]);
    if (bb < 7) {
#pragma unroll
      for (int i = 0; i < 8; i++) {
        __builtin_amdgcn_sched_group_barrier(0x8, 1, 0);
        __builtin_amdgcn_sched_group_barrier(0x100, 1, 0);
      }
      __builtin_amdgcn_sched_group_barrier(0x8, 2, 0);
    }
  }
  __builtin_amdgcn_sched_barrier(0);
    lds_wait(sm, C_OK, 2);
    if (__builtin_expect(__builtin_amdgcn_readfirstlane(((const LAS u32*)(sm + L_CNT))[C_REDO]) == 0u, 1)) break;
    NOTE(26, 7);
  }
  if (Y_POST == 8 && wave == 0) (void)gate_post(P, G_Y, true, inc);
  {
    LAS char* yb = sm + L_H + wave * (HC_B + 64) + n16 * 32 + g4 * 8;
#pragma unroll
    for (int t = 0; t < 4; t++) {
      v2u y2 = {umin(pk2(acc[4 + t][0], acc[4 + t][1]), MAXV), umin(pk2(acc[4 + t][2], acc[4 + t][3]), MAXV)};
      *(LAS v2u*)(yb + t * 512) = y2;
    }
  }
  lds_signal(sm, C_Y);
  STAMP(7);
  lds_wait(sm, C_Y, 4);
  publish_y(P, sm + L_H, HC_B + 64, 128, tid, 64, 2);
  STAMP(8);
}

struct EarlyX { v4u xa[9], wb[9]; };
struct Early {
  u32 kk[11], vv[8], dd;
  v4u sk;
  u64 epoch;
  u32 cx;
};

__device__ __forceinline__ void tile_wave(const Ctx& P, const Early& E, const bool MOE) {
  LAS char* sm = P.sm;
  const int tid = P.tid, lane = P.lane, wave = P.wave, C = P.C;
  const bool act = tid < 288;
  const int j4 = min(tid, 287) * 4;
  const int row0 = 8 * C * (D * 2);
  const u32 (&kk)[11] = E.kk;
  const u32 (&vv)[8] = E.vv;
  const v4u sk = E.sk;
  u32 qq[8];
  u32 gold, uinc;
  bool poster;
  u32 up[2][4];
  {
    f2 w[4], kf[11], ks[8];
    w[0] = (f2){lo16(sk.x), lo16(sk.z)}; w[1] = (f2){hi16(sk.x), hi16(sk.z)};
    w[2] = (f2){lo16(sk.y), lo16(sk.w)}; w[3] = (f2){hi16(sk.y), hi16(sk.w)};
#pragma unroll
    for (int i = 0; i < 11; i++) kf[i] = (f2){lo16(kk[i]), hi16(kk[i])};
#pragma unroll
    for (int t = 0; t < 8; t++) {
      ks[t] = __builtin_elementwise_fma(kf[t + 3], w[0], __builtin_elementwise_fma(kf[t + 2], w[1], __builtin_elementwise_fma(kf[t + 1], w[2], kf[t] * w[3])));
      asm volatile("" : "+v"(ks[t]));
    }
    __builtin_amdgcn_sched_barrier(0);
    lds_wait(sm, C_TAG, 1);
    uinc = gate_inc(*(const LAS u32*)(sm + L_REC));
    STAMP(12);
#pragma unroll
    for (int t = 0; t < 8; t++) ks[t] *= (f2){lo16(vv[t]), hi16(vv[t])};
#pragma unroll
    for (int i = 0; i < 4; i++) {
      up[0][i] = umin(pk2(ks[7 - 2 * i].x, ks[6 - 2 * i].x), MAXV);
      up[1][i] = umin(pk2(ks[7 - 2 * i].y, ks[6 - 2 * i].y), MAXV);
    }
    STAMP(1);
    LAS char* stg = sm + L_STG + wave * 2048;
    v4u p0 = {up[0][0], up[0][1], up[0][2], up[0][3]}, p1 = {up[1][0], up[1][1], up[1][2], up[1][3]};
    if (act) { *(LAS v4u*)(stg + lane * 32) = p0; *(LAS v4u*)(stg + lane * 32 + 16) = p1; }
    asm volatile("" ::: "memory");
    p0 = *(const LAS v4u*)(stg + lane * 16);
    p1 = *(const LAS v4u*)(stg + (wave < 4 ? 1024 : 0) + lane * 16);
    STAMP(13);
    const int so = WS_U + C * TILE_B + wave * 2048 + lane * 16;
    __builtin_amdgcn_raw_buffer_store_b128(p0, P.rws, so, 0, AUX_SC1);
    if (wave < 4) __builtin_amdgcn_raw_buffer_store_b128(p1, P.rws, so + 1024, 0, AUX_SC1);
  }
  __builtin_amdgcn_sched_barrier(0);
  poster = lds_arrive(sm, C_VER, 4);
  if (poster) gold = gate_post(P, G_U, true, uinc);
  STAMP(2);
  lds_wait(sm, C_STAT, 1);
#pragma unroll
  for (int i = 0; i < 8; i++) qq[i] = *(const LAS u32*)(sm + xrow_off(i) + j4);
  lds_signal(sm, C_QQ);
  u32 dd = E.dd;
  asm volatile("" : "+v"(dd));
  float xn[2][8], c0[2][8];
  auto prepare = [&]()
  {
    const float d0 = lo16(dd), d1 = hi16(dd);
#pragma unroll
    for (int s2 = 0; s2 < 4; s2++) {
      const f4 st = *(const LAS f4*)(sm + L_STAT + s2 * 16);
#pragma unroll
      for (int e = 0; e < 2; e++) {
        const int s = 2 * s2 + e;
        const float xa = lo16(qq[s]), xb = hi16(qq[s]);
        const float na = fmaf(xa, st[2 * e], st[2 * e + 1]), nb = fmaf(xb, st[2 * e], st[2 * e + 1]);
        const u32 pa = up[0][(7 - s) >> 1], pb = up[1][(7 - s) >> 1];
        const float ua = ((7 - s) & 1) ? hi16(pa) : lo16(pa), ub = ((7 - s) & 1) ? hi16(pb) : lo16(pb);
        xn[0][s] = na; xn[1][s] = nb;
        c0[0][s] = fmaf(na, fmaf(ua, d0, 1.0f), xa);
        c0[1][s] = fmaf(nb, fmaf(ub, d1, 1.0f), xb);
        asm volatile("" : "+v"(xn[0][s]), "+v"(xn[1][s]), "+v"(c0[0][s]), "+v"(c0[1][s]));
      }
    }
  };
  prepare();
  if (poster) gate_confirm(P, G_U, gold, uinc);
  if (!(P.is_conv && wave < 4)) {
    asm volatile("s_waitcnt vmcnt(0)" ::: "memory");
    lds_signal(sm, C_DRAIN);
  }
  STAMP(14);
  if (MOE) {
    ffn_gate_up(P, []() {});
    if (wave < 2) { Down dn; dn.load(P, wave); dn.run(P, wave); }
  } else {
   if (P.is_conv) {
    if (wave < 4) {
      conv_phase(P, uinc);
    } else {
      lds_wait<1>(sm, C_QQ, 5);
      lds_wait<1>(sm, C_GU, 1);
      __builtin_amdgcn_s_sleep(3 + U_DELAY);
      v4u g[8];
      gather_half(P, 128, g);
#pragma unroll
      for (int it = 0; it < 8; it++) *(LAS v4u*)(sm + L_U + (lane & 3) * UL_B + (32 + 128 + it * 16 + (lane >> 2)) * 16) = g[it];
      lds_signal(sm, C_U2);
      STAMP(5);
      gate_wait(P, G_V, 16u, uinc, SPIN_CAP);
      lds_signal(sm, C_TRUST);
      STAMP(4);
      verify_half(P, 128);
      STAMP(6);
      lds_wait<2>(sm, C_Y1, 4);
      lds_wait<1>(sm, C_OK, 2);
      lds_wait<1>(sm, C_Y1, __builtin_amdgcn_readfirstlane(((const LAS u32*)(sm + L_CNT))[C_REDO]) ? 8 : 4);
      publish_y(P, sm + L_STG, Y1_B, 0, lane, 32, 4);
    }
  } else {
    STAMP(5);
   }
  }
  lds_wait<1>(sm, C_SEL, 1);
  const u32 skip = __builtin_amdgcn_readfirstlane(*(const LAS u32*)(sm + L_REC + 4));
  const __amdgpu_buffer_rsrc_t ro = mkrsrc(P.out, S * D * 2);
  const int oo = act ? j4 : OOB;
  const int uoff = WS_U + C * TILE_B + j4 * 8;
  lds_wait<1>(sm, C_GY, 1);
  STAMP(9);
  v4u y0, y1;
  _Pragma("nounroll") for (u32 spin = 0; spin < SPIN_CAP; spin++) {
    asm volatile("" ::: "memory");
    y0 = __builtin_amdgcn_raw_buffer_load_b128(P.rws, uoff, 0, AUX_SC1);
    y1 = __builtin_amdgcn_raw_buffer_load_b128(P.rws, uoff + 16, 0, AUX_SC1);
    bool ok = true;
#pragma unroll
    for (int e = 0; e < 4; e++) ok = ok & (y0[e] != up[0][e]) & (y1[e] != up[1][e]);
    if (__builtin_expect(__all(ok), 1)) break;
    NOTE(25, spin + 1);
    __builtin_amdgcn_s_sleep(4);
  }
  STAMP(16);
#pragma unroll
  for (int e = 0; e < 4; e++) {
    const u32 a = y0[e] == SAME ? up[0][e] : y0[e], c = y1[e] == SAME ? up[1][e] : y1[e];
    const u32 o0 = pk2(fmaf(xn[0][2 * e], lo16(a), c0[0][2 * e]), fmaf(xn[1][2 * e], lo16(c), c0[1][2 * e]));
    const u32 o1 = pk2(fmaf(xn[0][2 * e + 1], hi16(a), c0[0][2 * e + 1]), fmaf(xn[1][2 * e + 1], hi16(c), c0[1][2 * e + 1]));
    const int so = row0 + (e >> 1) * 4 * (D * 2);
    if (!((skip >> (2 * e)) & 1)) __builtin_amdgcn_raw_buffer_store_b32(o0, ro, oo + ((2 * e) & 3) * (D * 2), so, OUT_AUX);
    if (!((skip >> (2 * e + 1)) & 1)) __builtin_amdgcn_raw_buffer_store_b32(o1, ro, oo + ((2 * e + 1) & 3) * (D * 2), so, OUT_AUX);
  }
}

__device__ __forceinline__ void route(const Ctx& P, u32 tag, u64 epoch, const int ROLE) {
  LAS char* sm = P.sm;
  const int lane = P.lane, wave = P.wave;
  (void)wave;
  v4u g[16];
  if (ROLE == 2) __builtin_amdgcn_s_setprio(0);
  if (ROLE == 0) {
    if (HELPERS != 0 && PF_WHEN != 0 && PF_CE != 0) {
      lds_wait<1>(sm, C_VER, PF_WHEN);
      u32 t[4] = {0u, 0u, 0u, 0u};
#pragma unroll
      for (int i = 0; i < 2 * PF_CE; i++) t[i] = touch_w1(P, i & 1, 25 + 18 * (i >> 1) + (P.b >> 3));
      asm volatile("" :: "v"(t[0]), "v"(t[1]), "v"(t[2]), "v"(t[3]));
      STAMP(18);
    }
    gate_wait(P, G_U, 16u, gate_inc(tag));
    lds_signal(sm, C_GU);
    STAMP(15);
    __builtin_amdgcn_s_setprio(0);
    post_u_complete(P, gate_inc(tag));
    STAMP(13);
    lds_wait<1>(sm, C_U, 4);
  } else {
    gate_wait(P, ROLE == 1 ? G_R : G_U, 16u, gate_inc(tag));
  }
  STAMP(3);
  _Pragma("nounroll") for (u32 spin = 0; spin < SPIN_CAP; spin++) {
    asm volatile("" ::: "memory");
#pragma unroll
    for (int i = 0; i < 16; i++)
      g[i] = __builtin_amdgcn_raw_buffer_load_b128(P.rws, WS_R + lane * 16 + (i & 3) * 1024, (i >> 2) * 4096, AUX_SC1);
    if (spin == 0) lds_signal(sm, C_PUB);
    u32 bad = 0;
#pragma unroll
    for (int i = 0; i < 16; i++) {
      bad = __builtin_amdgcn_bitop3_b32(bad, g[i].y, tag, 0xF6);
      bad = __builtin_amdgcn_bitop3_b32(bad, g[i].w, tag, 0xF6);
    }
    if (__builtin_expect(__all(bad == 0), 1)) break;
    NOTE(24, spin + 1);
    __builtin_amdgcn_s_sleep(4);
  }
  STAMP(4);
  if (P.b == 0 && lane == 0) __hip_atomic_store((GAS u64*)(P.ws + WS_E), epoch, __ATOMIC_RELAXED, __HIP_MEMORY_SCOPE_AGENT);
  u32 k1 = umax(g[0].x, g[0].z), k2 = umin(g[0].x, g[0].z), k3 = 0;
#pragma unroll
  for (int i = 2; i < 32; i++) {
    const u32 x = (i & 1) ? g[i >> 1].z : g[i >> 1].x;
    k3 = umed3(k2, k3, x);
    k2 = umed3(k1, k2, x);
    k1 = umax(k1, x);
  }
  u32 tau = umax(k1, dppu<0xB1>(k1));
  tau = umax(tau, dppu<0x4E>(tau));
  tau = umax(tau, dppu<0x141>(tau));
  tau = umin(tau, dppu<0x140>(tau));
  tau = xrow_umin(tau);
  LAS u32* cand = (LAS u32*)(sm + L_CAND);
  const u64 m1 = __ballot(k1 >= tau), m2 = __ballot(k2 >= tau), m3 = __ballot(k3 >= tau);
  const u32 n1 = __builtin_popcountll(m1), n2 = n1 + __builtin_popcountll(m2), n = n2 + __builtin_popcountll(m3);
  bool slow = n > 64;
  if (__builtin_expect(!slow, 1)) {
    const u32 p1 = __builtin_amdgcn_mbcnt_hi((u32)(m1 >> 32), __builtin_amdgcn_mbcnt_lo((u32)m1, 0u));
    const u32 p2 = __builtin_amdgcn_mbcnt_hi((u32)(m2 >> 32), __builtin_amdgcn_mbcnt_lo((u32)m2, n1));
    const u32 p3 = __builtin_amdgcn_mbcnt_hi((u32)(m3 >> 32), __builtin_amdgcn_mbcnt_lo((u32)m3, n2));
    cand[k1 >= tau ? p1 : 64] = k1;
    cand[k2 >= tau ? p2 : 64] = k2;
    cand[k3 >= tau ? p3 : 64] = k3;
    asm volatile("" ::: "memory");
    const u32 mine = cand[lane];
    u32 rank = 0;
    _Pragma("nounroll") for (u32 jj = 0; jj < n; jj += 8) {
      const v4u c4 = *(const LAS v4u*)(cand + jj), d4 = *(const LAS v4u*)(cand + jj + 4);
      rank += (c4.x > mine) + (c4.y > mine) + (c4.z > mine) + (c4.w > mine);
      rank += (d4.x > mine) + (d4.y > mine) + (d4.z > mine) + (d4.w > mine);
    }
    const bool in = (u32)lane < n && rank < 8;
    if (in) ((LAS u32*)(sm + L_SEL))[rank] = mine;
    slow = __any(in && (u32)lane >= n2);
  }
  if (__builtin_expect(slow, 0)) {
    _Pragma("nounroll") for (int r = 0; r < 8; r++) {
      u32 mx = 0;
#pragma unroll
      for (int i = 0; i < 16; i++) mx = umax3(mx, g[i].x, g[i].z);
      mx = wave_umax(mx);
#pragma unroll
      for (int i = 0; i < 16; i++) { g[i].x = g[i].x == mx ? 0u : g[i].x; g[i].z = g[i].z == mx ? 0u : g[i].z; }
      if (lane == 0) ((LAS u32*)(sm + L_SEL))[r] = mx;
    }
  }
  asm volatile("" ::: "memory");
  {
    const u32 tk = 2047u - (((LAS u32*)(sm + L_SEL))[lane & 7] & 2047u);
    u32 mk = (int)(tk >> 3) == P.C ? 1u << (tk & 7) : 0u;
    mk |= dppu<0xB1>(mk);
    mk |= dppu<0x4E>(mk);
    mk |= dppu<0x141>(mk);
    if (lane == 0) *(LAS u32*)(sm + L_REC + 4) = mk;
  }
  lds_signal(sm, C_SEL);
  STAMP(5);
}

__device__ __forceinline__ u32 load_msd(const Ctx& P, int tok, u32 tag) {
  v2u g;
  _Pragma("nounroll") for (u32 spin = 0; spin < SPIN_CAP; spin++) {
    asm volatile("" ::: "memory");
    g = __builtin_amdgcn_raw_buffer_load_b64(P.rws, WS_M + tok * 8, 0, AUX_SC1);
    if (__builtin_expect(__all(g.y == tag), 1)) break;
    __builtin_amdgcn_s_sleep(4);
  }
  return g.x;
}

__device__ __forceinline__ void routed_rows(const Ctx& P) {
  LAS char* sm = P.sm;
  const int lane = P.lane, wave = P.wave, C = P.C, cg = P.cg;
  (void)wave; (void)C; (void)cg;
  const __amdgpu_buffer_rsrc_t rq = mkrsrc(P.q, S * D * 2);
  {
    const int cw = wave - 6;
    const u32 tag = *(const LAS u32*)(sm + L_REC);
    const v4u s4 = *(const LAS v4u*)(sm + L_SEL + cw * 16);
    Row rr[4];
#pragma unroll
    for (int e = 0; e < 4; e++) {
      const int tok = 2047 - (int)(__builtin_amdgcn_readfirstlane(s4[e]) & 2047u);
      rr[e] = load_row<0>(rq, tok * (D * 2), lane);
    }
    const u32 ks = (lane & 2) ? ((lane & 1) ? s4.w : s4.z) : ((lane & 1) ? s4.y : s4.x);
    const int mtok = 2047 - (int)(ks & 2047u);
    v2u mg = __builtin_amdgcn_raw_buffer_load_b64(P.rws, WS_M + mtok * 8, 0, AUX_SC1);
    __builtin_amdgcn_sched_barrier(0);
    lds_signal(sm, C_W1);
    __builtin_amdgcn_sched_barrier(0);
    u32 msd = mg.x;
    if (__builtin_expect(!__all(mg.y == tag), 0)) msd = load_msd(P, mtok, tag);
    STAMP(15);
    const float rsl = __builtin_amdgcn_rcpf(lo16(msd)), nml = -hi16(msd) * rsl;
#pragma unroll
    for (int e = 0; e < 4; e++) {
      const float rs = __uint_as_float(__builtin_amdgcn_readlane(__float_as_uint(rsl), e));
      const float nm = __uint_as_float(__builtin_amdgcn_readlane(__float_as_uint(nml), e));
      LAS char* dst = sm + L_XS + (cw * 4 + e) * XS_STRIDE;
      const Row& x = rr[e];
      v2u a = {pk2(fmaf(lo16(x.a0.x), rs, nm), fmaf(hi16(x.a0.x), rs, nm)), pk2(fmaf(lo16(x.a0.y), rs, nm), fmaf(hi16(x.a0.y), rs, nm))};
      v2u c = {pk2(fmaf(lo16(x.a1.x), rs, nm), fmaf(hi16(x.a1.x), rs, nm)), pk2(fmaf(lo16(x.a1.y), rs, nm), fmaf(hi16(x.a1.y), rs, nm))};
      *(LAS v2u*)(dst + lane * 8) = a;
      *(LAS v2u*)(dst + 512 + lane * 8) = c;
      *(LAS u16*)(dst + 1024 + lane * 2) = (u16)pk2(fmaf(lo16(x.a2), rs, nm), 0.f);
    }
    lds_signal(sm, C_XS);
    STAMP(6);
    __builtin_amdgcn_s_setprio(0);
    Down dn;
    dn.load(P, wave - 4);
    dn.run(P, wave - 4);
  }
}

__device__ __forceinline__ void aux_wave(const Ctx& P, const int ROLE) {
  LAS char* sm = P.sm;
  const int lane = P.lane, wave = P.wave, C = P.C, cg = P.cg;
  (void)wave; (void)C; (void)cg;
  u32 tag;
    if (CODE_PF && wave == 6) asm volatile("s_waitcnt vmcnt(0)" ::: "v255");
    if (ROLE != 0 && wave == 5) {
      __builtin_amdgcn_s_setprio(3);
      lds_wait<1>(sm, C_TAG, 1);
      post_u_complete(P, gate_inc(*(const LAS u32*)(sm + L_REC)));
      __builtin_amdgcn_s_setprio(0);
      STAMP(13);
    }
    if (ROLE == 2 && HELPERS && wave == 6) {
      lds_wait<1>(sm, C_VER, 5);
      const int idx = (P.b - NCONV - NMOE) >> 3;
      u32 ta = touch_w1(P, 0, idx), tb = touch_w1(P, 1, idx);
#pragma unroll
      for (int i = 1; i < 5; i++) {
        const u32 tc = touch_w1(P, 0, i * 5 + idx), td = touch_w1(P, 1, i * 5 + idx);
        asm volatile("" :: "v"(ta), "v"(tb));
        ta = tc; tb = td;
      }
      asm volatile("" :: "v"(ta), "v"(tb));
      STAMP(5);
    }
    if (ROLE == 0) {
      __builtin_amdgcn_s_sleep(H_DELAY);
      const u32 tt = hcopy_first(P, 2 * (wave - 5));
      lds_wait(sm, C_H0, 2);
      hcopy_rest<2>(P, 12 + 2 * (wave - 5));
      asm volatile("" :: "v"(tt));
      STAMP(3);
      if (wave == 5) {
        lds_wait<1>(sm, C_U, 4);
        verify_half(P, 0);
        STAMP(6);
        lds_wait<2>(sm, C_Y1, 4);
        lds_wait<1>(sm, C_OK, 2);
        lds_wait<1>(sm, C_Y1, __builtin_amdgcn_readfirstlane(((const LAS u32*)(sm + L_CNT))[C_REDO]) ? 8 : 4);
        publish_y(P, sm + L_STG + 256, Y1_B, 16, lane, 32, 4);
      } else if (HELPERS) {
        lds_wait<1>(sm, C_U2, 1);
        u32 t[9];
#pragma unroll
        for (int i = 0; i < 4; i++) {
          const int u = 50 + 36 * PF_CE + i * 18 + (P.b >> 3);
          t[5 + i] = (i >= 4 - 2 * PF_CE) ? 0u : touch_w1(P, u & 1, u >> 1);
        }
#pragma unroll
        for (int i = 0; i < 5; i++) t[i] = touch_w2(P, i * 18 + (P.b >> 3));
        asm volatile("" :: "v"(t[5]), "v"(t[6]), "v"(t[7]), "v"(t[8]));
        STAMP(5);
        asm volatile("" :: "v"(t[0]), "v"(t[1]), "v"(t[2]), "v"(t[3]), "v"(t[4]));
        STAMP(6);
      }
    }
    if (wave == 5) {
      if (ROLE == 1) {
        Down dn;
        ffn_gate_up(P, [&]() { dn.load(P, 4); });
        dn.run(P, 4);
        gate_wait(P, G_Y, 9u, gate_inc(*(const LAS u32*)(sm + L_REC)));
        lds_signal(sm, C_GY);
      }
      return;
    }
    lds_wait<1>(sm, C_TAG, 1);
    tag = *(const LAS u32*)(sm + L_REC);
    if (ROLE == 1) { lds_wait<1>(sm, C_SEL, 1); __builtin_amdgcn_s_setprio(3); }
  if (ROLE != 1) {
    gate_wait(P, G_Y, 9u, gate_inc(tag));
    lds_signal(sm, C_GY);
    STAMP(7);
  }
}

__device__ __forceinline__ void router_stats(const Ctx& P, Early& E, const EarlyX& X, const int ROLE) {
  LAS char* sm = P.sm;
  const int lane = P.lane, C = P.C;
  u32 tag;
    const int m = lane & 15, g4 = lane >> 4;
    u64 epoch = E.epoch;
    const v4u (&xa)[9] = X.xa;
    const v4u (&wb)[9] = X.wb;
    asm volatile("" : "+v"(epoch) :: "memory");
    epoch += 1;
    tag = (u32)epoch;
    if (lane == 0) *(LAS u32*)(sm + L_REC) = tag;
    lds_signal(sm, C_TAG);
    STAMP(1);
    f4 gx = {0.f, 0.f, 0.f, 0.f}, gw = {0.f, 0.f, 0.f, 0.f}, gs = {0.f, 0.f, 0.f, 0.f};
#pragma unroll
    for (int i = 0; i < 9; i++) gx = mfma16(xa[i], xa[i], gx);
    __builtin_amdgcn_sched_barrier(0);
    const u32 gold = gate_post(P, G_R, true, gate_inc(tag));
    STAMP(12);
    __builtin_amdgcn_sched_barrier(0);
#pragma unroll
    for (int i = 0; i < 9; i++) { gw = mfma16(wb[i], xa[i], gw); gs = mfma16(wb[i], wb[i], gs); }
    float sxx = (m & 2) ? ((m & 1) ? gx[3] : gx[2]) : ((m & 1) ? gx[1] : gx[0]);
    sxx = __uint_as_float(__builtin_amdgcn_ds_bpermute((m + (m & 12) * 4) * 4, __float_as_uint(sxx)));
    float sxw = m < 8 ? gw[0] : gw[1], sx = gw[2];
    sxw += dppf<0x128>(sxw);
    sx += dppf<0x128>(sx);
    sxx += dppf<0x128>(sxx);
    const float sw = __uint_as_float(__builtin_amdgcn_readlane(__float_as_uint(gs[0] + gs[1]), 2));
    const float mean = sx * (1.0f / D);
    const float var = fmaxf(fmaf(-mean, mean, sxx * (1.0f / D)), 0.f);
    const float mu = rbf(mean);
    const float sd = rbf(__builtin_amdgcn_sqrtf(rbf(rbf(var) + 1e-5f)));
    const float rs = __builtin_amdgcn_rcpf(sd);
    const u32 r = pk2(0.f, fmaf(-mu, sw, sxw) * rs);
    u32 key = ((r ^ (((int)r >> 31) | 0x80000000u)) & 0xFFFF0000u) | (u32)(2047 - (8 * C + (m & 7)));
    u32 msd = __float_as_uint(mu) | (__float_as_uint(sd) >> 16);
    asm volatile("" : "+v"(key), "+v"(msd));
    const v2u rec = {m < 8 ? key : msd, tag};
    const int ro = lane < 16 ? (m < 8 ? WS_R : WS_M) + (8 * C + (m & 7)) * 8 : OOB;
    __builtin_amdgcn_raw_buffer_store_b64(rec, P.rws, ro, 0, AUX_SC1);
    STAMP(2);
    if (lane < 8) { f2 st = {rs, -mu * rs}; *(LAS f2*)(sm + L_STAT + m * 8) = st; }
    {
      const int t = m & 7, t3 = (t * 11) >> 5;
      LAS char* xd = sm + L_U + t3 * UL_B + 512 + (t - 3 * t3) * (D * 2) + (m >> 3) * 64 + g4 * 16;
#pragma unroll
      for (int i = 0; i < 9; i++) *(LAS v4u*)(xd + i * 128) = xa[i];
    }
    lds_signal(sm, C_STAT);
    E.epoch = epoch;
    E.cx = gold;
}

__device__ __forceinline__ void router_wave(const Ctx& P, const Early& E, const int ROLE) {
  LAS char* sm = P.sm;
  const int lane = P.lane, wave = P.wave, C = P.C, cg = P.cg;
  (void)wave; (void)C; (void)cg;
  u32 tag;
    const u64 epoch = E.epoch;
    tag = (u32)epoch;
    const u32 gold = E.cx;
    gate_confirm(P, G_R, gold, gate_inc(tag));
    route(P, tag, epoch, ROLE);
  if (ROLE == 0) {
    const int pi = lane & 7, ti = 4 * (pi & 1) + (pi & 2) + (pi >> 2);
    const u32 kx = ((LAS u32*)(sm + L_SEL))[ti];
    const int tok = 2047 - (int)(kx & 2047u);
    const v2u x2 = *(const GAS v2u*)((const GAS char*)P.q + (size_t)tok * (D * 2) + cg * 8);
    const v2u d2 = *(const GAS v2u*)((const GAS char*)P.dsk + cg * 8);
    const u32 msd = load_msd(P, tok, tag);
    const float rs = __builtin_amdgcn_rcpf(lo16(msd)), nm = -hi16(msd) * rs;
    const u32 rb = kx & 0xFFFF0000u;
    const float r = __uint_as_float((rb & 0x80000000u) ? (rb ^ 0x80000000u) : (~rb & 0xFFFF0000u));
    const float rsig = rbf(__builtin_amdgcn_rcpf(1.0f + __expf(-r)));
    STAMP(6);
    lds_wait<2>(sm, C_Y, 4);
    float o[4];
#pragma unroll
    for (int e = 0; e < 4; e++) {
      const float y = lo16(*(const LAS u16*)(sm + (tok < 1024 ? L_STG + e * Y1_B : L_H + e * (HC_B + 64) - 2048) + tok * 2));
      const float u = lo16(*(const LAS u16*)(sm + L_U + e * UL_B + (32 + (tok >> 3)) * 16 + (7 - (tok & 7)) * 2));
      const float x = (e & 1) ? hi16(x2[e >> 1]) : lo16(x2[e >> 1]);
      const float dk = (e & 1) ? hi16(d2[e >> 1]) : lo16(d2[e >> 1]);
      const float n = fmaf(x, rs, nm);
      o[e] = fmaf(n, fmaf(u, dk, y), x) + n;
      asm volatile("" : "+v"(o[e]));
    }
    gate_wait(P, G_P, lane < 8 ? 5u : 4u, gate_inc(tag));
    STAMP(7);
    v4u g[9];
    const u32 tag12 = tag & 0xFFFu;
    _Pragma("nounroll") for (u32 spin = 0; spin < SPIN_CAP; spin++) {
      asm volatile("" ::: "memory");
#pragma unroll
      for (int i = 0; i < 9; i++)
        g[i] = __builtin_amdgcn_raw_buffer_load_b128(P.rws, WS_P + (lane >> 3) * PART_B + cg * 128 + pi * 16, 8 * i * PART_B, AUX_SC1);
      u32 bad = 0;
#pragma unroll
      for (int i = 0; i < 9; i++) {
        g[i] ^= tag12;
        bad |= g[i].x | g[i].y;
        bad |= g[i].z | g[i].w;
      }
      if (__builtin_expect(__all((bad & 0xFFFu) == 0), 1)) break;
      NOTE(25, spin + 1);
      __builtin_amdgcn_s_sleep(4);
    }
    STAMP(8);
    f4 s4 = __builtin_bit_cast(f4, g[0]);
#pragma unroll
    for (int i = 1; i < 9; i++) s4 += __builtin_bit_cast(f4, g[i]);
#pragma unroll
    for (int e = 0; e < 4; e++) {
      float sv = s4[e];
      sv += dppf<0x128>(sv);
      o[e] = fmaf(rbf(xrow_sum(sv)), rsig, o[e]);
    }
    if (lane < 8) {
      v2u o2 = {pk2(o[0], o[1]), pk2(o[2], o[3])};
      __builtin_amdgcn_raw_buffer_store_b64(o2, mkrsrc(P.out, S * D * 2), tok * (D * 2) + cg * 8, 0, OUT_AUX);
    }
  }
}

__global__ __launch_bounds__(NTHR, 2) void fused_kernel(
    const u16* __restrict__ q, const u16* __restrict__ k, const u16* __restrict__ v, const u16* __restrict__ shk,
    const u16* __restrict__ hk, const u16* __restrict__ dsk, const u16* __restrict__ wr, const u16* __restrict__ wg,
    const u16* __restrict__ wu, const u16* __restrict__ wd, u16* __restrict__ out, char* ws) {
  extern __shared__ __attribute__((aligned(16))) char smem_[];
  Ctx P;
  P.q = q; P.k = k; P.v = v; P.shk = shk; P.hk = hk; P.dsk = dsk; P.wr = wr; P.wg = wg; P.wu = wu; P.wd = wd;
  P.out = out; P.ws = ws;
  u64 pc;
  asm volatile("s_getpc_b64 %0" : "=s"(pc));
  P.code = (const char*)(pc & ~(u64)127);
  P.sm = (LAS char*)smem_;
  P.tid = threadIdx.x; P.lane = P.tid & 63;
  P.wave = __builtin_amdgcn_readfirstlane(P.tid >> 6);
  const int b = blockIdx.x;
  P.b = b;
  P.C = (b & 7) * 32 + (b >> 3);
  P.is_conv = b < NCONV;
  const bool is_moe = !P.is_conv && b < NCONV + NMOE;
  P.cg = (b & 7) * 18 + (b >> 3);
  P.mw = (b & 7) * 9 + ((b - NCONV) >> 3);
  P.rws = mkrsrc(ws, WS_END);
  const int lane = P.lane, wave = P.wave;
  (void)lane;
#pragma unroll
  for (int i = 0; i < 32; i++) P.ts[i] = 0;
  STAMP(0); STAMPC(30);
  Early E;
  if (wave == 6) {
    constexpr int NL = (CODE_PF * 8 + 31) / 32;
    if (CODE_PF)
      asm volatile("global_load_dword v255, %0, off" :: "v"(P.code + min(((b >> 3) * NL + min(lane, NL - 1)) * 128, CODE_PF * 1024 - 128)) : "v255");
    const v4u z = {0u, 0u, 0u, 0u};
    *(LAS v4u*)(P.sm + L_U + (lane >> 4) * UL_B + (lane & 15) * 16) = z;
    *(LAS v4u*)(P.sm + L_U + (lane >> 4) * UL_B + 256 + (lane & 15) * 16) = z;
    ((LAS u32*)(P.sm + L_MISC))[lane] = 0u;
    ((LAS u32*)(P.sm + L_MISC))[64 + lane] = 0u;
    asm volatile("s_waitcnt lgkmcnt(0)" ::: "memory");
  }
  asm volatile("s_barrier" ::: "memory");
  asm volatile("" :: "s"(q), "s"(k), "s"(v), "s"(shk), "s"(hk), "s"(dsk), "s"(wr), "s"(wg), "s"(wu), "s"(wd), "s"(out), "s"(ws));
  STAMP(11);
  const int row0 = 8 * P.C * (D * 2);
  EarlyX X;
  if (P.wave == 7) {
    __builtin_amdgcn_s_setprio(3);
    const int m = lane & 15, g4 = lane >> 4;
    E.epoch = __hip_atomic_load((GAS u64*)(P.ws + WS_E), __ATOMIC_RELAXED, __HIP_MEMORY_SCOPE_AGENT);
    if (ROUTER_DELAY) __builtin_amdgcn_s_sleep(ROUTER_DELAY);
    __builtin_amdgcn_sched_barrier(0);
    const __amdgpu_buffer_rsrc_t rq = mkrsrc(P.q, S * D * 2);
    const int xo = (m & 7) * (D * 2) + (m >> 3) * 64 + g4 * 16;
#pragma unroll
    for (int i = 0; i < 9; i++) X.xa[i] = __builtin_amdgcn_raw_buffer_load_b128(rq, xo + i * 128, row0, AUX_NT);
    __builtin_amdgcn_sched_barrier(0);
    const size_t pw = (size_t)P.wr + m * 64, po = (size_t)kOnes;
    const GAS char* wp = (const GAS char*)(m < 2 ? pw : po) + g4 * 16;
#pragma unroll
    for (int i = 0; i < 9; i++) X.wb[i] = *(const GAS v4u*)(wp + i * 128);
  }
  asm volatile("" : "+s"(P.wave));
  if (P.wave == 5) {
    const v4u g4 = {GMAGIC, GMAGIC, GMAGIC, GMAGIC};
    __builtin_amdgcn_raw_buffer_store_b128(g4, P.rws, lane < 8 * NGATE ? gate_off(lane >> 3, b >> 4, b & 15) + (lane & 7) * 16 : OOB, 0, AUX_SC1);
  }
  asm volatile("" : "+s"(P.wave));
  if (P.wave < 5) {
    const int j4 = min(P.tid, 287) * 4;
    const __amdgpu_buffer_rsrc_t rk = mkrsrc(P.k, S * D * 2), rv = mkrsrc(P.v, S * D * 2);
    if (TILE_DELAY) __builtin_amdgcn_s_sleep(TILE_DELAY);
    __builtin_amdgcn_sched_barrier(0);
#pragma unroll
    for (int i = 0; i < 3; i++) E.kk[i] = __builtin_amdgcn_raw_buffer_load_b32(rk, row0 + (i - 3) * (D * 2) + j4, 0, AUX_NT);
#pragma unroll
    for (int i = 0; i < 8; i++) E.kk[3 + i] = __builtin_amdgcn_raw_buffer_load_b32(rk, j4 + (i & 3) * (D * 2), row0 + (i & 4) * (D * 2), AUX_NT);
    __builtin_amdgcn_sched_barrier(0);
    if (SK_POS == 1) E.sk = *(const GAS v4u*)((const GAS char*)P.shk + j4 * 4);
    __builtin_amdgcn_sched_barrier(0);
#pragma unroll
    for (int i = 0; i < 8; i++) E.vv[i] = __builtin_amdgcn_raw_buffer_load_b32(rv, j4 + (i & 3) * (D * 2), row0 + (i & 4) * (D * 2), AUX_NT);
    __builtin_amdgcn_sched_barrier(0);
    if (SK_POS == 2) E.sk = *(const GAS v4u*)((const GAS char*)P.shk + j4 * 4);
    E.dd = *(const GAS u32*)((const GAS char*)P.dsk + j4);
  }
  const int role = P.is_conv ? 0 : (is_moe ? 1 : 2);
  asm volatile("" : "+s"(P.wave));
  STAMP(10);
  if (P.wave == 7) router_stats(P, E, X, role);
  asm volatile("" : "+s"(P.wave));
  if (P.wave != 7) {
    if (P.wave >= 5) aux_wave(P, role);
    else tile_wave(P, E, is_moe);
  } else {
    router_wave(P, E, role);
  }
  asm volatile("" : "+s"(P.wave));
  if (is_moe && P.wave >= 6) routed_rows(P);
  FLUSH();
}

}

extern "C" void kernel_launch(void* const* d_in, const int* in_sizes, int n_in, void* d_out, int out_size, void* d_ws,
                              size_t ws_size, hipStream_t stream) {
  (void)in_sizes; (void)n_in; (void)out_size; (void)ws_size;
  (void)hipFuncSetAttribute((const void*)fused_kernel, hipFuncAttributeMaxDynamicSharedMemorySize, L_TOTAL);
  fused_kernel<<<NWG, NTHR, L_TOTAL, stream>>>(
      (const u16*)d_in[0], (const u16*)d_in[1], (const u16*)d_in[2], (const u16*)d_in[3], (const u16*)d_in[4],
      (const u16*)d_in[5], (const u16*)d_in[7], (const u16*)d_in[8], (const u16*)d_in[9], (const u16*)d_in[10],
      (u16*)d_out, (char*)d_ws + (WS_AT_END ? ((ws_size - WS_END) & ~(size_t)0xFFFF) : 0));
}
```

```cpp
#include <hip/hip_runtime.h>
#include <hip/hip_bf16.h>

typedef unsigned int u32;
typedef unsigned long long u64;
typedef unsigned short u16;
typedef u32 v4u __attribute__((ext_vector_type(4)));
typedef u32 v2u __attribute__((ext_vector_type(2)));
typedef float f4 __attribute__((ext_vector_type(4)));
typedef float f2 __attribute__((ext_vector_type(2)));
typedef __bf16 bf8 __attribute__((ext_vector_type(8)));
typedef __bf16 bf2 __attribute__((ext_vector_type(2)));
#define LAS __attribute__((address_space(3)))
#define GAS __attribute__((address_space(1)))

namespace {

constexpr int S = 2048, D = 576, F = 4608;
constexpr int NWG = 256, NTHR = 512, NCONV = 144, NMOE = 72;
constexpr int AUX_SC1 = 16;
constexpr u32 SAME = 0xFFFFFFFFu, MAXV = 0xFFFFFFFEu;
constexpr u32 SPIN_CAP = 1u << 16, GATE_CAP = 96;
constexpr u32 GMAGIC = 0x5AC3E900u;
constexpr int OOB = 0x7FFFFF00;
#define U_POST 2
#define WS_AT_END 0
#define HELPERS 1
#define Y_POST 8
#define OUT_AUX 16
#define W2_GATE 2
#define PF_R0 192
#define STAGED 1
#define H_DELAY 30
#define PF_CE 2
#define PF_WHEN 0
#define W1_DEPTH 0
#define DRAIN_FIRST 1
#define U_DELAY 0
#define PF_FIRST 1
#define SK_POS 1
#define ROUTER_DELAY 0
#define W2_EARLY 0
#define TILE_DELAY 4
#define CODE_PF 40
#define NT_MASK 5
constexpr int NT_KV = (NT_MASK & 1) ? 2 : 0, NT_Q = (NT_MASK & 2) ? 2 : 0, NT_H = (NT_MASK & 4) ? 2 : 0, NT_T = (NT_MASK & 8) ? 2 : 0,
              NT_W = (NT_MASK & 16) ? 2 : 0;

constexpr int WS_E = 0;
constexpr int WS_G = 4096;
constexpr int GATE_B = 256 * 256;
constexpr int WS_R = WS_G + 5 * GATE_B;
constexpr int WS_M = WS_R + S * 8;
constexpr int WS_U = WS_M + S * 8;
constexpr int TILE_B = D * 16;
constexpr int WS_P = WS_U + NWG * TILE_B;
constexpr int PART_B = NCONV * 128;
constexpr int WS_END = WS_P + NMOE * PART_B;
constexpr int WS_DBG = 4  __H2__ ;
static_assert(WS_END <= WS_DBG, "ws");
enum { G_R = 0, G_U, G_Y, G_P, G_V, NGATE };
#define ONES8 0x3F803F80u, 0x3F803F80u, 0x3F803F80u, 0x3F803F80u, 0x3F803F80u, 0x3F803F80u, 0x3F803F80u, 0x3F803F80u
#define ONES32 ONES8, ONES8, ONES8, ONES8
__device__ const u32 kOnes[288] __attribute__((aligned(128))) = {ONES32, ONES32, ONES32, ONES32, ONES32, ONES32, ONES32, ONES32, ONES32};

constexpr int HC_B = 2080 * 16;
constexpr int L_H = 0;
constexpr int UL_B = 290 * 16;
constexpr int L_U = L_H + 4 * HC_B;
constexpr int L_STG = L_U + 4 * UL_B;
constexpr int L_MISC = L_STG + TILE_B;
constexpr int L_CNT = L_MISC;
constexpr int L_CAND = L_CNT + 96;
constexpr int L_SEL = L_CAND + 288;
constexpr int L_REC = L_SEL + 32;
constexpr int L_STAT = L_REC + 32;
constexpr int Y1_B = 2048 + 64;
constexpr int L_END = L_STAT + 64;
constexpr int XS_STRIDE = 1184;
constexpr int L_XS = 0;
constexpr int L_RED = L_XS + 8 * XS_STRIDE;
constexpr int L_HB = L_RED + 6 * 8 * 128 * 4;
constexpr int HB_STRIDE = 160;
static_assert(L_END + 1024 <= 160 * 1024, "lds");
static_assert(L_HB + 8 * HB_STRIDE <= L_U, "lds");
static_assert(L_END - L_MISC == 512, "zero fill");

__device__ __forceinline__ constexpr int xrow_off(int t) { return L_U + (t / 3) * UL_B + 512 + (t % 3) * (D * 2); }
enum { C_DRAIN = 0, C_STAT, C_TRUST, C_GU, C_SEL, C_H, C_U, C_Y, C_XS, C_RED, C_HB, C_GY, C_PUB, C_TAG, C_QQ, C_U2, C_Y1, C_W1, C_DN, C_H0, C_VER, C_REDO, C_OK };

__device__ __forceinline__ float lo16(u32 x) { return __uint_as_float(x << 16); }
__device__ __forceinline__ float hi16(u32 x) { return __uint_as_float(x & 0xFFFF0000u); }
__device__ __forceinline__ u32 pk2(float a, float b) {
  bf2 p = {(__bf16)a, (__bf16)b};
  return __builtin_bit_cast(u32, p);
}
__device__ __forceinline__ float rbf(float a) { return lo16(pk2(a, a)); }
__device__ __forceinline__ float dot2(u32 a, u32 b, float c) {
  return __builtin_amdgcn_fdot2_f32_bf16(__builtin_bit_cast(bf2, a), __builtin_bit_cast(bf2, b), c, false);
}
template <int CTRL> __device__ __forceinline__ u32 dppu(u32 x) {
  return (u32)__builtin_amdgcn_mov_dpp((int)x, CTRL, 0xf, 0xf, true);
}
template <int CTRL> __device__ __forceinline__ float dppf(float x) { return __uint_as_float(dppu<CTRL>(__float_as_uint(x))); }
__device__ __forceinline__ float xrow_sum(float x) {
  auto s = __builtin_amdgcn_permlane16_swap(__float_as_uint(x), __float_as_uint(x), false, false);
  x = __uint_as_float(s[0]) + __uint_as_float(s[1]);
  auto t = __builtin_amdgcn_permlane32_swap(__float_as_uint(x), __float_as_uint(x), false, false);
  return __uint_as_float(t[0]) + __uint_as_float(t[1]);
}
__device__ __forceinline__ u32 umax(u32 a, u32 b) { return a > b ? a : b; }
__device__ __forceinline__ u32 umin(u32 a, u32 b) { return a < b ? a : b; }
__device__ __forceinline__ u32 umed3(u32 a, u32 b, u32 c) { return umax(umin(a, b), umin(umax(a, b), c)); }
__device__ __forceinline__ u32 xrow_umin(u32 x) {
  auto s = __builtin_amdgcn_permlane16_swap(x, x, false, false);
  x = umin(s[0], s[1]);
  auto t = __builtin_amdgcn_permlane32_swap(x, x, false, false);
  return umin(t[0], t[1]);
}
__device__ __forceinline__ u32 wave_umax(u32 x) {
  x = umax(x, dppu<0xB1>(x));
  x = umax(x, dppu<0x4E>(x));
  x = umax(x, dppu<0x141>(x));
  x = umax(x, dppu<0x140>(x));
  auto s = __builtin_amdgcn_permlane16_swap(x, x, false, false);
  x = umax(s[0], s[1]);
  auto t = __builtin_amdgcn_permlane32_swap(x, x, false, false);
  return umax(t[0], t[1]);
}
__device__ __forceinline__ void lds_signal(LAS char* sm, int c) {
  u64 keep;
  asm volatile("s_waitcnt lgkmcnt(0)\n\ts_mov_b64 %0, exec\n\ts_mov_b64 exec, 1\n\tds_add_u32 %1, %2\n\ts_mov_b64 exec, %0"
               : "=&s"(keep) : "v"((u32)(size_t)(sm + L_CNT + 4 * c)), "v"(1u) : "memory");
}
template <int SLEEP = 0> __device__ __forceinline__ void lds_wait(LAS char* sm, int c, u32 target) {
  u32 v, sv;
  if (SLEEP == 0)
    asm volatile("1:\n\tds_read_b32 %0, %2\n\ts_waitcnt lgkmcnt(0)\n\tv_readfirstlane_b32 %1, %0\n\ts_cmp_lt_u32 %1, %3\n\ts_cbranch_scc1 1b"
                 : "=&v"(v), "=&s"(sv) : "v"((u32)(size_t)(sm + L_CNT + 4 * c)), "s"(target) : "memory", "scc");
  else
    asm volatile("1:\n\ts_sleep %4\n\tds_read_b32 %0, %2\n\ts_waitcnt lgkmcnt(0)\n\tv_readfirstlane_b32 %1, %0\n\ts_cmp_lt_u32 %1, %3\n\ts_cbranch_scc1 1b"
                 : "=&v"(v), "=&s"(sv) : "v"((u32)(size_t)(sm + L_CNT + 4 * c)), "s"(target), "n"(SLEEP) : "memory", "scc");
}
__device__ __forceinline__ bool lds_arrive(LAS char* sm, int c, u32 last) {
  u64 keep;
  u32 v, sv;
  asm volatile("s_mov_b64 %0, exec\n\ts_mov_b64 exec, 1\n\tds_add_rtn_u32 %1, %3, %4\n\ts_waitcnt lgkmcnt(0)\n\tv_readfirstlane_b32 %2, %1\n\ts_mov_b64 exec, %0"
               : "=&s"(keep), "=&v"(v), "=&s"(sv) : "v"((u32)(size_t)(sm + L_CNT + 4 * c)), "v"(1u) : "memory");
  return sv == last;
}
__device__ __forceinline__ void wg_barrier() { asm volatile("s_waitcnt lgkmcnt(0)\n\ts_barrier" ::: "memory"); }
__device__ __forceinline__ u32 umax3(u32 a, u32 b, u32 c) { return umax(umax(a, b), c); }
__device__ __forceinline__ u32 vmax4(v4u g, u32 acc) { return umax3(umax3(g.x, g.y, acc), g.z, g.w); }
__device__ __forceinline__ f4 mfma16(v4u a, v4u b, f4 c) {
  return __builtin_amdgcn_mfma_f32_16x16x32_bf16(__builtin_bit_cast(bf8, a), __builtin_bit_cast(bf8, b), c, 0, 0, 0);
}
__device__ __forceinline__ __amdgpu_buffer_rsrc_t mkrsrc(const void* p, int bytes) {
  return __builtin_amdgcn_make_buffer_rsrc((void*)p, 0, bytes, 0x00020000);
}

#define STAMP(i) do {} while (0)
#define STAMPC(i) do {} while (0)
#define FLUSH() do {} while (0)
#define NOTE(i, v) do {} while (0)
constexpr int L_TOTAL = L_END;

struct Ctx {
  const u16 *q, *k, *v, *shk, *hk, *dsk, *wr, *wg, *wu, *wd;
  u16* out;
  char* ws;
  const char* code;
  __amdgpu_buffer_rsrc_t rws;
  LAS char* sm;
  int tid, lane, wave, b, C, cg, mw;
  bool is_conv;
  u32 t0;
};

__device__ __forceinline__ int gate_off(int gate, int rep, int shard) { return WS_G + gate * GATE_B + (rep * 16 + shard) * 256; }
__device__ __forceinline__ u32 gate_add(const Ctx& P, int off, int val) {
  return (u32)__builtin_amdgcn_raw_ptr_buffer_atomic_add_i32(val, P.rws, off, 0, 0);
}
__device__ __forceinline__ u32 gate_post(const Ctx& P, int gate, bool on, u32 inc) {
  return gate_add(P, (on && P.lane < 16) ? gate_off(gate, P.lane, P.b & 15) : OOB, (int)inc);
}
__device__ __forceinline__ void gate_confirm(const Ctx& P, int gate, u32 old, u32 inc) {
  bool bad = P.lane < 16;
  _Pragma("nounroll") for (u32 spin = 0; spin < SPIN_CAP; spin++) {
    const u32 d = old - GMAGIC;
    bad = bad && ((d & 0xF0u) != 0u || d != (d & 0xFu) * inc);
    if (__builtin_expect(!__any(bad), 1)) break;
    NOTE(26, spin + 1);
    __builtin_amdgcn_s_sleep(2);
    old = gate_add(P, bad ? gate_off(gate, P.lane, P.b & 15) : OOB, (int)inc);
  }
}
__device__ __forceinline__ void gate_wait(const Ctx& P, int gate, u32 expect, u32 inc, u32 cap = GATE_CAP) {
  const int off = P.lane < 16 ? gate_off(gate, P.b >> 4, P.lane) : OOB;
  const u32 want = P.lane < 16 ? GMAGIC + expect * inc : 0u;
  _Pragma("nounroll") for (u32 spin = 0; spin < cap; spin++)
    if (__all(gate_add(P, off, 0) == want)) break;
}
__device__ __forceinline__ u32 gate_inc(u32 tag) { return 1u + (tag << 8); }
__device__ __forceinline__ void post_u_complete(const Ctx& P, u32 inc) {
  lds_wait<1>(P.sm, C_DRAIN, 5);
  gate_confirm(P, G_V, gate_post(P, G_V, true, inc), inc);
}

struct Row { v2u a0, a1; u32 a2; };
template <int AUX> __device__ __forceinline__ Row load_row(__amdgpu_buffer_rsrc_t r, int soff, int lane) {
  Row x;
  x.a0 = __builtin_amdgcn_raw_buffer_load_b64(r, lane * 8, soff, AUX);
  x.a1 = __builtin_amdgcn_raw_buffer_load_b64(r, lane * 8 + 512, soff, AUX);
  x.a2 = __builtin_amdgcn_raw_buffer_load_b16(r, lane * 2 + 1024, soff, AUX);
  return x;
}
__device__ __forceinline__ void hcopy_store(LAS char* dst, v4u lo, v4u hi, bool first) {
  u32 d[8], o[7];
#pragma unroll
  for (int e = 0; e < 4; e++) { d[e] = lo[e]; d[4 + e] = hi[e]; }
#pragma unroll
  for (int e = 0; e < 7; e++) o[e] = __builtin_amdgcn_alignbit(d[e + 1], d[e], 16);
  constexpr int BASE[8] = {0, 260, 519, 778, 1037, 1296, 1556, 1815};
#pragma unroll
  for (int e = 0; e < 4; e++) {
    v4u a = {d[e], d[e + 1], d[e + 2], d[e + 3]}, c = {o[e], o[e + 1], o[e + 2], o[e + 3]};
    if (e > 0 || first) *(LAS v4u*)(dst + BASE[2 * e] * 16) = a;
    *(LAS v4u*)(dst + BASE[2 * e + 1] * 16) = c;
  }
}
__device__ __forceinline__ u32 touch_w1(const Ctx& P, int mat, int blk);
__device__ __forceinline__ u32 hcopy_first(const Ctx& P, int ch0) {
  const __amdgpu_buffer_rsrc_t rh = mkrsrc(P.hk + (size_t)(4 * P.cg + ch0) * S, 2 * S * 2);
  v4u lo[8];
#pragma unroll
  for (int r = 0; r < 8; r++) lo[r] = __builtin_amdgcn_raw_buffer_load_b128(rh, P.lane * 16 + (r & 3) * 1024, (r >> 2) * 4096, NT_H);
  __builtin_amdgcn_sched_barrier(0);
  STAMP(12);
  LAS char* dst = P.sm + L_H + ch0 * HC_B + (P.lane + 2) * 16;
#pragma unroll
  for (int r = 0; r < 8; r++) *(LAS v4u*)(dst + (r >> 2) * HC_B + (r & 3) * 1024) = lo[r];
  lds_signal(P.sm, C_H0);
  u32 t = 0, t2 = 0;
  if (HELPERS != 0 && PF_WHEN == 0 && PF_CE >= 1) t = touch_w1(P, ch0 >> 1, 25 + (P.b >> 3));
  if (HELPERS != 0 && PF_WHEN == 0 && PF_CE >= 2) t2 = touch_w1(P, ch0 >> 1, 43 + (P.b >> 3));
  __builtin_amdgcn_sched_barrier(0);
  STAMP(1);
  {
    const int c = P.lane & 1, m1 = (P.lane >> 1) & 1;
    const v4u z = {0u, 0u, 0u, 0u};
    v4u h0 = *(const LAS v4u*)(P.sm + L_H + (ch0 + c) * HC_B + 32);
    h0 = m1 ? h0 : z;
    if (P.lane < 4) hcopy_store(P.sm + L_H + (ch0 + c) * HC_B + m1 * 16, z, h0, true);
  }
  return t | t2;
}
template <int NR> __device__ __forceinline__ void hcopy_rest(const Ctx& P, int r0) {
  v4u lo[NR], hi[NR];
  LAS char* dst[NR];
#pragma unroll
  for (int r = 0; r < NR; r++) {
    dst[r] = P.sm + L_H + ((r0 + r) >> 2) * HC_B + ((r0 + r) & 3) * 1024 + (P.lane + 2) * 16;
    lo[r] = *(const LAS v4u*)dst[r];
    hi[r] = *(const LAS v4u*)(dst[r] + 16);
  }
#pragma unroll
  for (int r = 0; r < NR; r++) hcopy_store(dst[r], lo[r], hi[r], false);
  lds_signal(P.sm, C_H);
}

template <class FN> __device__ __forceinline__ void ffn_gate_up(const Ctx& P, FN&& between) {
  LAS char* sm = P.sm;
  const int lane = P.lane, wave = P.wave, n16 = lane & 15, g4 = lane >> 4;
  v4u W1[3][8];
  const GAS char* wp = (const GAS char*)(n16 < 8 ? P.wg : P.wu) + ((size_t)(96 * wave + 8 * g4) * F + 64 * P.mw + 8 * (n16 & 7)) * 2;
  if (wave >= PF_FIRST) lds_wait<1>(sm, wave < 2 ? C_PUB : C_W1, wave < 2 ? 1 : 2);
  __builtin_amdgcn_sched_barrier(0);
#pragma unroll
  for (int i = 0; i < 3; i++)
#pragma unroll
    for (int jj = 0; jj < 8; jj++) {
      const GAS v4u* a = (const GAS v4u*)(wp + (size_t)(32 * i + jj) * (F * 2));
      W1[i][jj] = NT_W ? __builtin_nontemporal_load(a) : *a;
    }
  __builtin_amdgcn_sched_barrier(0);
  lds_signal(sm, C_H0);
  STAMP(3);
  v4u Bf[3][8];
#pragma unroll
  for (int i = 0; i < 3; i++)
#pragma unroll
    for (int e = 0; e < 8; e++) {
#pragma unroll
      for (int d = 0; d < 4; d++)
        Bf[i][e][d] = __builtin_amdgcn_perm(W1[i][2 * d + 1][e >> 1], W1[i][2 * d][e >> 1], (e & 1) ? 0x07060302u : 0x05040100u);
      asm volatile("" : "+v"(Bf[i][e]));
    }
  STAMP(17);
  lds_wait<1>(sm, C_XS, 2);
  STAMP(5);
  f4 acc[8];
#pragma unroll
  for (int e = 0; e < 8; e++) acc[e] = (f4){0.f, 0.f, 0.f, 0.f};
#pragma unroll
  for (int i = 0; i < 3; i++) {
    const v4u A = *(const LAS v4u*)(sm + L_XS + (n16 & 7) * XS_STRIDE + (96 * wave + 32 * i + 8 * g4) * 2);
#pragma unroll
    for (int e = 0; e < 8; e++) acc[e] = mfma16(A, Bf[i][e], acc[e]);
  }
  {
    LAS char* dst = sm + L_RED + ((wave * 8 + 4 * (g4 & 1) + (g4 & 2)) * 128 + 8 * n16) * 4;
#pragma unroll
    for (int r = 0; r < 2; r++) {
      f4 a, c;
#pragma unroll
      for (int e = 0; e < 4; e++) { a[e] = g4 >= 2 ? acc[e][2 + r] : acc[e][r]; c[e] = g4 >= 2 ? acc[4 + e][2 + r] : acc[4 + e][r]; }
      *(LAS f4*)(dst + r * 512) = a;
      *(LAS f4*)(dst + r * 512 + 16) = c;
    }
  }
  lds_signal(sm, C_RED);
  STAMP(18);
  between();
  if (wave >= 2) {
    const int tk = 2 * (wave - 2) + (lane >> 5);
    const LAS char* src = sm + L_RED + (tk * 128 + 2 * (lane & 31)) * 4;
    lds_wait(sm, C_RED, 6);
    f2 G = {0.f, 0.f}, U = {0.f, 0.f};
#pragma unroll
    for (int w = 0; w < 6; w++) {
      G += *(const LAS f2*)(src + w * 4096);
      U += *(const LAS f2*)(src + w * 4096 + 256);
    }
    const u32 gb = pk2(G.x, G.y), ub = pk2(U.x, U.y);
    const float g0 = lo16(gb), g1 = hi16(gb);
    const u32 sg = pk2(g0 * __builtin_amdgcn_rcpf(1.0f + __expf(-g0)), g1 * __builtin_amdgcn_rcpf(1.0f + __expf(-g1)));
    *(LAS u32*)(sm + L_HB + tk * HB_STRIDE + (lane & 31) * 4) = pk2(lo16(sg) * lo16(ub), hi16(sg) * hi16(ub));
    lds_signal(sm, C_HB);
    STAMP(15);
  }
}

struct Down {
  v4u W2[2][8];
  __device__ __forceinline__ void load(const Ctx& P, int cu) {
    const int n16 = P.lane & 15, g4 = P.lane >> 4;
    const __amdgpu_buffer_rsrc_t rd = mkrsrc(P.wd + (size_t)64 * P.mw * D, 64 * D * 2);
    const int o2 = (cu == 4 && n16 >= 8) ? OOB : (8 * g4 * D + 128 * cu + 8 * n16) * 2;
    lds_wait<1>(P.sm, C_H0, 6);
    __builtin_amdgcn_sched_barrier(0);
#pragma unroll
    for (int ks = 0; ks < 2; ks++)
#pragma unroll
      for (int jj = 0; jj < 8; jj++)
        W2[ks][jj] = __builtin_amdgcn_raw_buffer_load_b128(rd, o2 + (jj & 3) * (D * 2), (32 * ks + (jj & 4)) * (D * 2), NT_W);
    __builtin_amdgcn_sched_barrier(0);
    STAMP(4);
  }
  __device__ __forceinline__ void run(const Ctx& P, int cu) {
    LAS char* sm = P.sm;
    const int n16 = P.lane & 15, g4 = P.lane >> 4;
    const u32 tag = *(const LAS u32*)(sm + L_REC), tag12 = tag & 0xFFFu;
    __builtin_amdgcn_sched_barrier(0);
    v4u Bd[2][8];
#pragma unroll
    for (int ks = 0; ks < 2; ks++)
#pragma unroll
      for (int e = 0; e < 8; e++) {
#pragma unroll
        for (int d = 0; d < 4; d++)
          Bd[ks][e][d] = __builtin_amdgcn_perm(W2[ks][2 * d + 1][e >> 1], W2[ks][2 * d][e >> 1], (e & 1) ? 0x07060302u : 0x05040100u);
        asm volatile("" : "+v"(Bd[ks][e]));
      }
    const int off = WS_P + P.mw * PART_B + (32 * cu + 2 * (n16 & 7)) * 128 + (n16 >> 3) * 64 + g4 * 16;
    STAMP(19);
    lds_wait(sm, C_HB, 4);
    const v4u A0 = *(const LAS v4u*)(sm + L_HB + (n16 & 7) * HB_STRIDE + g4 * 16);
    const v4u A1 = *(const LAS v4u*)(sm + L_HB + (n16 & 7) * HB_STRIDE + 64 + g4 * 16);
    if (lds_arrive(sm, C_DN, 4)) (void)gate_post(P, G_P, true, gate_inc(tag));
    STAMP(20);
    f4 ad[8];
#pragma unroll
    for (int e = 0; e < 8; e++) ad[e] = mfma16(A0, Bd[0][e], (f4){0.f, 0.f, 0.f, 0.f});
#pragma unroll
    for (int e = 0; e < 8; e++) ad[e] = mfma16(A1, Bd[1][e], ad[e]);
#pragma unroll
    for (int hh = 0; hh < 2; hh++) {
      v4u s0, s1;
#pragma unroll
      for (int e = 0; e < 4; e++) {
        const f4 a = ad[4 * hh + e];
        const u32 x = ((__float_as_uint(g4 >= 2 ? a[2] : a[0]) + 0x800u) & 0xFFFFF000u) | tag12;
        const u32 y = ((__float_as_uint(g4 >= 2 ? a[3] : a[1]) + 0x800u) & 0xFFFFF000u) | tag12;
        s0[e] = (u32)__builtin_amdgcn_update_dpp((int)x, (int)y, 0x128, 0xf, 0xc, false);
        s1[e] = (u32)__builtin_amdgcn_update_dpp((int)y, (int)x, 0x128, 0xf, 0x3, false);
      }
      __builtin_amdgcn_raw_buffer_store_b128(s0, P.rws, off + hh * 128, 0, AUX_SC1);
      if (cu != 4) __builtin_amdgcn_raw_buffer_store_b128(s1, P.rws, off + hh * 128 + 2048, 0, AUX_SC1);
    }
    STAMP(21);
  }
};

__device__ __forceinline__ u32 touch_w1(const Ctx& P, int mat, int blk) {
  const int r7 = (P.lane * 57) >> 9, c9 = P.lane - 9 * r7;
  return __builtin_amdgcn_raw_buffer_load_b32(mkrsrc(__builtin_amdgcn_readfirstlane(mat) ? P.wu : P.wg, D * F * 2), P.lane < 63 ? r7 * (F * 2) + c9 * 128 : OOB,
                                              (PF_R0 + 7 * blk) * (F * 2) + (P.b & 7) * 1152, NT_T);
}
__device__ __forceinline__ u32 touch_w2(const Ctx& P, int unit) {
  return __builtin_amdgcn_raw_buffer_load_b32(mkrsrc(P.wd, D * F * 2), unit < 81 ? P.lane * 128 : OOB,
                                              (P.b & 7) * (9 * 64 * D * 2) + unit * 8192, NT_T);
}

__device__ __forceinline__ void publish_y(const Ctx& P, const LAS char* yb, int ystride, int tile0, int t, int step, int n) {
  const int gch = t & 3;
  const int goff = WS_U + (tile0 + (t >> 2)) * TILE_B + (4 * P.cg + gch) * 16;
#pragma unroll
  for (int it = 0; it < n; it++) {
    const v4u y4 = *(const LAS v4u*)(yb + gch * ystride + (it * step + (t >> 2)) * 16);
    const v4u u4 = *(const LAS v4u*)(P.sm + L_U + gch * UL_B + (32 + tile0 + it * step + (t >> 2)) * 16);
    v4u w4;
#pragma unroll
    for (int e = 0; e < 4; e++) w4[e] = (y4[e] == u4[e]) ? SAME : y4[e];
    __builtin_amdgcn_raw_buffer_store_b128(w4, P.rws, goff, it * step * TILE_B, AUX_SC1);
  }
}

__device__ __forceinline__ void gather_half(const Ctx& P, int tile0, v4u (&g)[8]) {
  const int goff = WS_U + (tile0 + (P.lane >> 2)) * TILE_B + (4 * P.cg + (P.lane & 3)) * 16;
#pragma unroll
  for (int it = 0; it < 8; it++) g[it] = __builtin_amdgcn_raw_buffer_load_b128(P.rws, goff, it * 16 * TILE_B, AUX_SC1);
}

__device__ __forceinline__ void verify_half(const Ctx& P, int tile0) {
  LAS char* sm = P.sm;
  lds_wait<1>(sm, C_TRUST, 1);
  v4u g[8];
  gather_half(P, tile0, g);
  LAS char* ud = sm + L_U + (P.lane & 3) * UL_B + (32 + tile0 + (P.lane >> 2)) * 16;
  u32 df = 0;
#pragma unroll
  for (int it = 0; it < 8; it++) {
    const v4u x = *(const LAS v4u*)(ud + it * 256) ^ g[it];
    df |= x.x | x.y;
    df |= x.z | x.w;
  }
  if (__builtin_expect(__any(df != 0), 0)) {
#pragma unroll
    for (int it = 0; it < 8; it++) *(LAS v4u*)(ud + it * 256) = g[it];
    lds_signal(sm, C_REDO);
  }
  lds_signal(sm, C_OK);
}

__device__ __forceinline__ void conv_phase(const Ctx& P, u32 inc) {
  LAS char* sm = P.sm;
  const int tid = P.tid, lane = P.lane, wave = P.wave, n16 = lane & 15, g4 = lane >> 4, gch = tid & 3;
  const int goff = WS_U + (tid >> 2) * TILE_B + (4 * P.cg + gch) * 16;
  LAS char* ud = sm + L_U + gch * UL_B + (32 + (tid >> 2)) * 16;
  const int p1 = n16 + 1, kap = p1 & 7;
  const int base16 = 258 * kap + ((0x98654320u >> (4 * kap)) & 15);
  const LAS char* pa = sm + L_H + wave * HC_B + (base16 + g4 + (p1 >> 3)) * 16;
  const LAS char* pb = sm + L_U + wave * UL_B + (32 + 2 * n16 + 1 - g4 - 28) * 16;
  lds_wait<1>(sm, C_H0, 2);
  hcopy_rest<3>(P, 3 * wave);
  STAMP(17);
  asm volatile("s_waitcnt vmcnt(0)" ::: "memory");
  lds_signal(sm, C_DRAIN);
  lds_wait<1>(sm, C_QQ, 5);
  lds_wait<1>(sm, C_GU, 1);
  __builtin_amdgcn_s_setprio(2);
  STAMP(3);
  if (U_DELAY) __builtin_amdgcn_s_sleep(U_DELAY);
  STAMP(4);
  v4u g[2];
#pragma unroll
  for (int it = 0; it < 2; it++) g[it] = __builtin_amdgcn_raw_buffer_load_b128(P.rws, goff, it * 64 * TILE_B, AUX_SC1);
  *(LAS v4u*)ud = g[0];
  *(LAS v4u*)(ud + 1024) = g[1];
  lds_signal(sm, C_U);
  STAMP(5);
  lds_wait(sm, C_H, 6);
  lds_wait(sm, C_U, 4);
  STAMP(6);
  f4 acc[8];
  v4u A[2][8], B[2][4];
  _Pragma("nounroll") for (int pass = 0; pass < 2; pass++) {
#pragma unroll
  for (int t = 0; t < 8; t++) acc[t] = (f4){0.f, 0.f, 0.f, 0.f};
#pragma unroll
  for (int a = 0; a < 8; a++) {
    A[0][a] = *(const LAS v4u*)(pa + a * 512);
    if (a < 4) B[0][a] = *(const LAS v4u*)(pb + a * 512 + 7 * 64);
  }
#pragma unroll
  for (int bb = 0; bb < 8; bb++) {
    __builtin_amdgcn_sched_barrier(0);
    if (bb == 7) {
      lds_wait(sm, C_U2, 1);
      __builtin_amdgcn_sched_barrier(0);
    }
#pragma unroll
    for (int a = 0; a < 8; a++) {
      if (bb < 7) {
        A[(bb + 1) & 1][a] = *(const LAS v4u*)(pa + a * 512 + (bb + 1) * 64);
        if (a < 4) B[(bb + 1) & 1][a] = *(const LAS v4u*)(pb + a * 512 + (6 - bb) * 64);
      } else if (a < 4) {
        A[0][a] = *(const LAS v4u*)(pa + a * 512);
        B[0][a] = *(const LAS v4u*)(pb + (4 + a) * 512 + 7 * 64);
      }
    }
#pragma unroll
    for (int t = 0; t < 8; t++)
#pragma unroll
      for (int c = 0; c < 4; c++)
        if (c <= t) acc[t] = mfma16(A[bb & 1][t - c], B[bb & 1][c], acc[t]);
#pragma unroll
    for (int i = 0; i < (bb < 7 ? 12 : 8); i++) {
      __builtin_amdgcn_sched_group_barrier(0x8, 2, 0);
      __builtin_amdgcn_sched_group_barrier(0x100, 1, 0);
    }
    if (bb < 7) __builtin_amdgcn_sched_group_barrier(0x8, 2, 0);
    else __builtin_amdgcn_sched_group_barrier(0x8, 10, 0);
  }
#pragma unroll
  for (int bb = 0; bb < 8; bb++) {
    __builtin_amdgcn_sched_barrier(0);
    if (bb == 1) {
      LAS char* yb = sm + L_STG + wave * Y1_B + n16 * 32 + g4 * 8;
#pragma unroll
      for (int t = 0; t < 4; t++) {
        v2u y2 = {umin(pk2(acc[t][0], acc[t][1]), MAXV), umin(pk2(acc[t][2], acc[t][3]), MAXV)};
        *(LAS v2u*)(yb + t * 512) = y2;
      }
    }
    if (bb == 2) lds_signal(sm, C_Y1);
    if (bb == Y_POST) {
      (void)gate_post(P, G_Y, wave == 0 && pass == 0, inc);
      __builtin_amdgcn_sched_barrier(0);
    }
    if (bb < 7) {
#pragma unroll
      for (int a = 0; a < 4; a++) {
        A[(bb + 1) & 1][a] = *(const LAS v4u*)(pa + a * 512 + (bb + 1) * 64);
        B[(bb + 1) & 1][a] = *(const LAS v4u*)(pb + (4 + a) * 512 + (6 - bb) * 64);
      }
    }
#pragma unroll
    for (int t = 0; t < 4; t++)
#pragma unroll
      for (int c = 0; c <= t; c++) acc[4 + t] = mfma16(A[bb & 1][t - c], B[bb & 1][c], acc[4 + t]);
    if (bb < 7) {
#pragma unroll
      for (int i = 0; i < 8; i++) {
        __builtin_amdgcn_sched_group_barrier(0x8, 1, 0);
        __builtin_amdgcn_sched_group_barrier(0x100, 1, 0);
      }
      __builtin_amdgcn_sched_group_barrier(0x8, 2, 0);
    }
  }
  __builtin_amdgcn_sched_barrier(0);
    lds_wait(sm, C_OK, 2);
    if (__builtin_expect(__builtin_amdgcn_readfirstlane(((const LAS u32*)(sm + L_CNT))[C_REDO]) == 0u, 1)) break;
    NOTE(26, 7);
  }
  if (Y_POST == 8 && wave == 0) (void)gate_post(P, G_Y, true, inc);
  {
    LAS char* yb = sm + L_H + wave * (HC_B + 64) + n16 * 32 + g4 * 8;
#pragma unroll
    for (int t = 0; t < 4; t++) {
      v2u y2 = {umin(pk2(acc[4 + t][0], acc[4 + t][1]), MAXV), umin(pk2(acc[4 + t][2], acc[4 + t][3]), MAXV)};
      *(LAS v2u*)(yb + t * 512) = y2;
    }
  }
  lds_signal(sm, C_Y);
  STAMP(7);
  lds_wait(sm, C_Y, 4);
  publish_y(P, sm + L_H, HC_B + 64, 128, tid, 64, 2);
  STAMP(8);
}

struct EarlyX { v4u xa[9], wb[9]; };
struct Early {
  u32 kk[11], vv[8], dd;
  v4u sk;
  u64 epoch;
  u32 cx;
};

__device__ __forceinline__ void tile_wave(const Ctx& P, const Early& E, const bool MOE) {
  LAS char* sm = P.sm;
  const int tid = P.tid, lane = P.lane, wave = P.wave, C = P.C;
  const bool act = tid < 288;
  const int j4 = min(tid, 287) * 4;
  const int row0 = 8 * C * (D * 2);
  const u32 (&kk)[11] = E.kk;
  const u32 (&vv)[8] = E.vv;
  const v4u sk = E.sk;
  u32 qq[8];
  u32 gold, uinc;
  bool poster;
  u32 up[2][4];
  {
    f2 w[4], kf[11], ks[8];
    w[0] = (f2){lo16(sk.x), lo16(sk.z)}; w[1] = (f2){hi16(sk.x), hi16(sk.z)};
    w[2] = (f2){lo16(sk.y), lo16(sk.w)}; w[3] = (f2){hi16(sk.y), hi16(sk.w)};
#pragma unroll
    for (int i = 0; i < 11; i++) kf[i] = (f2){lo16(kk[i]), hi16(kk[i])};
#pragma unroll
    for (int t = 0; t < 8; t++) {
      ks[t] = __builtin_elementwise_fma(kf[t + 3], w[0], __builtin_elementwise_fma(kf[t + 2], w[1], __builtin_elementwise_fma(kf[t + 1], w[2], kf[t] * w[3])));
      asm volatile("" : "+v"(ks[t]));
    }
    __builtin_amdgcn_sched_barrier(0);
    lds_wait(sm, C_TAG, 1);
    uinc = gate_inc(*(const LAS u32*)(sm + L_REC));
    STAMP(12);
#pragma unroll
    for (int t = 0; t < 8; t++) ks[t] *= (f2){lo16(vv[t]), hi16(vv[t])};
#pragma unroll
    for (int i = 0; i < 4; i++) {
      up[0][i] = umin(pk2(ks[7 - 2 * i].x, ks[6 - 2 * i].x), MAXV);
      up[1][i] = umin(pk2(ks[7 - 2 * i].y, ks[6 - 2 * i].y), MAXV);
    }
    STAMP(1);
    LAS char* stg = sm + L_STG + wave * 2048;
    v4u p0 = {up[0][0], up[0][1], up[0][2], up[0][3]}, p1 = {up[1][0], up[1][1], up[1][2], up[1][3]};
    if (act) { *(LAS v4u*)(stg + lane * 32) = p0; *(LAS v4u*)(stg + lane * 32 + 16) = p1; }
    asm volatile("" ::: "memory");
    p0 = *(const LAS v4u*)(stg + lane * 16);
    p1 = *(const LAS v4u*)(stg + (wave < 4 ? 1024 : 0) + lane * 16);
    STAMP(13);
    const int so = WS_U + C * TILE_B + wave * 2048 + lane * 16;
    __builtin_amdgcn_raw_buffer_store_b128(p0, P.rws, so, 0, AUX_SC1);
    if (wave < 4) __builtin_amdgcn_raw_buffer_store_b128(p1, P.rws, so + 1024, 0, AUX_SC1);
  }
  __builtin_amdgcn_sched_barrier(0);
  poster = lds_arrive(sm, C_VER, 4);
  if (poster) gold = gate_post(P, G_U, true, uinc);
  STAMP(2);
  lds_wait(sm, C_STAT, 1);
#pragma unroll
  for (int i = 0; i < 8; i++) qq[i] = *(const LAS u32*)(sm + xrow_off(i) + j4);
  lds_signal(sm, C_QQ);
  u32 dd = E.dd;
  asm volatile("" : "+v"(dd));
  float xn[2][8], c0[2][8];
  auto prepare = [&]()
  {
    const float d0 = lo16(dd), d1 = hi16(dd);
#pragma unroll
    for (int s2 = 0; s2 < 4; s2++) {
      const f4 st = *(const LAS f4*)(sm + L_STAT + s2 * 16);
#pragma unroll
      for (int e = 0; e < 2; e++) {
        const int s = 2 * s2 + e;
        const float xa = lo16(qq[s]), xb = hi16(qq[s]);
        const float na = fmaf(xa, st[2 * e], st[2 * e + 1]), nb = fmaf(xb, st[2 * e], st[2 * e + 1]);
        const u32 pa = up[0][(7 - s) >> 1], pb = up[1][(7 - s) >> 1];
        const float ua = ((7 - s) & 1) ? hi16(pa) : lo16(pa), ub = ((7 - s) & 1) ? hi16(pb) : lo16(pb);
        xn[0][s] = na; xn[1][s] = nb;
        c0[0][s] = fmaf(na, fmaf(ua, d0, 1.0f), xa);
        c0[1][s] = fmaf(nb, fmaf(ub, d1, 1.0f), xb);
        asm volatile("" : "+v"(xn[0][s]), "+v"(xn[1][s]), "+v"(c0[0][s]), "+v"(c0[1][s]));
      }
    }
  };
  prepare();
  if (poster) gate_confirm(P, G_U, gold, uinc);
  if (!(P.is_conv && wave < 4)) {
    asm volatile("s_waitcnt vmcnt(0)" ::: "memory");
    lds_signal(sm, C_DRAIN);
  }
  STAMP(14);
  if (MOE) {
    ffn_gate_up(P, []() {});
    if (wave < 2) { Down dn; dn.load(P, wave); dn.run(P, wave); }
  } else {
   if (P.is_conv) {
    if (wave < 4) {
      conv_phase(P, uinc);
    } else {
      lds_wait<1>(sm, C_QQ, 5);
      lds_wait<1>(sm, C_GU, 1);
      __builtin_amdgcn_s_sleep(3 + U_DELAY);
      v4u g[8];
      gather_half(P, 128, g);
#pragma unroll
      for (int it = 0; it < 8; it++) *(LAS v4u*)(sm + L_U + (lane & 3) * UL_B + (32 + 128 + it * 16 + (lane >> 2)) * 16) = g[it];
      lds_signal(sm, C_U2);
      STAMP(5);
      gate_wait(P, G_V, 16u, uinc, SPIN_CAP);
      lds_signal(sm, C_TRUST);
      STAMP(4);
      verify_half(P, 128);
      STAMP(6);
      lds_wait<2>(sm, C_Y1, 4);
      lds_wait<1>(sm, C_OK, 2);
      lds_wait<1>(sm, C_Y1, __builtin_amdgcn_readfirstlane(((const LAS u32*)(sm + L_CNT))[C_REDO]) ? 8 : 4);
      publish_y(P, sm + L_STG, Y1_B, 0, lane, 32, 4);
    }
  } else {
    STAMP(5);
   }
  }
  lds_wait<1>(sm, C_SEL, 1);
  const u32 skip = __builtin_amdgcn_readfirstlane(*(const LAS u32*)(sm + L_REC + 4));
  const __amdgpu_buffer_rsrc_t ro = mkrsrc(P.out, S * D * 2);
  const int oo = act ? j4 : OOB;
  const int uoff = WS_U + C * TILE_B + j4 * 8;
  lds_wait<1>(sm, C_GY, 1);
  STAMP(9);
  v4u y0, y1;
  _Pragma("nounroll") for (u32 spin = 0; spin < SPIN_CAP; spin++) {
    asm volatile("" ::: "memory");
    y0 = __builtin_amdgcn_raw_buffer_load_b128(P.rws, uoff, 0, AUX_SC1);
    y1 = __builtin_amdgcn_raw_buffer_load_b128(P.rws, uoff + 16, 0, AUX_SC1);
    bool ok = true;
#pragma unroll
    for (int e = 0; e < 4; e++) ok = ok & (y0[e] != up[0][e]) & (y1[e] != up[1][e]);
    if (__builtin_expect(__all(ok), 1)) break;
    NOTE(25, spin + 1);
    __builtin_amdgcn_s_sleep(4);
  }
  STAMP(16);
#pragma unroll
  for (int e = 0; e < 4; e++) {
    const u32 a = y0[e] == SAME ? up[0][e] : y0[e], c = y1[e] == SAME ? up[1][e] : y1[e];
    const u32 o0 = pk2(fmaf(xn[0][2 * e], lo16(a), c0[0][2 * e]), fmaf(xn[1][2 * e], lo16(c), c0[1][2 * e]));
    const u32 o1 = pk2(fmaf(xn[0][2 * e + 1], hi16(a), c0[0][2 * e + 1]), fmaf(xn[1][2 * e + 1], hi16(c), c0[1][2 * e + 1]));
    const int so = row0 + (e >> 1) * 4 * (D * 2);
    if (!((skip >> (2 * e)) & 1)) __builtin_amdgcn_raw_buffer_store_b32(o0, ro, oo + ((2 * e) & 3) * (D * 2), so, OUT_AUX);
    if (!((skip >> (2 * e + 1)) & 1)) __builtin_amdgcn_raw_buffer_store_b32(o1, ro, oo + ((2 * e + 1) & 3) * (D * 2), so, OUT_AUX);
  }
}

__device__ __forceinline__ void route(const Ctx& P, u32 tag, u64 epoch, const int ROLE) {
  LAS char* sm = P.sm;
  const int lane = P.lane, wave = P.wave;
  (void)wave;
  v4u g[16];
  if (ROLE == 2) __builtin_amdgcn_s_setprio(0);
  if (ROLE == 0) {
    if (HELPERS != 0 && PF_WHEN != 0 && PF_CE != 0) {
      lds_wait<1>(sm, C_VER, PF_WHEN);
      u32 t[4] = {0u, 0u, 0u, 0u};
#pragma unroll
      for (int i = 0; i < 2 * PF_CE; i++) t[i] = touch_w1(P, i & 1, 25 + 18 * (i >> 1) + (P.b >> 3));
      asm volatile("" :: "v"(t[0]), "v"(t[1]), "v"(t[2]), "v"(t[3]));
      STAMP(18);
    }
    gate_wait(P, G_U, 16u, gate_inc(tag));
    lds_signal(sm, C_GU);
    STAMP(15);
    __builtin_amdgcn_s_setprio(0);
    post_u_complete(P, gate_inc(tag));
    STAMP(13);
    lds_wait<1>(sm, C_U, 4);
  } else {
    gate_wait(P, ROLE == 1 ? G_R : G_U, 16u, gate_inc(tag));
  }
  STAMP(3);
  _Pragma("nounroll") for (u32 spin = 0; spin < SPIN_CAP; spin++) {
    asm volatile("" ::: "memory");
#pragma unroll
    for (int i = 0; i < 16; i++)
      g[i] = __builtin_amdgcn_raw_buffer_load_b128(P.rws, WS_R + lane * 16 + (i & 3) * 1024, (i >> 2) * 4096, AUX_SC1);
    if (spin == 0) lds_signal(sm, C_PUB);
    u32 bad = 0;
#pragma unroll
    for (int i = 0; i < 16; i++) {
      bad = __builtin_amdgcn_bitop3_b32(bad, g[i].y, tag, 0xF6);
      bad = __builtin_amdgcn_bitop3_b32(bad, g[i].w, tag, 0xF6);
    }
    if (__builtin_expect(__all(bad == 0), 1)) break;
    NOTE(24, spin + 1);
    __builtin_amdgcn_s_sleep(4);
  }
  STAMP(4);
  if (P.b == 0 && lane == 0) __hip_atomic_store((GAS u64*)(P.ws + WS_E), epoch, __ATOMIC_RELAXED, __HIP_MEMORY_SCOPE_AGENT);
  u32 k1 = umax(g[0].x, g[0].z), k2 = umin(g[0].x, g[0].z), k3 = 0;
#pragma unroll
  for (int i = 2; i < 32; i++) {
    const u32 x = (i & 1) ? g[i >> 1].z : g[i >> 1].x;
    k3 = umed3(k2, k3, x);
    k2 = umed3(k1, k2, x);
    k1 = umax(k1, x);
  }
  u32 tau = umax(k1, dppu<0xB1>(k1));
  tau = umax(tau, dppu<0x4E>(tau));
  tau = umax(tau, dppu<0x141>(tau));
  tau = umin(tau, dppu<0x140>(tau));
  tau = xrow_umin(tau);
  LAS u32* cand = (LAS u32*)(sm + L_CAND);
  const u64 m1 = __ballot(k1 >= tau), m2 = __ballot(k2 >= tau), m3 = __ballot(k3 >= tau);
  const u32 n1 = __builtin_popcountll(m1), n2 = n1 + __builtin_popcountll(m2), n = n2 + __builtin_popcountll(m3);
  bool slow = n > 64;
  if (__builtin_expect(!slow, 1)) {
    const u32 p1 = __builtin_amdgcn_mbcnt_hi((u32)(m1 >> 32), __builtin_amdgcn_mbcnt_lo((u32)m1, 0u));
    const u32 p2 = __builtin_amdgcn_mbcnt_hi((u32)(m2 >> 32), __builtin_amdgcn_mbcnt_lo((u32)m2, n1));
    const u32 p3 = __builtin_amdgcn_mbcnt_hi((u32)(m3 >> 32), __builtin_amdgcn_mbcnt_lo((u32)m3, n2));
    cand[k1 >= tau ? p1 : 64] = k1;
    cand[k2 >= tau ? p2 : 64] = k2;
    cand[k3 >= tau ? p3 : 64] = k3;
    asm volatile("" ::: "memory");
    const u32 mine = cand[lane];
    u32 rank = 0;
    _Pragma("nounroll") for (u32 jj = 0; jj < n; jj += 8) {
      const v4u c4 = *(const LAS v4u*)(cand + jj), d4 = *(const LAS v4u*)(cand + jj + 4);
      rank += (c4.x > mine) + (c4.y > mine) + (c4.z > mine) + (c4.w > mine);
      rank += (d4.x > mine) + (d4.y > mine) + (d4.z > mine) + (d4.w > mine);
    }
    const bool in = (u32)lane < n && rank < 8;
    if (in) ((LAS u32*)(sm + L_SEL))[rank] = mine;
    slow = __any(in && (u32)lane >= n2);
  }
  if (__builtin_expect(slow, 0)) {
    _Pragma("nounroll") for (int r = 0; r < 8; r++) {
      u32 mx = 0;
#pragma unroll
      for (int i = 0; i < 16; i++) mx = umax3(mx, g[i].x, g[i].z);
      mx = wave_umax(mx);
#pragma unroll
      for (int i = 0; i < 16; i++) { g[i].x = g[i].x == mx ? 0u : g[i].x; g[i].z = g[i].z == mx ? 0u : g[i].z; }
      if (lane == 0) ((LAS u32*)(sm + L_SEL))[r] = mx;
    }
  }
  asm volatile("" ::: "memory");
  {
    const u32 tk = 2047u - (((LAS u32*)(sm + L_SEL))[lane & 7] & 2047u);
    u32 mk = (int)(tk >> 3) == P.C ? 1u << (tk & 7) : 0u;
    mk |= dppu<0xB1>(mk);
    mk |= dppu<0x4E>(mk);
    mk |= dppu<0x141>(mk);
    if (lane == 0) *(LAS u32*)(sm + L_REC + 4) = mk;
  }
  lds_signal(sm, C_SEL);
  STAMP(5);
}

__device__ __forceinline__ u32 load_msd(const Ctx& P, int tok, u32 tag) {
  v2u g;
  _Pragma("nounroll") for (u32 spin = 0; spin < SPIN_CAP; spin++) {
    asm volatile("" ::: "memory");
    g = __builtin_amdgcn_raw_buffer_load_b64(P.rws, WS_M + tok * 8, 0, AUX_SC1);
    if (__builtin_expect(__all(g.y == tag), 1)) break;
    __builtin_amdgcn_s_sleep(4);
  }
  return g.x;
}

__device__ __forceinline__ void routed_rows(const Ctx& P) {
  LAS char* sm = P.sm;
  const int lane = P.lane, wave = P.wave, C = P.C, cg = P.cg;
  (void)wave; (void)C; (void)cg;
  const __amdgpu_buffer_rsrc_t rq = mkrsrc(P.q, S * D * 2);
  {
    const int cw = wave - 6;
    const u32 tag = *(const LAS u32*)(sm + L_REC);
    const v4u s4 = *(const LAS v4u*)(sm + L_SEL + cw * 16);
    Row rr[4];
#pragma unroll
    for (int e = 0; e < 4; e++) {
      const int tok = 2047 - (int)(__builtin_amdgcn_readfirstlane(s4[e]) & 2047u);
      rr[e] = load_row<0>(rq, tok * (D * 2), lane);
    }
    const u32 ks = (lane & 2) ? ((lane & 1) ? s4.w : s4.z) : ((lane & 1) ? s4.y : s4.x);
    const int mtok = 2047 - (int)(ks & 2047u);
    v2u mg = __builtin_amdgcn_raw_buffer_load_b64(P.rws, WS_M + mtok * 8, 0, AUX_SC1);
    __builtin_amdgcn_sched_barrier(0);
    lds_signal(sm, C_W1);
    __builtin_amdgcn_sched_barrier(0);
    u32 msd = mg.x;
    if (__builtin_expect(!__all(mg.y == tag), 0)) msd = load_msd(P, mtok, tag);
    STAMP(15);
    const float rsl = __builtin_amdgcn_rcpf(lo16(msd)), nml = -hi16(msd) * rsl;
#pragma unroll
    for (int e = 0; e < 4; e++) {
      const float rs = __uint_as_float(__builtin_amdgcn_readlane(__float_as_uint(rsl), e));
      const float nm = __uint_as_float(__builtin_amdgcn_readlane(__float_as_uint(nml), e));
      LAS char* dst = sm + L_XS + (cw * 4 + e) * XS_STRIDE;
      const Row& x = rr[e];
      v2u a = {pk2(fmaf(lo16(x.a0.x), rs, nm), fmaf(hi16(x.a0.x), rs, nm)), pk2(fmaf(lo16(x.a0.y), rs, nm), fmaf(hi16(x.a0.y), rs, nm))};
      v2u c = {pk2(fmaf(lo16(x.a1.x), rs, nm), fmaf(hi16(x.a1.x), rs, nm)), pk2(fmaf(lo16(x.a1.y), rs, nm), fmaf(hi16(x.a1.y), rs, nm))};
      *(LAS v2u*)(dst + lane * 8) = a;
      *(LAS v2u*)(dst + 512 + lane * 8) = c;
      *(LAS u16*)(dst + 1024 + lane * 2) = (u16)pk2(fmaf(lo16(x.a2), rs, nm), 0.f);
    }
    lds_signal(sm, C_XS);
    STAMP(6);
    __builtin_amdgcn_s_setprio(0);
    Down dn;
    dn.load(P, wave - 4);
    dn.run(P, wave - 4);
  }
}

__device__ __forceinline__ void aux_wave(const Ctx& P, const int ROLE) {
  LAS char* sm = P.sm;
  const int lane = P.lane, wave = P.wave, C = P.C, cg = P.cg;
  (void)wave; (void)C; (void)cg;
  u32 tag;
    if (CODE_PF && wave == 6) asm volatile("s_waitcnt vmcnt(0)" ::: "v255");
    if (ROLE != 0 && wave == 5) {
      __builtin_amdgcn_s_setprio(3);
      lds_wait<1>(sm, C_TAG, 1);
      post_u_complete(P, gate_inc(*(const LAS u32*)(sm + L_REC)));
      __builtin_amdgcn_s_setprio(0);
      STAMP(13);
    }
    if (ROLE == 2 && HELPERS && wave == 6) {
      lds_wait<1>(sm, C_VER, 5);
      const int idx = (P.b - NCONV - NMOE) >> 3;
      u32 ta = touch_w1(P, 0, idx), tb = touch_w1(P, 1, idx);
#pragma unroll
      for (int i = 1; i < 5; i++) {
        const u32 tc = touch_w1(P, 0, i * 5 + idx), td = touch_w1(P, 1, i * 5 + idx);
        asm volatile("" :: "v"(ta), "v"(tb));
        ta = tc; tb = td;
      }
      asm volatile("" :: "v"(ta), "v"(tb));
      STAMP(5);
    }
    if (ROLE == 0) {
      __builtin_amdgcn_s_sleep(H_DELAY);
      const u32 tt = hcopy_first(P, 2 * (wave - 5));
      lds_wait(sm, C_H0, 2);
      hcopy_rest<2>(P, 12 + 2 * (wave - 5));
      asm volatile("" :: "v"(tt));
      STAMP(3);
      if (wave == 5) {
        lds_wait<1>(sm, C_U, 4);
        verify_half(P, 0);
        STAMP(6);
        lds_wait<2>(sm, C_Y1, 4);
        lds_wait<1>(sm, C_OK, 2);
        lds_wait<1>(sm, C_Y1, __builtin_amdgcn_readfirstlane(((const LAS u32*)(sm + L_CNT))[C_REDO]) ? 8 : 4);
        publish_y(P, sm + L_STG + 256, Y1_B, 16, lane, 32, 4);
      } else if (HELPERS) {
        lds_wait<1>(sm, C_U2, 1);
        u32 t[9];
#pragma unroll
        for (int i = 0; i < 4; i++) {
          const int u = 50 + 36 * PF_CE + i * 18 + (P.b >> 3);
          t[5 + i] = (i >= 4 - 2 * PF_CE) ? 0u : touch_w1(P, u & 1, u >> 1);
        }
#pragma unroll
        for (int i = 0; i < 5; i++) t[i] = touch_w2(P, i * 18 + (P.b >> 3));
        asm volatile("" :: "v"(t[5]), "v"(t[6]), "v"(t[7]), "v"(t[8]));
        STAMP(5);
        asm volatile("" :: "v"(t[0]), "v"(t[1]), "v"(t[2]), "v"(t[3]), "v"(t[4]));
        STAMP(6);
      }
    }
    if (wave == 5) {
      if (ROLE == 1) {
        Down dn;
        ffn_gate_up(P, [&]() { dn.load(P, 4); });
        dn.run(P, 4);
        gate_wait(P, G_Y, 9u, gate_inc(*(const LAS u32*)(sm + L_REC)));
        lds_signal(sm, C_GY);
      }
      return;
    }
    lds_wait<1>(sm, C_TAG, 1);
    tag = *(const LAS u32*)(sm + L_REC);
    if (ROLE == 1) { lds_wait<1>(sm, C_SEL, 1); __builtin_amdgcn_s_setprio(3); }
  if (ROLE != 1) {
    gate_wait(P, G_Y, 9u, gate_inc(tag));
    lds_signal(sm, C_GY);
    STAMP(7);
  }
}

__device__ __forceinline__ void router_stats(const Ctx& P, Early& E, const EarlyX& X, const int ROLE) {
  LAS char* sm = P.sm;
  const int lane = P.lane, C = P.C;
  u32 tag;
    const int m = lane & 15, g4 = lane >> 4;
    u64 epoch = E.epoch;
    const v4u (&xa)[9] = X.xa;
    const v4u (&wb)[9] = X.wb;
    asm volatile("" : "+v"(epoch) :: "memory");
    epoch += 1;
    tag = (u32)epoch;
    if (lane == 0) *(LAS u32*)(sm + L_REC) = tag;
    lds_signal(sm, C_TAG);
    STAMP(1);
    f4 gx = {0.f, 0.f, 0.f, 0.f}, gw = {0.f, 0.f, 0.f, 0.f}, gs = {0.f, 0.f, 0.f, 0.f};
#pragma unroll
    for (int i = 0; i < 9; i++) gx = mfma16(xa[i], xa[i], gx);
    __builtin_amdgcn_sched_barrier(0);
    const u32 gold = gate_post(P, G_R, true, gate_inc(tag));
    STAMP(12);
    __builtin_amdgcn_sched_barrier(0);
#pragma unroll
    for (int i = 0; i < 9; i++) { gw = mfma16(wb[i], xa[i], gw); gs = mfma16(wb[i], wb[i], gs); }
    float sxx = (m & 2) ? ((m & 1) ? gx[3] : gx[2]) : ((m & 1) ? gx[1] : gx[0]);
    sxx = __uint_as_float(__builtin_amdgcn_ds_bpermute((m + (m & 12) * 4) * 4, __float_as_uint(sxx)));
    float sxw = m < 8 ? gw[0] : gw[1], sx = gw[2];
    sxw += dppf<0x128>(sxw);
    sx += dppf<0x128>(sx);
    sxx += dppf<0x128>(sxx);
    const float sw = __uint_as_float(__builtin_amdgcn_readlane(__float_as_uint(gs[0] + gs[1]), 2));
    const float mean = sx * (1.0f / D);
    const float var = fmaxf(fmaf(-mean, mean, sxx * (1.0f / D)), 0.f);
    const float mu = rbf(mean);
    const float sd = rbf(__builtin_amdgcn_sqrtf(rbf(rbf(var) + 1e-5f)));
    const float rs = __builtin_amdgcn_rcpf(sd);
    const u32 r = pk2(0.f, fmaf(-mu, sw, sxw) * rs);
    u32 key = ((r ^ (((int)r >> 31) | 0x80000000u)) & 0xFFFF0000u) | (u32)(2047 - (8 * C + (m & 7)));
    u32 msd = __float_as_uint(mu) | (__float_as_uint(sd) >> 16);
    asm volatile("" : "+v"(key), "+v"(msd));
    const v2u rec = {m < 8 ? key : msd, tag};
    const int ro = lane < 16 ? (m < 8 ? WS_R : WS_M) + (8 * C + (m & 7)) * 8 : OOB;
    __builtin_amdgcn_raw_buffer_store_b64(rec, P.rws, ro, 0, AUX_SC1);
    STAMP(2);
    if (lane < 8) { f2 st = {rs, -mu * rs}; *(LAS f2*)(sm + L_STAT + m * 8) = st; }
    {
      const int t = m & 7, t3 = (t * 11) >> 5;
      LAS char* xd = sm + L_U + t3 * UL_B + 512 + (t - 3 * t3) * (D * 2) + (m >> 3) * 64 + g4 * 16;
#pragma unroll
      for (int i = 0; i < 9; i++) *(LAS v4u*)(xd + i * 128) = xa[i];
    }
    lds_signal(sm, C_STAT);
    E.epoch = epoch;
    E.cx = gold;
}

__device__ __forceinline__ void router_wave(const Ctx& P, const Early& E, const int ROLE) {
  LAS char* sm = P.sm;
  const int lane = P.lane, wave = P.wave, C = P.C, cg = P.cg;
  (void)wave; (void)C; (void)cg;
  u32 tag;
    const u64 epoch = E.epoch;
    tag = (u32)epoch;
    const u32 gold = E.cx;
    gate_confirm(P, G_R, gold, gate_inc(tag));
    route(P, tag, epoch, ROLE);
  if (ROLE == 0) {
    const int pi = lane & 7, ti = 4 * (pi & 1) + (pi & 2) + (pi >> 2);
    const u32 kx = ((LAS u32*)(sm + L_SEL))[ti];
    const int tok = 2047 - (int)(kx & 2047u);
    const v2u x2 = *(const GAS v2u*)((const GAS char*)P.q + (size_t)tok * (D * 2) + cg * 8);
    const v2u d2 = *(const GAS v2u*)((const GAS char*)P.dsk + cg * 8);
    const u32 msd = load_msd(P, tok, tag);
    const float rs = __builtin_amdgcn_rcpf(lo16(msd)), nm = -hi16(msd) * rs;
    const u32 rb = kx & 0xFFFF0000u;
    const float r = __uint_as_float((rb & 0x80000000u) ? (rb ^ 0x80000000u) : (~rb & 0xFFFF0000u));
    const float rsig = rbf(__builtin_amdgcn_rcpf(1.0f + __expf(-r)));
    STAMP(6);
    lds_wait<2>(sm, C_Y, 4);
    float o[4];
#pragma unroll
    for (int e = 0; e < 4; e++) {
      const float y = lo16(*(const LAS u16*)(sm + (tok < 1024 ? L_STG + e * Y1_B : L_H + e * (HC_B + 64) - 2048) + tok * 2));
      const float u = lo16(*(const LAS u16*)(sm + L_U + e * UL_B + (32 + (tok >> 3)) * 16 + (7 - (tok & 7)) * 2));
      const float x = (e & 1) ? hi16(x2[e >> 1]) : lo16(x2[e >> 1]);
      const float dk = (e & 1) ? hi16(d2[e >> 1]) : lo16(d2[e >> 1]);
      const float n = fmaf(x, rs, nm);
      o[e] = fmaf(n, fmaf(u, dk, y), x) + n;
      asm volatile("" : "+v"(o[e]));
    }
    gate_wait(P, G_P, lane < 8 ? 5u : 4u, gate_inc(tag));
    STAMP(7);
    v4u g[9];
    const u32 tag12 = tag & 0xFFFu;
    _Pragma("nounroll") for (u32 spin = 0; spin < SPIN_CAP; spin++) {
      asm volatile("" ::: "memory");
#pragma unroll
      for (int i = 0; i < 9; i++)
        g[i] = __builtin_amdgcn_raw_buffer_load_b128(P.rws, WS_P + (lane >> 3) * PART_B + cg * 128 + pi * 16, 8 * i * PART_B, AUX_SC1);
      u32 bad = 0;
#pragma unroll
      for (int i = 0; i < 9; i++) {
        g[i] ^= tag12;
        bad |= g[i].x | g[i].y;
        bad |= g[i].z | g[i].w;
      }
      if (__builtin_expect(__all((bad & 0xFFFu) == 0), 1)) break;
      NOTE(25, spin + 1);
      __builtin_amdgcn_s_sleep(4);
    }
    STAMP(8);
    f4 s4 = __builtin_bit_cast(f4, g[0]);
#pragma unroll
    for (int i = 1; i < 9; i++) s4 += __builtin_bit_cast(f4, g[i]);
#pragma unroll
    for (int e = 0; e < 4; e++) {
      float sv = s4[e];
      sv += dppf<0x128>(sv);
      o[e] = fmaf(rbf(xrow_sum(sv)), rsig, o[e]);
    }
    if (lane < 8) {
      v2u o2 = {pk2(o[0], o[1]), pk2(o[2], o[3])};
      __builtin_amdgcn_raw_buffer_store_b64(o2, mkrsrc(P.out, S * D * 2), tok * (D * 2) + cg * 8, 0, OUT_AUX);
    }
  }
}

__global__ __launch_bounds__(NTHR, 2) void fused_kernel(
    const u16* __restrict__ q, const u16* __restrict__ k, const u16* __restrict__ v, const u16* __restrict__ shk,
    const u16* __restrict__ hk, const u16* __restrict__ dsk, const u16* __restrict__ wr, const u16* __restrict__ wg,
    const u16* __restrict__ wu, const u16* __restrict__ wd, u16* __restrict__ out, char* ws) {
  extern __shared__ __attribute__((aligned(16))) char smem_[];
  Ctx P;
  P.q = q; P.k = k; P.v = v; P.shk = shk; P.hk = hk; P.dsk = dsk; P.wr = wr; P.wg = wg; P.wu = wu; P.wd = wd;
  P.out = out; P.ws = ws;
  u64 pc;
  asm volatile("s_getpc_b64 %0" : "=s"(pc));
  P.code = (const char*)(pc & ~(u64)127);
  P.sm = (LAS char*)smem_;
  P.tid = threadIdx.x; P.lane = P.tid & 63;
  P.wave = __builtin_amdgcn_readfirstlane(P.tid >> 6);
  const int b = blockIdx.x;
  P.b = b;
  P.C = (b & 7) * 32 + (b >> 3);
  P.is_conv = b < NCONV;
  const bool is_moe = !P.is_conv && b < NCONV + NMOE;
  P.cg = (b & 7) * 18 + (b >> 3);
  P.mw = (b & 7) * 9 + ((b - NCONV) >> 3);
  P.rws = mkrsrc(ws, WS_END);
  const int lane = P.lane, wave = P.wave;
  (void)lane;
  STAMP(0); STAMPC(30);
  Early E;
  if (wave == 6) {
    constexpr int NL = (CODE_PF * 8 + 31) / 32;
    if (CODE_PF)
      asm volatile("global_load_dword v255, %0, off" :: "v"(P.code + min(((b >> 3) * NL + min(lane, NL - 1)) * 128, CODE_PF * 1024 - 128)) : "v255");
    const v4u z = {0u, 0u, 0u, 0u};
    *(LAS v4u*)(P.sm + L_U + (lane >> 4) * UL_B + (lane & 15) * 16) = z;
    *(LAS v4u*)(P.sm + L_U + (lane >> 4) * UL_B + 256 + (lane & 15) * 16) = z;
    ((LAS u32*)(P.sm + L_MISC))[lane] = 0u;
    ((LAS u32*)(P.sm + L_MISC))[64 + lane] = 0u;
    asm volatile("s_waitcnt lgkmcnt(0)" ::: "memory");
  }
  asm volatile("s_barrier" ::: "memory");
  asm volatile("" :: "s"(q), "s"(k), "s"(v), "s"(shk), "s"(hk), "s"(dsk), "s"(wr), "s"(wg), "s"(wu), "s"(wd), "s"(out), "s"(ws));
  STAMP(11);
  const int row0 = 8 * P.C * (D * 2);
  EarlyX X;
  if (P.wave == 7) {
    __builtin_amdgcn_s_setprio(3);
    const int m = lane & 15, g4 = lane >> 4;
    E.epoch = __hip_atomic_load((GAS u64*)(P.ws + WS_E), __ATOMIC_RELAXED, __HIP_MEMORY_SCOPE_AGENT);
    if (ROUTER_DELAY) __builtin_amdgcn_s_sleep(ROUTER_DELAY);
    __builtin_amdgcn_sched_barrier(0);
    const __amdgpu_buffer_rsrc_t rq = mkrsrc(P.q, S * D * 2);
    const int xo = (m & 7) * (D * 2) + (m >> 3) * 64 + g4 * 16;
#pragma unroll
    for (int i = 0; i < 9; i++) X.xa[i] = __builtin_amdgcn_raw_buffer_load_b128(rq, xo + i * 128, row0, NT_Q);
    __builtin_amdgcn_sched_barrier(0);
    const size_t pw = (size_t)P.wr + m * 64, po = (size_t)kOnes;
    const GAS char* wp = (const GAS char*)(m < 2 ? pw : po) + g4 * 16;
#pragma unroll
    for (int i = 0; i < 9; i++) X.wb[i] = *(const GAS v4u*)(wp + i * 128);
  }
  asm volatile("" : "+s"(P.wave));
  if (P.wave == 5) {
    const v4u g4 = {GMAGIC, GMAGIC, GMAGIC, GMAGIC};
    __builtin_amdgcn_raw_buffer_store_b128(g4, P.rws, lane < 8 * NGATE ? gate_off(lane >> 3, b >> 4, b & 15) + (lane & 7) * 16 : OOB, 0, AUX_SC1);
  }
  asm volatile("" : "+s"(P.wave));
  if (P.wave < 5) {
    const int j4 = min(P.tid, 287) * 4;
    const __amdgpu_buffer_rsrc_t rk = mkrsrc(P.k, S * D * 2), rv = mkrsrc(P.v, S * D * 2);
    if (TILE_DELAY) __builtin_amdgcn_s_sleep(TILE_DELAY);
    __builtin_amdgcn_sched_barrier(0);
#pragma unroll
    for (int i = 0; i < 3; i++) E.kk[i] = __builtin_amdgcn_raw_buffer_load_b32(rk, row0 + (i - 3) * (D * 2) + j4, 0, NT_KV);
#pragma unroll
    for (int i = 0; i < 8; i++) E.kk[3 + i] = __builtin_amdgcn_raw_buffer_load_b32(rk, j4 + (i & 3) * (D * 2), row0 + (i & 4) * (D * 2), NT_KV);
    __builtin_amdgcn_sched_barrier(0);
    if (SK_POS == 1) E.sk = *(const GAS v4u*)((const GAS char*)P.shk + j4 * 4);
    __builtin_amdgcn_sched_barrier(0);
#pragma unroll
    for (int i = 0; i < 8; i++) E.vv[i] = __builtin_amdgcn_raw_buffer_load_b32(rv, j4 + (i & 3) * (D * 2), row0 + (i & 4) * (D * 2), NT_KV);
    __builtin_amdgcn_sched_barrier(0);
    if (SK_POS == 2) E.sk = *(const GAS v4u*)((const GAS char*)P.shk + j4 * 4);
    E.dd = *(const GAS u32*)((const GAS char*)P.dsk + j4);
  }
  const int role = P.is_conv ? 0 : (is_moe ? 1 : 2);
  asm volatile("" : "+s"(P.wave));
  STAMP(10);
  if (P.wave == 7) router_stats(P, E, X, role);
  asm volatile("" : "+s"(P.wave));
  if (P.wave != 7) {
    if (P.wave >= 5) aux_wave(P, role);
    else tile_wave(P, E, is_moe);
  } else {
    router_wave(P, E, role);
  }
  asm volatile("" : "+s"(P.wave));
  if (is_moe && P.wave >= 6) routed_rows(P);
  FLUSH();
}

}

extern "C" void kernel_launch(void* const* d_in, const int* in_sizes, int n_in, void* d_out, int out_size, void* d_ws,
                              size_t ws_size, hipStream_t stream) {
  (void)in_sizes; (void)n_in; (void)out_size; (void)ws_size;
  (void)hipFuncSetAttribute((const void*)fused_kernel, hipFuncAttributeMaxDynamicSharedMemorySize, L_TOTAL);
  fused_kernel<<<NWG, NTHR, L_TOTAL, stream>>>(
      (const u16*)d_in[0], (const u16*)d_in[1], (const u16*)d_in[2], (const u16*)d_in[3], (const u16*)d_in[4],
      (const u16*)d_in[5], (const u16*)d_in[7], (const u16*)d_in[8], (const u16*)d_in[9], (const u16*)d_in[10],
      (u16*)d_out, (char*)d_ws + (WS_AT_END ? ((ws_size - WS_END) & ~(size_t)0xFFFF) : 0));
}
```

```cpp
#include <hip/hip_runtime.h>
#include <hip/hip_bf16.h>

typedef unsigned int u32;
typedef unsigned long long u64;
typedef unsigned short u16;
typedef u32 v4u __attribute__((ext_vector_type(4)));
typedef u32 v2u __attribute__((ext_vector_type(2)));
typedef float f4 __attribute__((ext_vector_type(4)));
typedef float f2 __attribute__((ext_vector_type(2)));
typedef __bf16 bf8 __attribute__((ext_vector_type(8)));
typedef __bf16 bf2 __attribute__((ext_vector_type(2)));
#define LAS __attribute__((address_space(3)))
#define GAS __attribute__((address_space(1)))

namespace {

constexpr int S = 2048, D = 576, F = 4608;
constexpr int NWG = 256, NTHR = 512, NCONV = 144, NMOE = 72;
constexpr int AUX_SC1 = 16;
constexpr u32 SAME = 0xFFFFFFFFu, MAXV = 0xFFFFFFFEu;
constexpr u32 SPIN_CAP = 1u << 16, GATE_CAP = 96;
constexpr u32 GMAGIC = 0x5AC3E900u;
constexpr int OOB = 0x7FFFFF00;
#define U_POST 2
#define WS_AT_END 0
#define HELPERS 1
#define Y_POST 8
#define OUT_AUX 16
#define W2_GATE 2
#define PF_R0 192
#define STAGED 1
#define H_DELAY 30
#define PF_CE 2
#define PF_WHEN 0
#define W1_DEPTH 0
#define DRAIN_FIRST 1
#define U_DELAY 0
#define PF_FIRST 1
#define SK_POS 1
#define ROUTER_DELAY 0
#define W2_EARLY 0
#define TILE_DELAY 4
#define CODE_PF 40
#define NT_MASK 5
constexpr int NT_KV = (NT_MASK & 1) ? 2 : 0, NT_Q = (NT_MASK & 2) ? 2 : 0, NT_H = (NT_MASK & 4) ? 2 : 0, NT_T = (NT_MASK & 8) ? 2 : 0,
              NT_W = (NT_MASK & 16) ? 2 : 0;

constexpr int WS_E = 0;
constexpr int WS_G = 4096;
constexpr int GATE_B = 256 * 256;
constexpr int WS_R = WS_G + 5 * GATE_B;
constexpr int WS_M = WS_R + S * 8;
constexpr int WS_U = WS_M + S * 8;
constexpr int TILE_B = D * 16;
constexpr int WS_P = WS_U + NWG * TILE_B;
constexpr int PART_B = NCONV * 128;
constexpr int WS_END = WS_P + NMOE * PART_B;
constexpr int WS_DBG = 4  __H2__ ;
static_assert(WS_END <= WS_DBG, "ws");
enum { G_R = 0, G_U, G_Y, G_P, G_V, NGATE };
#define ONES8 0x3F803F80u, 0x3F803F80u, 0x3F803F80u, 0x3F803F80u, 0x3F803F80u, 0x3F803F80u, 0x3F803F80u, 0x3F803F80u
#define ONES32 ONES8, ONES8, ONES8, ONES8
__device__ const u32 kOnes[288] __attribute__((aligned(128))) = {ONES32, ONES32, ONES32, ONES32, ONES32, ONES32, ONES32, ONES32, ONES32};

constexpr int HC_B = 2080 * 16;
constexpr int L_H = 0;
constexpr int UL_B = 290 * 16;
constexpr int L_U = L_H + 4 * HC_B;
constexpr int L_STG = L_U + 4 * UL_B;
constexpr int L_MISC = L_STG + TILE_B;
constexpr int L_CNT = L_MISC;
constexpr int L_CAND = L_CNT + 96;
constexpr int L_SEL = L_CAND + 288;
constexpr int L_REC = L_SEL + 32;
constexpr int L_STAT = L_REC + 32;
constexpr int Y1_B = 2048 + 64;
constexpr int L_END = L_STAT + 64;
constexpr int XS_STRIDE = 1184;
constexpr int L_XS = 0;
constexpr int L_RED = L_XS + 8 * XS_STRIDE;
constexpr int L_HB = L_RED + 6 * 8 * 128 * 4;
constexpr int HB_STRIDE = 160;
static_assert(L_END + 1024 <= 160 * 1024, "lds");
static_assert(L_HB + 8 * HB_STRIDE <= L_U, "lds");
static_assert(L_END - L_MISC == 512, "zero fill");

__device__ __forceinline__ constexpr int xrow_off(int t) { return L_U + (t / 3) * UL_B + 512 + (t % 3) * (D * 2); }
enum { C_DRAIN = 0, C_STAT, C_TRUST, C_GU, C_SEL, C_H, C_U, C_Y, C_XS, C_RED, C_HB, C_GY, C_PUB, C_TAG, C_QQ, C_U2, C_Y1, C_W1, C_DN, C_H0, C_VER, C_REDO, C_OK };

__device__ __forceinline__ float lo16(u32 x) { return __uint_as_float(x << 16); }
__device__ __forceinline__ float hi16(u32 x) { return __uint_as_float(x & 0xFFFF0000u); }
__device__ __forceinline__ u32 pk2(float a, float b) {
  bf2 p = {(__bf16)a, (__bf16)b};
  return __builtin_bit_cast(u32, p);
}
__device__ __forceinline__ float rbf(float a) { return lo16(pk2(a, a)); }
__device__ __forceinline__ float dot2(u32 a, u32 b, float c) {
  return __builtin_amdgcn_fdot2_f32_bf16(__builtin_bit_cast(bf2, a), __builtin_bit_cast(bf2, b), c, false);
}
template <int CTRL> __device__ __forceinline__ u32 dppu(u32 x) {
  return (u32)__builtin_amdgcn_mov_dpp((int)x, CTRL, 0xf, 0xf, true);
}
template <int CTRL> __device__ __forceinline__ float dppf(float x) { return __uint_as_float(dppu<CTRL>(__float_as_uint(x))); }
__device__ __forceinline__ float xrow_sum(float x) {
  auto s = __builtin_amdgcn_permlane16_swap(__float_as_uint(x), __float_as_uint(x), false, false);
  x = __uint_as_float(s[0]) + __uint_as_float(s[1]);
  auto t = __builtin_amdgcn_permlane32_swap(__float_as_uint(x), __float_as_uint(x), false, false);
  return __uint_as_float(t[0]) + __uint_as_float(t[1]);
}
__device__ __forceinline__ u32 umax(u32 a, u32 b) { return a > b ? a : b; }
__device__ __forceinline__ u32 umin(u32 a, u32 b) { return a < b ? a : b; }
__device__ __forceinline__ u32 umed3(u32 a, u32 b, u32 c) { return umax(umin(a, b), umin(umax(a, b), c)); }
__device__ __forceinline__ u32 xrow_umin(u32 x) {
  auto s = __builtin_amdgcn_permlane16_swap(x, x, false, false);
  x = umin(s[0], s[1]);
  auto t = __builtin_amdgcn_permlane32_swap(x, x, false, false);
  return umin(t[0], t[1]);
}
__device__ __forceinline__ u32 wave_umax(u32 x) {
  x = umax(x, dppu<0xB1>(x));
  x = umax(x, dppu<0x4E>(x));
  x = umax(x, dppu<0x141>(x));
  x = umax(x, dppu<0x140>(x));
  auto s = __builtin_amdgcn_permlane16_swap(x, x, false, false);
  x = umax(s[0], s[1]);
  auto t = __builtin_amdgcn_permlane32_swap(x, x, false, false);
  return umax(t[0], t[1]);
}
__device__ __forceinline__ void lds_signal(LAS char* sm, int c) {
  u64 keep;
  asm volatile("s_waitcnt lgkmcnt(0)\n\ts_mov_b64 %0, exec\n\ts_mov_b64 exec, 1\n\tds_add_u32 %1, %2\n\ts_mov_b64 exec, %0"
               : "=&s"(keep) : "v"((u32)(size_t)(sm + L_CNT + 4 * c)), "v"(1u) : "memory");
}
template <int SLEEP = 0> __device__ __forceinline__ void lds_wait(LAS char* sm, int c, u32 target) {
  u32 v, sv;
  if (SLEEP == 0)
    asm volatile("1:\n\tds_read_b32 %0, %2\n\ts_waitcnt lgkmcnt(0)\n\tv_readfirstlane_b32 %1, %0\n\ts_cmp_lt_u32 %1, %3\n\ts_cbranch_scc1 1b"
                 : "=&v"(v), "=&s"(sv) : "v"((u32)(size_t)(sm + L_CNT + 4 * c)), "s"(target) : "memory", "scc");
  else
    asm volatile("1:\n\ts_sleep %4\n\tds_read_b32 %0, %2\n\ts_waitcnt lgkmcnt(0)\n\tv_readfirstlane_b32 %1, %0\n\ts_cmp_lt_u32 %1, %3\n\ts_cbranch_scc1 1b"
                 : "=&v"(v), "=&s"(sv) : "v"((u32)(size_t)(sm + L_CNT + 4 * c)), "s"(target), "n"(SLEEP) : "memory", "scc");
}
__device__ __forceinline__ bool lds_arrive(LAS char* sm, int c, u32 last) {
  u64 keep;
  u32 v, sv;
  asm volatile("s_mov_b64 %0, exec\n\ts_mov_b64 exec, 1\n\tds_add_rtn_u32 %1, %3, %4\n\ts_waitcnt lgkmcnt(0)\n\tv_readfirstlane_b32 %2, %1\n\ts_mov_b64 exec, %0"
               : "=&s"(keep), "=&v"(v), "=&s"(sv) : "v"((u32)(size_t)(sm + L_CNT + 4 * c)), "v"(1u) : "memory");
  return sv == last;
}
__device__ __forceinline__ void wg_barrier() { asm volatile("s_waitcnt lgkmcnt(0)\n\ts_barrier" ::: "memory"); }
__device__ __forceinline__ u32 umax3(u32 a, u32 b, u32 c) { return umax(umax(a, b), c); }
__device__ __forceinline__ u32 vmax4(v4u g, u32 acc) { return umax3(umax3(g.x, g.y, acc), g.z, g.w); }
__device__ __forceinline__ f4 mfma16(v4u a, v4u b, f4 c) {
  return __builtin_amdgcn_mfma_f32_16x16x32_bf16(__builtin_bit_cast(bf8, a), __builtin_bit_cast(bf8, b), c, 0, 0, 0);
}
__device__ __forceinline__ __amdgpu_buffer_rsrc_t mkrsrc(const void* p, int bytes) {
  return __builtin_amdgcn_make_buffer_rsrc((void*)p, 0, bytes, 0x00020000);
}

#define DBG_TS 1
#define STAMP(i) do { P.ts[i] = (u32)__builtin_amdgcn_s_memrealtime() | 1u; } while (0)
#define STAMPC(i) do { P.ts[i] = (u32)__builtin_amdgcn_s_memtime(); } while (0)
#define FLUSH() do { STAMP(29); if (P.wave == 7 && (!P.is_conv && P.b < 216)) { const u32 d_ = (P.ts[5] - P.ts[0]) * 4; while ((u32)__builtin_amdgcn_s_memrealtime() - P.ts[0] < d_ && d_ < 20000u) __builtin_amdgcn_s_sleep(1); } } while (0)
#define NOTE(i, v) do { P.ts[i] = (v); } while (0)
constexpr int L_TOTAL = L_END;

struct Ctx {
  const u16 *q, *k, *v, *shk, *hk, *dsk, *wr, *wg, *wu, *wd;
  u16* out;
  char* ws;
  const char* code;
  __amdgpu_buffer_rsrc_t rws;
  LAS char* sm;
  int tid, lane, wave, b, C, cg, mw;
  bool is_conv;
  u32 t0;
  mutable u32 ts[32];
};

__device__ __forceinline__ int gate_off(int gate, int rep, int shard) { return WS_G + gate * GATE_B + (rep * 16 + shard) * 256; }
__device__ __forceinline__ u32 gate_add(const Ctx& P, int off, int val) {
  return (u32)__builtin_amdgcn_raw_ptr_buffer_atomic_add_i32(val, P.rws, off, 0, 0);
}
__device__ __forceinline__ u32 gate_post(const Ctx& P, int gate, bool on, u32 inc) {
  return gate_add(P, (on && P.lane < 16) ? gate_off(gate, P.lane, P.b & 15) : OOB, (int)inc);
}
__device__ __forceinline__ void gate_confirm(const Ctx& P, int gate, u32 old, u32 inc) {
  bool bad = P.lane < 16;
  _Pragma("nounroll") for (u32 spin = 0; spin < SPIN_CAP; spin++) {
    const u32 d = old - GMAGIC;
    bad = bad && ((d & 0xF0u) != 0u || d != (d & 0xFu) * inc);
    if (__builtin_expect(!__any(bad), 1)) break;
    NOTE(26, spin + 1);
    __builtin_amdgcn_s_sleep(2);
    old = gate_add(P, bad ? gate_off(gate, P.lane, P.b & 15) : OOB, (int)inc);
  }
}
__device__ __forceinline__ void gate_wait(const Ctx& P, int gate, u32 expect, u32 inc, u32 cap = GATE_CAP) {
  const int off = P.lane < 16 ? gate_off(gate, P.b >> 4, P.lane) : OOB;
  const u32 want = P.lane < 16 ? GMAGIC + expect * inc : 0u;
  _Pragma("nounroll") for (u32 spin = 0; spin < cap; spin++)
    if (__all(gate_add(P, off, 0) == want)) break;
}
__device__ __forceinline__ u32 gate_inc(u32 tag) { return 1u + (tag << 8); }
__device__ __forceinline__ void post_u_complete(const Ctx& P, u32 inc) {
  lds_wait<1>(P.sm, C_DRAIN, 5);
  gate_confirm(P, G_V, gate_post(P, G_V, true, inc), inc);
}

struct Row { v2u a0, a1; u32 a2; };
template <int AUX> __device__ __forceinline__ Row load_row(__amdgpu_buffer_rsrc_t r, int soff, int lane) {
  Row x;
  x.a0 = __builtin_amdgcn_raw_buffer_load_b64(r, lane * 8, soff, AUX);
  x.a1 = __builtin_amdgcn_raw_buffer_load_b64(r, lane * 8 + 512, soff, AUX);
  x.a2 = __builtin_amdgcn_raw_buffer_load_b16(r, lane * 2 + 1024, soff, AUX);
  return x;
}
__device__ __forceinline__ void hcopy_store(LAS char* dst, v4u lo, v4u hi, bool first) {
  u32 d[8], o[7];
#pragma unroll
  for (int e = 0; e < 4; e++) { d[e] = lo[e]; d[4 + e] = hi[e]; }
#pragma unroll
  for (int e = 0; e < 7; e++) o[e] = __builtin_amdgcn_alignbit(d[e + 1], d[e], 16);
  constexpr int BASE[8] = {0, 260, 519, 778, 1037, 1296, 1556, 1815};
#pragma unroll
  for (int e = 0; e < 4; e++) {
    v4u a = {d[e], d[e + 1], d[e + 2], d[e + 3]}, c = {o[e], o[e + 1], o[e + 2], o[e + 3]};
    if (e > 0 || first) *(LAS v4u*)(dst + BASE[2 * e] * 16) = a;
    *(LAS v4u*)(dst + BASE[2 * e + 1] * 16) = c;
  }
}
__device__ __forceinline__ u32 touch_w1(const Ctx& P, int mat, int blk);
__device__ __forceinline__ u32 hcopy_first(const Ctx& P, int ch0) {
  const __amdgpu_buffer_rsrc_t rh = mkrsrc(P.hk + (size_t)(4 * P.cg + ch0) * S, 2 * S * 2);
  v4u lo[8];
#pragma unroll
  for (int r = 0; r < 8; r++) lo[r] = __builtin_amdgcn_raw_buffer_load_b128(rh, P.lane * 16 + (r & 3) * 1024, (r >> 2) * 4096, NT_H);
  __builtin_amdgcn_sched_barrier(0);
  STAMP(12);
  LAS char* dst = P.sm + L_H + ch0 * HC_B + (P.lane + 2) * 16;
#pragma unroll
  for (int r = 0; r < 8; r++) *(LAS v4u*)(dst + (r >> 2) * HC_B + (r & 3) * 1024) = lo[r];
  lds_signal(P.sm, C_H0);
  u32 t = 0, t2 = 0;
  if (HELPERS != 0 && PF_WHEN == 0 && PF_CE >= 1) t = touch_w1(P, ch0 >> 1, 25 + (P.b >> 3));
  if (HELPERS != 0 && PF_WHEN == 0 && PF_CE >= 2) t2 = touch_w1(P, ch0 >> 1, 43 + (P.b >> 3));
  __builtin_amdgcn_sched_barrier(0);
  STAMP(1);
  {
    const int c = P.lane & 1, m1 = (P.lane >> 1) & 1;
    const v4u z = {0u, 0u, 0u, 0u};
    v4u h0 = *(const LAS v4u*)(P.sm + L_H + (ch0 + c) * HC_B + 32);
    h0 = m1 ? h0 : z;
    if (P.lane < 4) hcopy_store(P.sm + L_H + (ch0 + c) * HC_B + m1 * 16, z, h0, true);
  }
  return t | t2;
}
template <int NR> __device__ __forceinline__ void hcopy_rest(const Ctx& P, int r0) {
  v4u lo[NR], hi[NR];
  LAS char* dst[NR];
#pragma unroll
  for (int r = 0; r < NR; r++) {
    dst[r] = P.sm + L_H + ((r0 + r) >> 2) * HC_B + ((r0 + r) & 3) * 1024 + (P.lane + 2) * 16;
    lo[r] = *(const LAS v4u*)dst[r];
    hi[r] = *(const LAS v4u*)(dst[r] + 16);
  }
#pragma unroll
  for (int r = 0; r < NR; r++) hcopy_store(dst[r], lo[r], hi[r], false);
  lds_signal(P.sm, C_H);
}

template <class FN> __device__ __forceinline__ void ffn_gate_up(const Ctx& P, FN&& between) {
  LAS char* sm = P.sm;
  const int lane = P.lane, wave = P.wave, n16 = lane & 15, g4 = lane >> 4;
  v4u W1[3][8];
  const GAS char* wp = (const GAS char*)(n16 < 8 ? P.wg : P.wu) + ((size_t)(96 * wave + 8 * g4) * F + 64 * P.mw + 8 * (n16 & 7)) * 2;
  if (wave >= PF_FIRST) lds_wait<1>(sm, wave < 2 ? C_PUB : C_W1, wave < 2 ? 1 : 2);
  __builtin_amdgcn_sched_barrier(0);
#pragma unroll
  for (int i = 0; i < 3; i++)
#pragma unroll
    for (int jj = 0; jj < 8; jj++) {
      const GAS v4u* a = (const GAS v4u*)(wp + (size_t)(32 * i + jj) * (F * 2));
      W1[i][jj] = NT_W ? __builtin_nontemporal_load(a) : *a;
    }
  __builtin_amdgcn_sched_barrier(0);
  lds_signal(sm, C_H0);
  STAMP(3);
  v4u Bf[3][8];
#pragma unroll
  for (int i = 0; i < 3; i++)
#pragma unroll
    for (int e = 0; e < 8; e++) {
#pragma unroll
      for (int d = 0; d < 4; d++)
        Bf[i][e][d] = __builtin_amdgcn_perm(W1[i][2 * d + 1][e >> 1], W1[i][2 * d][e >> 1], (e & 1) ? 0x07060302u : 0x05040100u);
      asm volatile("" : "+v"(Bf[i][e]));
    }
  STAMP(17);
  lds_wait<1>(sm, C_XS, 2);
  STAMP(5);
  f4 acc[8];
#pragma unroll
  for (int e = 0; e < 8; e++) acc[e] = (f4){0.f, 0.f, 0.f, 0.f};
#pragma unroll
  for (int i = 0; i < 3; i++) {
    const v4u A = *(const LAS v4u*)(sm + L_XS + (n16 & 7) * XS_STRIDE + (96 * wave + 32 * i + 8 * g4) * 2);
#pragma unroll
    for (int e = 0; e < 8; e++) acc[e] = mfma16(A, Bf[i][e], acc[e]);
  }
  {
    LAS char* dst = sm + L_RED + ((wave * 8 + 4 * (g4 & 1) + (g4 & 2)) * 128 + 8 * n16) * 4;
#pragma unroll
    for (int r = 0; r < 2; r++) {
      f4 a, c;
#pragma unroll
      for (int e = 0; e < 4; e++) { a[e] = g4 >= 2 ? acc[e][2 + r] : acc[e][r]; c[e] = g4 >= 2 ? acc[4 + e][2 + r] : acc[4 + e][r]; }
      *(LAS f4*)(dst + r * 512) = a;
      *(LAS f4*)(dst + r * 512 + 16) = c;
    }
  }
  lds_signal(sm, C_RED);
  STAMP(18);
  between();
  if (wave >= 2) {
    const int tk = 2 * (wave - 2) + (lane >> 5);
    const LAS char* src = sm + L_RED + (tk * 128 + 2 * (lane & 31)) * 4;
    lds_wait(sm, C_RED, 6);
    f2 G = {0.f, 0.f}, U = {0.f, 0.f};
#pragma unroll
    for (int w = 0; w < 6; w++) {
      G += *(const LAS f2*)(src + w * 4096);
      U += *(const LAS f2*)(src + w * 4096 + 256);
    }
    const u32 gb = pk2(G.x, G.y), ub = pk2(U.x, U.y);
    const float g0 = lo16(gb), g1 = hi16(gb);
    const u32 sg = pk2(g0 * __builtin_amdgcn_rcpf(1.0f + __expf(-g0)), g1 * __builtin_amdgcn_rcpf(1.0f + __expf(-g1)));
    *(LAS u32*)(sm + L_HB + tk * HB_STRIDE + (lane & 31) * 4) = pk2(lo16(sg) * lo16(ub), hi16(sg) * hi16(ub));
    lds_signal(sm, C_HB);
    STAMP(15);
  }
}

struct Down {
  v4u W2[2][8];
  __device__ __forceinline__ void load(const Ctx& P, int cu) {
    const int n16 = P.lane & 15, g4 = P.lane >> 4;
    const __amdgpu_buffer_rsrc_t rd = mkrsrc(P.wd + (size_t)64 * P.mw * D, 64 * D * 2);
    const int o2 = (cu == 4 && n16 >= 8) ? OOB : (8 * g4 * D + 128 * cu + 8 * n16) * 2;
    lds_wait<1>(P.sm, C_H0, 6);
    __builtin_amdgcn_sched_barrier(0);
#pragma unroll
    for (int ks = 0; ks < 2; ks++)
#pragma unroll
      for (int jj = 0; jj < 8; jj++)
        W2[ks][jj] = __builtin_amdgcn_raw_buffer_load_b128(rd, o2 + (jj & 3) * (D * 2), (32 * ks + (jj & 4)) * (D * 2), NT_W);
    __builtin_amdgcn_sched_barrier(0);
    STAMP(4);
  }
  __device__ __forceinline__ void run(const Ctx& P, int cu) {
    LAS char* sm = P.sm;
    const int n16 = P.lane & 15, g4 = P.lane >> 4;
    const u32 tag = *(const LAS u32*)(sm + L_REC), tag12 = tag & 0xFFFu;
    __builtin_amdgcn_sched_barrier(0);
    v4u Bd[2][8];
#pragma unroll
    for (int ks = 0; ks < 2; ks++)
#pragma unroll
      for (int e = 0; e < 8; e++) {
#pragma unroll
        for (int d = 0; d < 4; d++)
          Bd[ks][e][d] = __builtin_amdgcn_perm(W2[ks][2 * d + 1][e >> 1], W2[ks][2 * d][e >> 1], (e & 1) ? 0x07060302u : 0x05040100u);
        asm volatile("" : "+v"(Bd[ks][e]));
      }
    const int off = WS_P + P.mw * PART_B + (32 * cu + 2 * (n16 & 7)) * 128 + (n16 >> 3) * 64 + g4 * 16;
    STAMP(19);
    lds_wait(sm, C_HB, 4);
    const v4u A0 = *(const LAS v4u*)(sm + L_HB + (n16 & 7) * HB_STRIDE + g4 * 16);
    const v4u A1 = *(const LAS v4u*)(sm + L_HB + (n16 & 7) * HB_STRIDE + 64 + g4 * 16);
    if (lds_arrive(sm, C_DN, 4)) (void)gate_post(P, G_P, true, gate_inc(tag));
    STAMP(20);
    f4 ad[8];
#pragma unroll
    for (int e = 0; e < 8; e++) ad[e] = mfma16(A0, Bd[0][e], (f4){0.f, 0.f, 0.f, 0.f});
#pragma unroll
    for (int e = 0; e < 8; e++) ad[e] = mfma16(A1, Bd[1][e], ad[e]);
#pragma unroll
    for (int hh = 0; hh < 2; hh++) {
      v4u s0, s1;
#pragma unroll
      for (int e = 0; e < 4; e++) {
        const f4 a = ad[4 * hh + e];
        const u32 x = ((__float_as_uint(g4 >= 2 ? a[2] : a[0]) + 0x800u) & 0xFFFFF000u) | tag12;
        const u32 y = ((__float_as_uint(g4 >= 2 ? a[3] : a[1]) + 0x800u) & 0xFFFFF000u) | tag12;
        s0[e] = (u32)__builtin_amdgcn_update_dpp((int)x, (int)y, 0x128, 0xf, 0xc, false);
        s1[e] = (u32)__builtin_amdgcn_update_dpp((int)y, (int)x, 0x128, 0xf, 0x3, false);
      }
      __builtin_amdgcn_raw_buffer_store_b128(s0, P.rws, off + hh * 128, 0, AUX_SC1);
      if (cu != 4) __builtin_amdgcn_raw_buffer_store_b128(s1, P.rws, off + hh * 128 + 2048, 0, AUX_SC1);
    }
    STAMP(21);
  }
};

__device__ __forceinline__ u32 touch_w1(const Ctx& P, int mat, int blk) {
  const int r7 = (P.lane * 57) >> 9, c9 = P.lane - 9 * r7;
  return __builtin_amdgcn_raw_buffer_load_b32(mkrsrc(__builtin_amdgcn_readfirstlane(mat) ? P.wu : P.wg, D * F * 2), P.lane < 63 ? r7 * (F * 2) + c9 * 128 : OOB,
                                              (PF_R0 + 7 * blk) * (F * 2) + (P.b & 7) * 1152, NT_T);
}
__device__ __forceinline__ u32 touch_w2(const Ctx& P, int unit) {
  return __builtin_amdgcn_raw_buffer_load_b32(mkrsrc(P.wd, D * F * 2), unit < 81 ? P.lane * 128 : OOB,
                                              (P.b & 7) * (9 * 64 * D * 2) + unit * 8192, NT_T);
}

__device__ __forceinline__ void publish_y(const Ctx& P, const LAS char* yb, int ystride, int tile0, int t, int step, int n) {
  const int gch = t & 3;
  const int goff = WS_U + (tile0 + (t >> 2)) * TILE_B + (4 * P.cg + gch) * 16;
#pragma unroll
  for (int it = 0; it < n; it++) {
    const v4u y4 = *(const LAS v4u*)(yb + gch * ystride + (it * step + (t >> 2)) * 16);
    const v4u u4 = *(const LAS v4u*)(P.sm + L_U + gch * UL_B + (32 + tile0 + it * step + (t >> 2)) * 16);
    v4u w4;
#pragma unroll
    for (int e = 0; e < 4; e++) w4[e] = (y4[e] == u4[e]) ? SAME : y4[e];
    __builtin_amdgcn_raw_buffer_store_b128(w4, P.rws, goff, it * step * TILE_B, AUX_SC1);
  }
}

__device__ __forceinline__ void gather_half(const Ctx& P, int tile0, v4u (&g)[8]) {
  const int goff = WS_U + (tile0 + (P.lane >> 2)) * TILE_B + (4 * P.cg + (P.lane & 3)) * 16;
#pragma unroll
  for (int it = 0; it < 8; it++) g[it] = __builtin_amdgcn_raw_buffer_load_b128(P.rws, goff, it * 16 * TILE_B, AUX_SC1);
}

__device__ __forceinline__ void verify_half(const Ctx& P, int tile0) {
  LAS char* sm = P.sm;
  lds_wait<1>(sm, C_TRUST, 1);
  v4u g[8];
  gather_half(P, tile0, g);
  LAS char* ud = sm + L_U + (P.lane & 3) * UL_B + (32 + tile0 + (P.lane >> 2)) * 16;
  u32 df = 0;
#pragma unroll
  for (int it = 0; it < 8; it++) {
    const v4u x = *(const LAS v4u*)(ud + it * 256) ^ g[it];
    df |= x.x | x.y;
    df |= x.z | x.w;
  }
  if (__builtin_expect(__any(df != 0), 0)) {
#pragma unroll
    for (int it = 0; it < 8; it++) *(LAS v4u*)(ud + it * 256) = g[it];
    lds_signal(sm, C_REDO);
  }
  lds_signal(sm, C_OK);
}

__device__ __forceinline__ void conv_phase(const Ctx& P, u32 inc) {
  LAS char* sm = P.sm;
  const int tid = P.tid, lane = P.lane, wave = P.wave, n16 = lane & 15, g4 = lane >> 4, gch = tid & 3;
  const int goff = WS_U + (tid >> 2) * TILE_B + (4 * P.cg + gch) * 16;
  LAS char* ud = sm + L_U + gch * UL_B + (32 + (tid >> 2)) * 16;
  const int p1 = n16 + 1, kap = p1 & 7;
  const int base16 = 258 * kap + ((0x98654320u >> (4 * kap)) & 15);
  const LAS char* pa = sm + L_H + wave * HC_B + (base16 + g4 + (p1 >> 3)) * 16;
  const LAS char* pb = sm + L_U + wave * UL_B + (32 + 2 * n16 + 1 - g4 - 28) * 16;
  lds_wait<1>(sm, C_H0, 2);
  hcopy_rest<3>(P, 3 * wave);
  STAMP(17);
  asm volatile("s_waitcnt vmcnt(0)" ::: "memory");
  lds_signal(sm, C_DRAIN);
  lds_wait<1>(sm, C_QQ, 5);
  lds_wait<1>(sm, C_GU, 1);
  __builtin_amdgcn_s_setprio(2);
  STAMP(3);
  if (U_DELAY) __builtin_amdgcn_s_sleep(U_DELAY);
  STAMP(4);
  v4u g[2];
#pragma unroll
  for (int it = 0; it < 2; it++) g[it] = __builtin_amdgcn_raw_buffer_load_b128(P.rws, goff, it * 64 * TILE_B, AUX_SC1);
  *(LAS v4u*)ud = g[0];
  *(LAS v4u*)(ud + 1024) = g[1];
  lds_signal(sm, C_U);
  STAMP(5);
  lds_wait(sm, C_H, 6);
  lds_wait(sm, C_U, 4);
  STAMP(6);
  f4 acc[8];
  v4u A[2][8], B[2][4];
  _Pragma("nounroll") for (int pass = 0; pass < 2; pass++) {
#pragma unroll
  for (int t = 0; t < 8; t++) acc[t] = (f4){0.f, 0.f, 0.f, 0.f};
#pragma unroll
  for (int a = 0; a < 8; a++) {
    A[0][a] = *(const LAS v4u*)(pa + a * 512);
    if (a < 4) B[0][a] = *(const LAS v4u*)(pb + a * 512 + 7 * 64);
  }
#pragma unroll
  for (int bb = 0; bb < 8; bb++) {
    __builtin_amdgcn_sched_barrier(0);
    if (bb == 7) {
      lds_wait(sm, C_U2, 1);
      __builtin_amdgcn_sched_barrier(0);
    }
#pragma unroll
    for (int a = 0; a < 8; a++) {
      if (bb < 7) {
        A[(bb + 1) & 1][a] = *(const LAS v4u*)(pa + a * 512 + (bb + 1) * 64);
        if (a < 4) B[(bb + 1) & 1][a] = *(const LAS v4u*)(pb + a * 512 + (6 - bb) * 64);
      } else if (a < 4) {
        A[0][a] = *(const LAS v4u*)(pa + a * 512);
        B[0][a] = *(const LAS v4u*)(pb + (4 + a) * 512 + 7 * 64);
      }
    }
#pragma unroll
    for (int t = 0; t < 8; t++)
#pragma unroll
      for (int c = 0; c < 4; c++)
        if (c <= t) acc[t] = mfma16(A[bb & 1][t - c], B[bb & 1][c], acc[t]);
#pragma unroll
    for (int i = 0; i < (bb < 7 ? 12 : 8); i++) {
      __builtin_amdgcn_sched_group_barrier(0x8, 2, 0);
      __builtin_amdgcn_sched_group_barrier(0x100, 1, 0);
    }
    if (bb < 7) __builtin_amdgcn_sched_group_barrier(0x8, 2, 0);
    else __builtin_amdgcn_sched_group_barrier(0x8, 10, 0);
  }
#pragma unroll
  for (int bb = 0; bb < 8; bb++) {
    __builtin_amdgcn_sched_barrier(0);
    if (bb == 1) {
      LAS char* yb = sm + L_STG + wave * Y1_B + n16 * 32 + g4 * 8;
#pragma unroll
      for (int t = 0; t < 4; t++) {
        v2u y2 = {umin(pk2(acc[t][0], acc[t][1]), MAXV), umin(pk2(acc[t][2], acc[t][3]), MAXV)};
        *(LAS v2u*)(yb + t * 512) = y2;
      }
    }
    if (bb == 2) lds_signal(sm, C_Y1);
    if (bb == Y_POST) {
      (void)gate_post(P, G_Y, wave == 0 && pass == 0, inc);
      __builtin_amdgcn_sched_barrier(0);
    }
    if (bb < 7) {
#pragma unroll
      for (int a = 0; a < 4; a++) {
        A[(bb + 1) & 1][a] = *(const LAS v4u*)(pa + a * 512 + (bb + 1) * 64);
        B[(bb + 1) & 1][a] = *(const LAS v4u*)(pb + (4 + a) * 512 + (6 - bb) * 64);
      }
    }
#pragma unroll
    for (int t = 0; t < 4; t++)
#pragma unroll
      for (int c = 0; c <= t; c++) acc[4 + t] = mfma16(A[bb & 1][t - c], B[bb & 1][c], acc[4 + t]);
    if (bb < 7) {
#pragma unroll
      for (int i = 0; i < 8; i++) {
        __builtin_amdgcn_sched_group_barrier(0x8, 1, 0);
        __builtin_amdgcn_sched_group_barrier(0x100, 1, 0);
      }
      __builtin_amdgcn_sched_group_barrier(0x8, 2, 0);
    }
  }
  __builtin_amdgcn_sched_barrier(0);
    lds_wait(sm, C_OK, 2);
    if (__builtin_expect(__builtin_amdgcn_readfirstlane(((const LAS u32*)(sm + L_CNT))[C_REDO]) == 0u, 1)) break;
    NOTE(26, 7);
  }
  if (Y_POST == 8 && wave == 0) (void)gate_post(P, G_Y, true, inc);
  {
    LAS char* yb = sm + L_H + wave * (HC_B + 64) + n16 * 32 + g4 * 8;
#pragma unroll
    for (int t = 0; t < 4; t++) {
      v2u y2 = {umin(pk2(acc[4 + t][0], acc[4 + t][1]), MAXV), umin(pk2(acc[4 + t][2], acc[4 + t][3]), MAXV)};
      *(LAS v2u*)(yb + t * 512) = y2;
    }
  }
  lds_signal(sm, C_Y);
  STAMP(7);
  lds_wait(sm, C_Y, 4);
  publish_y(P, sm + L_H, HC_B + 64, 128, tid, 64, 2);
  STAMP(8);
}

struct EarlyX { v4u xa[9], wb[9]; };
struct Early {
  u32 kk[11], vv[8], dd;
  v4u sk;
  u64 epoch;
  u32 cx;
};

__device__ __forceinline__ void tile_wave(const Ctx& P, const Early& E, const bool MOE) {
  LAS char* sm = P.sm;
  const int tid = P.tid, lane = P.lane, wave = P.wave, C = P.C;
  const bool act = tid < 288;
  const int j4 = min(tid, 287) * 4;
  const int row0 = 8 * C * (D * 2);
  const u32 (&kk)[11] = E.kk;
  const u32 (&vv)[8] = E.vv;
  const v4u sk = E.sk;
  u32 qq[8];
  u32 gold, uinc;
  bool poster;
  u32 up[2][4];
  {
    f2 w[4], kf[11], ks[8];
    w[0] = (f2){lo16(sk.x), lo16(sk.z)}; w[1] = (f2){hi16(sk.x), hi16(sk.z)};
    w[2] = (f2){lo16(sk.y), lo16(sk.w)}; w[3] = (f2){hi16(sk.y), hi16(sk.w)};
#pragma unroll
    for (int i = 0; i < 11; i++) kf[i] = (f2){lo16(kk[i]), hi16(kk[i])};
#pragma unroll
    for (int t = 0; t < 8; t++) {
      ks[t] = __builtin_elementwise_fma(kf[t + 3], w[0], __builtin_elementwise_fma(kf[t + 2], w[1], __builtin_elementwise_fma(kf[t + 1], w[2], kf[t] * w[3])));
      asm volatile("" : "+v"(ks[t]));
    }
    __builtin_amdgcn_sched_barrier(0);
    lds_wait(sm, C_TAG, 1);
    uinc = gate_inc(*(const LAS u32*)(sm + L_REC));
    STAMP(12);
#pragma unroll
    for (int t = 0; t < 8; t++) ks[t] *= (f2){lo16(vv[t]), hi16(vv[t])};
#pragma unroll
    for (int i = 0; i < 4; i++) {
      up[0][i] = umin(pk2(ks[7 - 2 * i].x, ks[6 - 2 * i].x), MAXV);
      up[1][i] = umin(pk2(ks[7 - 2 * i].y, ks[6 - 2 * i].y), MAXV);
    }
    STAMP(1);
    LAS char* stg = sm + L_STG + wave * 2048;
    v4u p0 = {up[0][0], up[0][1], up[0][2], up[0][3]}, p1 = {up[1][0], up[1][1], up[1][2], up[1][3]};
    if (act) { *(LAS v4u*)(stg + lane * 32) = p0; *(LAS v4u*)(stg + lane * 32 + 16) = p1; }
    asm volatile("" ::: "memory");
    p0 = *(const LAS v4u*)(stg + lane * 16);
    p1 = *(const LAS v4u*)(stg + (wave < 4 ? 1024 : 0) + lane * 16);
    STAMP(13);
    const int so = WS_U + C * TILE_B + wave * 2048 + lane * 16;
    __builtin_amdgcn_raw_buffer_store_b128(p0, P.rws, so, 0, AUX_SC1);
    if (wave < 4) __builtin_amdgcn_raw_buffer_store_b128(p1, P.rws, so + 1024, 0, AUX_SC1);
  }
  __builtin_amdgcn_sched_barrier(0);
  poster = lds_arrive(sm, C_VER, 4);
  if (poster) gold = gate_post(P, G_U, true, uinc);
  STAMP(2);
  lds_wait(sm, C_STAT, 1);
#pragma unroll
  for (int i = 0; i < 8; i++) qq[i] = *(const LAS u32*)(sm + xrow_off(i) + j4);
  lds_signal(sm, C_QQ);
  u32 dd = E.dd;
  asm volatile("" : "+v"(dd));
  float xn[2][8], c0[2][8];
  auto prepare = [&]()
  {
    const float d0 = lo16(dd), d1 = hi16(dd);
#pragma unroll
    for (int s2 = 0; s2 < 4; s2++) {
      const f4 st = *(const LAS f4*)(sm + L_STAT + s2 * 16);
#pragma unroll
      for (int e = 0; e < 2; e++) {
        const int s = 2 * s2 + e;
        const float xa = lo16(qq[s]), xb = hi16(qq[s]);
        const float na = fmaf(xa, st[2 * e], st[2 * e + 1]), nb = fmaf(xb, st[2 * e], st[2 * e + 1]);
        const u32 pa = up[0][(7 - s) >> 1], pb = up[1][(7 - s) >> 1];
        const float ua = ((7 - s) & 1) ? hi16(pa) : lo16(pa), ub = ((7 - s) & 1) ? hi16(pb) : lo16(pb);
        xn[0][s] = na; xn[1][s] = nb;
        c0[0][s] = fmaf(na, fmaf(ua, d0, 1.0f), xa);
        c0[1][s] = fmaf(nb, fmaf(ub, d1, 1.0f), xb);
        asm volatile("" : "+v"(xn[0][s]), "+v"(xn[1][s]), "+v"(c0[0][s]), "+v"(c0[1][s]));
      }
    }
  };
  prepare();
  if (poster) gate_confirm(P, G_U, gold, uinc);
  if (!(P.is_conv && wave < 4)) {
    asm volatile("s_waitcnt vmcnt(0)" ::: "memory");
    lds_signal(sm, C_DRAIN);
  }
  STAMP(14);
  if (MOE) {
    ffn_gate_up(P, []() {});
    if (wave < 2) { Down dn; dn.load(P, wave); dn.run(P, wave); }
  } else {
   if (P.is_conv) {
    if (wave < 4) {
      conv_phase(P, uinc);
    } else {
      lds_wait<1>(sm, C_QQ, 5);
      lds_wait<1>(sm, C_GU, 1);
      __builtin_amdgcn_s_sleep(3 + U_DELAY);
      v4u g[8];
      gather_half(P, 128, g);
#pragma unroll
      for (int it = 0; it < 8; it++) *(LAS v4u*)(sm + L_U + (lane & 3) * UL_B + (32 + 128 + it * 16 + (lane >> 2)) * 16) = g[it];
      lds_signal(sm, C_U2);
      STAMP(5);
      gate_wait(P, G_V, 16u, uinc, SPIN_CAP);
      lds_signal(sm, C_TRUST);
      STAMP(4);
      verify_half(P, 128);
      STAMP(6);
      lds_wait<2>(sm, C_Y1, 4);
      lds_wait<1>(sm, C_OK, 2);
      lds_wait<1>(sm, C_Y1, __builtin_amdgcn_readfirstlane(((const LAS u32*)(sm + L_CNT))[C_REDO]) ? 8 : 4);
      publish_y(P, sm + L_STG, Y1_B, 0, lane, 32, 4);
    }
  } else {
    STAMP(5);
   }
  }
  lds_wait<1>(sm, C_SEL, 1);
  const u32 skip = __builtin_amdgcn_readfirstlane(*(const LAS u32*)(sm + L_REC + 4));
  const __amdgpu_buffer_rsrc_t ro = mkrsrc(P.out, S * D * 2);
  const int oo = act ? j4 : OOB;
  const int uoff = WS_U + C * TILE_B + j4 * 8;
  lds_wait<1>(sm, C_GY, 1);
  STAMP(9);
  v4u y0, y1;
  _Pragma("nounroll") for (u32 spin = 0; spin < SPIN_CAP; spin++) {
    asm volatile("" ::: "memory");
    y0 = __builtin_amdgcn_raw_buffer_load_b128(P.rws, uoff, 0, AUX_SC1);
    y1 = __builtin_amdgcn_raw_buffer_load_b128(P.rws, uoff + 16, 0, AUX_SC1);
    bool ok = true;
#pragma unroll
    for (int e = 0; e < 4; e++) ok = ok & (y0[e] != up[0][e]) & (y1[e] != up[1][e]);
    if (__builtin_expect(__all(ok), 1)) break;
    NOTE(25, spin + 1);
    __builtin_amdgcn_s_sleep(4);
  }
  STAMP(16);
#pragma unroll
  for (int e = 0; e < 4; e++) {
    const u32 a = y0[e] == SAME ? up[0][e] : y0[e], c = y1[e] == SAME ? up[1][e] : y1[e];
    const u32 o0 = pk2(fmaf(xn[0][2 * e], lo16(a), c0[0][2 * e]), fmaf(xn[1][2 * e], lo16(c), c0[1][2 * e]));
    const u32 o1 = pk2(fmaf(xn[0][2 * e + 1], hi16(a), c0[0][2 * e + 1]), fmaf(xn[1][2 * e + 1], hi16(c), c0[1][2 * e + 1]));
    const int so = row0 + (e >> 1) * 4 * (D * 2);
    if (!((skip >> (2 * e)) & 1)) __builtin_amdgcn_raw_buffer_store_b32(o0, ro, oo + ((2 * e) & 3) * (D * 2), so, OUT_AUX);
    if (!((skip >> (2 * e + 1)) & 1)) __builtin_amdgcn_raw_buffer_store_b32(o1, ro, oo + ((2 * e + 1) & 3) * (D * 2), so, OUT_AUX);
  }
}

__device__ __forceinline__ void route(const Ctx& P, u32 tag, u64 epoch, const int ROLE) {
  LAS char* sm = P.sm;
  const int lane = P.lane, wave = P.wave;
  (void)wave;
  v4u g[16];
  if (ROLE == 2) __builtin_amdgcn_s_setprio(0);
  if (ROLE == 0) {
    if (HELPERS != 0 && PF_WHEN != 0 && PF_CE != 0) {
      lds_wait<1>(sm, C_VER, PF_WHEN);
      u32 t[4] = {0u, 0u, 0u, 0u};
#pragma unroll
      for (int i = 0; i < 2 * PF_CE; i++) t[i] = touch_w1(P, i & 1, 25 + 18 * (i >> 1) + (P.b >> 3));
      asm volatile("" :: "v"(t[0]), "v"(t[1]), "v"(t[2]), "v"(t[3]));
      STAMP(18);
    }
    gate_wait(P, G_U, 16u, gate_inc(tag));
    lds_signal(sm, C_GU);
    STAMP(15);
    __builtin_amdgcn_s_setprio(0);
    post_u_complete(P, gate_inc(tag));
    STAMP(13);
    lds_wait<1>(sm, C_U, 4);
  } else {
    gate_wait(P, ROLE == 1 ? G_R : G_U, 16u, gate_inc(tag));
  }
  STAMP(3);
  _Pragma("nounroll") for (u32 spin = 0; spin < SPIN_CAP; spin++) {
    asm volatile("" ::: "memory");
#pragma unroll
    for (int i = 0; i < 16; i++)
      g[i] = __builtin_amdgcn_raw_buffer_load_b128(P.rws, WS_R + lane * 16 + (i & 3) * 1024, (i >> 2) * 4096, AUX_SC1);
    if (spin == 0) lds_signal(sm, C_PUB);
    u32 bad = 0;
#pragma unroll
    for (int i = 0; i < 16; i++) {
      bad = __builtin_amdgcn_bitop3_b32(bad, g[i].y, tag, 0xF6);
      bad = __builtin_amdgcn_bitop3_b32(bad, g[i].w, tag, 0xF6);
    }
    if (__builtin_expect(__all(bad == 0), 1)) break;
    NOTE(24, spin + 1);
    __builtin_amdgcn_s_sleep(4);
  }
  STAMP(4);
  if (P.b == 0 && lane == 0) __hip_atomic_store((GAS u64*)(P.ws + WS_E), epoch, __ATOMIC_RELAXED, __HIP_MEMORY_SCOPE_AGENT);
  u32 k1 = umax(g[0].x, g[0].z), k2 = umin(g[0].x, g[0].z), k3 = 0;
#pragma unroll
  for (int i = 2; i < 32; i++) {
    const u32 x = (i & 1) ? g[i >> 1].z : g[i >> 1].x;
    k3 = umed3(k2, k3, x);
    k2 = umed3(k1, k2, x);
    k1 = umax(k1, x);
  }
  u32 tau = umax(k1, dppu<0xB1>(k1));
  tau = umax(tau, dppu<0x4E>(tau));
  tau = umax(tau, dppu<0x141>(tau));
  tau = umin(tau, dppu<0x140>(tau));
  tau = xrow_umin(tau);
  LAS u32* cand = (LAS u32*)(sm + L_CAND);
  const u64 m1 = __ballot(k1 >= tau), m2 = __ballot(k2 >= tau), m3 = __ballot(k3 >= tau);
  const u32 n1 = __builtin_popcountll(m1), n2 = n1 + __builtin_popcountll(m2), n = n2 + __builtin_popcountll(m3);
  bool slow = n > 64;
  if (__builtin_expect(!slow, 1)) {
    const u32 p1 = __builtin_amdgcn_mbcnt_hi((u32)(m1 >> 32), __builtin_amdgcn_mbcnt_lo((u32)m1, 0u));
    const u32 p2 = __builtin_amdgcn_mbcnt_hi((u32)(m2 >> 32), __builtin_amdgcn_mbcnt_lo((u32)m2, n1));
    const u32 p3 = __builtin_amdgcn_mbcnt_hi((u32)(m3 >> 32), __builtin_amdgcn_mbcnt_lo((u32)m3, n2));
    cand[k1 >= tau ? p1 : 64] = k1;
    cand[k2 >= tau ? p2 : 64] = k2;
    cand[k3 >= tau ? p3 : 64] = k3;
    asm volatile("" ::: "memory");
    const u32 mine = cand[lane];
    u32 rank = 0;
    _Pragma("nounroll") for (u32 jj = 0; jj < n; jj += 8) {
      const v4u c4 = *(const LAS v4u*)(cand + jj), d4 = *(const LAS v4u*)(cand + jj + 4);
      rank += (c4.x > mine) + (c4.y > mine) + (c4.z > mine) + (c4.w > mine);
      rank += (d4.x > mine) + (d4.y > mine) + (d4.z > mine) + (d4.w > mine);
    }
    const bool in = (u32)lane < n && rank < 8;
    if (in) ((LAS u32*)(sm + L_SEL))[rank] = mine;
    slow = __any(in && (u32)lane >= n2);
  }
  if (__builtin_expect(slow, 0)) {
    _Pragma("nounroll") for (int r = 0; r < 8; r++) {
      u32 mx = 0;
#pragma unroll
      for (int i = 0; i < 16; i++) mx = umax3(mx, g[i].x, g[i].z);
      mx = wave_umax(mx);
#pragma unroll
      for (int i = 0; i < 16; i++) { g[i].x = g[i].x == mx ? 0u : g[i].x; g[i].z = g[i].z == mx ? 0u : g[i].z; }
      if (lane == 0) ((LAS u32*)(sm + L_SEL))[r] = mx;
    }
  }
  asm volatile("" ::: "memory");
  {
    const u32 tk = 2047u - (((LAS u32*)(sm + L_SEL))[lane & 7] & 2047u);
    u32 mk = (int)(tk >> 3) == P.C ? 1u << (tk & 7) : 0u;
    mk |= dppu<0xB1>(mk);
    mk |= dppu<0x4E>(mk);
    mk |= dppu<0x141>(mk);
    if (lane == 0) *(LAS u32*)(sm + L_REC + 4) = mk;
  }
  lds_signal(sm, C_SEL);
  STAMP(5);
}

__device__ __forceinline__ u32 load_msd(const Ctx& P, int tok, u32 tag) {
  v2u g;
  _Pragma("nounroll") for (u32 spin = 0; spin < SPIN_CAP; spin++) {
    asm volatile("" ::: "memory");
    g = __builtin_amdgcn_raw_buffer_load_b64(P.rws, WS_M + tok * 8, 0, AUX_SC1);
    if (__builtin_expect(__all(g.y == tag), 1)) break;
    __builtin_amdgcn_s_sleep(4);
  }
  return g.x;
}

__device__ __forceinline__ void routed_rows(const Ctx& P) {
  LAS char* sm = P.sm;
  const int lane = P.lane, wave = P.wave, C = P.C, cg = P.cg;
  (void)wave; (void)C; (void)cg;
  const __amdgpu_buffer_rsrc_t rq = mkrsrc(P.q, S * D * 2);
  {
    const int cw = wave - 6;
    const u32 tag = *(const LAS u32*)(sm + L_REC);
    const v4u s4 = *(const LAS v4u*)(sm + L_SEL + cw * 16);
    Row rr[4];
#pragma unroll
    for (int e = 0; e < 4; e++) {
      const int tok = 2047 - (int)(__builtin_amdgcn_readfirstlane(s4[e]) & 2047u);
      rr[e] = load_row<0>(rq, tok * (D * 2), lane);
    }
    const u32 ks = (lane & 2) ? ((lane & 1) ? s4.w : s4.z) : ((lane & 1) ? s4.y : s4.x);
    const int mtok = 2047 - (int)(ks & 2047u);
    v2u mg = __builtin_amdgcn_raw_buffer_load_b64(P.rws, WS_M + mtok * 8, 0, AUX_SC1);
    __builtin_amdgcn_sched_barrier(0);
    lds_signal(sm, C_W1);
    __builtin_amdgcn_sched_barrier(0);
    u32 msd = mg.x;
    if (__builtin_expect(!__all(mg.y == tag), 0)) msd = load_msd(P, mtok, tag);
    STAMP(15);
    const float rsl = __builtin_amdgcn_rcpf(lo16(msd)), nml = -hi16(msd) * rsl;
#pragma unroll
    for (int e = 0; e < 4; e++) {
      const float rs = __uint_as_float(__builtin_amdgcn_readlane(__float_as_uint(rsl), e));
      const float nm = __uint_as_float(__builtin_amdgcn_readlane(__float_as_uint(nml), e));
      LAS char* dst = sm + L_XS + (cw * 4 + e) * XS_STRIDE;
      const Row& x = rr[e];
      v2u a = {pk2(fmaf(lo16(x.a0.x), rs, nm), fmaf(hi16(x.a0.x), rs, nm)), pk2(fmaf(lo16(x.a0.y), rs, nm), fmaf(hi16(x.a0.y), rs, nm))};
      v2u c = {pk2(fmaf(lo16(x.a1.x), rs, nm), fmaf(hi16(x.a1.x), rs, nm)), pk2(fmaf(lo16(x.a1.y), rs, nm), fmaf(hi16(x.a1.y), rs, nm))};
      *(LAS v2u*)(dst + lane * 8) = a;
      *(LAS v2u*)(dst + 512 + lane * 8) = c;
      *(LAS u16*)(dst + 1024 + lane * 2) = (u16)pk2(fmaf(lo16(x.a2), rs, nm), 0.f);
    }
    lds_signal(sm, C_XS);
    STAMP(6);
    __builtin_amdgcn_s_setprio(0);
    Down dn;
    dn.load(P, wave - 4);
    dn.run(P, wave - 4);
  }
}

__device__ __forceinline__ void aux_wave(const Ctx& P, const int ROLE) {
  LAS char* sm = P.sm;
  const int lane = P.lane, wave = P.wave, C = P.C, cg = P.cg;
  (void)wave; (void)C; (void)cg;
  u32 tag;
    if (CODE_PF && wave == 6) asm volatile("s_waitcnt vmcnt(0)" ::: "v255");
    if (ROLE != 0 && wave == 5) {
      __builtin_amdgcn_s_setprio(3);
      lds_wait<1>(sm, C_TAG, 1);
      post_u_complete(P, gate_inc(*(const LAS u32*)(sm + L_REC)));
      __builtin_amdgcn_s_setprio(0);
      STAMP(13);
    }
    if (ROLE == 2 && HELPERS && wave == 6) {
      lds_wait<1>(sm, C_VER, 5);
      const int idx = (P.b - NCONV - NMOE) >> 3;
      u32 ta = touch_w1(P, 0, idx), tb = touch_w1(P, 1, idx);
#pragma unroll
      for (int i = 1; i < 5; i++) {
        const u32 tc = touch_w1(P, 0, i * 5 + idx), td = touch_w1(P, 1, i * 5 + idx);
        asm volatile("" :: "v"(ta), "v"(tb));
        ta = tc; tb = td;
      }
      asm volatile("" :: "v"(ta), "v"(tb));
      STAMP(5);
    }
    if (ROLE == 0) {
      __builtin_amdgcn_s_sleep(H_DELAY);
      const u32 tt = hcopy_first(P, 2 * (wave - 5));
      lds_wait(sm, C_H0, 2);
      hcopy_rest<2>(P, 12 + 2 * (wave - 5));
      asm volatile("" :: "v"(tt));
      STAMP(3);
      if (wave == 5) {
        lds_wait<1>(sm, C_U, 4);
        verify_half(P, 0);
        STAMP(6);
        lds_wait<2>(sm, C_Y1, 4);
        lds_wait<1>(sm, C_OK, 2);
        lds_wait<1>(sm, C_Y1, __builtin_amdgcn_readfirstlane(((const LAS u32*)(sm + L_CNT))[C_REDO]) ? 8 : 4);
        publish_y(P, sm + L_STG + 256, Y1_B, 16, lane, 32, 4);
      } else if (HELPERS) {
        lds_wait<1>(sm, C_U2, 1);
        u32 t[9];
#pragma unroll
        for (int i = 0; i < 4; i++) {
          const int u = 50 + 36 * PF_CE + i * 18 + (P.b >> 3);
          t[5 + i] = (i >= 4 - 2 * PF_CE) ? 0u : touch_w1(P, u & 1, u >> 1);
        }
#pragma unroll
        for (int i = 0; i < 5; i++) t[i] = touch_w2(P, i * 18 + (P.b >> 3));
        asm volatile("" :: "v"(t[5]), "v"(t[6]), "v"(t[7]), "v"(t[8]));
        STAMP(5);
        asm volatile("" :: "v"(t[0]), "v"(t[1]), "v"(t[2]), "v"(t[3]), "v"(t[4]));
        STAMP(6);
      }
    }
    if (wave == 5) {
      if (ROLE == 1) {
        Down dn;
        ffn_gate_up(P, [&]() { dn.load(P, 4); });
        dn.run(P, 4);
        gate_wait(P, G_Y, 9u, gate_inc(*(const LAS u32*)(sm + L_REC)));
        lds_signal(sm, C_GY);
      }
      return;
    }
    lds_wait<1>(sm, C_TAG, 1);
    tag = *(const LAS u32*)(sm + L_REC);
    if (ROLE == 1) { lds_wait<1>(sm, C_SEL, 1); __builtin_amdgcn_s_setprio(3); }
  if (ROLE != 1) {
    gate_wait(P, G_Y, 9u, gate_inc(tag));
    lds_signal(sm, C_GY);
    STAMP(7);
  }
}

__device__ __forceinline__ void router_stats(const Ctx& P, Early& E, const EarlyX& X, const int ROLE) {
  LAS char* sm = P.sm;
  const int lane = P.lane, C = P.C;
  u32 tag;
    const int m = lane & 15, g4 = lane >> 4;
    u64 epoch = E.epoch;
    const v4u (&xa)[9] = X.xa;
    const v4u (&wb)[9] = X.wb;
    asm volatile("" : "+v"(epoch) :: "memory");
    epoch += 1;
    tag = (u32)epoch;
    if (lane == 0) *(LAS u32*)(sm + L_REC) = tag;
    lds_signal(sm, C_TAG);
    STAMP(1);
    f4 gx = {0.f, 0.f, 0.f, 0.f}, gw = {0.f, 0.f, 0.f, 0.f}, gs = {0.f, 0.f, 0.f, 0.f};
#pragma unroll
    for (int i = 0; i < 9; i++) gx = mfma16(xa[i], xa[i], gx);
    __builtin_amdgcn_sched_barrier(0);
    const u32 gold = gate_post(P, G_R, true, gate_inc(tag));
    STAMP(12);
    __builtin_amdgcn_sched_barrier(0);
#pragma unroll
    for (int i = 0; i < 9; i++) { gw = mfma16(wb[i], xa[i], gw); gs = mfma16(wb[i], wb[i], gs); }
    float sxx = (m & 2) ? ((m & 1) ? gx[3] : gx[2]) : ((m & 1) ? gx[1] : gx[0]);
    sxx = __uint_as_float(__builtin_amdgcn_ds_bpermute((m + (m & 12) * 4) * 4, __float_as_uint(sxx)));
    float sxw = m < 8 ? gw[0] : gw[1], sx = gw[2];
    sxw += dppf<0x128>(sxw);
    sx += dppf<0x128>(sx);
    sxx += dppf<0x128>(sxx);
    const float sw = __uint_as_float(__builtin_amdgcn_readlane(__float_as_uint(gs[0] + gs[1]), 2));
    const float mean = sx * (1.0f / D);
    const float var = fmaxf(fmaf(-mean, mean, sxx * (1.0f / D)), 0.f);
    const float mu = rbf(mean);
    const float sd = rbf(__builtin_amdgcn_sqrtf(rbf(rbf(var) + 1e-5f)));
    const float rs = __builtin_amdgcn_rcpf(sd);
    const u32 r = pk2(0.f, fmaf(-mu, sw, sxw) * rs);
    u32 key = ((r ^ (((int)r >> 31) | 0x80000000u)) & 0xFFFF0000u) | (u32)(2047 - (8 * C + (m & 7)));
    u32 msd = __float_as_uint(mu) | (__float_as_uint(sd) >> 16);
    asm volatile("" : "+v"(key), "+v"(msd));
    const v2u rec = {m < 8 ? key : msd, tag};
    const int ro = lane < 16 ? (m < 8 ? WS_R : WS_M) + (8 * C + (m & 7)) * 8 : OOB;
    __builtin_amdgcn_raw_buffer_store_b64(rec, P.rws, ro, 0, AUX_SC1);
    STAMP(2);
    if (lane < 8) { f2 st = {rs, -mu * rs}; *(LAS f2*)(sm + L_STAT + m * 8) = st; }
    {
      const int t = m & 7, t3 = (t * 11) >> 5;
      LAS char* xd = sm + L_U + t3 * UL_B + 512 + (t - 3 * t3) * (D * 2) + (m >> 3) * 64 + g4 * 16;
#pragma unroll
      for (int i = 0; i < 9; i++) *(LAS v4u*)(xd + i * 128) = xa[i];
    }
    lds_signal(sm, C_STAT);
    E.epoch = epoch;
    E.cx = gold;
}

__device__ __forceinline__ void router_wave(const Ctx& P, const Early& E, const int ROLE) {
  LAS char* sm = P.sm;
  const int lane = P.lane, wave = P.wave, C = P.C, cg = P.cg;
  (void)wave; (void)C; (void)cg;
  u32 tag;
    const u64 epoch = E.epoch;
    tag = (u32)epoch;
    const u32 gold = E.cx;
    gate_confirm(P, G_R, gold, gate_inc(tag));
    route(P, tag, epoch, ROLE);
  if (ROLE == 0) {
    const int pi = lane & 7, ti = 4 * (pi & 1) + (pi & 2) + (pi >> 2);
    const u32 kx = ((LAS u32*)(sm + L_SEL))[ti];
    const int tok = 2047 - (int)(kx & 2047u);
    const v2u x2 = *(const GAS v2u*)((const GAS char*)P.q + (size_t)tok * (D * 2) + cg * 8);
    const v2u d2 = *(const GAS v2u*)((const GAS char*)P.dsk + cg * 8);
    const u32 msd = load_msd(P, tok, tag);
    const float rs = __builtin_amdgcn_rcpf(lo16(msd)), nm = -hi16(msd) * rs;
    const u32 rb = kx & 0xFFFF0000u;
    const float r = __uint_as_float((rb & 0x80000000u) ? (rb ^ 0x80000000u) : (~rb & 0xFFFF0000u));
    const float rsig = rbf(__builtin_amdgcn_rcpf(1.0f + __expf(-r)));
    STAMP(6);
    lds_wait<2>(sm, C_Y, 4);
    float o[4];
#pragma unroll
    for (int e = 0; e < 4; e++) {
      const float y = lo16(*(const LAS u16*)(sm + (tok < 1024 ? L_STG + e * Y1_B : L_H + e * (HC_B + 64) - 2048) + tok * 2));
      const float u = lo16(*(const LAS u16*)(sm + L_U + e * UL_B + (32 + (tok >> 3)) * 16 + (7 - (tok & 7)) * 2));
      const float x = (e & 1) ? hi16(x2[e >> 1]) : lo16(x2[e >> 1]);
      const float dk = (e & 1) ? hi16(d2[e >> 1]) : lo16(d2[e >> 1]);
      const float n = fmaf(x, rs, nm);
      o[e] = fmaf(n, fmaf(u, dk, y), x) + n;
      asm volatile("" : "+v"(o[e]));
    }
    gate_wait(P, G_P, lane < 8 ? 5u : 4u, gate_inc(tag));
    STAMP(7);
    v4u g[9];
    const u32 tag12 = tag & 0xFFFu;
    _Pragma("nounroll") for (u32 spin = 0; spin < SPIN_CAP; spin++) {
      asm volatile("" ::: "memory");
#pragma unroll
      for (int i = 0; i < 9; i++)
        g[i] = __builtin_amdgcn_raw_buffer_load_b128(P.rws, WS_P + (lane >> 3) * PART_B + cg * 128 + pi * 16, 8 * i * PART_B, AUX_SC1);
      u32 bad = 0;
#pragma unroll
      for (int i = 0; i < 9; i++) {
        g[i] ^= tag12;
        bad |= g[i].x | g[i].y;
        bad |= g[i].z | g[i].w;
      }
      if (__builtin_expect(__all((bad & 0xFFFu) == 0), 1)) break;
      NOTE(25, spin + 1);
      __builtin_amdgcn_s_sleep(4);
    }
    STAMP(8);
    f4 s4 = __builtin_bit_cast(f4, g[0]);
#pragma unroll
    for (int i = 1; i < 9; i++) s4 += __builtin_bit_cast(f4, g[i]);
#pragma unroll
    for (int e = 0; e < 4; e++) {
      float sv = s4[e];
      sv += dppf<0x128>(sv);
      o[e] = fmaf(rbf(xrow_sum(sv)), rsig, o[e]);
    }
    if (lane < 8) {
      v2u o2 = {pk2(o[0], o[1]), pk2(o[2], o[3])};
      __builtin_amdgcn_raw_buffer_store_b64(o2, mkrsrc(P.out, S * D * 2), tok * (D * 2) + cg * 8, 0, OUT_AUX);
    }
  }
}

__global__ __launch_bounds__(NTHR, 2) void fused_kernel(
    const u16* __restrict__ q, const u16* __restrict__ k, const u16* __restrict__ v, const u16* __restrict__ shk,
    const u16* __restrict__ hk, const u16* __restrict__ dsk, const u16* __restrict__ wr, const u16* __restrict__ wg,
    const u16* __restrict__ wu, const u16* __restrict__ wd, u16* __restrict__ out, char* ws) {
  extern __shared__ __attribute__((aligned(16))) char smem_[];
  Ctx P;
  P.q = q; P.k = k; P.v = v; P.shk = shk; P.hk = hk; P.dsk = dsk; P.wr = wr; P.wg = wg; P.wu = wu; P.wd = wd;
  P.out = out; P.ws = ws;
  u64 pc;
  asm volatile("s_getpc_b64 %0" : "=s"(pc));
  P.code = (const char*)(pc & ~(u64)127);
  P.sm = (LAS char*)smem_;
  P.tid = threadIdx.x; P.lane = P.tid & 63;
  P.wave = __builtin_amdgcn_readfirstlane(P.tid >> 6);
  const int b = blockIdx.x;
  P.b = b;
  P.C = (b & 7) * 32 + (b >> 3);
  P.is_conv = b < NCONV;
  const bool is_moe = !P.is_conv && b < NCONV + NMOE;
  P.cg = (b & 7) * 18 + (b >> 3);
  P.mw = (b & 7) * 9 + ((b - NCONV) >> 3);
  P.rws = mkrsrc(ws, WS_END);
  const int lane = P.lane, wave = P.wave;
  (void)lane;
#pragma unroll
  for (int i = 0; i < 32; i++) P.ts[i] = 0;
  STAMP(0); STAMPC(30);
  Early E;
  if (wave == 6) {
    constexpr int NL = (CODE_PF * 8 + 31) / 32;
    if (CODE_PF)
      asm volatile("global_load_dword v255, %0, off" :: "v"(P.code + min(((b >> 3) * NL + min(lane, NL - 1)) * 128, CODE_PF * 1024 - 128)) : "v255");
    const v4u z = {0u, 0u, 0u, 0u};
    *(LAS v4u*)(P.sm + L_U + (lane >> 4) * UL_B + (lane & 15) * 16) = z;
    *(LAS v4u*)(P.sm + L_U + (lane >> 4) * UL_B + 256 + (lane & 15) * 16) = z;
    ((LAS u32*)(P.sm + L_MISC))[lane] = 0u;
    ((LAS u32*)(P.sm + L_MISC))[64 + lane] = 0u;
    asm volatile("s_waitcnt lgkmcnt(0)" ::: "memory");
  }
  asm volatile("s_barrier" ::: "memory");
  asm volatile("" :: "s"(q), "s"(k), "s"(v), "s"(shk), "s"(hk), "s"(dsk), "s"(wr), "s"(wg), "s"(wu), "s"(wd), "s"(out), "s"(ws));
  STAMP(11);
  const int row0 = 8 * P.C * (D * 2);
  EarlyX X;
  if (P.wave == 7) {
    __builtin_amdgcn_s_setprio(3);
    const int m = lane & 15, g4 = lane >> 4;
    E.epoch = __hip_atomic_load((GAS u64*)(P.ws + WS_E), __ATOMIC_RELAXED, __HIP_MEMORY_SCOPE_AGENT);
    if (ROUTER_DELAY) __builtin_amdgcn_s_sleep(ROUTER_DELAY);
    __builtin_amdgcn_sched_barrier(0);
    const __amdgpu_buffer_rsrc_t rq = mkrsrc(P.q, S * D * 2);
    const int xo = (m & 7) * (D * 2) + (m >> 3) * 64 + g4 * 16;
#pragma unroll
    for (int i = 0; i < 9; i++) X.xa[i] = __builtin_amdgcn_raw_buffer_load_b128(rq, xo + i * 128, row0, NT_Q);
    __builtin_amdgcn_sched_barrier(0);
    const size_t pw = (size_t)P.wr + m * 64, po = (size_t)kOnes;
    const GAS char* wp = (const GAS char*)(m < 2 ? pw : po) + g4 * 16;
#pragma unroll
    for (int i = 0; i < 9; i++) X.wb[i] = *(const GAS v4u*)(wp + i * 128);
  }
  asm volatile("" : "+s"(P.wave));
  if (P.wave == 5) {
    const v4u g4 = {GMAGIC, GMAGIC, GMAGIC, GMAGIC};
    __builtin_amdgcn_raw_buffer_store_b128(g4, P.rws, lane < 8 * NGATE ? gate_off(lane >> 3, b >> 4, b & 15) + (lane & 7) * 16 : OOB, 0, AUX_SC1);
  }
  asm volatile("" : "+s"(P.wave));
  if (P.wave < 5) {
    const int j4 = min(P.tid, 287) * 4;
    const __amdgpu_buffer_rsrc_t rk = mkrsrc(P.k, S * D * 2), rv = mkrsrc(P.v, S * D * 2);
    if (TILE_DELAY) __builtin_amdgcn_s_sleep(TILE_DELAY);
    __builtin_amdgcn_sched_barrier(0);
#pragma unroll
    for (int i = 0; i < 3; i++) E.kk[i] = __builtin_amdgcn_raw_buffer_load_b32(rk, row0 + (i - 3) * (D * 2) + j4, 0, NT_KV);
#pragma unroll
    for (int i = 0; i < 8; i++) E.kk[3 + i] = __builtin_amdgcn_raw_buffer_load_b32(rk, j4 + (i & 3) * (D * 2), row0 + (i & 4) * (D * 2), NT_KV);
    __builtin_amdgcn_sched_barrier(0);
    if (SK_POS == 1) E.sk = *(const GAS v4u*)((const GAS char*)P.shk + j4 * 4);
    __builtin_amdgcn_sched_barrier(0);
#pragma unroll
    for (int i = 0; i < 8; i++) E.vv[i] = __builtin_amdgcn_raw_buffer_load_b32(rv, j4 + (i & 3) * (D * 2), row0 + (i & 4) * (D * 2), NT_KV);
    __builtin_amdgcn_sched_barrier(0);
    if (SK_POS == 2) E.sk = *(const GAS v4u*)((const GAS char*)P.shk + j4 * 4);
    E.dd = *(const GAS u32*)((const GAS char*)P.dsk + j4);
  }
  const int role = P.is_conv ? 0 : (is_moe ? 1 : 2);
  asm volatile("" : "+s"(P.wave));
  STAMP(10);
  if (P.wave == 7) router_stats(P, E, X, role);
  asm volatile("" : "+s"(P.wave));
  if (P.wave != 7) {
    if (P.wave >= 5) aux_wave(P, role);
    else tile_wave(P, E, is_moe);
  } else {
    router_wave(P, E, role);
  }
  asm volatile("" : "+s"(P.wave));
  if (is_moe && P.wave >= 6) routed_rows(P);
  FLUSH();
}

}

extern "C" void kernel_launch(void* const* d_in, const int* in_sizes, int n_in, void* d_out, int out_size, void* d_ws,
                              size_t ws_size, hipStream_t stream) {
  (void)in_sizes; (void)n_in; (void)out_size; (void)ws_size;
  (void)hipFuncSetAttribute((const void*)fused_kernel, hipFuncAttributeMaxDynamicSharedMemorySize, L_TOTAL);
  fused_kernel<<<NWG, NTHR, L_TOTAL, stream>>>(
      (const u16*)d_in[0], (const u16*)d_in[1], (const u16*)d_in[2], (const u16*)d_in[3], (const u16*)d_in[4],
      (const u16*)d_in[5], (const u16*)d_in[7], (const u16*)d_in[8], (const u16*)d_in[9], (const u16*)d_in[10],
      (u16*)d_out, (char*)d_ws + (WS_AT_END ? ((ws_size - WS_END) & ~(size_t)0xFFFF) : 0));
}
```

```cpp
#include <hip/hip_runtime.h>
#include <hip/hip_bf16.h>

typedef unsigned int u32;
typedef unsigned long long u64;
typedef unsigned short u16;
typedef u32 v4u __attribute__((ext_vector_type(4)));
typedef u32 v2u __attribute__((ext_vector_type(2)));
typedef float f4 __attribute__((ext_vector_type(4)));
typedef float f2 __attribute__((ext_vector_type(2)));
typedef __bf16 bf8 __attribute__((ext_vector_type(8)));
typedef __bf16 bf2 __attribute__((ext_vector_type(2)));
#define LAS __attribute__((address_space(3)))
#define GAS __attribute__((address_space(1)))

namespace {

constexpr int S = 2048, D = 576, F = 4608;
constexpr int NWG = 256, NTHR = 512, NCONV = 144, NMOE = 72;
constexpr int AUX_SC1 = 16;
constexpr u32 SAME = 0xFFFFFFFFu, MAXV = 0xFFFFFFFEu;
constexpr u32 SPIN_CAP = 1u << 16, GATE_CAP = 96;
constexpr u32 GMAGIC = 0x5AC3E900u;
constexpr int OOB = 0x7FFFFF00;
#define U_POST 2
#define WS_AT_END 0
#define HELPERS 1
#define Y_POST 8
#define OUT_AUX 16
#define W2_GATE 2
#define PF_R0 192
#define STAGED 1
#define H_DELAY 30
#define PF_CE 2
#define PF_WHEN 0
#define W1_DEPTH 0
#define DRAIN_FIRST 1
#define U_DELAY 0
#define PF_FIRST 1
#define SK_POS 1
#define ROUTER_DELAY 0
#define W2_EARLY 0
#define SLOW_FIX 1
#define P_POST 1
#define TILE_DELAY 4
#define CODE_PF 41
#define NT_MASK 5
constexpr int NT_KV = (NT_MASK & 1) ? 2 : 0, NT_Q = (NT_MASK & 2) ? 2 : 0, NT_H = (NT_MASK & 4) ? 2 : 0, NT_T = (NT_MASK & 8) ? 2 : 0,
              NT_W = (NT_MASK & 16) ? 2 : 0;

constexpr int WS_E = 0;
constexpr int WS_G = 4096;
constexpr int GATE_B = 256 * 256;
constexpr int WS_R = WS_G + 5 * GATE_B;
constexpr int WS_M = WS_R + S * 8;
constexpr int WS_U = WS_M + S * 8;
constexpr int TILE_B = D * 16;
constexpr int WS_P = WS_U + NWG * TILE_B;
constexpr int PART_B = NCONV * 128;
constexpr int WS_END = WS_P + NMOE * PART_B;
constexpr int WS_DBG = 4  __H2__ ;
static_assert(WS_END <= WS_DBG, "ws");
enum { G_R = 0, G_U, G_Y, G_P, G_V, NGATE };
#define ONES8 0x3F803F80u, 0x3F803F80u, 0x3F803F80u, 0x3F803F80u, 0x3F803F80u, 0x3F803F80u, 0x3F803F80u, 0x3F803F80u
#define ONES32 ONES8, ONES8, ONES8, ONES8
__device__ const u32 kOnes[288] __attribute__((aligned(128))) = {ONES32, ONES32, ONES32, ONES32, ONES32, ONES32, ONES32, ONES32, ONES32};

constexpr int HC_B = 2080 * 16;
constexpr int L_H = 0;
constexpr int UL_B = 290 * 16;
constexpr int L_U = L_H + 4 * HC_B;
constexpr int L_STG = L_U + 4 * UL_B;
constexpr int L_MISC = L_STG + TILE_B;
constexpr int L_CNT = L_MISC;
constexpr int L_CAND = L_CNT + 96;
constexpr int L_SEL = L_CAND + 288;
constexpr int L_REC = L_SEL + 32;
constexpr int L_STAT = L_REC + 32;
constexpr int Y1_B = 2048 + 64;
constexpr int L_END = L_STAT + 64;
constexpr int XS_STRIDE = 1184;
constexpr int L_XS = 0;
constexpr int L_RED = L_XS + 8 * XS_STRIDE;
constexpr int L_HB = L_RED + 6 * 8 * 128 * 4;
constexpr int HB_STRIDE = 160;
static_assert(L_END + 1024 <= 160 * 1024, "lds");
static_assert(L_HB + 8 * HB_STRIDE <= L_U, "lds");
static_assert(L_END - L_MISC == 512, "zero fill");

__device__ __forceinline__ constexpr int xrow_off(int t) { return L_U + (t / 3) * UL_B + 512 + (t % 3) * (D * 2); }
enum { C_DRAIN = 0, C_STAT, C_TRUST, C_GU, C_SEL, C_H, C_U, C_Y, C_XS, C_RED, C_HB, C_GY, C_PUB, C_TAG, C_QQ, C_U2, C_Y1, C_W1, C_DN, C_H0, C_VER, C_REDO, C_OK };

__device__ __forceinline__ float lo16(u32 x) { return __uint_as_float(x << 16); }
__device__ __forceinline__ float hi16(u32 x) { return __uint_as_float(x & 0xFFFF0000u); }
__device__ __forceinline__ u32 pk2(float a, float b) {
  bf2 p = {(__bf16)a, (__bf16)b};
  return __builtin_bit_cast(u32, p);
}
__device__ __forceinline__ float rbf(float a) { return lo16(pk2(a, a)); }
__device__ __forceinline__ float dot2(u32 a, u32 b, float c) {
  return __builtin_amdgcn_fdot2_f32_bf16(__builtin_bit_cast(bf2, a), __builtin_bit_cast(bf2, b), c, false);
}
template <int CTRL> __device__ __forceinline__ u32 dppu(u32 x) {
  return (u32)__builtin_amdgcn_mov_dpp((int)x, CTRL, 0xf, 0xf, true);
}
template <int CTRL> __device__ __forceinline__ float dppf(float x) { return __uint_as_float(dppu<CTRL>(__float_as_uint(x))); }
__device__ __forceinline__ float xrow_sum(float x) {
  auto s = __builtin_amdgcn_permlane16_swap(__float_as_uint(x), __float_as_uint(x), false, false);
  x = __uint_as_float(s[0]) + __uint_as_float(s[1]);
  auto t = __builtin_amdgcn_permlane32_swap(__float_as_uint(x), __float_as_uint(x), false, false);
  return __uint_as_float(t[0]) + __uint_as_float(t[1]);
}
__device__ __forceinline__ u32 umax(u32 a, u32 b) { return a > b ? a : b; }
__device__ __forceinline__ u32 umin(u32 a, u32 b) { return a < b ? a : b; }
__device__ __forceinline__ u32 umed3(u32 a, u32 b, u32 c) { return umax(umin(a, b), umin(umax(a, b), c)); }
__device__ __forceinline__ u32 xrow_umin(u32 x) {
  auto s = __builtin_amdgcn_permlane16_swap(x, x, false, false);
  x = umin(s[0], s[1]);
  auto t = __builtin_amdgcn_permlane32_swap(x, x, false, false);
  return umin(t[0], t[1]);
}
__device__ __forceinline__ u32 wave_umax(u32 x) {
  x = umax(x, dppu<0xB1>(x));
  x = umax(x, dppu<0x4E>(x));
  x = umax(x, dppu<0x141>(x));
  x = umax(x, dppu<0x140>(x));
  auto s = __builtin_amdgcn_permlane16_swap(x, x, false, false);
  x = umax(s[0], s[1]);
  auto t = __builtin_amdgcn_permlane32_swap(x, x, false, false);
  return umax(t[0], t[1]);
}
__device__ __forceinline__ void lds_signal(LAS char* sm, int c, u32 add = 1u) {
  u64 keep;
  asm volatile("s_waitcnt lgkmcnt(0)\n\ts_mov_b64 %0, exec\n\ts_mov_b64 exec, 1\n\tds_add_u32 %1, %2\n\ts_mov_b64 exec, %0"
               : "=&s"(keep) : "v"((u32)(size_t)(sm + L_CNT + 4 * c)), "v"(add) : "memory");
}
__device__ __forceinline__ void lds_signal_nowait(LAS char* sm, int c) {
  u64 keep;
  asm volatile("s_mov_b64 %0, exec\n\ts_mov_b64 exec, 1\n\tds_add_u32 %1, %2\n\ts_mov_b64 exec, %0"
               : "=&s"(keep) : "v"((u32)(size_t)(sm + L_CNT + 4 * c)), "v"(1u) : "memory");
}
__device__ __forceinline__ u32 lds_peek(LAS char* sm, int c) {
  return __hip_atomic_load((LAS u32*)(sm + L_CNT) + c, __ATOMIC_RELAXED, __HIP_MEMORY_SCOPE_WORKGROUP);
}
__device__ __forceinline__ bool checked_redo(LAS char* sm, u32 seen) {
  u32 v = __builtin_amdgcn_readfirstlane(seen);
  while (__builtin_expect((v & 0xFFu) < 2u, 0)) {
    __builtin_amdgcn_s_sleep(1);
    v = __builtin_amdgcn_readfirstlane(lds_peek(sm, C_OK));
  }
  asm volatile("" ::: "memory");
  return v > 0xFFu;
}
template <int SLEEP = 0> __device__ __forceinline__ void lds_wait(LAS char* sm, int c, u32 target) {
  u32 v, sv;
  if (SLEEP == 0)
    asm volatile("1:\n\tds_read_b32 %0, %2\n\ts_waitcnt lgkmcnt(0)\n\tv_readfirstlane_b32 %1, %0\n\ts_cmp_lt_u32 %1, %3\n\ts_cbranch_scc1 1b"
                 : "=&v"(v), "=&s"(sv) : "v"((u32)(size_t)(sm + L_CNT + 4 * c)), "s"(target) : "memory", "scc");
  else
    asm volatile("1:\n\ts_sleep %4\n\tds_read_b32 %0, %2\n\ts_waitcnt lgkmcnt(0)\n\tv_readfirstlane_b32 %1, %0\n\ts_cmp_lt_u32 %1, %3\n\ts_cbranch_scc1 1b"
                 : "=&v"(v), "=&s"(sv) : "v"((u32)(size_t)(sm + L_CNT + 4 * c)), "s"(target), "n"(SLEEP) : "memory", "scc");
}
__device__ __forceinline__ bool lds_arrive(LAS char* sm, int c, u32 last) {
  u64 keep;
  u32 v, sv;
  asm volatile("s_mov_b64 %0, exec\n\ts_mov_b64 exec, 1\n\tds_add_rtn_u32 %1, %3, %4\n\ts_waitcnt lgkmcnt(0)\n\tv_readfirstlane_b32 %2, %1\n\ts_mov_b64 exec, %0"
               : "=&s"(keep), "=&v"(v), "=&s"(sv) : "v"((u32)(size_t)(sm + L_CNT + 4 * c)), "v"(1u) : "memory");
  return sv == last;
}
__device__ __forceinline__ void wg_barrier() { asm volatile("s_waitcnt lgkmcnt(0)\n\ts_barrier" ::: "memory"); }
__device__ __forceinline__ u32 umax3(u32 a, u32 b, u32 c) { return umax(umax(a, b), c); }
__device__ __forceinline__ u32 vmax4(v4u g, u32 acc) { return umax3(umax3(g.x, g.y, acc), g.z, g.w); }
__device__ __forceinline__ f4 mfma16(v4u a, v4u b, f4 c) {
  return __builtin_amdgcn_mfma_f32_16x16x32_bf16(__builtin_bit_cast(bf8, a), __builtin_bit_cast(bf8, b), c, 0, 0, 0);
}
__device__ __forceinline__ __amdgpu_buffer_rsrc_t mkrsrc(const void* p, int bytes) {
  return __builtin_amdgcn_make_buffer_rsrc((void*)p, 0, bytes, 0x00020000);
}

#define STAMP(i) do {} while (0)
#define STAMPC(i) do {} while (0)
#define FLUSH() do {} while (0)
#define NOTE(i, v) do {} while (0)
constexpr int L_TOTAL = L_END;

struct Ctx {
  const u16 *q, *k, *v, *shk, *hk, *dsk, *wr, *wg, *wu, *wd;
  u16* out;
  char* ws;
  const char* code;
  __amdgpu_buffer_rsrc_t rws;
  LAS char* sm;
  int tid, lane, wave, b, C, cg, mw;
  bool is_conv;
  u32 t0;
};

__device__ __forceinline__ int gate_off(int gate, int rep, int shard) { return WS_G + gate * GATE_B + (rep * 16 + shard) * 256; }
__device__ __forceinline__ u32 gate_add(const Ctx& P, int off, int val) {
  return (u32)__builtin_amdgcn_raw_ptr_buffer_atomic_add_i32(val, P.rws, off, 0, 0);
}
__device__ __forceinline__ u32 gate_post(const Ctx& P, int gate, bool on, u32 inc) {
  return gate_add(P, (on && P.lane < 16) ? gate_off(gate, P.lane, P.b & 15) : OOB, (int)inc);
}
__device__ __forceinline__ void gate_confirm(const Ctx& P, int gate, u32 old, u32 inc) {
  bool bad = P.lane < 16;
  _Pragma("nounroll") for (u32 spin = 0; spin < SPIN_CAP; spin++) {
    const u32 d = old - GMAGIC;
    bad = bad && ((d & 0xF0u) != 0u || d != (d & 0xFu) * inc);
    if (__builtin_expect(!__any(bad), 1)) break;
    NOTE(26, spin + 1);
    __builtin_amdgcn_s_sleep(2);
    old = gate_add(P, bad ? gate_off(gate, P.lane, P.b & 15) : OOB, (int)inc);
  }
}
__device__ __forceinline__ void gate_wait(const Ctx& P, int gate, u32 expect, u32 inc, u32 cap = GATE_CAP) {
  const int off = P.lane < 16 ? gate_off(gate, P.b >> 4, P.lane) : OOB;
  const u32 want = P.lane < 16 ? GMAGIC + expect * inc : 0u;
  _Pragma("nounroll") for (u32 spin = 0; spin < cap; spin++)
    if (__all(gate_add(P, off, 0) == want)) break;
}
__device__ __forceinline__ u32 gate_inc(u32 tag) { return 1u + (tag << 8); }
__device__ __forceinline__ void post_u_complete(const Ctx& P, u32 inc) {
  lds_wait<1>(P.sm, C_DRAIN, 5);
  gate_confirm(P, G_V, gate_post(P, G_V, true, inc), inc);
}

__device__ __forceinline__ void hcopy_store(LAS char* dst, v4u lo, v4u hi, bool first) {
  u32 d[8], o[7];
#pragma unroll
  for (int e = 0; e < 4; e++) { d[e] = lo[e]; d[4 + e] = hi[e]; }
#pragma unroll
  for (int e = 0; e < 7; e++) o[e] = __builtin_amdgcn_alignbit(d[e + 1], d[e], 16);
  constexpr int BASE[8] = {0, 260, 519, 778, 1037, 1296, 1556, 1815};
#pragma unroll
  for (int e = 0; e < 4; e++) {
    v4u a = {d[e], d[e + 1], d[e + 2], d[e + 3]}, c = {o[e], o[e + 1], o[e + 2], o[e + 3]};
    if (e > 0 || first) *(LAS v4u*)(dst + BASE[2 * e] * 16) = a;
    *(LAS v4u*)(dst + BASE[2 * e + 1] * 16) = c;
  }
}
__device__ __forceinline__ u32 touch_w1(const Ctx& P, int mat, int blk);
__device__ __forceinline__ u32 hcopy_first(const Ctx& P, int ch0) {
  const __amdgpu_buffer_rsrc_t rh = mkrsrc(P.hk + (size_t)(4 * P.cg + ch0) * S, 2 * S * 2);
  v4u lo[8];
#pragma unroll
  for (int r = 0; r < 8; r++) lo[r] = __builtin_amdgcn_raw_buffer_load_b128(rh, P.lane * 16 + (r & 3) * 1024, (r >> 2) * 4096, NT_H);
  __builtin_amdgcn_sched_barrier(0);
  STAMP(12);
  LAS char* dst = P.sm + L_H + ch0 * HC_B + (P.lane + 2) * 16;
#pragma unroll
  for (int r = 0; r < 8; r++) *(LAS v4u*)(dst + (r >> 2) * HC_B + (r & 3) * 1024) = lo[r];
  lds_signal(P.sm, C_H0);
  u32 t = 0, t2 = 0;
  if (HELPERS != 0 && PF_WHEN == 0 && PF_CE >= 1) t = touch_w1(P, ch0 >> 1, 25 + (P.b >> 3));
  if (HELPERS != 0 && PF_WHEN == 0 && PF_CE >= 2) t2 = touch_w1(P, ch0 >> 1, 43 + (P.b >> 3));
  __builtin_amdgcn_sched_barrier(0);
  STAMP(1);
  {
    const int c = P.lane & 1, m1 = (P.lane >> 1) & 1;
    const v4u z = {0u, 0u, 0u, 0u};
    v4u h0 = *(const LAS v4u*)(P.sm + L_H + (ch0 + c) * HC_B + 32);
    h0 = m1 ? h0 : z;
    if (P.lane < 4) hcopy_store(P.sm + L_H + (ch0 + c) * HC_B + m1 * 16, z, h0, true);
  }
  return t | t2;
}
template <int NR> __device__ __forceinline__ void hcopy_rest(const Ctx& P, int r0) {
  v4u lo[NR], hi[NR];
  LAS char* dst[NR];
#pragma unroll
  for (int r = 0; r < NR; r++) {
    dst[r] = P.sm + L_H + ((r0 + r) >> 2) * HC_B + ((r0 + r) & 3) * 1024 + (P.lane + 2) * 16;
    lo[r] = *(const LAS v4u*)dst[r];
    hi[r] = *(const LAS v4u*)(dst[r] + 16);
  }
#pragma unroll
  for (int r = 0; r < NR; r++) hcopy_store(dst[r], lo[r], hi[r], false);
  lds_signal(P.sm, C_H);
}

__device__ __forceinline__ void ffn_gate_up(const Ctx& P) {
  LAS char* sm = P.sm;
  const int lane = P.lane, wave = P.wave, n16 = lane & 15, g4 = lane >> 4;
  v4u W1[3][8];
  const GAS char* wp = (const GAS char*)(n16 < 8 ? P.wg : P.wu) + ((size_t)(96 * wave + 8 * g4) * F + 64 * P.mw + 8 * (n16 & 7)) * 2;
  if (wave >= PF_FIRST || (SLOW_FIX && (P.mw & 7) == 3)) lds_wait<1>(sm, wave < 2 ? C_PUB : C_W1, wave < 2 ? 1 : 2);
  __builtin_amdgcn_sched_barrier(0);
#pragma unroll
  for (int i = 0; i < 3; i++)
#pragma unroll
    for (int jj = 0; jj < 8; jj++) {
      const GAS v4u* a = (const GAS v4u*)(wp + (size_t)(32 * i + jj) * (F * 2));
      W1[i][jj] = NT_W ? __builtin_nontemporal_load(a) : *a;
    }
  __builtin_amdgcn_sched_barrier(0);
  lds_signal(sm, C_H0);
  STAMP(3);
  v4u Bf[3][8];
#pragma unroll
  for (int i = 0; i < 3; i++)
#pragma unroll
    for (int e = 0; e < 8; e++) {
#pragma unroll
      for (int d = 0; d < 4; d++)
        Bf[i][e][d] = __builtin_amdgcn_perm(W1[i][2 * d + 1][e >> 1], W1[i][2 * d][e >> 1], (e & 1) ? 0x07060302u : 0x05040100u);
      asm volatile("" : "+v"(Bf[i][e]));
    }
  STAMP(17);
  lds_wait<1>(sm, C_XS, 2);
  STAMP(5);
  f4 acc[8];
#pragma unroll
  for (int e = 0; e < 8; e++) acc[e] = (f4){0.f, 0.f, 0.f, 0.f};
#pragma unroll
  for (int i = 0; i < 3; i++) {
    const v4u A = *(const LAS v4u*)(sm + L_XS + (n16 & 7) * XS_STRIDE + (96 * wave + 32 * i + 8 * g4) * 2);
#pragma unroll
    for (int e = 0; e < 8; e++) acc[e] = mfma16(A, Bf[i][e], acc[e]);
  }
  {
    LAS char* dst = sm + L_RED + ((wave * 8 + 4 * (g4 & 1) + (g4 & 2)) * 128 + 8 * n16) * 4;
#pragma unroll
    for (int r = 0; r < 2; r++) {
      f4 a, c;
#pragma unroll
      for (int e = 0; e < 4; e++) { a[e] = g4 >= 2 ? acc[e][2 + r] : acc[e][r]; c[e] = g4 >= 2 ? acc[4 + e][2 + r] : acc[4 + e][r]; }
      *(LAS f4*)(dst + r * 512) = a;
      *(LAS f4*)(dst + r * 512 + 16) = c;
    }
  }
  lds_signal(sm, C_RED);
  STAMP(18);
  if (wave >= 2) {
    const int tk = 2 * (wave - 2) + (lane >> 5);
    const LAS char* src = sm + L_RED + (tk * 128 + 2 * (lane & 31)) * 4;
    lds_wait(sm, C_RED, 6);
    f2 G = {0.f, 0.f}, U = {0.f, 0.f};
#pragma unroll
    for (int w = 0; w < 6; w++) {
      G += *(const LAS f2*)(src + w * 4096);
      U += *(const LAS f2*)(src + w * 4096 + 256);
    }
    const u32 gb = pk2(G.x, G.y), ub = pk2(U.x, U.y);
    const float g0 = lo16(gb), g1 = hi16(gb);
    const u32 sg = pk2(g0 * __builtin_amdgcn_rcpf(1.0f + __expf(-g0)), g1 * __builtin_amdgcn_rcpf(1.0f + __expf(-g1)));
    *(LAS u32*)(sm + L_HB + tk * HB_STRIDE + (lane & 31) * 4) = pk2(lo16(sg) * lo16(ub), hi16(sg) * hi16(ub));
    lds_signal(sm, C_HB);
    STAMP(15);
  }
}

struct Down {
  v4u W2[2][8];
  __device__ __forceinline__ void load(const Ctx& P, int cu) {
    const int n16 = P.lane & 15, g4 = P.lane >> 4;
    const __amdgpu_buffer_rsrc_t rd = mkrsrc(P.wd + (size_t)64 * P.mw * D, 64 * D * 2);
    const int o2 = (cu == 4 && n16 >= 8) ? OOB : (8 * g4 * D + 128 * cu + 8 * n16) * 2;
    __builtin_amdgcn_sched_barrier(0);
#pragma unroll
    for (int ks = 0; ks < 2; ks++)
#pragma unroll
      for (int jj = 0; jj < 8; jj++)
        W2[ks][jj] = __builtin_amdgcn_raw_buffer_load_b128(rd, o2 + (jj & 3) * (D * 2), (32 * ks + (jj & 4)) * (D * 2), NT_W);
    __builtin_amdgcn_sched_barrier(0);
  }
  __device__ __forceinline__ void turn() {
    __builtin_amdgcn_sched_barrier(0);
#pragma unroll
    for (int ks = 0; ks < 2; ks++) {
      v4u t[8];
#pragma unroll
      for (int e = 0; e < 8; e++)
#pragma unroll
        for (int d = 0; d < 4; d++)
          t[e][d] = __builtin_amdgcn_perm(W2[ks][2 * d + 1][e >> 1], W2[ks][2 * d][e >> 1], (e & 1) ? 0x07060302u : 0x05040100u);
#pragma unroll
      for (int e = 0; e < 8; e++) { W2[ks][e] = t[e]; asm volatile("" : "+v"(W2[ks][e])); }
    }
  }
  __device__ __forceinline__ void mma(const Ctx& P, int cu, v4u A0, v4u A1, u32 tag12) {
    const int n16 = P.lane & 15, g4 = P.lane >> 4;
    const int off = WS_P + P.mw * PART_B + (32 * cu + 2 * (n16 & 7)) * 128 + (n16 >> 3) * 64 + g4 * 16;
    f4 ad[8];
#pragma unroll
    for (int e = 0; e < 8; e++) ad[e] = mfma16(A0, W2[0][e], (f4){0.f, 0.f, 0.f, 0.f});
#pragma unroll
    for (int e = 0; e < 8; e++) ad[e] = mfma16(A1, W2[1][e], ad[e]);
#pragma unroll
    for (int hh = 0; hh < 2; hh++) {
      v4u s0, s1;
#pragma unroll
      for (int e = 0; e < 4; e++) {
        const f4 a = ad[4 * hh + e];
        const u32 x = ((__float_as_uint(g4 >= 2 ? a[2] : a[0]) + 0x800u) & 0xFFFFF000u) | tag12;
        const u32 y = ((__float_as_uint(g4 >= 2 ? a[3] : a[1]) + 0x800u) & 0xFFFFF000u) | tag12;
        s0[e] = (u32)__builtin_amdgcn_update_dpp((int)x, (int)y, 0x128, 0xf, 0xc, false);
        s1[e] = (u32)__builtin_amdgcn_update_dpp((int)y, (int)x, 0x128, 0xf, 0x3, false);
      }
      __builtin_amdgcn_raw_buffer_store_b128(s0, P.rws, off + hh * 128, 0, AUX_SC1);
      if (cu != 4) __builtin_amdgcn_raw_buffer_store_b128(s1, P.rws, off + hh * 128 + 2048, 0, AUX_SC1);
    }
  }
};
template <bool EXTRA> __device__ __forceinline__ void ffn_down(const Ctx& P, int cu) {
  LAS char* sm = P.sm;
  const int n16 = P.lane & 15, g4 = P.lane >> 4;
  const u32 tag = *(const LAS u32*)(sm + L_REC), tag12 = tag & 0xFFFu;
  const bool two = EXTRA && cu == 2;
  Down d0, d1;
  lds_wait<1>(sm, C_H0, 6);
  d0.load(P, cu);
  STAMP(4);
  if (two) { d1.load(P, 4); d0.turn(); d1.turn(); } else { d0.turn(); }
  STAMP(19);
  lds_wait(sm, C_HB, 4);
  const v4u A0 = *(const LAS v4u*)(sm + L_HB + (n16 & 7) * HB_STRIDE + g4 * 16);
  const v4u A1 = *(const LAS v4u*)(sm + L_HB + (n16 & 7) * HB_STRIDE + 64 + g4 * 16);
  if (P_POST == 0 && lds_arrive(sm, C_DN, 3)) (void)gate_post(P, G_P, true, gate_inc(tag));
  STAMP(20);
  d0.mma(P, cu, A0, A1, tag12);
  if (P_POST == 1 && lds_arrive(sm, C_DN, 3)) (void)gate_post(P, G_P, true, gate_inc(tag));
  if (two) d1.mma(P, 4, A0, A1, tag12);
  if (P_POST == 2 && lds_arrive(sm, C_DN, 3)) (void)gate_post(P, G_P, true, gate_inc(tag));
  STAMP(21);
}

__device__ __forceinline__ u32 touch_w1(const Ctx& P, int mat, int blk) {
  const int r7 = (P.lane * 57) >> 9, c9 = P.lane - 9 * r7;
  return __builtin_amdgcn_raw_buffer_load_b32(mkrsrc(__builtin_amdgcn_readfirstlane(mat) ? P.wu : P.wg, D * F * 2), P.lane < 63 ? r7 * (F * 2) + c9 * 128 : OOB,
                                              (PF_R0 + 7 * blk) * (F * 2) + (P.b & 7) * 1152, NT_T);
}
__device__ __forceinline__ u32 touch_w2(const Ctx& P, int unit) {
  return __builtin_amdgcn_raw_buffer_load_b32(mkrsrc(P.wd, D * F * 2), unit < 81 ? P.lane * 128 : OOB,
                                              (P.b & 7) * (9 * 64 * D * 2) + unit * 8192, NT_T);
}

__device__ __forceinline__ v4u encode_y(v4u y, v4u u) {
  u64 c0, c1, c2, c3;
  asm("v_cmp_ne_u32_e64 %4, %0, %8\n\tv_cmp_ne_u32_e64 %5, %1, %9\n\tv_cmp_ne_u32_e64 %6, %2, %10\n\tv_cmp_ne_u32_e64 %7, %3, %11\n\t"
      "v_cndmask_b32_e64 %0, -1, %0, %4\n\tv_cndmask_b32_e64 %1, -1, %1, %5\n\ts_nop 0\n\t"
      "v_cndmask_b32_e64 %2, -1, %2, %6\n\tv_cndmask_b32_e64 %3, -1, %3, %7"
      : "+v"(y.x), "+v"(y.y), "+v"(y.z), "+v"(y.w), "=&s"(c0), "=&s"(c1), "=&s"(c2), "=&s"(c3)
      : "v"(u.x), "v"(u.y), "v"(u.z), "v"(u.w));
  return y;
}
__device__ __forceinline__ void publish_y(const Ctx& P, const LAS char* yb, int ystride, int tile0, int t, int step, int n) {
  const int gch = t & 3;
  const int goff = WS_U + (tile0 + (t >> 2)) * TILE_B + (4 * P.cg + gch) * 16;
#pragma unroll
  for (int it = 0; it < n; it++) {
    const v4u y4 = *(const LAS v4u*)(yb + gch * ystride + (it * step + (t >> 2)) * 16);
    const v4u u4 = *(const LAS v4u*)(P.sm + L_U + gch * UL_B + (32 + tile0 + it * step + (t >> 2)) * 16);
    __builtin_amdgcn_raw_buffer_store_b128(encode_y(y4, u4), P.rws, goff, it * step * TILE_B, AUX_SC1);
  }
}

__device__ __forceinline__ void gather_half(const Ctx& P, int tile0, v4u (&g)[8]) {
  const int goff = WS_U + (tile0 + (P.lane >> 2)) * TILE_B + (4 * P.cg + (P.lane & 3)) * 16;
#pragma unroll
  for (int it = 0; it < 8; it++) g[it] = __builtin_amdgcn_raw_buffer_load_b128(P.rws, goff, it * 16 * TILE_B, AUX_SC1);
}

__device__ __forceinline__ void verify_half(const Ctx& P, int tile0) {
  LAS char* sm = P.sm;
  lds_wait<1>(sm, C_TRUST, 1);
  v4u g[8];
  gather_half(P, tile0, g);
  LAS char* ud = sm + L_U + (P.lane & 3) * UL_B + (32 + tile0 + (P.lane >> 2)) * 16;
  u32 df = 0;
#pragma unroll
  for (int it = 0; it < 8; it++) {
    const v4u x = *(const LAS v4u*)(ud + it * 256) ^ g[it];
    df |= x.x | x.y;
    df |= x.z | x.w;
  }
  if (__builtin_expect(__any(df != 0), 0)) {
#pragma unroll
    for (int it = 0; it < 8; it++) *(LAS v4u*)(ud + it * 256) = g[it];
    lds_signal(sm, C_OK, 0x101u);
  } else {
    lds_signal(sm, C_OK);
  }
}

__device__ __forceinline__ void conv_phase(const Ctx& P, u32 inc) {
  LAS char* sm = P.sm;
  const int tid = P.tid, lane = P.lane, wave = P.wave, n16 = lane & 15, g4 = lane >> 4, gch = tid & 3;
  const int goff = WS_U + (tid >> 2) * TILE_B + (4 * P.cg + gch) * 16;
  LAS char* ud = sm + L_U + gch * UL_B + (32 + (tid >> 2)) * 16;
  const int p1 = n16 + 1, kap = p1 & 7;
  const int base16 = 258 * kap + ((0x98654320u >> (4 * kap)) & 15);
  const LAS char* pa = sm + L_H + wave * HC_B + (base16 + g4 + (p1 >> 3)) * 16;
  const LAS char* pb = sm + L_U + wave * UL_B + (32 + 2 * n16 + 1 - g4 - 28) * 16;
  lds_wait<1>(sm, C_H0, 2);
  hcopy_rest<3>(P, 3 * wave);
  STAMP(17);
  asm volatile("s_waitcnt vmcnt(0)" ::: "memory");
  lds_signal(sm, C_DRAIN);
  lds_wait<1>(sm, C_QQ, 5);
  lds_wait<1>(sm, C_GU, 1);
  __builtin_amdgcn_s_setprio(2);
  STAMP(3);
  if (U_DELAY) __builtin_amdgcn_s_sleep(U_DELAY);
  STAMP(4);
  v4u g[2];
#pragma unroll
  for (int it = 0; it < 2; it++) g[it] = __builtin_amdgcn_raw_buffer_load_b128(P.rws, goff, it * 64 * TILE_B, AUX_SC1);
  *(LAS v4u*)ud = g[0];
  *(LAS v4u*)(ud + 1024) = g[1];
  lds_signal(sm, C_U);
  STAMP(5);
  f4 acc[8];
  v4u A[2][8], B[2][4];
  lds_wait(sm, C_H, 6);
#pragma unroll
  for (int a = 0; a < 8; a++) A[0][a] = *(const LAS v4u*)(pa + a * 512);
  __builtin_amdgcn_sched_barrier(0);
  lds_wait(sm, C_U, 4);
  STAMP(6);
  v4u u4[2];
  _Pragma("nounroll") for (int pass = 0; pass < 2; pass++) {
#pragma unroll
    for (int t = 0; t < 8; t++) acc[t] = (f4){0.f, 0.f, 0.f, 0.f};
#pragma unroll
    for (int a = 0; a < 4; a++) B[0][a] = *(const LAS v4u*)(pb + a * 512 + 7 * 64);
    u32 seen;
    v4u A2[3][4], B2[3][4];
#pragma unroll
    for (int bb = 0; bb < 8; bb++) {
      __builtin_amdgcn_sched_barrier(0);
      if (bb == 6) seen = lds_peek(sm, C_U2);
      if (bb == 7) {
        if (__builtin_expect(__builtin_amdgcn_readfirstlane(seen) < 1u, 0)) lds_wait(sm, C_U2, 1);
        asm volatile("" ::: "memory");
        __builtin_amdgcn_sched_barrier(0);
      }
#pragma unroll
      for (int a = 0; a < 8; a++) {
        if (bb < 7) {
          A[(bb + 1) & 1][a] = *(const LAS v4u*)(pa + a * 512 + (bb + 1) * 64);
          if (a < 4) B[(bb + 1) & 1][a] = *(const LAS v4u*)(pb + a * 512 + (6 - bb) * 64);
        } else {
          A2[a >> 2][a & 3] = *(const LAS v4u*)(pa + (a & 3) * 512 + (a >> 2) * 64);
          B2[a >> 2][a & 3] = *(const LAS v4u*)(pb + (4 + (a & 3)) * 512 + (7 - (a >> 2)) * 64);
        }
      }
#pragma unroll
      for (int t = 0; t < 8; t++)
#pragma unroll
        for (int c = 0; c < 4; c++)
          if (c <= t) acc[t] = mfma16(A[bb & 1][t - c], B[bb & 1][c], acc[t]);
      if (bb < 7) {
#pragma unroll
        for (int i = 0; i < 12; i++) {
          __builtin_amdgcn_sched_group_barrier(0x8, 2, 0);
          __builtin_amdgcn_sched_group_barrier(0x100, 1, 0);
        }
        __builtin_amdgcn_sched_group_barrier(0x8, 2, 0);
      } else {
#pragma unroll
        for (int i = 0; i < 8; i++) {
          __builtin_amdgcn_sched_group_barrier(0x8, 3, 0);
          __builtin_amdgcn_sched_group_barrier(0x100, 2, 0);
        }
        __builtin_amdgcn_sched_group_barrier(0x8, 2, 0);
      }
    }
#pragma unroll
    for (int bb = 0; bb < 8; bb++) {
      __builtin_amdgcn_sched_barrier(0);
      if (bb == 1) {
        LAS char* yb = sm + L_STG + wave * Y1_B + n16 * 32 + g4 * 8;
#pragma unroll
        for (int t = 0; t < 4; t++) {
          v2u y2 = {umin(pk2(acc[t][0], acc[t][1]), MAXV), umin(pk2(acc[t][2], acc[t][3]), MAXV)};
          *(LAS v2u*)(yb + t * 512) = y2;
        }
      }
      if (bb == 2) lds_signal_nowait(sm, C_Y1);
      if (bb == 6) {
        seen = lds_peek(sm, C_OK);
        u4[0] = *(const LAS v4u*)(ud + 2048);
        u4[1] = *(const LAS v4u*)(ud + 3072);
      }
      if (bb == Y_POST) {
        if (wave == 0 && pass == 0) (void)gate_post(P, G_Y, true, inc);
        __builtin_amdgcn_sched_barrier(0);
      }
      if (bb < 6) {
#pragma unroll
        for (int a = 0; a < 4; a++) {
          A2[(bb + 2) % 3][a] = *(const LAS v4u*)(pa + a * 512 + (bb + 2) * 64);
          B2[(bb + 2) % 3][a] = *(const LAS v4u*)(pb + (4 + a) * 512 + (5 - bb) * 64);
        }
      }
#pragma unroll
      for (int c = 0; c < 4; c++)
#pragma unroll
        for (int t = c; t < 4; t++) acc[4 + t] = mfma16(A2[bb % 3][t - c], B2[bb % 3][c], acc[4 + t]);
      if (bb < 6) {
#pragma unroll
        for (int i = 0; i < 8; i++) {
          __builtin_amdgcn_sched_group_barrier(0x8, 1, 0);
          __builtin_amdgcn_sched_group_barrier(0x100, 1, 0);
        }
        __builtin_amdgcn_sched_group_barrier(0x8, 2, 0);
      }
    }
    __builtin_amdgcn_sched_barrier(0);
    if (__builtin_expect(!checked_redo(sm, seen), 1)) break;
    NOTE(26, 7);
#pragma unroll
    for (int a = 0; a < 8; a++) A[0][a] = *(const LAS v4u*)(pa + a * 512);
  }
  if (Y_POST == 8 && wave == 0) (void)gate_post(P, G_Y, true, inc);
  {
    LAS char* yb = sm + L_H + wave * (HC_B + 64) + n16 * 32 + g4 * 8;
#pragma unroll
    for (int t = 0; t < 4; t++) {
      v2u y2 = {umin(pk2(acc[4 + t][0], acc[4 + t][1]), MAXV), umin(pk2(acc[4 + t][2], acc[4 + t][3]), MAXV)};
      *(LAS v2u*)(yb + t * 512) = y2;
    }
  }
  lds_signal_nowait(sm, C_Y);
  const LAS char* yr = sm + L_H + gch * (HC_B + 64) + (tid >> 2) * 16;
  STAMP(7);
  lds_wait(sm, C_Y, 4);
  const v4u y0 = *(const LAS v4u*)yr, y1 = *(const LAS v4u*)(yr + 1024);
  __builtin_amdgcn_raw_buffer_store_b128(encode_y(y0, u4[0]), P.rws, goff, 128 * TILE_B, AUX_SC1);
  __builtin_amdgcn_raw_buffer_store_b128(encode_y(y1, u4[1]), P.rws, goff, 192 * TILE_B, AUX_SC1);
  STAMP(8);
}

struct EarlyX { v4u xa[9], wb[9]; };
struct Early {
  u32 kk[11], vv[8], dd;
  v4u sk;
  u64 epoch;
  u32 cx;
};

__device__ __forceinline__ void tile_wave(const Ctx& P, const Early& E, const bool MOE) {
  LAS char* sm = P.sm;
  const int tid = P.tid, lane = P.lane, wave = P.wave, C = P.C;
  const bool act = tid < 288;
  const int j4 = min(tid, 287) * 4;
  const int row0 = 8 * C * (D * 2);
  const u32 (&kk)[11] = E.kk;
  const u32 (&vv)[8] = E.vv;
  const v4u sk = E.sk;
  u32 qq[8];
  u32 gold, uinc;
  bool poster;
  u32 up[2][4];
  {
    f2 w[4], kf[11], ks[8];
    w[0] = (f2){lo16(sk.x), lo16(sk.z)}; w[1] = (f2){hi16(sk.x), hi16(sk.z)};
    w[2] = (f2){lo16(sk.y), lo16(sk.w)}; w[3] = (f2){hi16(sk.y), hi16(sk.w)};
#pragma unroll
    for (int i = 0; i < 11; i++) kf[i] = (f2){lo16(kk[i]), hi16(kk[i])};
#pragma unroll
    for (int t = 0; t < 8; t++) {
      ks[t] = __builtin_elementwise_fma(kf[t + 3], w[0], __builtin_elementwise_fma(kf[t + 2], w[1], __builtin_elementwise_fma(kf[t + 1], w[2], kf[t] * w[3])));
      asm volatile("" : "+v"(ks[t]));
    }
    __builtin_amdgcn_sched_barrier(0);
    lds_wait(sm, C_TAG, 1);
    uinc = gate_inc(*(const LAS u32*)(sm + L_REC));
    STAMP(12);
#pragma unroll
    for (int t = 0; t < 8; t++) ks[t] *= (f2){lo16(vv[t]), hi16(vv[t])};
#pragma unroll
    for (int i = 0; i < 4; i++) {
      up[0][i] = umin(pk2(ks[7 - 2 * i].x, ks[6 - 2 * i].x), MAXV);
      up[1][i] = umin(pk2(ks[7 - 2 * i].y, ks[6 - 2 * i].y), MAXV);
    }
    STAMP(1);
    LAS char* stg = sm + L_STG + wave * 2048;
    v4u p0 = {up[0][0], up[0][1], up[0][2], up[0][3]}, p1 = {up[1][0], up[1][1], up[1][2], up[1][3]};
    if (act) { *(LAS v4u*)(stg + lane * 32) = p0; *(LAS v4u*)(stg + lane * 32 + 16) = p1; }
    asm volatile("" ::: "memory");
    p0 = *(const LAS v4u*)(stg + lane * 16);
    p1 = *(const LAS v4u*)(stg + (wave < 4 ? 1024 : 0) + lane * 16);
    STAMP(13);
    const int so = WS_U + C * TILE_B + wave * 2048 + lane * 16;
    __builtin_amdgcn_raw_buffer_store_b128(p0, P.rws, so, 0, AUX_SC1);
    if (wave < 4) __builtin_amdgcn_raw_buffer_store_b128(p1, P.rws, so + 1024, 0, AUX_SC1);
  }
  __builtin_amdgcn_sched_barrier(0);
  poster = lds_arrive(sm, C_VER, 4);
  if (poster) gold = gate_post(P, G_U, true, uinc);
  STAMP(2);
  lds_wait(sm, C_STAT, 1);
#pragma unroll
  for (int i = 0; i < 8; i++) qq[i] = *(const LAS u32*)(sm + xrow_off(i) + j4);
  lds_signal(sm, C_QQ);
  u32 dd = E.dd;
  asm volatile("" : "+v"(dd));
  float xn[2][8], c0[2][8];
  auto prepare = [&]()
  {
    const float d0 = lo16(dd), d1 = hi16(dd);
#pragma unroll
    for (int s2 = 0; s2 < 4; s2++) {
      const f4 st = *(const LAS f4*)(sm + L_STAT + s2 * 16);
#pragma unroll
      for (int e = 0; e < 2; e++) {
        const int s = 2 * s2 + e;
        const float xa = lo16(qq[s]), xb = hi16(qq[s]);
        const float na = fmaf(xa, st[2 * e], st[2 * e + 1]), nb = fmaf(xb, st[2 * e], st[2 * e + 1]);
        const u32 pa = up[0][(7 - s) >> 1], pb = up[1][(7 - s) >> 1];
        const float ua = ((7 - s) & 1) ? hi16(pa) : lo16(pa), ub = ((7 - s) & 1) ? hi16(pb) : lo16(pb);
        xn[0][s] = na; xn[1][s] = nb;
        c0[0][s] = fmaf(na, fmaf(ua, d0, 1.0f), xa);
        c0[1][s] = fmaf(nb, fmaf(ub, d1, 1.0f), xb);
        asm volatile("" : "+v"(xn[0][s]), "+v"(xn[1][s]), "+v"(c0[0][s]), "+v"(c0[1][s]));
      }
    }
  };
  prepare();
  if (poster) gate_confirm(P, G_U, gold, uinc);
  if (!(P.is_conv && wave < 4)) {
    asm volatile("s_waitcnt vmcnt(0)" ::: "memory");
    lds_signal(sm, C_DRAIN);
  }
  STAMP(14);
  if (MOE) {
    ffn_gate_up(P);
    if (wave < 2) ffn_down<false>(P, wave);
  } else {
   if (P.is_conv) {
    if (wave < 4) {
      conv_phase(P, uinc);
    } else {
      lds_wait<1>(sm, C_QQ, 5);
      lds_wait<1>(sm, C_GU, 1);
      __builtin_amdgcn_s_sleep(3 + U_DELAY);
      v4u g[8];
      gather_half(P, 128, g);
#pragma unroll
      for (int it = 0; it < 8; it++) *(LAS v4u*)(sm + L_U + (lane & 3) * UL_B + (32 + 128 + it * 16 + (lane >> 2)) * 16) = g[it];
      lds_signal(sm, C_U2);
      STAMP(5);
      gate_wait(P, G_V, 16u, uinc, SPIN_CAP);
      lds_signal(sm, C_TRUST);
      STAMP(4);
      verify_half(P, 128);
      STAMP(6);
      lds_wait<2>(sm, C_Y1, 4);
      if (checked_redo(sm, 0u)) lds_wait<1>(sm, C_Y1, 8);
      publish_y(P, sm + L_STG, Y1_B, 0, lane, 32, 4);
    }
  } else {
    STAMP(5);
   }
  }
  lds_wait<1>(sm, C_SEL, 1);
  const u32 skip = __builtin_amdgcn_readfirstlane(*(const LAS u32*)(sm + L_REC + 4));
  const __amdgpu_buffer_rsrc_t ro = mkrsrc(P.out, S * D * 2);
  const int oo = act ? j4 : OOB;
  const int uoff = WS_U + C * TILE_B + j4 * 8;
  lds_wait<1>(sm, C_GY, 1);
  STAMP(9);
  v4u y0, y1;
  _Pragma("nounroll") for (u32 spin = 0; spin < SPIN_CAP; spin++) {
    asm volatile("" ::: "memory");
    y0 = __builtin_amdgcn_raw_buffer_load_b128(P.rws, uoff, 0, AUX_SC1);
    y1 = __builtin_amdgcn_raw_buffer_load_b128(P.rws, uoff + 16, 0, AUX_SC1);
    bool ok = true;
#pragma unroll
    for (int e = 0; e < 4; e++) ok = ok & (y0[e] != up[0][e]) & (y1[e] != up[1][e]);
    if (__builtin_expect(__all(ok), 1)) break;
    NOTE(25, spin + 1);
    __builtin_amdgcn_s_sleep(4);
  }
  STAMP(16);
#pragma unroll
  for (int e = 0; e < 4; e++) {
    const u32 a = y0[e] == SAME ? up[0][e] : y0[e], c = y1[e] == SAME ? up[1][e] : y1[e];
    const u32 o0 = pk2(fmaf(xn[0][2 * e], lo16(a), c0[0][2 * e]), fmaf(xn[1][2 * e], lo16(c), c0[1][2 * e]));
    const u32 o1 = pk2(fmaf(xn[0][2 * e + 1], hi16(a), c0[0][2 * e + 1]), fmaf(xn[1][2 * e + 1], hi16(c), c0[1][2 * e + 1]));
    const int so = row0 + (e >> 1) * 4 * (D * 2);
    if (!((skip >> (2 * e)) & 1)) __builtin_amdgcn_raw_buffer_store_b32(o0, ro, oo + ((2 * e) & 3) * (D * 2), so, OUT_AUX);
    if (!((skip >> (2 * e + 1)) & 1)) __builtin_amdgcn_raw_buffer_store_b32(o1, ro, oo + ((2 * e + 1) & 3) * (D * 2), so, OUT_AUX);
  }
}

__device__ __forceinline__ void route(const Ctx& P, u32 tag, u64 epoch, const int ROLE) {
  LAS char* sm = P.sm;
  const int lane = P.lane, wave = P.wave;
  (void)wave;
  v4u g[16];
  if (ROLE == 2) __builtin_amdgcn_s_setprio(0);
  if (ROLE == 0) {
    if (HELPERS != 0 && PF_WHEN != 0 && PF_CE != 0) {
      lds_wait<1>(sm, C_VER, PF_WHEN);
      u32 t[4] = {0u, 0u, 0u, 0u};
#pragma unroll
      for (int i = 0; i < 2 * PF_CE; i++) t[i] = touch_w1(P, i & 1, 25 + 18 * (i >> 1) + (P.b >> 3));
      asm volatile("" :: "v"(t[0]), "v"(t[1]), "v"(t[2]), "v"(t[3]));
      STAMP(18);
    }
    gate_wait(P, G_U, 16u, gate_inc(tag));
    lds_signal(sm, C_GU);
    STAMP(15);
    __builtin_amdgcn_s_setprio(0);
    post_u_complete(P, gate_inc(tag));
    STAMP(13);
    lds_wait<1>(sm, C_U, 4);
  } else {
    gate_wait(P, ROLE == 1 ? G_R : G_U, 16u, gate_inc(tag));
  }
  STAMP(3);
  _Pragma("nounroll") for (u32 spin = 0; spin < SPIN_CAP; spin++) {
    asm volatile("" ::: "memory");
#pragma unroll
    for (int i = 0; i < 16; i++)
      g[i] = __builtin_amdgcn_raw_buffer_load_b128(P.rws, WS_R + lane * 16 + (i & 3) * 1024, (i >> 2) * 4096, AUX_SC1);
    if (spin == 0) lds_signal(sm, C_PUB);
    u32 bad = 0;
#pragma unroll
    for (int i = 0; i < 16; i++) {
      bad = __builtin_amdgcn_bitop3_b32(bad, g[i].y, tag, 0xF6);
      bad = __builtin_amdgcn_bitop3_b32(bad, g[i].w, tag, 0xF6);
    }
    if (__builtin_expect(__all(bad == 0), 1)) break;
    NOTE(24, spin + 1);
    __builtin_amdgcn_s_sleep(4);
  }
  STAMP(4);
  if (P.b == 0 && lane == 0) __hip_atomic_store((GAS u64*)(P.ws + WS_E), epoch, __ATOMIC_RELAXED, __HIP_MEMORY_SCOPE_AGENT);
  u32 k1 = umax(g[0].x, g[0].z), k2 = umin(g[0].x, g[0].z), k3 = 0;
#pragma unroll
  for (int i = 2; i < 32; i++) {
    const u32 x = (i & 1) ? g[i >> 1].z : g[i >> 1].x;
    k3 = umed3(k2, k3, x);
    k2 = umed3(k1, k2, x);
    k1 = umax(k1, x);
  }
  u32 tau = umax(k1, dppu<0xB1>(k1));
  tau = umax(tau, dppu<0x4E>(tau));
  tau = umax(tau, dppu<0x141>(tau));
  tau = umin(tau, dppu<0x140>(tau));
  tau = xrow_umin(tau);
  LAS u32* cand = (LAS u32*)(sm + L_CAND);
  const u64 m1 = __ballot(k1 >= tau), m2 = __ballot(k2 >= tau), m3 = __ballot(k3 >= tau);
  const u32 n1 = __builtin_popcountll(m1), n2 = n1 + __builtin_popcountll(m2), n = n2 + __builtin_popcountll(m3);
  bool slow = n > 64;
  if (__builtin_expect(!slow, 1)) {
    const u32 p1 = __builtin_amdgcn_mbcnt_hi((u32)(m1 >> 32), __builtin_amdgcn_mbcnt_lo((u32)m1, 0u));
    const u32 p2 = __builtin_amdgcn_mbcnt_hi((u32)(m2 >> 32), __builtin_amdgcn_mbcnt_lo((u32)m2, n1));
    const u32 p3 = __builtin_amdgcn_mbcnt_hi((u32)(m3 >> 32), __builtin_amdgcn_mbcnt_lo((u32)m3, n2));
    cand[k1 >= tau ? p1 : 64] = k1;
    cand[k2 >= tau ? p2 : 64] = k2;
    cand[k3 >= tau ? p3 : 64] = k3;
    asm volatile("" ::: "memory");
    const u32 mine = cand[lane];
    u32 rank = 0;
    _Pragma("nounroll") for (u32 jj = 0; jj < n; jj += 8) {
      const v4u c4 = *(const LAS v4u*)(cand + jj), d4 = *(const LAS v4u*)(cand + jj + 4);
      rank += (c4.x > mine) + (c4.y > mine) + (c4.z > mine) + (c4.w > mine);
      rank += (d4.x > mine) + (d4.y > mine) + (d4.z > mine) + (d4.w > mine);
    }
    const bool in = (u32)lane < n && rank < 8;
    if (in) ((LAS u32*)(sm + L_SEL))[rank] = mine;
    slow = __any(in && (u32)lane >= n2);
  }
  if (__builtin_expect(slow, 0)) {
    _Pragma("nounroll") for (int r = 0; r < 8; r++) {
      u32 mx = 0;
#pragma unroll
      for (int i = 0; i < 16; i++) mx = umax3(mx, g[i].x, g[i].z);
      mx = wave_umax(mx);
#pragma unroll
      for (int i = 0; i < 16; i++) { g[i].x = g[i].x == mx ? 0u : g[i].x; g[i].z = g[i].z == mx ? 0u : g[i].z; }
      if (lane == 0) ((LAS u32*)(sm + L_SEL))[r] = mx;
    }
  }
  asm volatile("" ::: "memory");
  {
    const u32 tk = 2047u - (((LAS u32*)(sm + L_SEL))[lane & 7] & 2047u);
    u32 mk = (int)(tk >> 3) == P.C ? 1u << (tk & 7) : 0u;
    mk |= dppu<0xB1>(mk);
    mk |= dppu<0x4E>(mk);
    mk |= dppu<0x141>(mk);
    if (lane == 0) *(LAS u32*)(sm + L_REC + 4) = mk;
  }
  lds_signal(sm, C_SEL);
  STAMP(5);
}

__device__ __forceinline__ u32 load_msd(const Ctx& P, int tok, u32 tag) {
  v2u g;
  _Pragma("nounroll") for (u32 spin = 0; spin < SPIN_CAP; spin++) {
    asm volatile("" ::: "memory");
    g = __builtin_amdgcn_raw_buffer_load_b64(P.rws, WS_M + tok * 8, 0, AUX_SC1);
    if (__builtin_expect(__all(g.y == tag), 1)) break;
    __builtin_amdgcn_s_sleep(4);
  }
  return g.x;
}

__device__ __forceinline__ void routed_rows(const Ctx& P) {
  LAS char* sm = P.sm;
  const int lane = P.lane, wave = P.wave, C = P.C, cg = P.cg;
  (void)wave; (void)C; (void)cg;
  const __amdgpu_buffer_rsrc_t rq = mkrsrc(P.q, S * D * 2);
  {
    const int cw = wave - 6;
    const u32 tag = *(const LAS u32*)(sm + L_REC);
    const v4u s4 = *(const LAS v4u*)(sm + L_SEL + cw * 16);
    const int n16 = lane & 15, g4 = lane >> 4;
    const u32 ks = (g4 & 2) ? ((g4 & 1) ? s4.w : s4.z) : ((g4 & 1) ? s4.y : s4.x);
    const int mtok = 2047 - (int)(ks & 2047u);
    const int xo = mtok * (D * 2) + n16 * 16;
    v4u rr[5];
#pragma unroll
    for (int i = 0; i < 5; i++) rr[i] = __builtin_amdgcn_raw_buffer_load_b128(rq, (i < 4 || n16 < 8) ? xo + i * 256 : OOB, 0, 0);
    v2u mg = __builtin_amdgcn_raw_buffer_load_b64(P.rws, WS_M + mtok * 8, 0, AUX_SC1);
    __builtin_amdgcn_sched_barrier(0);
    lds_signal(sm, C_W1);
    __builtin_amdgcn_sched_barrier(0);
    u32 msd = mg.x;
    if (__builtin_expect(!__all(mg.y == tag), 0)) msd = load_msd(P, mtok, tag);
    STAMP(15);
    const float rs = __builtin_amdgcn_rcpf(lo16(msd)), nm = -hi16(msd) * rs;
    LAS char* dst = sm + L_XS + (cw * 4 + g4) * XS_STRIDE + n16 * 16;
#pragma unroll
    for (int i = 0; i < 5; i++) {
      v4u a;
#pragma unroll
      for (int e = 0; e < 4; e++) a[e] = pk2(fmaf(lo16(rr[i][e]), rs, nm), fmaf(hi16(rr[i][e]), rs, nm));
      if (i < 4 || n16 < 8) *(LAS v4u*)(dst + i * 256) = a;
    }
    lds_signal(sm, C_XS);
    STAMP(6);
    __builtin_amdgcn_s_setprio(0);
    ffn_down<true>(P, wave - 4);
  }
}

__device__ __forceinline__ void aux_wave(const Ctx& P, const int ROLE) {
  LAS char* sm = P.sm;
  const int lane = P.lane, wave = P.wave, C = P.C, cg = P.cg;
  (void)wave; (void)C; (void)cg;
  u32 tag;
    if (CODE_PF && wave == 6) asm volatile("s_waitcnt vmcnt(0)" ::: "v255");
    if (ROLE != 0 && wave == 5) {
      __builtin_amdgcn_s_setprio(3);
      lds_wait<1>(sm, C_TAG, 1);
      if (SLOW_FIX && ROLE == 2) {
        const int g = P.b & 7, idx = (P.b - NCONV - NMOE) >> 3, sl = 9 * g + ((3 - g) & 7);
        lds_wait<1>(sm, C_VER, 3);
        u32 t0 = 0, t1 = 0;
        if (idx < 3) {
          t0 = __builtin_amdgcn_raw_buffer_load_b32(mkrsrc(P.wg, D * F * 2), lane * (F * 2), idx * 64 * (F * 2) + sl * 128, 0);
          t1 = __builtin_amdgcn_raw_buffer_load_b32(mkrsrc(P.wu, D * F * 2), lane * (F * 2), idx * 64 * (F * 2) + sl * 128, 0);
        } else if (g == 3) {
          const u16* wm = idx == 3 ? P.wg : P.wu;
          t0 = __builtin_amdgcn_raw_buffer_load_b32(mkrsrc(wm, D * F * 2), lane * (F * 2), 35 * 128, 0);
          t1 = __builtin_amdgcn_raw_buffer_load_b32(mkrsrc(wm, D * F * 2), lane * (F * 2), 64 * (F * 2) + 35 * 128, 0);
          t1 |= __builtin_amdgcn_raw_buffer_load_b32(mkrsrc(wm, D * F * 2), lane * (F * 2), 128 * (F * 2) + 35 * 128, 0);
        }
        asm volatile("" :: "v"(t0), "v"(t1));
      }
      post_u_complete(P, gate_inc(*(const LAS u32*)(sm + L_REC)));
      __builtin_amdgcn_s_setprio(0);
      STAMP(13);
    }
    if (ROLE == 2 && HELPERS && wave == 6) {
      lds_wait<1>(sm, C_VER, 5);
      const int idx = (P.b - NCONV - NMOE) >> 3;
      u32 ta = touch_w1(P, 0, idx), tb = touch_w1(P, 1, idx);
#pragma unroll
      for (int i = 1; i < 5; i++) {
        const u32 tc = touch_w1(P, 0, i * 5 + idx), td = touch_w1(P, 1, i * 5 + idx);
        asm volatile("" :: "v"(ta), "v"(tb));
        ta = tc; tb = td;
      }
      asm volatile("" :: "v"(ta), "v"(tb));
      STAMP(5);
    }
    if (ROLE == 0) {
      __builtin_amdgcn_s_sleep(H_DELAY);
      const u32 tt = hcopy_first(P, 2 * (wave - 5));
      lds_wait(sm, C_H0, 2);
      hcopy_rest<2>(P, 12 + 2 * (wave - 5));
      asm volatile("" :: "v"(tt));
      STAMP(3);
      if (wave == 5) {
        lds_wait<1>(sm, C_U, 4);
        verify_half(P, 0);
        STAMP(6);
        lds_wait<2>(sm, C_Y1, 4);
        if (checked_redo(sm, 0u)) lds_wait<1>(sm, C_Y1, 8);
        publish_y(P, sm + L_STG + 256, Y1_B, 16, lane, 32, 4);
      } else if (HELPERS) {
        lds_wait<1>(sm, C_U2, 1);
        u32 t[9];
#pragma unroll
        for (int i = 0; i < 4; i++) {
          const int u = 50 + 36 * PF_CE + i * 18 + (P.b >> 3);
          t[5 + i] = (i >= 4 - 2 * PF_CE) ? 0u : touch_w1(P, u & 1, u >> 1);
        }
#pragma unroll
        for (int i = 0; i < 5; i++) t[i] = touch_w2(P, i * 18 + (P.b >> 3));
        asm volatile("" :: "v"(t[5]), "v"(t[6]), "v"(t[7]), "v"(t[8]));
        STAMP(5);
        asm volatile("" :: "v"(t[0]), "v"(t[1]), "v"(t[2]), "v"(t[3]), "v"(t[4]));
        STAMP(6);
      }
    }
    if (wave == 5) {
      if (ROLE == 1) {
        ffn_gate_up(P);
        gate_wait(P, G_Y, 9u, gate_inc(*(const LAS u32*)(sm + L_REC)));
        lds_signal(sm, C_GY);
      }
      return;
    }
    lds_wait<1>(sm, C_TAG, 1);
    tag = *(const LAS u32*)(sm + L_REC);
    if (ROLE == 1) { lds_wait<1>(sm, C_SEL, 1); __builtin_amdgcn_s_setprio(3); }
  if (ROLE != 1) {
    gate_wait(P, G_Y, 9u, gate_inc(tag));
    lds_signal(sm, C_GY);
    STAMP(7);
  }
}

__device__ __forceinline__ void router_stats(const Ctx& P, Early& E, const EarlyX& X, const int ROLE) {
  LAS char* sm = P.sm;
  const int lane = P.lane, C = P.C;
  u32 tag;
    const int m = lane & 15, g4 = lane >> 4;
    u64 epoch = E.epoch;
    const v4u (&xa)[9] = X.xa;
    const v4u (&wb)[9] = X.wb;
    asm volatile("" : "+v"(epoch) :: "memory");
    epoch += 1;
    tag = (u32)epoch;
    if (lane == 0) *(LAS u32*)(sm + L_REC) = tag;
    lds_signal(sm, C_TAG);
    STAMP(1);
    f4 gx = {0.f, 0.f, 0.f, 0.f}, gw = {0.f, 0.f, 0.f, 0.f}, gs = {0.f, 0.f, 0.f, 0.f};
#pragma unroll
    for (int i = 0; i < 9; i++) gx = mfma16(xa[i], xa[i], gx);
    __builtin_amdgcn_sched_barrier(0);
    const u32 gold = gate_post(P, G_R, true, gate_inc(tag));
    STAMP(12);
    __builtin_amdgcn_sched_barrier(0);
#pragma unroll
    for (int i = 0; i < 9; i++) { gw = mfma16(wb[i], xa[i], gw); gs = mfma16(wb[i], wb[i], gs); }
    float sxx = (m & 2) ? ((m & 1) ? gx[3] : gx[2]) : ((m & 1) ? gx[1] : gx[0]);
    sxx = __uint_as_float(__builtin_amdgcn_ds_bpermute((m + (m & 12) * 4) * 4, __float_as_uint(sxx)));
    float sxw = m < 8 ? gw[0] : gw[1], sx = gw[2];
    sxw += dppf<0x128>(sxw);
    sx += dppf<0x128>(sx);
    sxx += dppf<0x128>(sxx);
    const float sw = __uint_as_float(__builtin_amdgcn_readlane(__float_as_uint(gs[0] + gs[1]), 2));
    const float mean = sx * (1.0f / D);
    const float var = fmaxf(fmaf(-mean, mean, sxx * (1.0f / D)), 0.f);
    const float mu = rbf(mean);
    const float sd = rbf(__builtin_amdgcn_sqrtf(rbf(rbf(var) + 1e-5f)));
    const float rs = __builtin_amdgcn_rcpf(sd);
    const u32 r = pk2(0.f, fmaf(-mu, sw, sxw) * rs);
    u32 key = ((r ^ (((int)r >> 31) | 0x80000000u)) & 0xFFFF0000u) | (u32)(2047 - (8 * C + (m & 7)));
    u32 msd = __float_as_uint(mu) | (__float_as_uint(sd) >> 16);
    asm volatile("" : "+v"(key), "+v"(msd));
    const v2u rec = {m < 8 ? key : msd, tag};
    const int ro = lane < 16 ? (m < 8 ? WS_R : WS_M) + (8 * C + (m & 7)) * 8 : OOB;
    __builtin_amdgcn_raw_buffer_store_b64(rec, P.rws, ro, 0, AUX_SC1);
    STAMP(2);
    if (lane < 8) { f2 st = {rs, -mu * rs}; *(LAS f2*)(sm + L_STAT + m * 8) = st; }
    {
      const int t = m & 7, t3 = (t * 11) >> 5;
      LAS char* xd = sm + L_U + t3 * UL_B + 512 + (t - 3 * t3) * (D * 2) + (m >> 3) * 64 + g4 * 16;
#pragma unroll
      for (int i = 0; i < 9; i++) *(LAS v4u*)(xd + i * 128) = xa[i];
    }
    lds_signal(sm, C_STAT);
    E.epoch = epoch;
    E.cx = gold;
}

__device__ __forceinline__ void router_wave(const Ctx& P, const Early& E, const int ROLE) {
  LAS char* sm = P.sm;
  const int lane = P.lane, wave = P.wave, C = P.C, cg = P.cg;
  (void)wave; (void)C; (void)cg;
  u32 tag;
    const u64 epoch = E.epoch;
    tag = (u32)epoch;
    const u32 gold = E.cx;
    gate_confirm(P, G_R, gold, gate_inc(tag));
    route(P, tag, epoch, ROLE);
  if (ROLE == 0) {
    const int pi = lane & 7, ti = 4 * (pi & 1) + (pi & 2) + (pi >> 2);
    const u32 kx = ((LAS u32*)(sm + L_SEL))[ti];
    const int tok = 2047 - (int)(kx & 2047u);
    const v2u x2 = *(const GAS v2u*)((const GAS char*)P.q + (size_t)tok * (D * 2) + cg * 8);
    const v2u d2 = *(const GAS v2u*)((const GAS char*)P.dsk + cg * 8);
    const u32 msd = load_msd(P, tok, tag);
    const float rs = __builtin_amdgcn_rcpf(lo16(msd)), nm = -hi16(msd) * rs;
    const u32 rb = kx & 0xFFFF0000u;
    const float r = __uint_as_float((rb & 0x80000000u) ? (rb ^ 0x80000000u) : (~rb & 0xFFFF0000u));
    const float rsig = rbf(__builtin_amdgcn_rcpf(1.0f + __expf(-r)));
    STAMP(6);
    gate_wait(P, G_P, lane < 8 ? 5u : 4u, gate_inc(tag));
    STAMP(7);
    v4u g[9];
    const u32 tag12 = tag & 0xFFFu;
    const int po = WS_P + (lane >> 3) * PART_B + cg * 128 + pi * 16;
#pragma unroll
    for (int i = 0; i < 9; i++) g[i] = __builtin_amdgcn_raw_buffer_load_b128(P.rws, po, 8 * i * PART_B, AUX_SC1);
    __builtin_amdgcn_sched_barrier(0);
    lds_wait<1>(sm, C_Y, 4);
    float o[4];
#pragma unroll
    for (int e = 0; e < 4; e++) {
      const float y = lo16(*(const LAS u16*)(sm + (tok < 1024 ? L_STG + e * Y1_B : L_H + e * (HC_B + 64) - 2048) + tok * 2));
      const float u = lo16(*(const LAS u16*)(sm + L_U + e * UL_B + (32 + (tok >> 3)) * 16 + (7 - (tok & 7)) * 2));
      const float x = (e & 1) ? hi16(x2[e >> 1]) : lo16(x2[e >> 1]);
      const float dk = (e & 1) ? hi16(d2[e >> 1]) : lo16(d2[e >> 1]);
      const float n = fmaf(x, rs, nm);
      o[e] = fmaf(n, fmaf(u, dk, y), x) + n;
      asm volatile("" : "+v"(o[e]));
    }
    __builtin_amdgcn_sched_barrier(0);
    _Pragma("nounroll") for (u32 spin = 0; spin < SPIN_CAP; spin++) {
      u32 bad = 0;
#pragma unroll
      for (int i = 0; i < 9; i++) {
        g[i] ^= tag12;
        bad |= g[i].x | g[i].y;
        bad |= g[i].z | g[i].w;
      }
      if (__builtin_expect(__all((bad & 0xFFFu) == 0), 1)) break;
      NOTE(25, spin + 1);
      __builtin_amdgcn_s_sleep(4);
      asm volatile("" ::: "memory");
#pragma unroll
      for (int i = 0; i < 9; i++) g[i] = __builtin_amdgcn_raw_buffer_load_b128(P.rws, po, 8 * i * PART_B, AUX_SC1);
    }
    STAMP(8);
    f4 s4 = __builtin_bit_cast(f4, g[0]);
#pragma unroll
    for (int i = 1; i < 9; i++) s4 += __builtin_bit_cast(f4, g[i]);
#pragma unroll
    for (int e = 0; e < 4; e++) {
      float sv = s4[e];
      sv += dppf<0x128>(sv);
      o[e] = fmaf(rbf(xrow_sum(sv)), rsig, o[e]);
    }
    if (lane < 8) {
      v2u o2 = {pk2(o[0], o[1]), pk2(o[2], o[3])};
      __builtin_amdgcn_raw_buffer_store_b64(o2, mkrsrc(P.out, S * D * 2), tok * (D * 2) + cg * 8, 0, OUT_AUX);
    }
  }
}

__global__ __launch_bounds__(NTHR, 2) void fused_kernel(
    const u16* __restrict__ q, const u16* __restrict__ k, const u16* __restrict__ v, const u16* __restrict__ shk,
    const u16* __restrict__ hk, const u16* __restrict__ dsk, const u16* __restrict__ wr, const u16* __restrict__ wg,
    const u16* __restrict__ wu, const u16* __restrict__ wd, u16* __restrict__ out, char* ws) {
  extern __shared__ __attribute__((aligned(16))) char smem_[];
  Ctx P;
  P.q = q; P.k = k; P.v = v; P.shk = shk; P.hk = hk; P.dsk = dsk; P.wr = wr; P.wg = wg; P.wu = wu; P.wd = wd;
  P.out = out; P.ws = ws;
  u64 pc;
  asm volatile("s_getpc_b64 %0" : "=s"(pc));
  P.code = (const char*)(pc & ~(u64)127);
  P.sm = (LAS char*)smem_;
  P.tid = threadIdx.x; P.lane = P.tid & 63;
  P.wave = __builtin_amdgcn_readfirstlane(P.tid >> 6);
  const int b = blockIdx.x;
  P.b = b;
  P.C = (b & 7) * 32 + (b >> 3);
  P.is_conv = b < NCONV;
  const bool is_moe = !P.is_conv && b < NCONV + NMOE;
  P.cg = (b & 7) * 18 + (b >> 3);
  P.mw = (b & 7) * 9 + ((b - NCONV) >> 3);
  P.rws = mkrsrc(ws, WS_END);
  const int lane = P.lane, wave = P.wave;
  (void)lane;
  STAMP(0); STAMPC(30);
  Early E;
  if (wave == 6) {
    constexpr int NL = (CODE_PF * 8 + 31) / 32;
    if (CODE_PF)
      asm volatile("global_load_dword v255, %0, off" :: "v"(P.code + min(((b >> 3) * NL + min(lane, NL - 1)) * 128, CODE_PF * 1024 - 128)) : "v255");
    const v4u z = {0u, 0u, 0u, 0u};
    *(LAS v4u*)(P.sm + L_U + (lane >> 4) * UL_B + (lane & 15) * 16) = z;
    *(LAS v4u*)(P.sm + L_U + (lane >> 4) * UL_B + 256 + (lane & 15) * 16) = z;
    ((LAS u32*)(P.sm + L_MISC))[lane] = 0u;
    ((LAS u32*)(P.sm + L_MISC))[64 + lane] = 0u;
    asm volatile("s_waitcnt lgkmcnt(0)" ::: "memory");
  }
  asm volatile("s_barrier" ::: "memory");
  asm volatile("" :: "s"(q), "s"(k), "s"(v), "s"(shk), "s"(hk), "s"(dsk), "s"(wr), "s"(wg), "s"(wu), "s"(wd), "s"(out), "s"(ws));
  STAMP(11);
  const int row0 = 8 * P.C * (D * 2);
  EarlyX X;
  if (P.wave == 7) {
    __builtin_amdgcn_s_setprio(3);
    const int m = lane & 15, g4 = lane >> 4;
    E.epoch = __hip_atomic_load((GAS u64*)(P.ws + WS_E), __ATOMIC_RELAXED, __HIP_MEMORY_SCOPE_AGENT);
    if (ROUTER_DELAY) __builtin_amdgcn_s_sleep(ROUTER_DELAY);
    __builtin_amdgcn_sched_barrier(0);
    const __amdgpu_buffer_rsrc_t rq = mkrsrc(P.q, S * D * 2);
    const int xo = (m & 7) * (D * 2) + (m >> 3) * 64 + g4 * 16;
#pragma unroll
    for (int i = 0; i < 9; i++) X.xa[i] = __builtin_amdgcn_raw_buffer_load_b128(rq, xo + i * 128, row0, NT_Q);
    __builtin_amdgcn_sched_barrier(0);
    const size_t pw = (size_t)P.wr + m * 64, po = (size_t)kOnes;
    const GAS char* wp = (const GAS char*)(m < 2 ? pw : po) + g4 * 16;
#pragma unroll
    for (int i = 0; i < 9; i++) X.wb[i] = *(const GAS v4u*)(wp + i * 128);
  }
  asm volatile("" : "+s"(P.wave));
  if (P.wave == 5) {
    const v4u g4 = {GMAGIC, GMAGIC, GMAGIC, GMAGIC};
    __builtin_amdgcn_raw_buffer_store_b128(g4, P.rws, lane < 8 * NGATE ? gate_off(lane >> 3, b >> 4, b & 15) + (lane & 7) * 16 : OOB, 0, AUX_SC1);
  }
  asm volatile("" : "+s"(P.wave));
  if (P.wave < 5) {
    const int j4 = min(P.tid, 287) * 4;
    const __amdgpu_buffer_rsrc_t rk = mkrsrc(P.k, S * D * 2), rv = mkrsrc(P.v, S * D * 2);
    if (TILE_DELAY) __builtin_amdgcn_s_sleep(TILE_DELAY);
    __builtin_amdgcn_sched_barrier(0);
#pragma unroll
    for (int i = 0; i < 3; i++) E.kk[i] = __builtin_amdgcn_raw_buffer_load_b32(rk, row0 + (i - 3) * (D * 2) + j4, 0, NT_KV);
#pragma unroll
    for (int i = 0; i < 8; i++) E.kk[3 + i] = __builtin_amdgcn_raw_buffer_load_b32(rk, j4 + (i & 3) * (D * 2), row0 + (i & 4) * (D * 2), NT_KV);
    __builtin_amdgcn_sched_barrier(0);
    if (SK_POS == 1) E.sk = *(const GAS v4u*)((const GAS char*)P.shk + j4 * 4);
    __builtin_amdgcn_sched_barrier(0);
#pragma unroll
    for (int i = 0; i < 8; i++) E.vv[i] = __builtin_amdgcn_raw_buffer_load_b32(rv, j4 + (i & 3) * (D * 2), row0 + (i & 4) * (D * 2), NT_KV);
    __builtin_amdgcn_sched_barrier(0);
    if (SK_POS == 2) E.sk = *(const GAS v4u*)((const GAS char*)P.shk + j4 * 4);
    E.dd = *(const GAS u32*)((const GAS char*)P.dsk + j4);
  }
  const int role = P.is_conv ? 0 : (is_moe ? 1 : 2);
  asm volatile("" : "+s"(P.wave));
  STAMP(10);
  if (P.wave == 7) router_stats(P, E, X, role);
  asm volatile("" : "+s"(P.wave));
  if (P.wave != 7) {
    if (P.wave >= 5) aux_wave(P, role);
    else tile_wave(P, E, is_moe);
  } else {
    router_wave(P, E, role);
  }
  asm volatile("" : "+s"(P.wave));
  if (is_moe && P.wave >= 6) routed_rows(P);
  FLUSH();
}

}

extern "C" void kernel_launch(void* const* d_in, const int* in_sizes, int n_in, void* d_out, int out_size, void* d_ws,
                              size_t ws_size, hipStream_t stream) {
  (void)in_sizes; (void)n_in; (void)out_size; (void)ws_size;
  (void)hipFuncSetAttribute((const void*)fused_kernel, hipFuncAttributeMaxDynamicSharedMemorySize, L_TOTAL);
  fused_kernel<<<NWG, NTHR, L_TOTAL, stream>>>(
      (const u16*)d_in[0], (const u16*)d_in[1], (const u16*)d_in[2], (const u16*)d_in[3], (const u16*)d_in[4],
      (const u16*)d_in[5], (const u16*)d_in[7], (const u16*)d_in[8], (const u16*)d_in[9], (const u16*)d_in[10],
      (u16*)d_out, (char*)d_ws + (WS_AT_END ? ((ws_size - WS_END) & ~(size_t)0xFFFF) : 0));
}
```

```cpp
#include <hip/hip_runtime.h>
#include <hip/hip_bf16.h>

typedef unsigned int u32;
typedef unsigned long long u64;
typedef unsigned short u16;
typedef u32 v4u __attribute__((ext_vector_type(4)));
typedef u32 v2u __attribute__((ext_vector_type(2)));
typedef float f4 __attribute__((ext_vector_type(4)));
typedef float f2 __attribute__((ext_vector_type(2)));
typedef __bf16 bf8 __attribute__((ext_vector_type(8)));
typedef __bf16 bf2 __attribute__((ext_vector_type(2)));
#define LAS __attribute__((address_space(3)))
#define GAS __attribute__((address_space(1)))

namespace {

constexpr int S = 2048, D = 576, F = 4608;
constexpr int NWG = 256, NTHR = 512, NCONV = 144, NMOE = 72;
constexpr int AUX_SC1 = 16;
constexpr u32 SAME = 0xFFFFFFFFu, MAXV = 0xFFFFFFFEu;
constexpr u32 SPIN_CAP = 1u << 16, GATE_CAP = 96;
constexpr u32 GMAGIC = 0x5AC3E900u;
constexpr int OOB = 0x7FFFFF00;
#define U_POST 2
#define WS_AT_END 0
#define HELPERS 1
#define Y_POST 8
#define OUT_AUX 16
#define W2_GATE 2
#define PF_R0 192
#define STAGED 1
#define H_DELAY 30
#define PF_CE 2
#define PF_WHEN 0
#define W1_DEPTH 0
#define DRAIN_FIRST 1
#define U_DELAY 0
#define PF_FIRST 1
#define SK_POS 1
#define ROUTER_DELAY 0
#define W2_EARLY 0
#define TILE_DELAY 4
#define CODE_PF 40
#define NT_MASK 5
constexpr int NT_KV = (NT_MASK & 1) ? 2 : 0, NT_Q = (NT_MASK & 2) ? 2 : 0, NT_H = (NT_MASK & 4) ? 2 : 0, NT_T = (NT_MASK & 8) ? 2 : 0,
              NT_W = (NT_MASK & 16) ? 2 : 0;

constexpr int WS_E = 0;
constexpr int WS_G = 4096;
constexpr int GATE_B = 256 * 256;
constexpr int WS_R = WS_G + 5 * GATE_B;
constexpr int WS_M = WS_R + S * 8;
constexpr int WS_U = WS_M + S * 8;
constexpr int TILE_B = D * 16;
constexpr int WS_P = WS_U + NWG * TILE_B;
constexpr int PART_B = NCONV * 128;
constexpr int WS_END = WS_P + NMOE * PART_B;
constexpr int WS_DBG = 4  __H2__ ;
static_assert(WS_END <= WS_DBG, "ws");
enum { G_R = 0, G_U, G_Y, G_P, G_V, NGATE };
#define ONES8 0x3F803F80u, 0x3F803F80u, 0x3F803F80u, 0x3F803F80u, 0x3F803F80u, 0x3F803F80u, 0x3F803F80u, 0x3F803F80u
#define ONES32 ONES8, ONES8, ONES8, ONES8
__device__ const u32 kOnes[288] __attribute__((aligned(128))) = {ONES32, ONES32, ONES32, ONES32, ONES32, ONES32, ONES32, ONES32, ONES32};

constexpr int HC_B = 2080 * 16;
constexpr int L_H = 0;
constexpr int UL_B = 290 * 16;
constexpr int L_U = L_H + 4 * HC_B;
constexpr int L_STG = L_U + 4 * UL_B;
constexpr int L_MISC = L_STG + TILE_B;
constexpr int L_CNT = L_MISC;
constexpr int L_CAND = L_CNT + 96;
constexpr int L_SEL = L_CAND + 288;
constexpr int L_REC = L_SEL + 32;
constexpr int L_STAT = L_REC + 32;
constexpr int Y1_B = 2048 + 64;
constexpr int L_END = L_STAT + 64;
constexpr int XS_STRIDE = 1184;
constexpr int L_XS = 0;
constexpr int L_RED = L_XS + 8 * XS_STRIDE;
constexpr int L_HB = L_RED + 6 * 8 * 128 * 4;
constexpr int HB_STRIDE = 160;
static_assert(L_END + 1024 <= 160 * 1024, "lds");
static_assert(L_HB + 8 * HB_STRIDE <= L_U, "lds");
static_assert(L_END - L_MISC == 512, "zero fill");

__device__ __forceinline__ constexpr int xrow_off(int t) { return L_U + (t / 3) * UL_B + 512 + (t % 3) * (D * 2); }
enum { C_DRAIN = 0, C_STAT, C_TRUST, C_GU, C_SEL, C_H, C_U, C_Y, C_XS, C_RED, C_HB, C_GY, C_PUB, C_TAG, C_QQ, C_U2, C_Y1, C_W1, C_DN, C_H0, C_VER, C_REDO, C_OK };

__device__ __forceinline__ float lo16(u32 x) { return __uint_as_float(x << 16); }
__device__ __forceinline__ float hi16(u32 x) { return __uint_as_float(x & 0xFFFF0000u); }
__device__ __forceinline__ u32 pk2(float a, float b) {
  bf2 p = {(__bf16)a, (__bf16)b};
  return __builtin_bit_cast(u32, p);
}
__device__ __forceinline__ float rbf(float a) { return lo16(pk2(a, a)); }
__device__ __forceinline__ float dot2(u32 a, u32 b, float c) {
  return __builtin_amdgcn_fdot2_f32_bf16(__builtin_bit_cast(bf2, a), __builtin_bit_cast(bf2, b), c, false);
}
template <int CTRL> __device__ __forceinline__ u32 dppu(u32 x) {
  return (u32)__builtin_amdgcn_mov_dpp((int)x, CTRL, 0xf, 0xf, true);
}
template <int CTRL> __device__ __forceinline__ float dppf(float x) { return __uint_as_float(dppu<CTRL>(__float_as_uint(x))); }
__device__ __forceinline__ float xrow_sum(float x) {
  auto s = __builtin_amdgcn_permlane16_swap(__float_as_uint(x), __float_as_uint(x), false, false);
  x = __uint_as_float(s[0]) + __uint_as_float(s[1]);
  auto t = __builtin_amdgcn_permlane32_swap(__float_as_uint(x), __float_as_uint(x), false, false);
  return __uint_as_float(t[0]) + __uint_as_float(t[1]);
}
__device__ __forceinline__ u32 umax(u32 a, u32 b) { return a > b ? a : b; }
__device__ __forceinline__ u32 umin(u32 a, u32 b) { return a < b ? a : b; }
__device__ __forceinline__ u32 umed3(u32 a, u32 b, u32 c) { return umax(umin(a, b), umin(umax(a, b), c)); }
__device__ __forceinline__ u32 xrow_umin(u32 x) {
  auto s = __builtin_amdgcn_permlane16_swap(x, x, false, false);
  x = umin(s[0], s[1]);
  auto t = __builtin_amdgcn_permlane32_swap(x, x, false, false);
  return umin(t[0], t[1]);
}
__device__ __forceinline__ u32 wave_umax(u32 x) {
  x = umax(x, dppu<0xB1>(x));
  x = umax(x, dppu<0x4E>(x));
  x = umax(x, dppu<0x141>(x));
  x = umax(x, dppu<0x140>(x));
  auto s = __builtin_amdgcn_permlane16_swap(x, x, false, false);
  x = umax(s[0], s[1]);
  auto t = __builtin_amdgcn_permlane32_swap(x, x, false, false);
  return umax(t[0], t[1]);
}
__device__ __forceinline__ void lds_signal(LAS char* sm, int c) {
  u64 keep;
  asm volatile("s_waitcnt lgkmcnt(0)\n\ts_mov_b64 %0, exec\n\ts_mov_b64 exec, 1\n\tds_add_u32 %1, %2\n\ts_mov_b64 exec, %0"
               : "=&s"(keep) : "v"((u32)(size_t)(sm + L_CNT + 4 * c)), "v"(1u) : "memory");
}
template <int SLEEP = 0> __device__ __forceinline__ void lds_wait(LAS char* sm, int c, u32 target) {
  u32 v, sv;
  if (SLEEP == 0)
    asm volatile("1:\n\tds_read_b32 %0, %2\n\ts_waitcnt lgkmcnt(0)\n\tv_readfirstlane_b32 %1, %0\n\ts_cmp_lt_u32 %1, %3\n\ts_cbranch_scc1 1b"
                 : "=&v"(v), "=&s"(sv) : "v"((u32)(size_t)(sm + L_CNT + 4 * c)), "s"(target) : "memory", "scc");
  else
    asm volatile("1:\n\ts_sleep %4\n\tds_read_b32 %0, %2\n\ts_waitcnt lgkmcnt(0)\n\tv_readfirstlane_b32 %1, %0\n\ts_cmp_lt_u32 %1, %3\n\ts_cbranch_scc1 1b"
                 : "=&v"(v), "=&s"(sv) : "v"((u32)(size_t)(sm + L_CNT + 4 * c)), "s"(target), "n"(SLEEP) : "memory", "scc");
}
__device__ __forceinline__ bool lds_arrive(LAS char* sm, int c, u32 last) {
  u64 keep;
  u32 v, sv;
  asm volatile("s_mov_b64 %0, exec\n\ts_mov_b64 exec, 1\n\tds_add_rtn_u32 %1, %3, %4\n\ts_waitcnt lgkmcnt(0)\n\tv_readfirstlane_b32 %2, %1\n\ts_mov_b64 exec, %0"
               : "=&s"(keep), "=&v"(v), "=&s"(sv) : "v"((u32)(size_t)(sm + L_CNT + 4 * c)), "v"(1u) : "memory");
  return sv == last;
}
__device__ __forceinline__ void wg_barrier() { asm volatile("s_waitcnt lgkmcnt(0)\n\ts_barrier" ::: "memory"); }
__device__ __forceinline__ u32 umax3(u32 a, u32 b, u32 c) { return umax(umax(a, b), c); }
__device__ __forceinline__ u32 vmax4(v4u g, u32 acc) { return umax3(umax3(g.x, g.y, acc), g.z, g.w); }
__device__ __forceinline__ f4 mfma16(v4u a, v4u b, f4 c) {
  return __builtin_amdgcn_mfma_f32_16x16x32_bf16(__builtin_bit_cast(bf8, a), __builtin_bit_cast(bf8, b), c, 0, 0, 0);
}
__device__ __forceinline__ __amdgpu_buffer_rsrc_t mkrsrc(const void* p, int bytes) {
  return __builtin_amdgcn_make_buffer_rsrc((void*)p, 0, bytes, 0x00020000);
}

#define DBG_TS 1
#define STAMP(i) do { P.ts[i] = (u32)__builtin_amdgcn_s_memrealtime() | 1u; } while (0)
#define STAMPC(i) do { P.ts[i] = (u32)__builtin_amdgcn_s_memtime(); } while (0)
#define FLUSH() do { STAMP(29); if (P.wave == 0 && P.is_conv) { const u32 d_ = (P.ts[29] - P.ts[0]) * 4; while ((u32)__builtin_amdgcn_s_memrealtime() - P.ts[0] < d_ && d_ < 20000u) __builtin_amdgcn_s_sleep(1); } } while (0)
#define NOTE(i, v) do { P.ts[i] = (v); } while (0)
constexpr int L_TOTAL = L_END;

struct Ctx {
  const u16 *q, *k, *v, *shk, *hk, *dsk, *wr, *wg, *wu, *wd;
  u16* out;
  char* ws;
  const char* code;
  __amdgpu_buffer_rsrc_t rws;
  LAS char* sm;
  int tid, lane, wave, b, C, cg, mw;
  bool is_conv;
  u32 t0;
  mutable u32 ts[32];
};

__device__ __forceinline__ int gate_off(int gate, int rep, int shard) { return WS_G + gate * GATE_B + (rep * 16 + shard) * 256; }
__device__ __forceinline__ u32 gate_add(const Ctx& P, int off, int val) {
  return (u32)__builtin_amdgcn_raw_ptr_buffer_atomic_add_i32(val, P.rws, off, 0, 0);
}
__device__ __forceinline__ u32 gate_post(const Ctx& P, int gate, bool on, u32 inc) {
  return gate_add(P, (on && P.lane < 16) ? gate_off(gate, P.lane, P.b & 15) : OOB, (int)inc);
}
__device__ __forceinline__ void gate_confirm(const Ctx& P, int gate, u32 old, u32 inc) {
  bool bad = P.lane < 16;
  _Pragma("nounroll") for (u32 spin = 0; spin < SPIN_CAP; spin++) {
    const u32 d = old - GMAGIC;
    bad = bad && ((d & 0xF0u) != 0u || d != (d & 0xFu) * inc);
    if (__builtin_expect(!__any(bad), 1)) break;
    NOTE(26, spin + 1);
    __builtin_amdgcn_s_sleep(2);
    old = gate_add(P, bad ? gate_off(gate, P.lane, P.b & 15) : OOB, (int)inc);
  }
}
__device__ __forceinline__ void gate_wait(const Ctx& P, int gate, u32 expect, u32 inc, u32 cap = GATE_CAP) {
  const int off = P.lane < 16 ? gate_off(gate, P.b >> 4, P.lane) : OOB;
  const u32 want = P.lane < 16 ? GMAGIC + expect * inc : 0u;
  _Pragma("nounroll") for (u32 spin = 0; spin < cap; spin++)
    if (__all(gate_add(P, off, 0) == want)) break;
}
__device__ __forceinline__ u32 gate_inc(u32 tag) { return 1u + (tag << 8); }
__device__ __forceinline__ void post_u_complete(const Ctx& P, u32 inc) {
  lds_wait<1>(P.sm, C_DRAIN, 5);
  gate_confirm(P, G_V, gate_post(P, G_V, true, inc), inc);
}

struct Row { v2u a0, a1; u32 a2; };
template <int AUX> __device__ __forceinline__ Row load_row(__amdgpu_buffer_rsrc_t r, int soff, int lane) {
  Row x;
  x.a0 = __builtin_amdgcn_raw_buffer_load_b64(r, lane * 8, soff, AUX);
  x.a1 = __builtin_amdgcn_raw_buffer_load_b64(r, lane * 8 + 512, soff, AUX);
  x.a2 = __builtin_amdgcn_raw_buffer_load_b16(r, lane * 2 + 1024, soff, AUX);
  return x;
}
__device__ __forceinline__ void hcopy_store(LAS char* dst, v4u lo, v4u hi, bool first) {
  u32 d[8], o[7];
#pragma unroll
  for (int e = 0; e < 4; e++) { d[e] = lo[e]; d[4 + e] = hi[e]; }
#pragma unroll
  for (int e = 0; e < 7; e++) o[e] = __builtin_amdgcn_alignbit(d[e + 1], d[e], 16);
  constexpr int BASE[8] = {0, 260, 519, 778, 1037, 1296, 1556, 1815};
#pragma unroll
  for (int e = 0; e < 4; e++) {
    v4u a = {d[e], d[e + 1], d[e + 2], d[e + 3]}, c = {o[e], o[e + 1], o[e + 2], o[e + 3]};
    if (e > 0 || first) *(LAS v4u*)(dst + BASE[2 * e] * 16) = a;
    *(LAS v4u*)(dst + BASE[2 * e + 1] * 16) = c;
  }
}
__device__ __forceinline__ u32 touch_w1(const Ctx& P, int mat, int blk);
__device__ __forceinline__ u32 hcopy_first(const Ctx& P, int ch0) {
  const __amdgpu_buffer_rsrc_t rh = mkrsrc(P.hk + (size_t)(4 * P.cg + ch0) * S, 2 * S * 2);
  v4u lo[8];
#pragma unroll
  for (int r = 0; r < 8; r++) lo[r] = __builtin_amdgcn_raw_buffer_load_b128(rh, P.lane * 16 + (r & 3) * 1024, (r >> 2) * 4096, NT_H);
  __builtin_amdgcn_sched_barrier(0);
  STAMP(12);
  LAS char* dst = P.sm + L_H + ch0 * HC_B + (P.lane + 2) * 16;
#pragma unroll
  for (int r = 0; r < 8; r++) *(LAS v4u*)(dst + (r >> 2) * HC_B + (r & 3) * 1024) = lo[r];
  lds_signal(P.sm, C_H0);
  u32 t = 0, t2 = 0;
  if (HELPERS != 0 && PF_WHEN == 0 && PF_CE >= 1) t = touch_w1(P, ch0 >> 1, 25 + (P.b >> 3));
  if (HELPERS != 0 && PF_WHEN == 0 && PF_CE >= 2) t2 = touch_w1(P, ch0 >> 1, 43 + (P.b >> 3));
  __builtin_amdgcn_sched_barrier(0);
  STAMP(1);
  {
    const int c = P.lane & 1, m1 = (P.lane >> 1) & 1;
    const v4u z = {0u, 0u, 0u, 0u};
    v4u h0 = *(const LAS v4u*)(P.sm + L_H + (ch0 + c) * HC_B + 32);
    h0 = m1 ? h0 : z;
    if (P.lane < 4) hcopy_store(P.sm + L_H + (ch0 + c) * HC_B + m1 * 16, z, h0, true);
  }
  return t | t2;
}
template <int NR> __device__ __forceinline__ void hcopy_rest(const Ctx& P, int r0) {
  v4u lo[NR], hi[NR];
  LAS char* dst[NR];
#pragma unroll
  for (int r = 0; r < NR; r++) {
    dst[r] = P.sm + L_H + ((r0 + r) >> 2) * HC_B + ((r0 + r) & 3) * 1024 + (P.lane + 2) * 16;
    lo[r] = *(const LAS v4u*)dst[r];
    hi[r] = *(const LAS v4u*)(dst[r] + 16);
  }
#pragma unroll
  for (int r = 0; r < NR; r++) hcopy_store(dst[r], lo[r], hi[r], false);
  lds_signal(P.sm, C_H);
}

template <class FN> __device__ __forceinline__ void ffn_gate_up(const Ctx& P, FN&& between) {
  LAS char* sm = P.sm;
  const int lane = P.lane, wave = P.wave, n16 = lane & 15, g4 = lane >> 4;
  v4u W1[3][8];
  const GAS char* wp = (const GAS char*)(n16 < 8 ? P.wg : P.wu) + ((size_t)(96 * wave + 8 * g4) * F + 64 * P.mw + 8 * (n16 & 7)) * 2;
  if (wave >= PF_FIRST) lds_wait<1>(sm, wave < 2 ? C_PUB : C_W1, wave < 2 ? 1 : 2);
  __builtin_amdgcn_sched_barrier(0);
#pragma unroll
  for (int i = 0; i < 3; i++)
#pragma unroll
    for (int jj = 0; jj < 8; jj++) {
      const GAS v4u* a = (const GAS v4u*)(wp + (size_t)(32 * i + jj) * (F * 2));
      W1[i][jj] = NT_W ? __builtin_nontemporal_load(a) : *a;
    }
  __builtin_amdgcn_sched_barrier(0);
  lds_signal(sm, C_H0);
  STAMP(3);
  v4u Bf[3][8];
#pragma unroll
  for (int i = 0; i < 3; i++)
#pragma unroll
    for (int e = 0; e < 8; e++) {
#pragma unroll
      for (int d = 0; d < 4; d++)
        Bf[i][e][d] = __builtin_amdgcn_perm(W1[i][2 * d + 1][e >> 1], W1[i][2 * d][e >> 1], (e & 1) ? 0x07060302u : 0x05040100u);
      asm volatile("" : "+v"(Bf[i][e]));
    }
  STAMP(17);
  lds_wait<1>(sm, C_XS, 2);
  STAMP(5);
  f4 acc[8];
#pragma unroll
  for (int e = 0; e < 8; e++) acc[e] = (f4){0.f, 0.f, 0.f, 0.f};
#pragma unroll
  for (int i = 0; i < 3; i++) {
    const v4u A = *(const LAS v4u*)(sm + L_XS + (n16 & 7) * XS_STRIDE + (96 * wave + 32 * i + 8 * g4) * 2);
#pragma unroll
    for (int e = 0; e < 8; e++) acc[e] = mfma16(A, Bf[i][e], acc[e]);
  }
  {
    LAS char* dst = sm + L_RED + ((wave * 8 + 4 * (g4 & 1) + (g4 & 2)) * 128 + 8 * n16) * 4;
#pragma unroll
    for (int r = 0; r < 2; r++) {
      f4 a, c;
#pragma unroll
      for (int e = 0; e < 4; e++) { a[e] = g4 >= 2 ? acc[e][2 + r] : acc[e][r]; c[e] = g4 >= 2 ? acc[4 + e][2 + r] : acc[4 + e][r]; }
      *(LAS f4*)(dst + r * 512) = a;
      *(LAS f4*)(dst + r * 512 + 16) = c;
    }
  }
  lds_signal(sm, C_RED);
  STAMP(18);
  between();
  if (wave >= 2) {
    const int tk = 2 * (wave - 2) + (lane >> 5);
    const LAS char* src = sm + L_RED + (tk * 128 + 2 * (lane & 31)) * 4;
    lds_wait(sm, C_RED, 6);
    f2 G = {0.f, 0.f}, U = {0.f, 0.f};
#pragma unroll
    for (int w = 0; w < 6; w++) {
      G += *(const LAS f2*)(src + w * 4096);
      U += *(const LAS f2*)(src + w * 4096 + 256);
    }
    const u32 gb = pk2(G.x, G.y), ub = pk2(U.x, U.y);
    const float g0 = lo16(gb), g1 = hi16(gb);
    const u32 sg = pk2(g0 * __builtin_amdgcn_rcpf(1.0f + __expf(-g0)), g1 * __builtin_amdgcn_rcpf(1.0f + __expf(-g1)));
    *(LAS u32*)(sm + L_HB + tk * HB_STRIDE + (lane & 31) * 4) = pk2(lo16(sg) * lo16(ub), hi16(sg) * hi16(ub));
    lds_signal(sm, C_HB);
    STAMP(15);
  }
}

struct Down {
  v4u W2[2][8];
  __device__ __forceinline__ void load(const Ctx& P, int cu) {
    const int n16 = P.lane & 15, g4 = P.lane >> 4;
    const __amdgpu_buffer_rsrc_t rd = mkrsrc(P.wd + (size_t)64 * P.mw * D, 64 * D * 2);
    const int o2 = (cu == 4 && n16 >= 8) ? OOB : (8 * g4 * D + 128 * cu + 8 * n16) * 2;
    lds_wait<1>(P.sm, C_H0, 6);
    __builtin_amdgcn_sched_barrier(0);
#pragma unroll
    for (int ks = 0; ks < 2; ks++)
#pragma unroll
      for (int jj = 0; jj < 8; jj++)
        W2[ks][jj] = __builtin_amdgcn_raw_buffer_load_b128(rd, o2 + (jj & 3) * (D * 2), (32 * ks + (jj & 4)) * (D * 2), NT_W);
    __builtin_amdgcn_sched_barrier(0);
    STAMP(4);
  }
  __device__ __forceinline__ void run(const Ctx& P, int cu) {
    LAS char* sm = P.sm;
    const int n16 = P.lane & 15, g4 = P.lane >> 4;
    const u32 tag = *(const LAS u32*)(sm + L_REC), tag12 = tag & 0xFFFu;
    __builtin_amdgcn_sched_barrier(0);
    v4u Bd[2][8];
#pragma unroll
    for (int ks = 0; ks < 2; ks++)
#pragma unroll
      for (int e = 0; e < 8; e++) {
#pragma unroll
        for (int d = 0; d < 4; d++)
          Bd[ks][e][d] = __builtin_amdgcn_perm(W2[ks][2 * d + 1][e >> 1], W2[ks][2 * d][e >> 1], (e & 1) ? 0x07060302u : 0x05040100u);
        asm volatile("" : "+v"(Bd[ks][e]));
      }
    const int off = WS_P + P.mw * PART_B + (32 * cu + 2 * (n16 & 7)) * 128 + (n16 >> 3) * 64 + g4 * 16;
    STAMP(19);
    lds_wait(sm, C_HB, 4);
    const v4u A0 = *(const LAS v4u*)(sm + L_HB + (n16 & 7) * HB_STRIDE + g4 * 16);
    const v4u A1 = *(const LAS v4u*)(sm + L_HB + (n16 & 7) * HB_STRIDE + 64 + g4 * 16);
    if (lds_arrive(sm, C_DN, 4)) (void)gate_post(P, G_P, true, gate_inc(tag));
    STAMP(20);
    f4 ad[8];
#pragma unroll
    for (int e = 0; e < 8; e++) ad[e] = mfma16(A0, Bd[0][e], (f4){0.f, 0.f, 0.f, 0.f});
#pragma unroll
    for (int e = 0; e < 8; e++) ad[e] = mfma16(A1, Bd[1][e], ad[e]);
#pragma unroll
    for (int hh = 0; hh < 2; hh++) {
      v4u s0, s1;
#pragma unroll
      for (int e = 0; e < 4; e++) {
        const f4 a = ad[4 * hh + e];
        const u32 x = ((__float_as_uint(g4 >= 2 ? a[2] : a[0]) + 0x800u) & 0xFFFFF000u) | tag12;
        const u32 y = ((__float_as_uint(g4 >= 2 ? a[3] : a[1]) + 0x800u) & 0xFFFFF000u) | tag12;
        s0[e] = (u32)__builtin_amdgcn_update_dpp((int)x, (int)y, 0x128, 0xf, 0xc, false);
        s1[e] = (u32)__builtin_amdgcn_update_dpp((int)y, (int)x, 0x128, 0xf, 0x3, false);
      }
      __builtin_amdgcn_raw_buffer_store_b128(s0, P.rws, off + hh * 128, 0, AUX_SC1);
      if (cu != 4) __builtin_amdgcn_raw_buffer_store_b128(s1, P.rws, off + hh * 128 + 2048, 0, AUX_SC1);
    }
    STAMP(21);
  }
};

__device__ __forceinline__ u32 touch_w1(const Ctx& P, int mat, int blk) {
  const int r7 = (P.lane * 57) >> 9, c9 = P.lane - 9 * r7;
  return __builtin_amdgcn_raw_buffer_load_b32(mkrsrc(__builtin_amdgcn_readfirstlane(mat) ? P.wu : P.wg, D * F * 2), P.lane < 63 ? r7 * (F * 2) + c9 * 128 : OOB,
                                              (PF_R0 + 7 * blk) * (F * 2) + (P.b & 7) * 1152, NT_T);
}
__device__ __forceinline__ u32 touch_w2(const Ctx& P, int unit) {
  return __builtin_amdgcn_raw_buffer_load_b32(mkrsrc(P.wd, D * F * 2), unit < 81 ? P.lane * 128 : OOB,
                                              (P.b & 7) * (9 * 64 * D * 2) + unit * 8192, NT_T);
}

__device__ __forceinline__ void publish_y(const Ctx& P, const LAS char* yb, int ystride, int tile0, int t, int step, int n) {
  const int gch = t & 3;
  const int goff = WS_U + (tile0 + (t >> 2)) * TILE_B + (4 * P.cg + gch) * 16;
#pragma unroll
  for (int it = 0; it < n; it++) {
    const v4u y4 = *(const LAS v4u*)(yb + gch * ystride + (it * step + (t >> 2)) * 16);
    const v4u u4 = *(const LAS v4u*)(P.sm + L_U + gch * UL_B + (32 + tile0 + it * step + (t >> 2)) * 16);
    v4u w4;
#pragma unroll
    for (int e = 0; e < 4; e++) w4[e] = (y4[e] == u4[e]) ? SAME : y4[e];
    __builtin_amdgcn_raw_buffer_store_b128(w4, P.rws, goff, it * step * TILE_B, AUX_SC1);
  }
}

__device__ __forceinline__ void gather_half(const Ctx& P, int tile0, v4u (&g)[8]) {
  const int goff = WS_U + (tile0 + (P.lane >> 2)) * TILE_B + (4 * P.cg + (P.lane & 3)) * 16;
#pragma unroll
  for (int it = 0; it < 8; it++) g[it] = __builtin_amdgcn_raw_buffer_load_b128(P.rws, goff, it * 16 * TILE_B, AUX_SC1);
}

__device__ __forceinline__ void verify_half(const Ctx& P, int tile0) {
  LAS char* sm = P.sm;
  lds_wait<1>(sm, C_TRUST, 1);
  v4u g[8];
  gather_half(P, tile0, g);
  LAS char* ud = sm + L_U + (P.lane & 3) * UL_B + (32 + tile0 + (P.lane >> 2)) * 16;
  u32 df = 0;
#pragma unroll
  for (int it = 0; it < 8; it++) {
    const v4u x = *(const LAS v4u*)(ud + it * 256) ^ g[it];
    df |= x.x | x.y;
    df |= x.z | x.w;
  }
  if (__builtin_expect(__any(df != 0), 0)) {
#pragma unroll
    for (int it = 0; it < 8; it++) *(LAS v4u*)(ud + it * 256) = g[it];
    lds_signal(sm, C_REDO);
  }
  lds_signal(sm, C_OK);
}

__device__ __forceinline__ void conv_phase(const Ctx& P, u32 inc) {
  LAS char* sm = P.sm;
  const int tid = P.tid, lane = P.lane, wave = P.wave, n16 = lane & 15, g4 = lane >> 4, gch = tid & 3;
  const int goff = WS_U + (tid >> 2) * TILE_B + (4 * P.cg + gch) * 16;
  LAS char* ud = sm + L_U + gch * UL_B + (32 + (tid >> 2)) * 16;
  const int p1 = n16 + 1, kap = p1 & 7;
  const int base16 = 258 * kap + ((0x98654320u >> (4 * kap)) & 15);
  const LAS char* pa = sm + L_H + wave * HC_B + (base16 + g4 + (p1 >> 3)) * 16;
  const LAS char* pb = sm + L_U + wave * UL_B + (32 + 2 * n16 + 1 - g4 - 28) * 16;
  lds_wait<1>(sm, C_H0, 2);
  hcopy_rest<3>(P, 3 * wave);
  STAMP(17);
  asm volatile("s_waitcnt vmcnt(0)" ::: "memory");
  lds_signal(sm, C_DRAIN);
  lds_wait<1>(sm, C_QQ, 5);
  lds_wait<1>(sm, C_GU, 1);
  __builtin_amdgcn_s_setprio(2);
  STAMP(3);
  if (U_DELAY) __builtin_amdgcn_s_sleep(U_DELAY);
  STAMP(4);
  v4u g[2];
#pragma unroll
  for (int it = 0; it < 2; it++) g[it] = __builtin_amdgcn_raw_buffer_load_b128(P.rws, goff, it * 64 * TILE_B, AUX_SC1);
  *(LAS v4u*)ud = g[0];
  *(LAS v4u*)(ud + 1024) = g[1];
  lds_signal(sm, C_U);
  STAMP(5);
  lds_wait(sm, C_H, 6);
  lds_wait(sm, C_U, 4);
  STAMP(6);
  f4 acc[8];
  v4u A[2][8], B[2][4];
  _Pragma("nounroll") for (int pass = 0; pass < 2; pass++) {
#pragma unroll
  for (int t = 0; t < 8; t++) acc[t] = (f4){0.f, 0.f, 0.f, 0.f};
#pragma unroll
  for (int a = 0; a < 8; a++) {
    A[0][a] = *(const LAS v4u*)(pa + a * 512);
    if (a < 4) B[0][a] = *(const LAS v4u*)(pb + a * 512 + 7 * 64);
  }
#pragma unroll
  for (int bb = 0; bb < 8; bb++) {
    __builtin_amdgcn_sched_barrier(0);
    if (bb == 7) {
      lds_wait(sm, C_U2, 1);
      __builtin_amdgcn_sched_barrier(0);
    }
#pragma unroll
    for (int a = 0; a < 8; a++) {
      if (bb < 7) {
        A[(bb + 1) & 1][a] = *(const LAS v4u*)(pa + a * 512 + (bb + 1) * 64);
        if (a < 4) B[(bb + 1) & 1][a] = *(const LAS v4u*)(pb + a * 512 + (6 - bb) * 64);
      } else if (a < 4) {
        A[0][a] = *(const LAS v4u*)(pa + a * 512);
        B[0][a] = *(const LAS v4u*)(pb + (4 + a) * 512 + 7 * 64);
      }
    }
#pragma unroll
    for (int t = 0; t < 8; t++)
#pragma unroll
      for (int c = 0; c < 4; c++)
        if (c <= t) acc[t] = mfma16(A[bb & 1][t - c], B[bb & 1][c], acc[t]);
#pragma unroll
    for (int i = 0; i < (bb < 7 ? 12 : 8); i++) {
      __builtin_amdgcn_sched_group_barrier(0x8, 2, 0);
      __builtin_amdgcn_sched_group_barrier(0x100, 1, 0);
    }
    if (bb < 7) __builtin_amdgcn_sched_group_barrier(0x8, 2, 0);
    else __builtin_amdgcn_sched_group_barrier(0x8, 10, 0);
  }
#pragma unroll
  for (int bb = 0; bb < 8; bb++) {
    __builtin_amdgcn_sched_barrier(0);
    if (bb == 1) {
      LAS char* yb = sm + L_STG + wave * Y1_B + n16 * 32 + g4 * 8;
#pragma unroll
      for (int t = 0; t < 4; t++) {
        v2u y2 = {umin(pk2(acc[t][0], acc[t][1]), MAXV), umin(pk2(acc[t][2], acc[t][3]), MAXV)};
        *(LAS v2u*)(yb + t * 512) = y2;
      }
    }
    if (bb == 2) lds_signal(sm, C_Y1);
    if (bb == Y_POST) {
      (void)gate_post(P, G_Y, wave == 0 && pass == 0, inc);
      __builtin_amdgcn_sched_barrier(0);
    }
    if (bb < 7) {
#pragma unroll
      for (int a = 0; a < 4; a++) {
        A[(bb + 1) & 1][a] = *(const LAS v4u*)(pa + a * 512 + (bb + 1) * 64);
        B[(bb + 1) & 1][a] = *(const LAS v4u*)(pb + (4 + a) * 512 + (6 - bb) * 64);
      }
    }
#pragma unroll
    for (int t = 0; t < 4; t++)
#pragma unroll
      for (int c = 0; c <= t; c++) acc[4 + t] = mfma16(A[bb & 1][t - c], B[bb & 1][c], acc[4 + t]);
    if (bb < 7) {
#pragma unroll
      for (int i = 0; i < 8; i++) {
        __builtin_amdgcn_sched_group_barrier(0x8, 1, 0);
        __builtin_amdgcn_sched_group_barrier(0x100, 1, 0);
      }
      __builtin_amdgcn_sched_group_barrier(0x8, 2, 0);
    }
  }
  __builtin_amdgcn_sched_barrier(0);
    lds_wait(sm, C_OK, 2);
    if (__builtin_expect(__builtin_amdgcn_readfirstlane(((const LAS u32*)(sm + L_CNT))[C_REDO]) == 0u, 1)) break;
    NOTE(26, 7);
  }
  if (Y_POST == 8 && wave == 0) (void)gate_post(P, G_Y, true, inc);
  {
    LAS char* yb = sm + L_H + wave * (HC_B + 64) + n16 * 32 + g4 * 8;
#pragma unroll
    for (int t = 0; t < 4; t++) {
      v2u y2 = {umin(pk2(acc[4 + t][0], acc[4 + t][1]), MAXV), umin(pk2(acc[4 + t][2], acc[4 + t][3]), MAXV)};
      *(LAS v2u*)(yb + t * 512) = y2;
    }
  }
  lds_signal(sm, C_Y);
  STAMP(7);
  lds_wait(sm, C_Y, 4);
  publish_y(P, sm + L_H, HC_B + 64, 128, tid, 64, 2);
  STAMP(8);
}

struct EarlyX { v4u xa[9], wb[9]; };
struct Early {
  u32 kk[11], vv[8], dd;
  v4u sk;
  u64 epoch;
  u32 cx;
};

__device__ __forceinline__ void tile_wave(const Ctx& P, const Early& E, const bool MOE) {
  LAS char* sm = P.sm;
  const int tid = P.tid, lane = P.lane, wave = P.wave, C = P.C;
  const bool act = tid < 288;
  const int j4 = min(tid, 287) * 4;
  const int row0 = 8 * C * (D * 2);
  const u32 (&kk)[11] = E.kk;
  const u32 (&vv)[8] = E.vv;
  const v4u sk = E.sk;
  u32 qq[8];
  u32 gold, uinc;
  bool poster;
  u32 up[2][4];
  {
    f2 w[4], kf[11], ks[8];
    w[0] = (f2){lo16(sk.x), lo16(sk.z)}; w[1] = (f2){hi16(sk.x), hi16(sk.z)};
    w[2] = (f2){lo16(sk.y), lo16(sk.w)}; w[3] = (f2){hi16(sk.y), hi16(sk.w)};
#pragma unroll
    for (int i = 0; i < 11; i++) kf[i] = (f2){lo16(kk[i]), hi16(kk[i])};
#pragma unroll
    for (int t = 0; t < 8; t++) {
      ks[t] = __builtin_elementwise_fma(kf[t + 3], w[0], __builtin_elementwise_fma(kf[t + 2], w[1], __builtin_elementwise_fma(kf[t + 1], w[2], kf[t] * w[3])));
      asm volatile("" : "+v"(ks[t]));
    }
    __builtin_amdgcn_sched_barrier(0);
    lds_wait(sm, C_TAG, 1);
    uinc = gate_inc(*(const LAS u32*)(sm + L_REC));
    STAMP(12);
#pragma unroll
    for (int t = 0; t < 8; t++) ks[t] *= (f2){lo16(vv[t]), hi16(vv[t])};
#pragma unroll
    for (int i = 0; i < 4; i++) {
      up[0][i] = umin(pk2(ks[7 - 2 * i].x, ks[6 - 2 * i].x), MAXV);
      up[1][i] = umin(pk2(ks[7 - 2 * i].y, ks[6 - 2 * i].y), MAXV);
    }
    STAMP(1);
    LAS char* stg = sm + L_STG + wave * 2048;
    v4u p0 = {up[0][0], up[0][1], up[0][2], up[0][3]}, p1 = {up[1][0], up[1][1], up[1][2], up[1][3]};
    if (act) { *(LAS v4u*)(stg + lane * 32) = p0; *(LAS v4u*)(stg + lane * 32 + 16) = p1; }
    asm volatile("" ::: "memory");
    p0 = *(const LAS v4u*)(stg + lane * 16);
    p1 = *(const LAS v4u*)(stg + (wave < 4 ? 1024 : 0) + lane * 16);
    STAMP(13);
    const int so = WS_U + C * TILE_B + wave * 2048 + lane * 16;
    __builtin_amdgcn_raw_buffer_store_b128(p0, P.rws, so, 0, AUX_SC1);
    if (wave < 4) __builtin_amdgcn_raw_buffer_store_b128(p1, P.rws, so + 1024, 0, AUX_SC1);
  }
  __builtin_amdgcn_sched_barrier(0);
  poster = lds_arrive(sm, C_VER, 4);
  if (poster) gold = gate_post(P, G_U, true, uinc);
  STAMP(2);
  lds_wait(sm, C_STAT, 1);
#pragma unroll
  for (int i = 0; i < 8; i++) qq[i] = *(const LAS u32*)(sm + xrow_off(i) + j4);
  lds_signal(sm, C_QQ);
  u32 dd = E.dd;
  asm volatile("" : "+v"(dd));
  float xn[2][8], c0[2][8];
  auto prepare = [&]()
  {
    const float d0 = lo16(dd), d1 = hi16(dd);
#pragma unroll
    for (int s2 = 0; s2 < 4; s2++) {
      const f4 st = *(const LAS f4*)(sm + L_STAT + s2 * 16);
#pragma unroll
      for (int e = 0; e < 2; e++) {
        const int s = 2 * s2 + e;
        const float xa = lo16(qq[s]), xb = hi16(qq[s]);
        const float na = fmaf(xa, st[2 * e], st[2 * e + 1]), nb = fmaf(xb, st[2 * e], st[2 * e + 1]);
        const u32 pa = up[0][(7 - s) >> 1], pb = up[1][(7 - s) >> 1];
        const float ua = ((7 - s) & 1) ? hi16(pa) : lo16(pa), ub = ((7 - s) & 1) ? hi16(pb) : lo16(pb);
        xn[0][s] = na; xn[1][s] = nb;
        c0[0][s] = fmaf(na, fmaf(ua, d0, 1.0f), xa);
        c0[1][s] = fmaf(nb, fmaf(ub, d1, 1.0f), xb);
        asm volatile("" : "+v"(xn[0][s]), "+v"(xn[1][s]), "+v"(c0[0][s]), "+v"(c0[1][s]));
      }
    }
  };
  prepare();
  if (poster) gate_confirm(P, G_U, gold, uinc);
  if (!(P.is_conv && wave < 4)) {
    asm volatile("s_waitcnt vmcnt(0)" ::: "memory");
    lds_signal(sm, C_DRAIN);
  }
  STAMP(14);
  if (MOE) {
    ffn_gate_up(P, []() {});
    if (wave < 2) { Down dn; dn.load(P, wave); dn.run(P, wave); }
  } else {
   if (P.is_conv) {
    if (wave < 4) {
      conv_phase(P, uinc);
    } else {
      lds_wait<1>(sm, C_QQ, 5);
      lds_wait<1>(sm, C_GU, 1);
      __builtin_amdgcn_s_sleep(3 + U_DELAY);
      v4u g[8];
      gather_half(P, 128, g);
#pragma unroll
      for (int it = 0; it < 8; it++) *(LAS v4u*)(sm + L_U + (lane & 3) * UL_B + (32 + 128 + it * 16 + (lane >> 2)) * 16) = g[it];
      lds_signal(sm, C_U2);
      STAMP(5);
      gate_wait(P, G_V, 16u, uinc, SPIN_CAP);
      lds_signal(sm, C_TRUST);
      STAMP(4);
      verify_half(P, 128);
      STAMP(6);
      lds_wait<2>(sm, C_Y1, 4);
      lds_wait<1>(sm, C_OK, 2);
      lds_wait<1>(sm, C_Y1, __builtin_amdgcn_readfirstlane(((const LAS u32*)(sm + L_CNT))[C_REDO]) ? 8 : 4);
      publish_y(P, sm + L_STG, Y1_B, 0, lane, 32, 4);
    }
  } else {
    STAMP(5);
   }
  }
  lds_wait<1>(sm, C_SEL, 1);
  const u32 skip = __builtin_amdgcn_readfirstlane(*(const LAS u32*)(sm + L_REC + 4));
  const __amdgpu_buffer_rsrc_t ro = mkrsrc(P.out, S * D * 2);
  const int oo = act ? j4 : OOB;
  const int uoff = WS_U + C * TILE_B + j4 * 8;
  lds_wait<1>(sm, C_GY, 1);
  STAMP(9);
  v4u y0, y1;
  _Pragma("nounroll") for (u32 spin = 0; spin < SPIN_CAP; spin++) {
    asm volatile("" ::: "memory");
    y0 = __builtin_amdgcn_raw_buffer_load_b128(P.rws, uoff, 0, AUX_SC1);
    y1 = __builtin_amdgcn_raw_buffer_load_b128(P.rws, uoff + 16, 0, AUX_SC1);
    bool ok = true;
#pragma unroll
    for (int e = 0; e < 4; e++) ok = ok & (y0[e] != up[0][e]) & (y1[e] != up[1][e]);
    if (__builtin_expect(__all(ok), 1)) break;
    NOTE(25, spin + 1);
    __builtin_amdgcn_s_sleep(4);
  }
  STAMP(16);
#pragma unroll
  for (int e = 0; e < 4; e++) {
    const u32 a = y0[e] == SAME ? up[0][e] : y0[e], c = y1[e] == SAME ? up[1][e] : y1[e];
    const u32 o0 = pk2(fmaf(xn[0][2 * e], lo16(a), c0[0][2 * e]), fmaf(xn[1][2 * e], lo16(c), c0[1][2 * e]));
    const u32 o1 = pk2(fmaf(xn[0][2 * e + 1], hi16(a), c0[0][2 * e + 1]), fmaf(xn[1][2 * e + 1], hi16(c), c0[1][2 * e + 1]));
    const int so = row0 + (e >> 1) * 4 * (D * 2);
    if (!((skip >> (2 * e)) & 1)) __builtin_amdgcn_raw_buffer_store_b32(o0, ro, oo + ((2 * e) & 3) * (D * 2), so, OUT_AUX);
    if (!((skip >> (2 * e + 1)) & 1)) __builtin_amdgcn_raw_buffer_store_b32(o1, ro, oo + ((2 * e + 1) & 3) * (D * 2), so, OUT_AUX);
  }
}

__device__ __forceinline__ void route(const Ctx& P, u32 tag, u64 epoch, const int ROLE) {
  LAS char* sm = P.sm;
  const int lane = P.lane, wave = P.wave;
  (void)wave;
  v4u g[16];
  if (ROLE == 2) __builtin_amdgcn_s_setprio(0);
  if (ROLE == 0) {
    if (HELPERS != 0 && PF_WHEN != 0 && PF_CE != 0) {
      lds_wait<1>(sm, C_VER, PF_WHEN);
      u32 t[4] = {0u, 0u, 0u, 0u};
#pragma unroll
      for (int i = 0; i < 2 * PF_CE; i++) t[i] = touch_w1(P, i & 1, 25 + 18 * (i >> 1) + (P.b >> 3));
      asm volatile("" :: "v"(t[0]), "v"(t[1]), "v"(t[2]), "v"(t[3]));
      STAMP(18);
    }
    gate_wait(P, G_U, 16u, gate_inc(tag));
    lds_signal(sm, C_GU);
    STAMP(15);
    __builtin_amdgcn_s_setprio(0);
    post_u_complete(P, gate_inc(tag));
    STAMP(13);
    lds_wait<1>(sm, C_U, 4);
  } else {
    gate_wait(P, ROLE == 1 ? G_R : G_U, 16u, gate_inc(tag));
  }
  STAMP(3);
  _Pragma("nounroll") for (u32 spin = 0; spin < SPIN_CAP; spin++) {
    asm volatile("" ::: "memory");
#pragma unroll
    for (int i = 0; i < 16; i++)
      g[i] = __builtin_amdgcn_raw_buffer_load_b128(P.rws, WS_R + lane * 16 + (i & 3) * 1024, (i >> 2) * 4096, AUX_SC1);
    if (spin == 0) lds_signal(sm, C_PUB);
    u32 bad = 0;
#pragma unroll
    for (int i = 0; i < 16; i++) {
      bad = __builtin_amdgcn_bitop3_b32(bad, g[i].y, tag, 0xF6);
      bad = __builtin_amdgcn_bitop3_b32(bad, g[i].w, tag, 0xF6);
    }
    if (__builtin_expect(__all(bad == 0), 1)) break;
    NOTE(24, spin + 1);
    __builtin_amdgcn_s_sleep(4);
  }
  STAMP(4);
  if (P.b == 0 && lane == 0) __hip_atomic_store((GAS u64*)(P.ws + WS_E), epoch, __ATOMIC_RELAXED, __HIP_MEMORY_SCOPE_AGENT);
  u32 k1 = umax(g[0].x, g[0].z), k2 = umin(g[0].x, g[0].z), k3 = 0;
#pragma unroll
  for (int i = 2; i < 32; i++) {
    const u32 x = (i & 1) ? g[i >> 1].z : g[i >> 1].x;
    k3 = umed3(k2, k3, x);
    k2 = umed3(k1, k2, x);
    k1 = umax(k1, x);
  }
  u32 tau = umax(k1, dppu<0xB1>(k1));
  tau = umax(tau, dppu<0x4E>(tau));
  tau = umax(tau, dppu<0x141>(tau));
  tau = umin(tau, dppu<0x140>(tau));
  tau = xrow_umin(tau);
  LAS u32* cand = (LAS u32*)(sm + L_CAND);
  const u64 m1 = __ballot(k1 >= tau), m2 = __ballot(k2 >= tau), m3 = __ballot(k3 >= tau);
  const u32 n1 = __builtin_popcountll(m1), n2 = n1 + __builtin_popcountll(m2), n = n2 + __builtin_popcountll(m3);
  bool slow = n > 64;
  if (__builtin_expect(!slow, 1)) {
    const u32 p1 = __builtin_amdgcn_mbcnt_hi((u32)(m1 >> 32), __builtin_amdgcn_mbcnt_lo((u32)m1, 0u));
    const u32 p2 = __builtin_amdgcn_mbcnt_hi((u32)(m2 >> 32), __builtin_amdgcn_mbcnt_lo((u32)m2, n1));
    const u32 p3 = __builtin_amdgcn_mbcnt_hi((u32)(m3 >> 32), __builtin_amdgcn_mbcnt_lo((u32)m3, n2));
    cand[k1 >= tau ? p1 : 64] = k1;
    cand[k2 >= tau ? p2 : 64] = k2;
    cand[k3 >= tau ? p3 : 64] = k3;
    asm volatile("" ::: "memory");
    const u32 mine = cand[lane];
    u32 rank = 0;
    _Pragma("nounroll") for (u32 jj = 0; jj < n; jj += 8) {
      const v4u c4 = *(const LAS v4u*)(cand + jj), d4 = *(const LAS v4u*)(cand + jj + 4);
      rank += (c4.x > mine) + (c4.y > mine) + (c4.z > mine) + (c4.w > mine);
      rank += (d4.x > mine) + (d4.y > mine) + (d4.z > mine) + (d4.w > mine);
    }
    const bool in = (u32)lane < n && rank < 8;
    if (in) ((LAS u32*)(sm + L_SEL))[rank] = mine;
    slow = __any(in && (u32)lane >= n2);
  }
  if (__builtin_expect(slow, 0)) {
    _Pragma("nounroll") for (int r = 0; r < 8; r++) {
      u32 mx = 0;
#pragma unroll
      for (int i = 0; i < 16; i++) mx = umax3(mx, g[i].x, g[i].z);
      mx = wave_umax(mx);
#pragma unroll
      for (int i = 0; i < 16; i++) { g[i].x = g[i].x == mx ? 0u : g[i].x; g[i].z = g[i].z == mx ? 0u : g[i].z; }
      if (lane == 0) ((LAS u32*)(sm + L_SEL))[r] = mx;
    }
  }
  asm volatile("" ::: "memory");
  {
    const u32 tk = 2047u - (((LAS u32*)(sm + L_SEL))[lane & 7] & 2047u);
    u32 mk = (int)(tk >> 3) == P.C ? 1u << (tk & 7) : 0u;
    mk |= dppu<0xB1>(mk);
    mk |= dppu<0x4E>(mk);
    mk |= dppu<0x141>(mk);
    if (lane == 0) *(LAS u32*)(sm + L_REC + 4) = mk;
  }
  lds_signal(sm, C_SEL);
  STAMP(5);
}

__device__ __forceinline__ u32 load_msd(const Ctx& P, int tok, u32 tag) {
  v2u g;
  _Pragma("nounroll") for (u32 spin = 0; spin < SPIN_CAP; spin++) {
    asm volatile("" ::: "memory");
    g = __builtin_amdgcn_raw_buffer_load_b64(P.rws, WS_M + tok * 8, 0, AUX_SC1);
    if (__builtin_expect(__all(g.y == tag), 1)) break;
    __builtin_amdgcn_s_sleep(4);
  }
  return g.x;
}

__device__ __forceinline__ void routed_rows(const Ctx& P) {
  LAS char* sm = P.sm;
  const int lane = P.lane, wave = P.wave, C = P.C, cg = P.cg;
  (void)wave; (void)C; (void)cg;
  const __amdgpu_buffer_rsrc_t rq = mkrsrc(P.q, S * D * 2);
  {
    const int cw = wave - 6;
    const u32 tag = *(const LAS u32*)(sm + L_REC);
    const v4u s4 = *(const LAS v4u*)(sm + L_SEL + cw * 16);
    Row rr[4];
#pragma unroll
    for (int e = 0; e < 4; e++) {
      const int tok = 2047 - (int)(__builtin_amdgcn_readfirstlane(s4[e]) & 2047u);
      rr[e] = load_row<0>(rq, tok * (D * 2), lane);
    }
    const u32 ks = (lane & 2) ? ((lane & 1) ? s4.w : s4.z) : ((lane & 1) ? s4.y : s4.x);
    const int mtok = 2047 - (int)(ks & 2047u);
    v2u mg = __builtin_amdgcn_raw_buffer_load_b64(P.rws, WS_M + mtok * 8, 0, AUX_SC1);
    __builtin_amdgcn_sched_barrier(0);
    lds_signal(sm, C_W1);
    __builtin_amdgcn_sched_barrier(0);
    u32 msd = mg.x;
    if (__builtin_expect(!__all(mg.y == tag), 0)) msd = load_msd(P, mtok, tag);
    STAMP(15);
    const float rsl = __builtin_amdgcn_rcpf(lo16(msd)), nml = -hi16(msd) * rsl;
#pragma unroll
    for (int e = 0; e < 4; e++) {
      const float rs = __uint_as_float(__builtin_amdgcn_readlane(__float_as_uint(rsl), e));
      const float nm = __uint_as_float(__builtin_amdgcn_readlane(__float_as_uint(nml), e));
      LAS char* dst = sm + L_XS + (cw * 4 + e) * XS_STRIDE;
      const Row& x = rr[e];
      v2u a = {pk2(fmaf(lo16(x.a0.x), rs, nm), fmaf(hi16(x.a0.x), rs, nm)), pk2(fmaf(lo16(x.a0.y), rs, nm), fmaf(hi16(x.a0.y), rs, nm))};
      v2u c = {pk2(fmaf(lo16(x.a1.x), rs, nm), fmaf(hi16(x.a1.x), rs, nm)), pk2(fmaf(lo16(x.a1.y), rs, nm), fmaf(hi16(x.a1.y), rs, nm))};
      *(LAS v2u*)(dst + lane * 8) = a;
      *(LAS v2u*)(dst + 512 + lane * 8) = c;
      *(LAS u16*)(dst + 1024 + lane * 2) = (u16)pk2(fmaf(lo16(x.a2), rs, nm), 0.f);
    }
    lds_signal(sm, C_XS);
    STAMP(6);
    __builtin_amdgcn_s_setprio(0);
    Down dn;
    dn.load(P, wave - 4);
    dn.run(P, wave - 4);
  }
}

__device__ __forceinline__ void aux_wave(const Ctx& P, const int ROLE) {
  LAS char* sm = P.sm;
  const int lane = P.lane, wave = P.wave, C = P.C, cg = P.cg;
  (void)wave; (void)C; (void)cg;
  u32 tag;
    if (CODE_PF && wave == 6) asm volatile("s_waitcnt vmcnt(0)" ::: "v255");
    if (ROLE != 0 && wave == 5) {
      __builtin_amdgcn_s_setprio(3);
      lds_wait<1>(sm, C_TAG, 1);
      post_u_complete(P, gate_inc(*(const LAS u32*)(sm + L_REC)));
      __builtin_amdgcn_s_setprio(0);
      STAMP(13);
    }
    if (ROLE == 2 && HELPERS && wave == 6) {
      lds_wait<1>(sm, C_VER, 5);
      const int idx = (P.b - NCONV - NMOE) >> 3;
      u32 ta = touch_w1(P, 0, idx), tb = touch_w1(P, 1, idx);
#pragma unroll
      for (int i = 1; i < 5; i++) {
        const u32 tc = touch_w1(P, 0, i * 5 + idx), td = touch_w1(P, 1, i * 5 + idx);
        asm volatile("" :: "v"(ta), "v"(tb));
        ta = tc; tb = td;
      }
      asm volatile("" :: "v"(ta), "v"(tb));
      STAMP(5);
    }
    if (ROLE == 0) {
      __builtin_amdgcn_s_sleep(H_DELAY);
      const u32 tt = hcopy_first(P, 2 * (wave - 5));
      lds_wait(sm, C_H0, 2);
      hcopy_rest<2>(P, 12 + 2 * (wave - 5));
      asm volatile("" :: "v"(tt));
      STAMP(3);
      if (wave == 5) {
        lds_wait<1>(sm, C_U, 4);
        verify_half(P, 0);
        STAMP(6);
        lds_wait<2>(sm, C_Y1, 4);
        lds_wait<1>(sm, C_OK, 2);
        lds_wait<1>(sm, C_Y1, __builtin_amdgcn_readfirstlane(((const LAS u32*)(sm + L_CNT))[C_REDO]) ? 8 : 4);
        publish_y(P, sm + L_STG + 256, Y1_B, 16, lane, 32, 4);
      } else if (HELPERS) {
        lds_wait<1>(sm, C_U2, 1);
        u32 t[9];
#pragma unroll
        for (int i = 0; i < 4; i++) {
          const int u = 50 + 36 * PF_CE + i * 18 + (P.b >> 3);
          t[5 + i] = (i >= 4 - 2 * PF_CE) ? 0u : touch_w1(P, u & 1, u >> 1);
        }
#pragma unroll
        for (int i = 0; i < 5; i++) t[i] = touch_w2(P, i * 18 + (P.b >> 3));
        asm volatile("" :: "v"(t[5]), "v"(t[6]), "v"(t[7]), "v"(t[8]));
        STAMP(5);
        asm volatile("" :: "v"(t[0]), "v"(t[1]), "v"(t[2]), "v"(t[3]), "v"(t[4]));
        STAMP(6);
      }
    }
    if (wave == 5) {
      if (ROLE == 1) {
        Down dn;
        ffn_gate_up(P, [&]() { dn.load(P, 4); });
        dn.run(P, 4);
        gate_wait(P, G_Y, 9u, gate_inc(*(const LAS u32*)(sm + L_REC)));
        lds_signal(sm, C_GY);
      }
      return;
    }
    lds_wait<1>(sm, C_TAG, 1);
    tag = *(const LAS u32*)(sm + L_REC);
    if (ROLE == 1) { lds_wait<1>(sm, C_SEL, 1); __builtin_amdgcn_s_setprio(3); }
  if (ROLE != 1) {
    gate_wait(P, G_Y, 9u, gate_inc(tag));
    lds_signal(sm, C_GY);
    STAMP(7);
  }
}

__device__ __forceinline__ void router_stats(const Ctx& P, Early& E, const EarlyX& X, const int ROLE) {
  LAS char* sm = P.sm;
  const int lane = P.lane, C = P.C;
  u32 tag;
    const int m = lane & 15, g4 = lane >> 4;
    u64 epoch = E.epoch;
    const v4u (&xa)[9] = X.xa;
    const v4u (&wb)[9] = X.wb;
    asm volatile("" : "+v"(epoch) :: "memory");
    epoch += 1;
    tag = (u32)epoch;
    if (lane == 0) *(LAS u32*)(sm + L_REC) = tag;
    lds_signal(sm, C_TAG);
    STAMP(1);
    f4 gx = {0.f, 0.f, 0.f, 0.f}, gw = {0.f, 0.f, 0.f, 0.f}, gs = {0.f, 0.f, 0.f, 0.f};
#pragma unroll
    for (int i = 0; i < 9; i++) gx = mfma16(xa[i], xa[i], gx);
    __builtin_amdgcn_sched_barrier(0);
    const u32 gold = gate_post(P, G_R, true, gate_inc(tag));
    STAMP(12);
    __builtin_amdgcn_sched_barrier(0);
#pragma unroll
    for (int i = 0; i < 9; i++) { gw = mfma16(wb[i], xa[i], gw); gs = mfma16(wb[i], wb[i], gs); }
    float sxx = (m & 2) ? ((m & 1) ? gx[3] : gx[2]) : ((m & 1) ? gx[1] : gx[0]);
    sxx = __uint_as_float(__builtin_amdgcn_ds_bpermute((m + (m & 12) * 4) * 4, __float_as_uint(sxx)));
    float sxw = m < 8 ? gw[0] : gw[1], sx = gw[2];
    sxw += dppf<0x128>(sxw);
    sx += dppf<0x128>(sx);
    sxx += dppf<0x128>(sxx);
    const float sw = __uint_as_float(__builtin_amdgcn_readlane(__float_as_uint(gs[0] + gs[1]), 2));
    const float mean = sx * (1.0f / D);
    const float var = fmaxf(fmaf(-mean, mean, sxx * (1.0f / D)), 0.f);
    const float mu = rbf(mean);
    const float sd = rbf(__builtin_amdgcn_sqrtf(rbf(rbf(var) + 1e-5f)));
    const float rs = __builtin_amdgcn_rcpf(sd);
    const u32 r = pk2(0.f, fmaf(-mu, sw, sxw) * rs);
    u32 key = ((r ^ (((int)r >> 31) | 0x80000000u)) & 0xFFFF0000u) | (u32)(2047 - (8 * C + (m & 7)));
    u32 msd = __float_as_uint(mu) | (__float_as_uint(sd) >> 16);
    asm volatile("" : "+v"(key), "+v"(msd));
    const v2u rec = {m < 8 ? key : msd, tag};
    const int ro = lane < 16 ? (m < 8 ? WS_R : WS_M) + (8 * C + (m & 7)) * 8 : OOB;
    __builtin_amdgcn_raw_buffer_store_b64(rec, P.rws, ro, 0, AUX_SC1);
    STAMP(2);
    if (lane < 8) { f2 st = {rs, -mu * rs}; *(LAS f2*)(sm + L_STAT + m * 8) = st; }
    {
      const int t = m & 7, t3 = (t * 11) >> 5;
      LAS char* xd = sm + L_U + t3 * UL_B + 512 + (t - 3 * t3) * (D * 2) + (m >> 3) * 64 + g4 * 16;
#pragma unroll
      for (int i = 0; i < 9; i++) *(LAS v4u*)(xd + i * 128) = xa[i];
    }
    lds_signal(sm, C_STAT);
    E.epoch = epoch;
    E.cx = gold;
}

__device__ __forceinline__ void router_wave(const Ctx& P, const Early& E, const int ROLE) {
  LAS char* sm = P.sm;
  const int lane = P.lane, wave = P.wave, C = P.C, cg = P.cg;
  (void)wave; (void)C; (void)cg;
  u32 tag;
    const u64 epoch = E.epoch;
    tag = (u32)epoch;
    const u32 gold = E.cx;
    gate_confirm(P, G_R, gold, gate_inc(tag));
    route(P, tag, epoch, ROLE);
  if (ROLE == 0) {
    const int pi = lane & 7, ti = 4 * (pi & 1) + (pi & 2) + (pi >> 2);
    const u32 kx = ((LAS u32*)(sm + L_SEL))[ti];
    const int tok = 2047 - (int)(kx & 2047u);
    const v2u x2 = *(const GAS v2u*)((const GAS char*)P.q + (size_t)tok * (D * 2) + cg * 8);
    const v2u d2 = *(const GAS v2u*)((const GAS char*)P.dsk + cg * 8);
    const u32 msd = load_msd(P, tok, tag);
    const float rs = __builtin_amdgcn_rcpf(lo16(msd)), nm = -hi16(msd) * rs;
    const u32 rb = kx & 0xFFFF0000u;
    const float r = __uint_as_float((rb & 0x80000000u) ? (rb ^ 0x80000000u) : (~rb & 0xFFFF0000u));
    const float rsig = rbf(__builtin_amdgcn_rcpf(1.0f + __expf(-r)));
    STAMP(6);
    lds_wait<2>(sm, C_Y, 4);
    float o[4];
#pragma unroll
    for (int e = 0; e < 4; e++) {
      const float y = lo16(*(const LAS u16*)(sm + (tok < 1024 ? L_STG + e * Y1_B : L_H + e * (HC_B + 64) - 2048) + tok * 2));
      const float u = lo16(*(const LAS u16*)(sm + L_U + e * UL_B + (32 + (tok >> 3)) * 16 + (7 - (tok & 7)) * 2));
      const float x = (e & 1) ? hi16(x2[e >> 1]) : lo16(x2[e >> 1]);
      const float dk = (e & 1) ? hi16(d2[e >> 1]) : lo16(d2[e >> 1]);
      const float n = fmaf(x, rs, nm);
      o[e] = fmaf(n, fmaf(u, dk, y), x) + n;
      asm volatile("" : "+v"(o[e]));
    }
    gate_wait(P, G_P, lane < 8 ? 5u : 4u, gate_inc(tag));
    STAMP(7);
    v4u g[9];
    const u32 tag12 = tag & 0xFFFu;
    _Pragma("nounroll") for (u32 spin = 0; spin < SPIN_CAP; spin++) {
      asm volatile("" ::: "memory");
#pragma unroll
      for (int i = 0; i < 9; i++)
        g[i] = __builtin_amdgcn_raw_buffer_load_b128(P.rws, WS_P + (lane >> 3) * PART_B + cg * 128 + pi * 16, 8 * i * PART_B, AUX_SC1);
      u32 bad = 0;
#pragma unroll
      for (int i = 0; i < 9; i++) {
        g[i] ^= tag12;
        bad |= g[i].x | g[i].y;
        bad |= g[i].z | g[i].w;
      }
      if (__builtin_expect(__all((bad & 0xFFFu) == 0), 1)) break;
      NOTE(25, spin + 1);
      __builtin_amdgcn_s_sleep(4);
    }
    STAMP(8);
    f4 s4 = __builtin_bit_cast(f4, g[0]);
#pragma unroll
    for (int i = 1; i < 9; i++) s4 += __builtin_bit_cast(f4, g[i]);
#pragma unroll
    for (int e = 0; e < 4; e++) {
      float sv = s4[e];
      sv += dppf<0x128>(sv);
      o[e] = fmaf(rbf(xrow_sum(sv)), rsig, o[e]);
    }
    if (lane < 8) {
      v2u o2 = {pk2(o[0], o[1]), pk2(o[2], o[3])};
      __builtin_amdgcn_raw_buffer_store_b64(o2, mkrsrc(P.out, S * D * 2), tok * (D * 2) + cg * 8, 0, OUT_AUX);
    }
  }
}

__global__ __launch_bounds__(NTHR, 2) void fused_kernel(
    const u16* __restrict__ q, const u16* __restrict__ k, const u16* __restrict__ v, const u16* __restrict__ shk,
    const u16* __restrict__ hk, const u16* __restrict__ dsk, const u16* __restrict__ wr, const u16* __restrict__ wg,
    const u16* __restrict__ wu, const u16* __restrict__ wd, u16* __restrict__ out, char* ws) {
  extern __shared__ __attribute__((aligned(16))) char smem_[];
  Ctx P;
  P.q = q; P.k = k; P.v = v; P.shk = shk; P.hk = hk; P.dsk = dsk; P.wr = wr; P.wg = wg; P.wu = wu; P.wd = wd;
  P.out = out; P.ws = ws;
  u64 pc;
  asm volatile("s_getpc_b64 %0" : "=s"(pc));
  P.code = (const char*)(pc & ~(u64)127);
  P.sm = (LAS char*)smem_;
  P.tid = threadIdx.x; P.lane = P.tid & 63;
  P.wave = __builtin_amdgcn_readfirstlane(P.tid >> 6);
  const int b = blockIdx.x;
  P.b = b;
  P.C = (b & 7) * 32 + (b >> 3);
  P.is_conv = b < NCONV;
  const bool is_moe = !P.is_conv && b < NCONV + NMOE;
  P.cg = (b & 7) * 18 + (b >> 3);
  P.mw = (b & 7) * 9 + ((b - NCONV) >> 3);
  P.rws = mkrsrc(ws, WS_END);
  const int lane = P.lane, wave = P.wave;
  (void)lane;
#pragma unroll
  for (int i = 0; i < 32; i++) P.ts[i] = 0;
  STAMP(0); STAMPC(30);
  Early E;
  if (wave == 6) {
    constexpr int NL = (CODE_PF * 8 + 31) / 32;
    if (CODE_PF)
      asm volatile("global_load_dword v255, %0, off" :: "v"(P.code + min(((b >> 3) * NL + min(lane, NL - 1)) * 128, CODE_PF * 1024 - 128)) : "v255");
    const v4u z = {0u, 0u, 0u, 0u};
    *(LAS v4u*)(P.sm + L_U + (lane >> 4) * UL_B + (lane & 15) * 16) = z;
    *(LAS v4u*)(P.sm + L_U + (lane >> 4) * UL_B + 256 + (lane & 15) * 16) = z;
    ((LAS u32*)(P.sm + L_MISC))[lane] = 0u;
    ((LAS u32*)(P.sm + L_MISC))[64 + lane] = 0u;
    asm volatile("s_waitcnt lgkmcnt(0)" ::: "memory");
  }
  asm volatile("s_barrier" ::: "memory");
  asm volatile("" :: "s"(q), "s"(k), "s"(v), "s"(shk), "s"(hk), "s"(dsk), "s"(wr), "s"(wg), "s"(wu), "s"(wd), "s"(out), "s"(ws));
  STAMP(11);
  const int row0 = 8 * P.C * (D * 2);
  EarlyX X;
  if (P.wave == 7) {
    __builtin_amdgcn_s_setprio(3);
    const int m = lane & 15, g4 = lane >> 4;
    E.epoch = __hip_atomic_load((GAS u64*)(P.ws + WS_E), __ATOMIC_RELAXED, __HIP_MEMORY_SCOPE_AGENT);
    if (ROUTER_DELAY) __builtin_amdgcn_s_sleep(ROUTER_DELAY);
    __builtin_amdgcn_sched_barrier(0);
    const __amdgpu_buffer_rsrc_t rq = mkrsrc(P.q, S * D * 2);
    const int xo = (m & 7) * (D * 2) + (m >> 3) * 64 + g4 * 16;
#pragma unroll
    for (int i = 0; i < 9; i++) X.xa[i] = __builtin_amdgcn_raw_buffer_load_b128(rq, xo + i * 128, row0, NT_Q);
    __builtin_amdgcn_sched_barrier(0);
    const size_t pw = (size_t)P.wr + m * 64, po = (size_t)kOnes;
    const GAS char* wp = (const GAS char*)(m < 2 ? pw : po) + g4 * 16;
#pragma unroll
    for (int i = 0; i < 9; i++) X.wb[i] = *(const GAS v4u*)(wp + i * 128);
  }
  asm volatile("" : "+s"(P.wave));
  if (P.wave == 5) {
    const v4u g4 = {GMAGIC, GMAGIC, GMAGIC, GMAGIC};
    __builtin_amdgcn_raw_buffer_store_b128(g4, P.rws, lane < 8 * NGATE ? gate_off(lane >> 3, b >> 4, b & 15) + (lane & 7) * 16 : OOB, 0, AUX_SC1);
  }
  asm volatile("" : "+s"(P.wave));
  if (P.wave < 5) {
    const int j4 = min(P.tid, 287) * 4;
    const __amdgpu_buffer_rsrc_t rk = mkrsrc(P.k, S * D * 2), rv = mkrsrc(P.v, S * D * 2);
    if (TILE_DELAY) __builtin_amdgcn_s_sleep(TILE_DELAY);
    __builtin_amdgcn_sched_barrier(0);
#pragma unroll
    for (int i = 0; i < 3; i++) E.kk[i] = __builtin_amdgcn_raw_buffer_load_b32(rk, row0 + (i - 3) * (D * 2) + j4, 0, NT_KV);
#pragma unroll
    for (int i = 0; i < 8; i++) E.kk[3 + i] = __builtin_amdgcn_raw_buffer_load_b32(rk, j4 + (i & 3) * (D * 2), row0 + (i & 4) * (D * 2), NT_KV);
    __builtin_amdgcn_sched_barrier(0);
    if (SK_POS == 1) E.sk = *(const GAS v4u*)((const GAS char*)P.shk + j4 * 4);
    __builtin_amdgcn_sched_barrier(0);
#pragma unroll
    for (int i = 0; i < 8; i++) E.vv[i] = __builtin_amdgcn_raw_buffer_load_b32(rv, j4 + (i & 3) * (D * 2), row0 + (i & 4) * (D * 2), NT_KV);
    __builtin_amdgcn_sched_barrier(0);
    if (SK_POS == 2) E.sk = *(const GAS v4u*)((const GAS char*)P.shk + j4 * 4);
    E.dd = *(const GAS u32*)((const GAS char*)P.dsk + j4);
  }
  const int role = P.is_conv ? 0 : (is_moe ? 1 : 2);
  asm volatile("" : "+s"(P.wave));
  STAMP(10);
  if (P.wave == 7) router_stats(P, E, X, role);
  asm volatile("" : "+s"(P.wave));
  if (P.wave != 7) {
    if (P.wave >= 5) aux_wave(P, role);
    else tile_wave(P, E, is_moe);
  } else {
    router_wave(P, E, role);
  }
  asm volatile("" : "+s"(P.wave));
  if (is_moe && P.wave >= 6) routed_rows(P);
  FLUSH();
}

}

extern "C" void kernel_launch(void* const* d_in, const int* in_sizes, int n_in, void* d_out, int out_size, void* d_ws,
                              size_t ws_size, hipStream_t stream) {
  (void)in_sizes; (void)n_in; (void)out_size; (void)ws_size;
  (void)hipFuncSetAttribute((const void*)fused_kernel, hipFuncAttributeMaxDynamicSharedMemorySize, L_TOTAL);
  fused_kernel<<<NWG, NTHR, L_TOTAL, stream>>>(
      (const u16*)d_in[0], (const u16*)d_in[1], (const u16*)d_in[2], (const u16*)d_in[3], (const u16*)d_in[4],
      (const u16*)d_in[5], (const u16*)d_in[7], (const u16*)d_in[8], (const u16*)d_in[9], (const u16*)d_in[10],
      (u16*)d_out, (char*)d_ws + (WS_AT_END ? ((ws_size - WS_END) & ~(size_t)0xFFFF) : 0));
}
```

```cpp
#include <hip/hip_runtime.h>
#include <hip/hip_bf16.h>

typedef unsigned int u32;
typedef unsigned long long u64;
typedef unsigned short u16;
typedef u32 v4u __attribute__((ext_vector_type(4)));
typedef u32 v2u __attribute__((ext_vector_type(2)));
typedef float f4 __attribute__((ext_vector_type(4)));
typedef float f2 __attribute__((ext_vector_type(2)));
typedef __bf16 bf8 __attribute__((ext_vector_type(8)));
typedef __bf16 bf2 __attribute__((ext_vector_type(2)));
#define LAS __attribute__((address_space(3)))
#define GAS __attribute__((address_space(1)))

namespace {

constexpr int S = 2048, D = 576, F = 4608;
constexpr int NWG = 256, NTHR = 512, NCONV = 144, NMOE = 72;
constexpr int AUX_SC1 = 16;
constexpr u32 SAME = 0xFFFFFFFFu, MAXV = 0xFFFFFFFEu;
constexpr u32 SPIN_CAP = 1u << 16, GATE_CAP = 96;

constexpr u32 GMAGIC = 0x5AC3E900u;
constexpr int OOB = 0x7FFFFF00;
#define U_POST 2
#define WS_AT_END 0
#define HELPERS 1
#define Y_POST 8
#define OUT_AUX 16
#define W2_GATE 2
#define PF_R0 192
#define STAGED 1
#define H_DELAY 10
#define PF_CE 1
#define PF_WHEN 0
#define W1_DEPTH 0
#define DRAIN_FIRST 1
#define U_DELAY 0
#define PF_FIRST 1
#define SK_POS 1
#define ROUTER_DELAY 0
#define W2_EARLY 0
#define SLOW_FIX 1
#define P_POST 1
#define SPLIT_U 1
#define SPLIT_Y 0
#define TILE_DELAY2 0
#define LATE_MODE 0
#define R_POST 8
#define COPY_FIRST 1
#define MID_W 4
#define W5_EARLY 0
#define POLL_SLOW 12
#define POLL_FAST 0
#define POLL_THR 1
#define GSTRIDE 256
#define Y_GATE 1
#define W5_DIRECT 1
#define TOUCH2 0
#define SP_DEPTH 2
#define LT_DEPTH 9
#define BAR_LATE 1
#define W5_START 6
#define W6_DIRECT 6
#define TILE_DELAY 0
#define CODE_PF 43
#define NT_MASK 23
constexpr int NT_KV = (NT_MASK & 1) ? 2 : 0, NT_Q = (NT_MASK & 2) ? 2 : 0, NT_H = (NT_MASK & 4) ? 2 : 0, NT_T = (NT_MASK & 8) ? 2 : 0,
              NT_W = (NT_MASK & 16) ? 2 : 0;

constexpr int WS_E = 0;
constexpr int WS_G = 4096;
#define NREP 16
constexpr int GATE_B = NREP * 16 * GSTRIDE;
constexpr int WS_R = WS_G + 6 * GATE_B;
constexpr int WS_M = WS_R + S * 8;
constexpr int WS_U = WS_M + S * 8;
constexpr int TILE_B = D * 16;
constexpr int WS_P = WS_U + NWG * TILE_B;
constexpr int PART_B = NCONV * 128;
constexpr int WS_END = WS_P + NMOE * PART_B;
constexpr int WS_DBG = 4 << 20;
static_assert(WS_END <= WS_DBG, "ws");
enum { G_R = 0, G_U, G_Y, G_P, G_V, G_Y1, NGATE };
#define ONES8 0x3F803F80u, 0x3F803F80u, 0x3F803F80u, 0x3F803F80u, 0x3F803F80u, 0x3F803F80u, 0x3F803F80u, 0x3F803F80u
#define ONES32 ONES8, ONES8, ONES8, ONES8
__device__ const u32 kOnes[288] __attribute__((aligned(128))) = {ONES32, ONES32, ONES32, ONES32, ONES32, ONES32, ONES32, ONES32, ONES32};

constexpr int HC_B = 2080 * 16;
constexpr int L_H = 0;
constexpr int L_KV = L_H + 4352;
constexpr int UL_B = 290 * 16;
constexpr int L_U = L_H + 4 * HC_B;
constexpr int L_STG = L_U + 4 * UL_B;
constexpr int L_MISC = L_STG + TILE_B;
constexpr int L_CNT = L_MISC;
constexpr int L_CAND = L_CNT + 96;
constexpr int L_SEL = L_CAND + 288;
constexpr int L_REC = L_SEL + 32;
constexpr int L_STAT = L_REC + 32;
constexpr int Y1_B = 2048 + 64;
constexpr int L_END = L_STAT + 64;
constexpr int XS_STRIDE = 1184;
constexpr int L_XS = 0;
constexpr int L_RED = L_XS + 8 * XS_STRIDE;
constexpr int L_HB = L_RED + 6 * 8 * 128 * 4;
constexpr int HB_STRIDE = 160;
static_assert(L_END + 1024 <= 160 * 1024, "lds");
static_assert(L_HB + 8 * HB_STRIDE <= L_U, "lds");
static_assert(L_END - L_MISC == 512, "zero fill");

__device__ __forceinline__ constexpr int xrow_off(int t) { return L_U + (t / 3) * UL_B + 512 + (t % 3) * (D * 2); }
enum { C_DRAIN = 0, C_STAT, C_TRUST, C_GU, C_SEL, C_H, C_U, C_Y, C_XS, C_RED, C_HB, C_GY, C_PUB, C_TAG, C_QQ, C_U2, C_Y1, C_W1, C_DN, C_H0, C_VER, C_REDO, C_OK, C_MASK };

__device__ __forceinline__ float lo16(u32 x) { return __uint_as_float(x << 16); }
__device__ __forceinline__ float hi16(u32 x) { return __uint_as_float(x & 0xFFFF0000u); }
__device__ __forceinline__ u32 pk2(float a, float b) {
  bf2 p = {(__bf16)a, (__bf16)b};
  return __builtin_bit_cast(u32, p);
}
__device__ __forceinline__ float rbf(float a) { return lo16(pk2(a, a)); }
__device__ __forceinline__ float dot2(u32 a, u32 b, float c) {
  return __builtin_amdgcn_fdot2_f32_bf16(__builtin_bit_cast(bf2, a), __builtin_bit_cast(bf2, b), c, false);
}
template <int CTRL> __device__ __forceinline__ u32 dppu(u32 x) {
  return (u32)__builtin_amdgcn_mov_dpp((int)x, CTRL, 0xf, 0xf, true);
}
template <int CTRL> __device__ __forceinline__ float dppf(float x) { return __uint_as_float(dppu<CTRL>(__float_as_uint(x))); }
__device__ __forceinline__ float xrow_sum(float x) {
  auto s = __builtin_amdgcn_permlane16_swap(__float_as_uint(x), __float_as_uint(x), false, false);
  x = __uint_as_float(s[0]) + __uint_as_float(s[1]);
  auto t = __builtin_amdgcn_permlane32_swap(__float_as_uint(x), __float_as_uint(x), false, false);
  return __uint_as_float(t[0]) + __uint_as_float(t[1]);
}
__device__ __forceinline__ u32 umax(u32 a, u32 b) { return a > b ? a : b; }
__device__ __forceinline__ u32 umin(u32 a, u32 b) { return a < b ? a : b; }
__device__ __forceinline__ u32 umed3(u32 a, u32 b, u32 c) { return umax(umin(a, b), umin(umax(a, b), c)); }
__device__ __forceinline__ u32 xrow_umin(u32 x) {
  auto s = __builtin_amdgcn_permlane16_swap(x, x, false, false);
  x = umin(s[0], s[1]);
  auto t = __builtin_amdgcn_permlane32_swap(x, x, false, false);
  return umin(t[0], t[1]);
}
__device__ __forceinline__ u32 wave_umax(u32 x) {
  x = umax(x, dppu<0xB1>(x));
  x = umax(x, dppu<0x4E>(x));
  x = umax(x, dppu<0x141>(x));
  x = umax(x, dppu<0x140>(x));
  auto s = __builtin_amdgcn_permlane16_swap(x, x, false, false);
  x = umax(s[0], s[1]);
  auto t = __builtin_amdgcn_permlane32_swap(x, x, false, false);
  return umax(t[0], t[1]);
}
__device__ __forceinline__ void lds_signal(LAS char* sm, int c, u32 add = 1u) {
  u64 keep;
  asm volatile("s_waitcnt lgkmcnt(0)\n\ts_mov_b64 %0, exec\n\ts_mov_b64 exec, 1\n\tds_add_u32 %1, %2\n\ts_mov_b64 exec, %0"
               : "=&s"(keep) : "v"((u32)(size_t)(sm + L_CNT + 4 * c)), "v"(add) : "memory");
}
__device__ __forceinline__ void lds_signal_nowait(LAS char* sm, int c) {
  u64 keep;
  asm volatile("s_mov_b64 %0, exec\n\ts_mov_b64 exec, 1\n\tds_add_u32 %1, %2\n\ts_mov_b64 exec, %0"
               : "=&s"(keep) : "v"((u32)(size_t)(sm + L_CNT + 4 * c)), "v"(1u) : "memory");
}
__device__ __forceinline__ u32 lds_peek(LAS char* sm, int c) {
  return __hip_atomic_load((LAS u32*)(sm + L_CNT) + c, __ATOMIC_RELAXED, __HIP_MEMORY_SCOPE_WORKGROUP);
}
__device__ __forceinline__ bool checked_redo(LAS char* sm, u32 seen) {
  u32 v = __builtin_amdgcn_readfirstlane(seen);
  while (__builtin_expect((v & 0xFFu) < 2u, 0)) {
    __builtin_amdgcn_s_sleep(1);
    v = __builtin_amdgcn_readfirstlane(lds_peek(sm, C_OK));
  }
  asm volatile("" ::: "memory");
  return v > 0xFFu;
}
template <int SLEEP = 0> __device__ __forceinline__ void lds_wait(LAS char* sm, int c, u32 target) {
  u32 v, sv;
  if (SLEEP == 0)
    asm volatile("1:\n\tds_read_b32 %0, %2\n\ts_waitcnt lgkmcnt(0)\n\tv_readfirstlane_b32 %1, %0\n\ts_cmp_lt_u32 %1, %3\n\ts_cbranch_scc1 1b"
                 : "=&v"(v), "=&s"(sv) : "v"((u32)(size_t)(sm + L_CNT + 4 * c)), "s"(target) : "memory", "scc");
  else
    asm volatile("1:\n\ts_sleep %4\n\tds_read_b32 %0, %2\n\ts_waitcnt lgkmcnt(0)\n\tv_readfirstlane_b32 %1, %0\n\ts_cmp_lt_u32 %1, %3\n\ts_cbranch_scc1 1b"
                 : "=&v"(v), "=&s"(sv) : "v"((u32)(size_t)(sm + L_CNT + 4 * c)), "s"(target), "n"(SLEEP) : "memory", "scc");
}
__device__ __forceinline__ bool lds_arrive(LAS char* sm, int c, u32 last) {
  u64 keep;
  u32 v, sv;
  asm volatile("s_mov_b64 %0, exec\n\ts_mov_b64 exec, 1\n\tds_add_rtn_u32 %1, %3, %4\n\ts_waitcnt lgkmcnt(0)\n\tv_readfirstlane_b32 %2, %1\n\ts_mov_b64 exec, %0"
               : "=&s"(keep), "=&v"(v), "=&s"(sv) : "v"((u32)(size_t)(sm + L_CNT + 4 * c)), "v"(1u) : "memory");
  return sv == last;
}
__device__ __forceinline__ void wg_barrier() { asm volatile("s_waitcnt lgkmcnt(0)\n\ts_barrier" ::: "memory"); }
__device__ __forceinline__ u32 umax3(u32 a, u32 b, u32 c) { return umax(umax(a, b), c); }
__device__ __forceinline__ u32 vmax4(v4u g, u32 acc) { return umax3(umax3(g.x, g.y, acc), g.z, g.w); }
__device__ __forceinline__ f4 mfma16(v4u a, v4u b, f4 c) {
  return __builtin_amdgcn_mfma_f32_16x16x32_bf16(__builtin_bit_cast(bf8, a), __builtin_bit_cast(bf8, b), c, 0, 0, 0);
}
__device__ __forceinline__ __amdgpu_buffer_rsrc_t mkrsrc(const void* p, int bytes) {
  return __builtin_amdgcn_make_buffer_rsrc((void*)p, 0, bytes, 0x00020000);
}

#define STAMP(i) do {} while (0)
#define STAMPC(i) do {} while (0)
#define FLUSH() do {} while (0)
#define NOTE(i, v) do {} while (0)
constexpr int L_TOTAL = L_END;

struct Ctx {
  const u16 *q, *k, *v, *shk, *hk, *dsk, *wr, *wg, *wu, *wd;
  u16* out;
  char* ws;
  const char* code;
  __amdgpu_buffer_rsrc_t rws;
  LAS char* sm;
  int tid, lane, wave, b, C, cg, mw;
  bool is_conv;
  u32 t0;
};

__device__ __forceinline__ int gate_off(int gate, int rep, int shard) { return WS_G + gate * GATE_B + (rep * 16 + shard) * GSTRIDE; }
__device__ __forceinline__ u32 gate_add(const Ctx& P, int off, int val) {
  return (u32)__builtin_amdgcn_raw_ptr_buffer_atomic_add_i32(val, P.rws, off, 0, 0);
}
__device__ __forceinline__ u32 gate_post(const Ctx& P, int gate, bool on, u32 inc) {
  return gate_add(P, (on && P.lane < NREP) ? gate_off(gate, P.lane, P.b & 15) : OOB, (int)inc);
}
__device__ __forceinline__ void gate_confirm(const Ctx& P, int gate, u32 old, u32 inc) {
  bool bad = P.lane < NREP;
  _Pragma("nounroll") for (u32 spin = 0; spin < SPIN_CAP; spin++) {
    const u32 d = old - GMAGIC;
    bad = bad && ((d & 0xF0u) != 0u || d != (d & 0xFu) * inc);
    if (__builtin_expect(!__any(bad), 1)) break;
    NOTE(26, spin + 1);
    __builtin_amdgcn_s_sleep(2);
    old = gate_add(P, bad ? gate_off(gate, P.lane, P.b & 15) : OOB, (int)inc);
  }
}
__device__ __forceinline__ void gate_wait(const Ctx& P, int gate, u32 expect, u32 inc, u32 cap = GATE_CAP) {
  const bool on = P.lane < 16 && expect != 0u;
  const int off = on ? gate_off(gate, P.b / (256 / NREP), P.lane) : OOB;
  const u32 want = on ? GMAGIC + expect * inc : 0u;
  _Pragma("nounroll") for (u32 spin = 0; spin < cap; spin++) {
    const u32 v = gate_add(P, off, 0);
    const u64 full = __ballot(v == want);
    if (full == ~0ull) break;
    if (POLL_SLOW && (u32)__builtin_popcountll(__ballot(on && v != GMAGIC)) < POLL_THR) __builtin_amdgcn_s_sleep(POLL_SLOW);
    else if (POLL_FAST) __builtin_amdgcn_s_sleep(POLL_FAST);
  }
}
__device__ __forceinline__ u32 gate_inc(u32 tag) { return 1u + (tag << 8); }
__device__ __forceinline__ void post_u_complete(const Ctx& P, u32 inc) {
  lds_wait<1>(P.sm, C_DRAIN, 5);
  gate_confirm(P, G_V, gate_post(P, G_V, true, inc), inc);
}

__device__ __forceinline__ void hcopy_store(LAS char* dst, v4u lo, v4u hi, bool first) {
  u32 d[8], o[7];
#pragma unroll
  for (int e = 0; e < 4; e++) { d[e] = lo[e]; d[4 + e] = hi[e]; }
#pragma unroll
  for (int e = 0; e < 7; e++) o[e] = __builtin_amdgcn_alignbit(d[e + 1], d[e], 16);
  constexpr int BASE[8] = {0, 260, 519, 778, 1037, 1296, 1556, 1815};
#pragma unroll
  for (int e = 0; e < 4; e++) {
    v4u a = {d[e], d[e + 1], d[e + 2], d[e + 3]}, c = {o[e], o[e + 1], o[e + 2], o[e + 3]};
    if (e > 0 || first) *(LAS v4u*)(dst + BASE[2 * e] * 16) = a;
    *(LAS v4u*)(dst + BASE[2 * e + 1] * 16) = c;
  }
}
template <bool TWO> __device__ __forceinline__ void touch_w1(const Ctx& P, int mat, int blk);
__device__ __forceinline__ void hcopy_first(const Ctx& P, int ch0) {
  const __amdgpu_buffer_rsrc_t rh = mkrsrc(P.hk + (size_t)(4 * P.cg + ch0) * S, 2 * S * 2);
  v4u lo[8];
#pragma unroll
  for (int r = 0; r < 8; r++) lo[r] = __builtin_amdgcn_raw_buffer_load_b128(rh, P.lane * 16 + (r & 3) * 1024, (r >> 2) * 4096, NT_H);
  __builtin_amdgcn_sched_barrier(0);
  STAMP(12);
  LAS char* dst = P.sm + L_H + ch0 * HC_B + (P.lane + 2) * 16;
#pragma unroll
  for (int r = 0; r < 8; r++) *(LAS v4u*)(dst + (r >> 2) * HC_B + (r & 3) * 1024) = lo[r];
  lds_signal(P.sm, C_H0);
  if (HELPERS != 0 && PF_CE >= 1) touch_w1<(TOUCH2 & 2) != 0>(P, ch0 >> 1, 25 + (P.b >> 3));
  if (HELPERS != 0 && PF_CE >= 2) touch_w1<(TOUCH2 & 2) != 0>(P, ch0 >> 1, 43 + (P.b >> 3));
  __builtin_amdgcn_sched_barrier(0);
  STAMP(1);
  lds_wait<1>(P.sm, C_VER, 5);
  {
    const int c = P.lane & 1, m1 = (P.lane >> 1) & 1;
    const v4u z = {0u, 0u, 0u, 0u};
    v4u h0 = *(const LAS v4u*)(P.sm + L_H + (ch0 + c) * HC_B + 32);
    h0 = m1 ? h0 : z;
    if (P.lane < 4) hcopy_store(P.sm + L_H + (ch0 + c) * HC_B + m1 * 16, z, h0, true);
  }
}
template <int NR> __device__ __forceinline__ void hcopy_rest(const Ctx& P, int r0) {
  v4u lo[NR], hi[NR];
  lds_wait<1>(P.sm, C_VER, 5);
  LAS char* dst[NR];
#pragma unroll
  for (int r = 0; r < NR; r++) {
    dst[r] = P.sm + L_H + ((r0 + r) >> 2) * HC_B + ((r0 + r) & 3) * 1024 + (P.lane + 2) * 16;
    lo[r] = *(const LAS v4u*)dst[r];
    hi[r] = *(const LAS v4u*)(dst[r] + 16);
  }
#pragma unroll
  for (int r = 0; r < NR; r++) hcopy_store(dst[r], lo[r], hi[r], false);
  lds_signal(P.sm, C_H);
}

struct GateUp {
  v4u Bf[3][8];
  __device__ __forceinline__ void fetch(const Ctx& P, int kw) {
    const int n16 = P.lane & 15, g4 = P.lane >> 4;
    v4u W1[3][8];
    const GAS char* wp = (const GAS char*)(n16 < 8 ? P.wg : P.wu) + ((size_t)(96 * kw + 8 * g4) * F + 64 * P.mw + 8 * (n16 & 7)) * 2;
    __builtin_amdgcn_sched_barrier(0);
#pragma unroll
    for (int i = 0; i < 3; i++)
#pragma unroll
      for (int jj = 0; jj < 8; jj++) {
        const GAS v4u* a = (const GAS v4u*)(wp + (size_t)(32 * i + jj) * (F * 2));
        W1[i][jj] = NT_W ? __builtin_nontemporal_load(a) : *a;
      }
    __builtin_amdgcn_sched_barrier(0);
    lds_signal(P.sm, C_H0);
    STAMP(3);
#pragma unroll
    for (int i = 0; i < 3; i++)
#pragma unroll
      for (int e = 0; e < 8; e++) {
#pragma unroll
        for (int d = 0; d < 4; d++)
          Bf[i][e][d] = __builtin_amdgcn_perm(W1[i][2 * d + 1][e >> 1], W1[i][2 * d][e >> 1], (e & 1) ? 0x07060302u : 0x05040100u);
        asm volatile("" : "+v"(Bf[i][e]));
      }
    STAMP(17);
  }
  __device__ __forceinline__ void mma(const Ctx& P, int kw) {
    LAS char* sm = P.sm;
    const int n16 = P.lane & 15, g4 = P.lane >> 4;
    lds_wait<1>(sm, C_XS, 2);
    STAMP(5);
    f4 acc[8];
#pragma unroll
    for (int e = 0; e < 8; e++) acc[e] = (f4){0.f, 0.f, 0.f, 0.f};
#pragma unroll
    for (int i = 0; i < 3; i++) {
      const v4u A = *(const LAS v4u*)(sm + L_XS + (n16 & 7) * XS_STRIDE + (96 * kw + 32 * i + 8 * g4) * 2);
#pragma unroll
      for (int e = 0; e < 8; e++) acc[e] = mfma16(A, Bf[i][e], acc[e]);
    }
    LAS char* dst = sm + L_RED + ((kw * 8 + 4 * (g4 & 1) + (g4 & 2)) * 128 + 8 * n16) * 4;
#pragma unroll
    for (int r = 0; r < 2; r++) {
      f4 a, c;
#pragma unroll
      for (int e = 0; e < 4; e++) { a[e] = g4 >= 2 ? acc[e][2 + r] : acc[e][r]; c[e] = g4 >= 2 ? acc[4 + e][2 + r] : acc[4 + e][r]; }
      *(LAS f4*)(dst + r * 512) = a;
      *(LAS f4*)(dst + r * 512 + 16) = c;
    }
    lds_signal_nowait(sm, C_RED);
  }
};

template <class FN> __device__ __forceinline__ void ffn_gate_up(const Ctx& P, FN&& between) {
  LAS char* sm = P.sm;
  const int lane = P.lane, wave = P.wave;
  if (!(W6_DIRECT && wave == 1)) {
    if (W5_DIRECT && wave == 5) { if (W5_START) __builtin_amdgcn_s_sleep(W5_START); else lds_wait<1>(sm, C_VER, 5); }
    else if (wave >= PF_FIRST || (SLOW_FIX && (P.mw & 7) == 3))
      lds_wait<1>(sm, (wave < PF_R0 / 96 || wave >= MID_W) ? C_PUB : C_W1, wave < PF_R0 / 96 ? 1 : 2);
    GateUp gu;
    gu.fetch(P, wave);
    if (W5_EARLY) between();
    gu.mma(P, wave);
  }
  STAMP(18);
  if (!W5_EARLY) between();
  if (wave >= 2) {
    const int tk = 2 * (wave - 2) + (lane >> 5);
    const LAS char* src = sm + L_RED + (tk * 128 + 2 * (lane & 31)) * 4;
    lds_wait(sm, C_RED, 6);
    f2 G = {0.f, 0.f}, U = {0.f, 0.f};
#pragma unroll
    for (int w = 0; w < 6; w++) {
      G += *(const LAS f2*)(src + w * 4096);
      U += *(const LAS f2*)(src + w * 4096 + 256);
    }
    const u32 gb = pk2(G.x, G.y), ub = pk2(U.x, U.y);
    const float g0 = lo16(gb), g1 = hi16(gb);
    const u32 sg = pk2(g0 * __builtin_amdgcn_rcpf(1.0f + __expf(-g0)), g1 * __builtin_amdgcn_rcpf(1.0f + __expf(-g1)));
    *(LAS u32*)(sm + L_HB + tk * HB_STRIDE + (lane & 31) * 4) = pk2(lo16(sg) * lo16(ub), hi16(sg) * hi16(ub));
    lds_signal(sm, C_HB);
    STAMP(15);
  }
}

struct Down {
  v4u W2[2][8];
  __device__ __forceinline__ void load(const Ctx& P, int cu) {
    const int n16 = P.lane & 15, g4 = P.lane >> 4;
    const __amdgpu_buffer_rsrc_t rd = mkrsrc(P.wd + (size_t)64 * P.mw * D, 64 * D * 2);
    const int o2 = (cu == 4 && n16 >= 8) ? OOB : (8 * g4 * D + 128 * cu + 8 * n16) * 2;
    __builtin_amdgcn_sched_barrier(0);
#pragma unroll
    for (int ks = 0; ks < 2; ks++)
#pragma unroll
      for (int jj = 0; jj < 8; jj++)
        W2[ks][jj] = __builtin_amdgcn_raw_buffer_load_b128(rd, o2 + (jj & 3) * (D * 2), (32 * ks + (jj & 4)) * (D * 2), NT_W);
    __builtin_amdgcn_sched_barrier(0);
  }
  __device__ __forceinline__ void turn() {
    __builtin_amdgcn_sched_barrier(0);
#pragma unroll
    for (int ks = 0; ks < 2; ks++) {
      v4u t[8];
#pragma unroll
      for (int e = 0; e < 8; e++)
#pragma unroll
        for (int d = 0; d < 4; d++)
          t[e][d] = __builtin_amdgcn_perm(W2[ks][2 * d + 1][e >> 1], W2[ks][2 * d][e >> 1], (e & 1) ? 0x07060302u : 0x05040100u);
#pragma unroll
      for (int e = 0; e < 8; e++) { W2[ks][e] = t[e]; asm volatile("" : "+v"(W2[ks][e])); }
    }
  }
  __device__ __forceinline__ void mma(const Ctx& P, int cu, v4u A0, v4u A1, u32 tag12) {
    const int n16 = P.lane & 15, g4 = P.lane >> 4;
    const int off = WS_P + P.mw * PART_B + (32 * cu + 2 * (n16 & 7)) * 128 + (n16 >> 3) * 64 + g4 * 16;
    f4 ad[8];
#pragma unroll
    for (int e = 0; e < 8; e++) ad[e] = mfma16(A0, W2[0][e], (f4){0.f, 0.f, 0.f, 0.f});
#pragma unroll
    for (int e = 0; e < 8; e++) ad[e] = mfma16(A1, W2[1][e], ad[e]);
#pragma unroll
    for (int hh = 0; hh < 2; hh++) {
      v4u s0, s1;
#pragma unroll
      for (int e = 0; e < 4; e++) {
        const f4 a = ad[4 * hh + e];
        const u32 x = ((__float_as_uint(g4 >= 2 ? a[2] : a[0]) + 0x800u) & 0xFFFFF000u) | tag12;
        const u32 y = ((__float_as_uint(g4 >= 2 ? a[3] : a[1]) + 0x800u) & 0xFFFFF000u) | tag12;
        s0[e] = (u32)__builtin_amdgcn_update_dpp((int)x, (int)y, 0x128, 0xf, 0xc, false);
        s1[e] = (u32)__builtin_amdgcn_update_dpp((int)y, (int)x, 0x128, 0xf, 0x3, false);
      }
      __builtin_amdgcn_raw_buffer_store_b128(s0, P.rws, off + hh * 128, 0, AUX_SC1);
      if (cu != 4) __builtin_amdgcn_raw_buffer_store_b128(s1, P.rws, off + hh * 128 + 2048, 0, AUX_SC1);
    }
  }
};
__device__ __forceinline__ void down_fetch(const Ctx& P, Down& d, int cu) {
  lds_wait<1>(P.sm, C_H0, 6);
  d.load(P, cu);
  STAMP(4);
  d.turn();
  STAMP(19);
}
__device__ __forceinline__ void down_finish(const Ctx& P, Down& d, int cu) {
  LAS char* sm = P.sm;
  const int n16 = P.lane & 15, g4 = P.lane >> 4;
  const u32 tag = *(const LAS u32*)(sm + L_REC), tag12 = tag & 0xFFFu;
  int gpo = P.lane < NREP ? gate_off(G_P, P.lane, P.b & 15) : OOB;
  u32 inc = gate_inc(tag);
  asm volatile("" : "+v"(gpo), "+v"(inc));
  lds_wait(sm, C_HB, 4);
  const v4u A0 = *(const LAS v4u*)(sm + L_HB + (n16 & 7) * HB_STRIDE + g4 * 16);
  const v4u A1 = *(const LAS v4u*)(sm + L_HB + (n16 & 7) * HB_STRIDE + 64 + g4 * 16);
  if (P_POST == 0 && lds_arrive(sm, C_DN, 4)) (void)gate_add(P, gpo, (int)inc);
  STAMP(20);
  d.mma(P, cu, A0, A1, tag12);
  if (P_POST == 1 && lds_arrive(sm, C_DN, 4)) (void)gate_add(P, gpo, (int)inc);
  STAMP(21);
}

template <bool TWO> __device__ __forceinline__ void touch(const u16* base, int voff, int soff) {
  const v4u r = {(u32)(size_t)base, (u32)((size_t)base >> 32) & 0xFFFFu, (u32)(D * F * 2), 0x00020000u};
  if (TWO)
    asm volatile("buffer_load_dword v255, %0, %1, %2 offen nt\n\tbuffer_load_dword v255, %0, %1, %2 offen offset:64"
                 :: "v"(voff), "s"(r), "s"(soff) : "v255");
  else
    asm volatile("buffer_load_dword v255, %0, %1, %2 offen" :: "v"(voff), "s"(r), "s"(soff) : "v255");
}
template <int N, bool TWO> __device__ __forceinline__ void touch_wait() {
  asm volatile("s_waitcnt vmcnt(%0)" :: "n"(N * (TWO ? 2 : 1)) : "v255");
}
template <bool TWO> __device__ __forceinline__ void touch_w1(const Ctx& P, int mat, int blk) {
  const int r7 = (P.lane * 57) >> 9, c9 = P.lane - 9 * r7;
  touch<TWO>(mat ? P.wu : P.wg, P.lane < 63 ? r7 * (F * 2) + c9 * 128 : OOB, (PF_R0 + 7 * blk) * (F * 2) + (P.b & 7) * 1152);
}
template <bool TWO> __device__ __forceinline__ void touch_w2(const Ctx& P, int unit) {
  if (unit < 81) touch<TWO>(P.wd, P.lane * 128, (P.b & 7) * (9 * 64 * D * 2) + unit * 8192);
}

__device__ __forceinline__ v4u encode_y(v4u y, v4u u) {
  u64 c0, c1, c2, c3;
  asm("v_cmp_ne_u32_e64 %4, %0, %8\n\tv_cmp_ne_u32_e64 %5, %1, %9\n\tv_cmp_ne_u32_e64 %6, %2, %10\n\tv_cmp_ne_u32_e64 %7, %3, %11\n\t"
      "v_cndmask_b32_e64 %0, -1, %0, %4\n\tv_cndmask_b32_e64 %1, -1, %1, %5\n\ts_nop 0\n\t"
      "v_cndmask_b32_e64 %2, -1, %2, %6\n\tv_cndmask_b32_e64 %3, -1, %3, %7"
      : "+v"(y.x), "+v"(y.y), "+v"(y.z), "+v"(y.w), "=&s"(c0), "=&s"(c1), "=&s"(c2), "=&s"(c3)
      : "v"(u.x), "v"(u.y), "v"(u.z), "v"(u.w));
  return y;
}
__device__ __forceinline__ void publish_y(const Ctx& P, const LAS char* yb, int ystride, int tile0, int t, int step, int n) {
  const int gch = t & 3;
  const int goff = WS_U + (tile0 + (t >> 2)) * TILE_B + (4 * P.cg + gch) * 16;
#pragma unroll
  for (int it = 0; it < n; it++) {
    const v4u y4 = *(const LAS v4u*)(yb + gch * ystride + (it * step + (t >> 2)) * 16);
    const v4u u4 = *(const LAS v4u*)(P.sm + L_U + gch * UL_B + (32 + tile0 + it * step + (t >> 2)) * 16);
    __builtin_amdgcn_raw_buffer_store_b128(encode_y(y4, u4), P.rws, goff, it * step * TILE_B, AUX_SC1);
  }
}

__device__ __forceinline__ void gather_half(const Ctx& P, int tile0, v4u (&g)[8]) {
  const int goff = WS_U + (tile0 + (P.lane >> 2)) * TILE_B + (4 * P.cg + (P.lane & 3)) * 16;
#pragma unroll
  for (int it = 0; it < 8; it++) g[it] = __builtin_amdgcn_raw_buffer_load_b128(P.rws, goff, it * 16 * TILE_B, AUX_SC1);
}

__device__ __forceinline__ void verify_half(const Ctx& P, int tile0) {
  LAS char* sm = P.sm;
  lds_wait<1>(sm, C_TRUST, 1);
  v4u g[8];
  gather_half(P, tile0, g);
  LAS char* ud = sm + L_U + (P.lane & 3) * UL_B + (32 + tile0 + (P.lane >> 2)) * 16;
  u32 df = 0;
#pragma unroll
  for (int it = 0; it < 8; it++) {
    const v4u x = *(const LAS v4u*)(ud + it * 256) ^ g[it];
    df |= x.x | x.y;
    df |= x.z | x.w;
  }
  if (__builtin_expect(__any(df != 0), 0)) {
#pragma unroll
    for (int it = 0; it < 8; it++) *(LAS v4u*)(ud + it * 256) = g[it];
    lds_signal(sm, C_OK, 0x101u);
  } else {
    lds_signal(sm, C_OK);
  }
}

__device__ __forceinline__ void conv_phase(const Ctx& P, u32 inc) {
  LAS char* sm = P.sm;
  const int tid = P.tid, lane = P.lane, wave = P.wave, n16 = lane & 15, g4 = lane >> 4, gch = tid & 3;
  const int goff = WS_U + (tid >> 2) * TILE_B + (4 * P.cg + gch) * 16;
  LAS char* ud = sm + L_U + gch * UL_B + (32 + (tid >> 2)) * 16;
  const int p1 = n16 + 1, kap = p1 & 7;
  const int base16 = 258 * kap + ((0x98654320u >> (4 * kap)) & 15);
  const LAS char* pa = sm + L_H + wave * HC_B + (base16 + g4 + (p1 >> 3)) * 16;
  const LAS char* pb = sm + L_U + wave * UL_B + (32 + 2 * n16 + 1 - g4 - 28) * 16;
  if (!COPY_FIRST) {
    lds_wait<1>(sm, C_H0, 2);
    hcopy_rest<3>(P, 3 * wave);
    STAMP(17);
  }
  asm volatile("s_waitcnt vmcnt(0)" ::: "memory");
  lds_signal(sm, C_DRAIN);
  lds_wait<1>(sm, C_QQ, 5);
  lds_wait<1>(sm, C_GU, 1);
  __builtin_amdgcn_s_setprio(2);
  STAMP(3);
  if (U_DELAY) __builtin_amdgcn_s_sleep(U_DELAY);
  STAMP(4);
  v4u g[2];
#pragma unroll
  for (int it = 0; it < 2; it++) g[it] = __builtin_amdgcn_raw_buffer_load_b128(P.rws, goff, it * 64 * TILE_B, AUX_SC1);
  *(LAS v4u*)ud = g[0];
  *(LAS v4u*)(ud + 1024) = g[1];
  lds_signal(sm, C_U);
  STAMP(5);
  f4 acc[8];
  v4u A[2][8], B[2][4];
  lds_wait(sm, C_H, 6);
#pragma unroll
  for (int a = 0; a < 8; a++) A[0][a] = *(const LAS v4u*)(pa + a * 512);
  __builtin_amdgcn_sched_barrier(0);
  lds_wait(sm, C_U, 4);
  STAMP(6);
  v4u u4[2];
  _Pragma("nounroll") for (int pass = 0; pass < 2; pass++) {
#pragma unroll
    for (int t = 0; t < 8; t++) acc[t] = (f4){0.f, 0.f, 0.f, 0.f};
#pragma unroll
    for (int a = 0; a < 4; a++) B[0][a] = *(const LAS v4u*)(pb + a * 512 + 7 * 64);
    u32 seen;
    v4u A2[3][4], B2[3][4];
#pragma unroll
    for (int bb = 0; bb < 8; bb++) {
      __builtin_amdgcn_sched_barrier(0);
      if (bb == 6) seen = lds_peek(sm, C_U2);
      if (bb == 7) {
        if (__builtin_expect(__builtin_amdgcn_readfirstlane(seen) < 1u, 0)) lds_wait(sm, C_U2, 1);
        asm volatile("" ::: "memory");
        __builtin_amdgcn_sched_barrier(0);
      }
#pragma unroll
      for (int a = 0; a < 8; a++) {
        if (bb < 7) {
          A[(bb + 1) & 1][a] = *(const LAS v4u*)(pa + a * 512 + (bb + 1) * 64);
          if (a < 4) B[(bb + 1) & 1][a] = *(const LAS v4u*)(pb + a * 512 + (6 - bb) * 64);
        } else {
          A2[a >> 2][a & 3] = *(const LAS v4u*)(pa + (a & 3) * 512 + (a >> 2) * 64);
          B2[a >> 2][a & 3] = *(const LAS v4u*)(pb + (4 + (a & 3)) * 512 + (7 - (a >> 2)) * 64);
        }
      }
#pragma unroll
      for (int t = 0; t < 8; t++)
#pragma unroll
        for (int c = 0; c < 4; c++)
          if (c <= t) acc[t] = mfma16(A[bb & 1][t - c], B[bb & 1][c], acc[t]);
      if (bb < 7) {
#pragma unroll
        for (int i = 0; i < 12; i++) {
          __builtin_amdgcn_sched_group_barrier(0x8, 2, 0);
          __builtin_amdgcn_sched_group_barrier(0x100, 1, 0);
        }
        __builtin_amdgcn_sched_group_barrier(0x8, 2, 0);
      } else {
#pragma unroll
        for (int i = 0; i < 8; i++) {
          __builtin_amdgcn_sched_group_barrier(0x8, 3, 0);
          __builtin_amdgcn_sched_group_barrier(0x100, 2, 0);
        }
        __builtin_amdgcn_sched_group_barrier(0x8, 2, 0);
      }
    }
#pragma unroll
    for (int bb = 0; bb < 8; bb++) {
      __builtin_amdgcn_sched_barrier(0);
      if (bb == 1) {
        LAS char* yb = sm + L_STG + wave * Y1_B + n16 * 32 + g4 * 8;
#pragma unroll
        for (int t = 0; t < 4; t++) {
          v2u y2 = {umin(pk2(acc[t][0], acc[t][1]), MAXV), umin(pk2(acc[t][2], acc[t][3]), MAXV)};
          *(LAS v2u*)(yb + t * 512) = y2;
        }
      }
      if (bb == 2) lds_signal_nowait(sm, C_Y1);
      if (bb == 6) {
        seen = lds_peek(sm, C_OK);
        u4[0] = *(const LAS v4u*)(ud + 2048);
        u4[1] = *(const LAS v4u*)(ud + 3072);
      }
      if (bb == Y_POST) {
        if (wave == 0 && pass == 0) (void)gate_post(P, G_Y, true, inc);
        __builtin_amdgcn_sched_barrier(0);
      }
      if (bb < 6) {
#pragma unroll
        for (int a = 0; a < 4; a++) {
          A2[(bb + 2) % 3][a] = *(const LAS v4u*)(pa + a * 512 + (bb + 2) * 64);
          B2[(bb + 2) % 3][a] = *(const LAS v4u*)(pb + (4 + a) * 512 + (5 - bb) * 64);
        }
      }
#pragma unroll
      for (int c = 0; c < 4; c++)
#pragma unroll
        for (int t = c; t < 4; t++) acc[4 + t] = mfma16(A2[bb % 3][t - c], B2[bb % 3][c], acc[4 + t]);
      if (bb < 6) {
#pragma unroll
        for (int i = 0; i < 8; i++) {
          __builtin_amdgcn_sched_group_barrier(0x8, 1, 0);
          __builtin_amdgcn_sched_group_barrier(0x100, 1, 0);
        }
        __builtin_amdgcn_sched_group_barrier(0x8, 2, 0);
      }
    }
    __builtin_amdgcn_sched_barrier(0);
    if (__builtin_expect(!checked_redo(sm, seen), 1)) break;
    NOTE(26, 7);
#pragma unroll
    for (int a = 0; a < 8; a++) A[0][a] = *(const LAS v4u*)(pa + a * 512);
  }
  if (Y_POST == 8 && wave == 0) (void)gate_post(P, G_Y, true, inc);
  {
    LAS char* yb = sm + L_H + wave * (HC_B + 64) + n16 * 32 + g4 * 8;
#pragma unroll
    for (int t = 0; t < 4; t++) {
      v2u y2 = {umin(pk2(acc[4 + t][0], acc[4 + t][1]), MAXV), umin(pk2(acc[4 + t][2], acc[4 + t][3]), MAXV)};
      *(LAS v2u*)(yb + t * 512) = y2;
    }
  }
  lds_signal_nowait(sm, C_Y);
  const LAS char* yr = sm + L_H + gch * (HC_B + 64) + (tid >> 2) * 16;
  STAMP(7);
  lds_wait(sm, C_Y, 4);
  const v4u y0 = *(const LAS v4u*)yr, y1 = *(const LAS v4u*)(yr + 1024);
  __builtin_amdgcn_raw_buffer_store_b128(encode_y(y0, u4[0]), P.rws, goff, 128 * TILE_B, AUX_SC1);
  __builtin_amdgcn_raw_buffer_store_b128(encode_y(y1, u4[1]), P.rws, goff, 192 * TILE_B, AUX_SC1);
  STAMP(8);
}

struct EarlyX { v4u xa[9], wb[9]; };
struct Early {
  v4u sk;
  u32 dd;
  u64 epoch;
  u32 cx;
};

__device__ __forceinline__ void tile_wave(const Ctx& P, const Early& E, const bool MOE) {
  LAS char* sm = P.sm;
  const int tid = P.tid, lane = P.lane, wave = P.wave, C = P.C;
  const bool act = tid < 288;
  const int j4 = min(tid, 287) * 4;
  const int row0 = 8 * C * (D * 2);
  const v4u sk = E.sk;
  u32 kk[11], vv[8];
  const LAS char* kv = sm + L_KV + wave * 5120 + lane * 4;
  u32 uinc;
  lds_wait<1>(sm, C_TAG, 1);
  asm volatile("ds_read_b32 %0, %1\n\ts_waitcnt lgkmcnt(0)" : "=v"(uinc) : "v"((u32)(size_t)(sm + L_REC)) : "memory");
  uinc = gate_inc(uinc);
  int guo = lane < NREP ? gate_off(G_U, lane, P.b & 15) : OOB;
  int so = WS_U + C * TILE_B + wave * 2048 + lane * 16;
  asm volatile("s_waitcnt vmcnt(2)" : "+v"(uinc), "+v"(guo), "+v"(so) :: "memory");
  STAMP(22);
  {
    v2u k01, k23, k45, k67, k89;
    asm volatile("ds_read2st64_b32 %0, %6 offset0:1 offset1:2\n\tds_read2st64_b32 %1, %6 offset0:3 offset1:4\n\t"
                 "ds_read2st64_b32 %2, %6 offset0:5 offset1:6\n\tds_read2st64_b32 %3, %6 offset0:7 offset1:8\n\t"
                 "ds_read2st64_b32 %4, %6 offset0:9 offset1:10\n\tds_read_b32 %5, %6 offset:2816\n\ts_waitcnt lgkmcnt(0)"
                 : "=&v"(k01), "=&v"(k23), "=&v"(k45), "=&v"(k67), "=&v"(k89), "=&v"(kk[10]) : "v"((u32)(size_t)kv) : "memory");
    kk[0] = k01.x; kk[1] = k01.y; kk[2] = k23.x; kk[3] = k23.y; kk[4] = k45.x; kk[5] = k45.y;
    kk[6] = k67.x; kk[7] = k67.y; kk[8] = k89.x; kk[9] = k89.y;
  }
  u32 qq[8];
  u32 gold;
  bool poster;
  u32 up[2][4];
  {
    f2 w[4], kf[11], ks[8];
    w[0] = (f2){lo16(sk.x), lo16(sk.z)}; w[1] = (f2){hi16(sk.x), hi16(sk.z)};
    w[2] = (f2){lo16(sk.y), lo16(sk.w)}; w[3] = (f2){hi16(sk.y), hi16(sk.w)};
#pragma unroll
    for (int i = 0; i < 11; i++) kf[i] = (f2){lo16(kk[i]), hi16(kk[i])};
#pragma unroll
    for (int t = 0; t < 8; t++) {
      ks[t] = __builtin_elementwise_fma(kf[t + 3], w[0], __builtin_elementwise_fma(kf[t + 2], w[1], __builtin_elementwise_fma(kf[t + 1], w[2], kf[t] * w[3])));
      asm volatile("" : "+v"(ks[t]));
    }
    __builtin_amdgcn_sched_barrier(0);
    v2u v01, v23, v45, v67;
    asm volatile("s_waitcnt vmcnt(0)\n\t"
                 "ds_read2st64_b32 %0, %4 offset0:12 offset1:13\n\tds_read2st64_b32 %1, %4 offset0:14 offset1:15\n\t"
                 "ds_read2st64_b32 %2, %4 offset0:16 offset1:17\n\tds_read2st64_b32 %3, %4 offset0:18 offset1:19\n\ts_waitcnt lgkmcnt(0)"
                 : "=&v"(v01), "=&v"(v23), "=&v"(v45), "=&v"(v67) : "v"((u32)(size_t)kv) : "memory");
    vv[0] = v01.x; vv[1] = v01.y; vv[2] = v23.x; vv[3] = v23.y; vv[4] = v45.x; vv[5] = v45.y; vv[6] = v67.x; vv[7] = v67.y;
    STAMP(12);
#pragma unroll
    for (int t = 0; t < 8; t++) ks[t] *= (f2){lo16(vv[t]), hi16(vv[t])};
#pragma unroll
    for (int i = 0; i < 4; i++) {
      up[0][i] = umin(pk2(ks[7 - 2 * i].x, ks[6 - 2 * i].x), MAXV);
      up[1][i] = umin(pk2(ks[7 - 2 * i].y, ks[6 - 2 * i].y), MAXV);
    }
    STAMP(1);
    LAS char* stg = sm + L_STG + wave * 2048;
    v4u p0 = {up[0][0], up[0][1], up[0][2], up[0][3]}, p1 = {up[1][0], up[1][1], up[1][2], up[1][3]};
    if (act) { *(LAS v4u*)(stg + lane * 32) = p0; *(LAS v4u*)(stg + lane * 32 + 16) = p1; }
    asm volatile("" ::: "memory");
    p0 = *(const LAS v4u*)(stg + lane * 16);
    p1 = *(const LAS v4u*)(stg + (wave < 4 ? 1024 : 0) + lane * 16);
    asm volatile("" : "+v"(p0), "+v"(p1));
    STAMP(13);
    __builtin_amdgcn_raw_buffer_store_b128(p0, P.rws, so, 0, AUX_SC1);
    if (wave < 4) __builtin_amdgcn_raw_buffer_store_b128(p1, P.rws, so + 1024, 0, AUX_SC1);
  }
  __builtin_amdgcn_sched_barrier(0);
  poster = lds_arrive(sm, C_VER, 4);
  if (poster) gold = gate_add(P, guo, (int)uinc);
  STAMP(2);
  if (COPY_FIRST && P.is_conv && wave < 4) {
    lds_wait<1>(sm, C_H0, 2);
    hcopy_rest<3>(P, 3 * wave);
    STAMP(17);
  }
  lds_wait<1>(sm, C_STAT, 1);
#pragma unroll
  for (int i = 0; i < 8; i++) qq[i] = *(const LAS u32*)(sm + xrow_off(i) + j4);
  lds_signal(sm, C_QQ);
  const u32 dd = E.dd;
  float xn[2][8], c0[2][8];
  auto prepare = [&]()
  {
    const float d0 = lo16(dd), d1 = hi16(dd);
#pragma unroll
    for (int s2 = 0; s2 < 4; s2++) {
      const f4 st = *(const LAS f4*)(sm + L_STAT + s2 * 16);
#pragma unroll
      for (int e = 0; e < 2; e++) {
        const int s = 2 * s2 + e;
        const float xa = lo16(qq[s]), xb = hi16(qq[s]);
        const float na = fmaf(xa, st[2 * e], st[2 * e + 1]), nb = fmaf(xb, st[2 * e], st[2 * e + 1]);
        const u32 pa = up[0][(7 - s) >> 1], pb = up[1][(7 - s) >> 1];
        const float ua = ((7 - s) & 1) ? hi16(pa) : lo16(pa), ub = ((7 - s) & 1) ? hi16(pb) : lo16(pb);
        xn[0][s] = na; xn[1][s] = nb;
        c0[0][s] = fmaf(na, fmaf(ua, d0, 1.0f), xa);
        c0[1][s] = fmaf(nb, fmaf(ub, d1, 1.0f), xb);
        asm volatile("" : "+v"(xn[0][s]), "+v"(xn[1][s]), "+v"(c0[0][s]), "+v"(c0[1][s]));
      }
    }
  };
  prepare();
  if (poster) gate_confirm(P, G_U, gold, uinc);
  if (!(P.is_conv && wave < 4)) {
    asm volatile("s_waitcnt vmcnt(0)" ::: "memory");
    lds_signal(sm, C_DRAIN);
  }
  STAMP(14);
  if (MOE) {
    ffn_gate_up(P, []() {});
    if (wave < 2) { Down dn; down_fetch(P, dn, wave); down_finish(P, dn, wave); }
  } else {
   if (P.is_conv) {
    if (wave < 4) {
      conv_phase(P, uinc);
    } else {
      lds_wait<1>(sm, C_QQ, 5);
      lds_wait<1>(sm, C_GU, 1);
      __builtin_amdgcn_s_sleep(3 + U_DELAY);
      if (SPLIT_U) gate_wait(P, G_U, (lane & 4) ? 16u : 0u, uinc);
      v4u g[8];
      gather_half(P, 128, g);
#pragma unroll
      for (int it = 0; it < 8; it++) *(LAS v4u*)(sm + L_U + (lane & 3) * UL_B + (32 + 128 + it * 16 + (lane >> 2)) * 16) = g[it];
      lds_signal(sm, C_U2);
      STAMP(5);
      gate_wait(P, G_V, 16u, uinc, SPIN_CAP);
      lds_signal(sm, C_TRUST);
      STAMP(4);
      verify_half(P, 128);
      STAMP(6);
      lds_wait<2>(sm, C_Y1, 4);
      if (checked_redo(sm, 0u)) lds_wait<1>(sm, C_Y1, 8);
      __builtin_amdgcn_s_setprio(3);
      publish_y(P, sm + L_STG, Y1_B, 0, lane, 32, 4);
      if (SPLIT_Y && lds_arrive(sm, C_REDO, 1)) (void)gate_post(P, G_Y1, true, uinc);
    }
  } else {
    STAMP(5);
   }
  }
  lds_wait<1>(sm, C_MASK, 1);
  const u32 skip = __builtin_amdgcn_readfirstlane(*(const LAS u32*)(sm + L_REC + 4));
  const __amdgpu_buffer_rsrc_t ro = mkrsrc(P.out, S * D * 2);
  const int oo = act ? j4 : OOB;
  const int uoff = WS_U + C * TILE_B + j4 * 8;
  lds_wait<1>(sm, C_GY, 1);
  STAMP(9);
  v4u y0, y1;
  _Pragma("nounroll") for (u32 spin = 0; spin < SPIN_CAP; spin++) {
    asm volatile("" ::: "memory");
    y0 = __builtin_amdgcn_raw_buffer_load_b128(P.rws, uoff, 0, AUX_SC1);
    y1 = __builtin_amdgcn_raw_buffer_load_b128(P.rws, uoff + 16, 0, AUX_SC1);
    bool ok = true;
#pragma unroll
    for (int e = 0; e < 4; e++) ok = ok & (y0[e] != up[0][e]) & (y1[e] != up[1][e]);
    if (__builtin_expect(__all(ok || !act), 1)) break;
    NOTE(25, spin + 1);
    __builtin_amdgcn_s_sleep(4);
  }
  STAMP(16);
#pragma unroll
  for (int e = 0; e < 4; e++) {
    const u32 a = y0[e] == SAME ? up[0][e] : y0[e], c = y1[e] == SAME ? up[1][e] : y1[e];
    const u32 o0 = pk2(fmaf(xn[0][2 * e], lo16(a), c0[0][2 * e]), fmaf(xn[1][2 * e], lo16(c), c0[1][2 * e]));
    const u32 o1 = pk2(fmaf(xn[0][2 * e + 1], hi16(a), c0[0][2 * e + 1]), fmaf(xn[1][2 * e + 1], hi16(c), c0[1][2 * e + 1]));
    const int so = row0 + (e >> 1) * 4 * (D * 2);
    if (!((skip >> (2 * e)) & 1)) __builtin_amdgcn_raw_buffer_store_b32(o0, ro, oo + ((2 * e) & 3) * (D * 2), so, OUT_AUX);
    if (!((skip >> (2 * e + 1)) & 1)) __builtin_amdgcn_raw_buffer_store_b32(o1, ro, oo + ((2 * e + 1) & 3) * (D * 2), so, OUT_AUX);
  }
}

__device__ __forceinline__ void route(const Ctx& P, u32 tag, u64 epoch, const int ROLE) {
  LAS char* sm = P.sm;
  const int lane = P.lane, wave = P.wave;
  (void)wave;
  v4u g[16];
  if (ROLE == 2) __builtin_amdgcn_s_setprio(0);
  if (ROLE == 0) {
    gate_wait(P, G_U, (!SPLIT_U || (lane & 4) == 0) ? 16u : 0u, gate_inc(tag));
    lds_signal(sm, C_GU);
    STAMP(15);
    __builtin_amdgcn_s_setprio(0);
    post_u_complete(P, gate_inc(tag));
    STAMP(13);
    lds_wait<1>(sm, C_U, 4);
  } else {
    gate_wait(P, ROLE == 1 ? G_R : G_U, 16u, gate_inc(tag));
  }
  STAMP(3);
  _Pragma("nounroll") for (u32 spin = 0; spin < SPIN_CAP; spin++) {
    asm volatile("" ::: "memory");
#pragma unroll
    for (int i = 0; i < 16; i++)
      g[i] = __builtin_amdgcn_raw_buffer_load_b128(P.rws, WS_R + lane * 16 + (i & 3) * 1024, (i >> 2) * 4096, AUX_SC1);
    if (spin == 0) lds_signal_nowait(sm, C_PUB);
    u32 bad = 0;
#pragma unroll
    for (int i = 0; i < 16; i++) {
      bad = __builtin_amdgcn_bitop3_b32(bad, g[i].y, tag, 0xF6);
      bad = __builtin_amdgcn_bitop3_b32(bad, g[i].w, tag, 0xF6);
    }
    if (__builtin_expect(__all(bad == 0), 1)) break;
    NOTE(24, spin + 1);
    __builtin_amdgcn_s_sleep(4);
  }
  lds_signal_nowait(sm, C_PUB);
  STAMP(4);
  if (P.b == 0 && lane == 0) __hip_atomic_store((GAS u64*)(P.ws + WS_E), epoch, __ATOMIC_RELAXED, __HIP_MEMORY_SCOPE_AGENT);
  u32 k1 = umax(g[0].x, g[0].z), k2 = umin(g[0].x, g[0].z), k3 = 0;
#pragma unroll
  for (int i = 2; i < 32; i++) {
    const u32 x = (i & 1) ? g[i >> 1].z : g[i >> 1].x;
    k3 = umed3(k2, k3, x);
    k2 = umed3(k1, k2, x);
    k1 = umax(k1, x);
  }
  u32 tau = umax(k1, dppu<0xB1>(k1));
  tau = umax(tau, dppu<0x4E>(tau));
  tau = umax(tau, dppu<0x141>(tau));
  tau = umin(tau, dppu<0x140>(tau));
  tau = xrow_umin(tau);
  LAS u32* cand = (LAS u32*)(sm + L_CAND);
  const u64 m1 = __ballot(k1 >= tau), m2 = __ballot(k2 >= tau), m3 = __ballot(k3 >= tau);
  const u32 n1 = __builtin_popcountll(m1), n2 = n1 + __builtin_popcountll(m2), n = n2 + __builtin_popcountll(m3);
  bool slow = n > 64;
  if (__builtin_expect(!slow, 1)) {
    const u32 p1 = __builtin_amdgcn_mbcnt_hi((u32)(m1 >> 32), __builtin_amdgcn_mbcnt_lo((u32)m1, 0u));
    const u32 p2 = __builtin_amdgcn_mbcnt_hi((u32)(m2 >> 32), __builtin_amdgcn_mbcnt_lo((u32)m2, n1));
    const u32 p3 = __builtin_amdgcn_mbcnt_hi((u32)(m3 >> 32), __builtin_amdgcn_mbcnt_lo((u32)m3, n2));
    cand[k1 >= tau ? p1 : 64] = k1;
    cand[k2 >= tau ? p2 : 64] = k2;
    cand[k3 >= tau ? p3 : 64] = k3;
    asm volatile("" ::: "memory");
    const u32 mine = cand[lane];
    u32 rank = 0;
    _Pragma("nounroll") for (u32 jj = 0; jj < n; jj += 8) {
      const v4u c4 = *(const LAS v4u*)(cand + jj), d4 = *(const LAS v4u*)(cand + jj + 4);
      rank += (c4.x > mine) + (c4.y > mine) + (c4.z > mine) + (c4.w > mine);
      rank += (d4.x > mine) + (d4.y > mine) + (d4.z > mine) + (d4.w > mine);
    }
    const bool in = (u32)lane < n && rank < 8;
    if (in) ((LAS u32*)(sm + L_SEL))[rank] = mine;
    slow = __any(in && (u32)lane >= n2);
  }
  if (__builtin_expect(slow, 0)) {
    _Pragma("nounroll") for (int r = 0; r < 8; r++) {
      u32 mx = 0;
#pragma unroll
      for (int i = 0; i < 16; i++) mx = umax3(mx, g[i].x, g[i].z);
      mx = wave_umax(mx);
#pragma unroll
      for (int i = 0; i < 16; i++) { g[i].x = g[i].x == mx ? 0u : g[i].x; g[i].z = g[i].z == mx ? 0u : g[i].z; }
      if (lane == 0) ((LAS u32*)(sm + L_SEL))[r] = mx;
    }
  }
  lds_signal_nowait(sm, C_SEL);
  STAMP(5);
}
__device__ __forceinline__ void tile_mask(const Ctx& P) {
  LAS char* sm = P.sm;
  const int lane = P.lane;
  {
    const u32 tk = 2047u - (((LAS u32*)(sm + L_SEL))[lane & 7] & 2047u);
    u32 mk = (int)(tk >> 3) == P.C ? 1u << (tk & 7) : 0u;
    mk |= dppu<0xB1>(mk);
    mk |= dppu<0x4E>(mk);
    mk |= dppu<0x141>(mk);
    if (lane == 0) *(LAS u32*)(sm + L_REC + 4) = mk;
  }
  lds_signal_nowait(sm, C_MASK);
}

__device__ __forceinline__ u32 load_msd(const Ctx& P, int tok, u32 tag) {
  v2u g;
  _Pragma("nounroll") for (u32 spin = 0; spin < SPIN_CAP; spin++) {
    asm volatile("" ::: "memory");
    g = __builtin_amdgcn_raw_buffer_load_b64(P.rws, WS_M + tok * 8, 0, AUX_SC1);
    if (__builtin_expect(__all(g.y == tag), 1)) break;
    __builtin_amdgcn_s_sleep(4);
  }
  return g.x;
}

__device__ __forceinline__ void routed_rows(const Ctx& P, GateUp& gu) {
  LAS char* sm = P.sm;
  const int lane = P.lane, wave = P.wave, C = P.C, cg = P.cg;
  (void)wave; (void)C; (void)cg;
  const __amdgpu_buffer_rsrc_t rq = mkrsrc(P.q, S * D * 2);
  {
    const int cw = wave - 6;
    lds_wait(sm, C_VER, 5);
    const u32 tag = *(const LAS u32*)(sm + L_REC);
    const v4u s4 = *(const LAS v4u*)(sm + L_SEL + cw * 16);
    const int n16 = lane & 15, g4 = lane >> 4;
    const u32 ks = (g4 & 2) ? ((g4 & 1) ? s4.w : s4.z) : ((g4 & 1) ? s4.y : s4.x);
    const int mtok = 2047 - (int)(ks & 2047u);
    const int xo = mtok * (D * 2) + n16 * 16;
    v4u rr[5];
#pragma unroll
    for (int i = 0; i < 5; i++) rr[i] = __builtin_amdgcn_raw_buffer_load_b128(rq, (i < 4 || n16 < 8) ? xo + i * 256 : OOB, 0, 0);
    v2u mg = __builtin_amdgcn_raw_buffer_load_b64(P.rws, WS_M + mtok * 8, 0, AUX_SC1);
    __builtin_amdgcn_sched_barrier(0);
    lds_signal(sm, C_W1);
    __builtin_amdgcn_sched_barrier(0);
    u32 msd = mg.x;
    if (__builtin_expect(!__all(mg.y == tag), 0)) msd = load_msd(P, mtok, tag);
    STAMP(15);
    const float rs = __builtin_amdgcn_rcpf(lo16(msd)), nm = -hi16(msd) * rs;
    LAS char* dst = sm + L_XS + (cw * 4 + g4) * XS_STRIDE + n16 * 16;
#pragma unroll
    for (int i = 0; i < 5; i++) {
      v4u a;
#pragma unroll
      for (int e = 0; e < 4; e++) a[e] = pk2(fmaf(lo16(rr[i][e]), rs, nm), fmaf(hi16(rr[i][e]), rs, nm));
      if (i < 4 || n16 < 8) *(LAS v4u*)(dst + i * 256) = a;
    }
    lds_signal_nowait(sm, C_XS);
    if (wave == 7) tile_mask(P);
    STAMP(6);
    if (W6_DIRECT && wave == 6) gu.mma(P, 1);
    __builtin_amdgcn_s_setprio(0);
    Down dn;
    down_fetch(P, dn, wave - 4);
    down_finish(P, dn, wave - 4);
  }
}

__device__ __forceinline__ void aux_wave(const Ctx& P, const int ROLE) {
  LAS char* sm = P.sm;
  const int lane = P.lane, wave = P.wave, C = P.C, cg = P.cg;
  (void)wave; (void)C; (void)cg;
  u32 tag;
    if (CODE_PF && wave == 6) asm volatile("s_waitcnt vmcnt(0)" ::: "v255");
    if (ROLE != 0 && wave == ((W5_DIRECT && ROLE == 1) ? 6 : 5)) {
      __builtin_amdgcn_s_setprio(3);
      lds_wait<1>(sm, C_TAG, 1);
      if (SLOW_FIX && ROLE == 2) {
        const int g = P.b & 7, idx = (P.b - NCONV - NMOE) >> 3, sl = 9 * g + ((3 - g) & 7);
        lds_wait<1>(sm, C_VER, 3);
        if (idx < 3) {
          touch<(TOUCH2 & 1) != 0>(P.wg, lane * (F * 2), idx * 64 * (F * 2) + sl * 128);
          touch<(TOUCH2 & 1) != 0>(P.wu, lane * (F * 2), idx * 64 * (F * 2) + sl * 128);
        } else if (g == 3) {
#pragma unroll
          for (int i = 0; i < 3; i++) touch<(TOUCH2 & 1) != 0>(idx == 3 ? P.wg : P.wu, lane * (F * 2), i * 64 * (F * 2) + 35 * 128);
        }
      }
      post_u_complete(P, gate_inc(*(const LAS u32*)(sm + L_REC)));
      __builtin_amdgcn_s_setprio(0);
      STAMP(13);
    }
    if (ROLE == 2 && HELPERS && wave == 6) {
      lds_wait<1>(sm, C_VER, 5);
      const int idx = (P.b - NCONV - NMOE) >> 3;
#pragma unroll
      for (int i = 0; i < 5; i++) {
        touch_w1<(TOUCH2 & 1) != 0>(P, 0, i * 5 + idx);
        touch_w1<(TOUCH2 & 1) != 0>(P, 1, i * 5 + idx);
        touch_wait<SP_DEPTH, (TOUCH2 & 1) != 0>();
      }
      touch_wait<0, false>();
      STAMP(5);
    }
    if (ROLE == 0) {
      __builtin_amdgcn_s_sleep(H_DELAY);
      hcopy_first(P, 2 * (wave - 5));
      lds_wait(sm, C_H0, 2);
      hcopy_rest<2>(P, 12 + 2 * (wave - 5));
      STAMP(3);
      if (wave == 5) {
        lds_wait<1>(sm, C_U, 4);
        verify_half(P, 0);
        STAMP(6);
        lds_wait<2>(sm, C_Y1, 4);
        if (checked_redo(sm, 0u)) lds_wait<1>(sm, C_Y1, 8);
        __builtin_amdgcn_s_setprio(3);
        publish_y(P, sm + L_STG + 256, Y1_B, 16, lane, 32, 4);
        if (SPLIT_Y && lds_arrive(sm, C_REDO, 1)) (void)gate_post(P, G_Y1, true, gate_inc(*(const LAS u32*)(sm + L_REC)));
      } else if (HELPERS) {
        lds_wait<1>(sm, LATE_MODE ? C_U : C_U2, LATE_MODE ? 4 : 1);
#pragma unroll
        for (int i = 0; i < 4; i++) {
          const int u = 50 + 36 * PF_CE + i * 18 + (P.b >> 3);
          if (50 + 36 * PF_CE + i * 18 < 2 * (((W5_DIRECT ? 480 : D) - PF_R0 + 6) / 7)) touch_w1<(TOUCH2 & 4) != 0>(P, u & 1, u >> 1);
        }
#pragma unroll
        for (int i = 0; i < 5; i++) { touch_wait<LT_DEPTH, (TOUCH2 & 4) != 0>(); touch_w2<(TOUCH2 & 4) != 0>(P, i * 18 + (P.b >> 3)); }
        touch_wait<0, false>();
        STAMP(6);
      }
    }
    if (wave == 5) {
      if (ROLE == 1) {
        Down dn;
        ffn_gate_up(P, [&]() { if (W5_EARLY) lds_wait<1>(sm, C_W1, 2); else lds_wait<1>(sm, C_H0, 6); dn.load(P, 4); });
        dn.turn();
        STAMP(19);
        down_finish(P, dn, 4);
        gate_wait(P, (SPLIT_Y && !(P.b & 4)) ? G_Y1 : G_Y, 9u, gate_inc(*(const LAS u32*)(sm + L_REC)));
        lds_signal(sm, C_GY);
      }
      return;
    }
    lds_wait<1>(sm, C_TAG, 1);
    tag = *(const LAS u32*)(sm + L_REC);
    if (ROLE == 1) { __builtin_amdgcn_s_setprio(3); lds_wait<1>(sm, C_PUB, 2); lds_wait(sm, C_SEL, 1); }
  if (ROLE != 1) {
    if (ROLE == 0 && Y_GATE) lds_wait<2>(sm, C_Y, 1);
    gate_wait(P, (SPLIT_Y && !(P.b & 4)) ? G_Y1 : G_Y, 9u, gate_inc(tag));
    lds_signal(sm, C_GY);
    STAMP(7);
  }
}

__device__ __forceinline__ void router_stats(const Ctx& P, Early& E, const EarlyX& X, const int ROLE) {
  LAS char* sm = P.sm;
  const int lane = P.lane, C = P.C;
  u32 tag;
    const int m = lane & 15, g4 = lane >> 4;
    u64 epoch = E.epoch;
    const v4u (&xa)[9] = X.xa;
    const v4u (&wb)[9] = X.wb;
    asm volatile("" : "+v"(epoch) :: "memory");
    epoch += 1;
    tag = (u32)epoch;
    if (lane == 0) *(LAS u32*)(sm + L_REC) = tag;
    lds_signal(sm, C_TAG);
    STAMP(1);
    f4 gx = {0.f, 0.f, 0.f, 0.f}, gw = {0.f, 0.f, 0.f, 0.f}, gs = {0.f, 0.f, 0.f, 0.f};
#pragma unroll
    for (int i = 0; i < 9; i++) gx = mfma16(xa[i], xa[i], gx);
    asm volatile("" :: "v"(xa[R_POST]) : "memory");
    __builtin_amdgcn_sched_barrier(0);
    const u32 gold = gate_post(P, G_R, true, gate_inc(tag));
    STAMP(12);
    __builtin_amdgcn_sched_barrier(0);
#pragma unroll
    for (int i = 0; i < 9; i++) { gw = mfma16(wb[i], xa[i], gw); gs = mfma16(wb[i], wb[i], gs); }
    float sxx = (m & 2) ? ((m & 1) ? gx[3] : gx[2]) : ((m & 1) ? gx[1] : gx[0]);
    sxx = __uint_as_float(__builtin_amdgcn_ds_bpermute((m + (m & 12) * 4) * 4, __float_as_uint(sxx)));
    float sxw = m < 8 ? gw[0] : gw[1], sx = gw[2];
    sxw += dppf<0x128>(sxw);
    sx += dppf<0x128>(sx);
    sxx += dppf<0x128>(sxx);
    const float sw = __uint_as_float(__builtin_amdgcn_readlane(__float_as_uint(gs[0] + gs[1]), 2));
    const float mean = sx * (1.0f / D);
    const float var = fmaxf(fmaf(-mean, mean, sxx * (1.0f / D)), 0.f);
    const float mu = rbf(mean);
    const float sd = rbf(__builtin_amdgcn_sqrtf(rbf(rbf(var) + 1e-5f)));
    const float rs = __builtin_amdgcn_rcpf(sd);
    const u32 r = pk2(0.f, fmaf(-mu, sw, sxw) * rs);
    u32 key = ((r ^ (((int)r >> 31) | 0x80000000u)) & 0xFFFF0000u) | (u32)(2047 - (8 * C + (m & 7)));
    u32 msd = __float_as_uint(mu) | (__float_as_uint(sd) >> 16);
    asm volatile("" : "+v"(key), "+v"(msd));
    const v2u rec = {m < 8 ? key : msd, tag};
    const int ro = lane < 16 ? (m < 8 ? WS_R : WS_M) + (8 * C + (m & 7)) * 8 : OOB;
    __builtin_amdgcn_raw_buffer_store_b64(rec, P.rws, ro, 0, AUX_SC1);
    STAMP(2);
    if (lane < 8) { f2 st = {rs, -mu * rs}; *(LAS f2*)(sm + L_STAT + m * 8) = st; }
    {
      const int t = m & 7, t3 = (t * 11) >> 5;
      LAS char* xd = sm + L_U + t3 * UL_B + 512 + (t - 3 * t3) * (D * 2) + (m >> 3) * 64 + g4 * 16;
#pragma unroll
      for (int i = 0; i < 9; i++) *(LAS v4u*)(xd + i * 128) = xa[i];
    }
    lds_signal(sm, C_STAT);
    E.epoch = epoch;
    E.cx = gold;
}

__device__ __forceinline__ void router_wave(const Ctx& P, const Early& E, const int ROLE) {
  LAS char* sm = P.sm;
  const int lane = P.lane, wave = P.wave, C = P.C, cg = P.cg;
  (void)wave; (void)C; (void)cg;
  u32 tag;
    const u64 epoch = E.epoch;
    tag = (u32)epoch;
    const u32 gold = E.cx;
    gate_confirm(P, G_R, gold, gate_inc(tag));
    route(P, tag, epoch, ROLE);
    if (ROLE != 1) tile_mask(P);
  if (ROLE == 0) {
    const int pi = lane & 7, ti = 4 * (pi & 1) + (pi & 2) + (pi >> 2);
    const u32 kx = ((LAS u32*)(sm + L_SEL))[ti];
    const int tok = 2047 - (int)(kx & 2047u);
    const v2u x2 = *(const GAS v2u*)((const GAS char*)P.q + (size_t)tok * (D * 2) + cg * 8);
    const v2u d2 = *(const GAS v2u*)((const GAS char*)P.dsk + cg * 8);
    const u32 msd = load_msd(P, tok, tag);
    const float rs = __builtin_amdgcn_rcpf(lo16(msd)), nm = -hi16(msd) * rs;
    const u32 rb = kx & 0xFFFF0000u;
    const float r = __uint_as_float((rb & 0x80000000u) ? (rb ^ 0x80000000u) : (~rb & 0xFFFF0000u));
    const float rsig = rbf(__builtin_amdgcn_rcpf(1.0f + __expf(-r)));
    STAMP(6);
    gate_wait(P, G_P, lane < 8 ? 5u : 4u, gate_inc(tag));
    STAMP(7);
    v4u g[9];
    const u32 tag12 = tag & 0xFFFu;
    const int po = WS_P + (lane >> 3) * PART_B + cg * 128 + pi * 16;
#pragma unroll
    for (int i = 0; i < 9; i++) g[i] = __builtin_amdgcn_raw_buffer_load_b128(P.rws, po, 8 * i * PART_B, AUX_SC1);
    __builtin_amdgcn_sched_barrier(0);
    lds_wait<1>(sm, C_Y, 4);
    float o[4];
#pragma unroll
    for (int e = 0; e < 4; e++) {
      const float y = lo16(*(const LAS u16*)(sm + (tok < 1024 ? L_STG + e * Y1_B : L_H + e * (HC_B + 64) - 2048) + tok * 2));
      const float u = lo16(*(const LAS u16*)(sm + L_U + e * UL_B + (32 + (tok >> 3)) * 16 + (7 - (tok & 7)) * 2));
      const float x = (e & 1) ? hi16(x2[e >> 1]) : lo16(x2[e >> 1]);
      const float dk = (e & 1) ? hi16(d2[e >> 1]) : lo16(d2[e >> 1]);
      const float n = fmaf(x, rs, nm);
      o[e] = fmaf(n, fmaf(u, dk, y), x) + n;
      asm volatile("" : "+v"(o[e]));
    }
    __builtin_amdgcn_sched_barrier(0);
    _Pragma("nounroll") for (u32 spin = 0; spin < SPIN_CAP; spin++) {
      u32 bad = 0;
#pragma unroll
      for (int i = 0; i < 9; i++) {
        g[i] ^= tag12;
        bad |= g[i].x | g[i].y;
        bad |= g[i].z | g[i].w;
      }
      if (__builtin_expect(__all((bad & 0xFFFu) == 0), 1)) break;
      NOTE(25, spin + 1);
      __builtin_amdgcn_s_sleep(4);
      asm volatile("" ::: "memory");
#pragma unroll
      for (int i = 0; i < 9; i++) g[i] = __builtin_amdgcn_raw_buffer_load_b128(P.rws, po, 8 * i * PART_B, AUX_SC1);
    }
    STAMP(8);
    f4 s4 = __builtin_bit_cast(f4, g[0]);
#pragma unroll
    for (int i = 1; i < 9; i++) s4 += __builtin_bit_cast(f4, g[i]);
#pragma unroll
    for (int e = 0; e < 4; e++) {
      float sv = s4[e];
      sv += dppf<0x128>(sv);
      o[e] = fmaf(rbf(xrow_sum(sv)), rsig, o[e]);
    }
    if (lane < 8) {
      v2u o2 = {pk2(o[0], o[1]), pk2(o[2], o[3])};
      __builtin_amdgcn_raw_buffer_store_b64(o2, mkrsrc(P.out, S * D * 2), tok * (D * 2) + cg * 8, 0, OUT_AUX);
    }
  }
}

__global__ __launch_bounds__(NTHR, 2) void fused_kernel(
    const u16* __restrict__ q, const u16* __restrict__ k, const u16* __restrict__ v, const u16* __restrict__ shk,
    const u16* __restrict__ hk, const u16* __restrict__ dsk, const u16* __restrict__ wr, const u16* __restrict__ wg,
    const u16* __restrict__ wu, const u16* __restrict__ wd, u16* __restrict__ out, char* ws) {
  extern __shared__ __attribute__((aligned(16))) char smem_[];
  Ctx P;
  P.q = q; P.k = k; P.v = v; P.shk = shk; P.hk = hk; P.dsk = dsk; P.wr = wr; P.wg = wg; P.wu = wu; P.wd = wd;
  P.out = out; P.ws = ws;
  u64 pc;
  asm volatile("s_getpc_b64 %0" : "=s"(pc));
  P.code = (const char*)(pc & ~(u64)127);
  P.sm = (LAS char*)smem_;
  P.tid = threadIdx.x; P.lane = P.tid & 63;
  P.wave = __builtin_amdgcn_readfirstlane(P.tid >> 6);
  const int b = blockIdx.x;
  P.b = b;
  P.C = (b & 7) * 32 + (b >> 3);
  P.is_conv = b < NCONV;
  const bool is_moe = !P.is_conv && b < NCONV + NMOE;
  P.cg = (b & 7) * 18 + (b >> 3);
  P.mw = (b & 7) * 9 + ((b - NCONV) >> 3);
  P.rws = mkrsrc(ws, WS_END);
  const int lane = P.lane, wave = P.wave;
  (void)lane;
  STAMP(0); STAMPC(30);
  Early E;
  if (wave == 6) {
    constexpr int NL = (CODE_PF * 8 + 31) / 32;
    if (CODE_PF)
      asm volatile("global_load_dword v255, %0, off" :: "v"(P.code + min(((b >> 3) * NL + min(lane, NL - 1)) * 128, CODE_PF * 1024 - 128)) : "v255");
    const v4u z = {0u, 0u, 0u, 0u};
    *(LAS v4u*)(P.sm + L_U + (lane >> 4) * UL_B + (lane & 15) * 16) = z;
    *(LAS v4u*)(P.sm + L_U + (lane >> 4) * UL_B + 256 + (lane & 15) * 16) = z;
    ((LAS u32*)(P.sm + L_MISC))[lane] = 0u;
    ((LAS u32*)(P.sm + L_MISC))[64 + lane] = 0u;
    asm volatile("s_waitcnt lgkmcnt(0)" ::: "memory");
  }
  const int row0 = 8 * P.C * (D * 2);
  const bool col = wave < 4 || (lane & 8) == 0;
  int vo = (lane >> 4) * (D * 2) + wave * 256 + (lane & 15) * 16;
  int vo0 = (col && lane >= 16) ? row0 - 4 * (D * 2) + vo : OOB;
  int so4 = min(P.tid, 287) * 16;
  vo = col ? vo : OOB;
  int xo = (lane & 7) * (D * 2) + (lane & 8) * 8 + (lane >> 4) * 16;
  if (BAR_LATE) asm volatile("" : "+v"(vo), "+v"(vo0), "+v"(so4), "+v"(xo) :: "memory");
  else asm volatile("s_barrier" : "+v"(vo), "+v"(vo0), "+v"(so4), "+v"(xo) :: "memory");
  asm volatile("" :: "s"(q), "s"(k), "s"(v), "s"(shk), "s"(hk), "s"(dsk), "s"(wr), "s"(wg), "s"(wu), "s"(wd), "s"(out), "s"(ws));
  STAMP(11);
  EarlyX X;
  if (P.wave == 7) {
    __builtin_amdgcn_s_setprio(3);
    const int m = lane & 15, g4 = lane >> 4;
    E.epoch = __hip_atomic_load((GAS u64*)(P.ws + WS_E), __ATOMIC_RELAXED, __HIP_MEMORY_SCOPE_AGENT);
    if (ROUTER_DELAY && !(b & 4)) __builtin_amdgcn_s_sleep(ROUTER_DELAY);
    __builtin_amdgcn_sched_barrier(0);
    const __amdgpu_buffer_rsrc_t rq = mkrsrc(P.q, S * D * 2);
#pragma unroll
    for (int i = 0; i < 9; i++) X.xa[i] = __builtin_amdgcn_raw_buffer_load_b128(rq, xo + i * 128, row0, NT_Q);
    __builtin_amdgcn_sched_barrier(0);
    const size_t pw = (size_t)P.wr + m * 64, po = (size_t)kOnes;
    const GAS char* wp = (const GAS char*)(m < 2 ? pw : po) + g4 * 16;
#pragma unroll
    for (int i = 0; i < 9; i++) X.wb[i] = *(const GAS v4u*)(wp + i * 128);
  }
  asm volatile("" : "+s"(P.wave));
  if (P.wave < 5) {
    const __amdgpu_buffer_rsrc_t rk = mkrsrc(P.k, S * D * 2), rv = mkrsrc(P.v, S * D * 2);
    if (TILE_DELAY) __builtin_amdgcn_s_sleep(TILE_DELAY);
    if (TILE_DELAY2 && (b & 4)) __builtin_amdgcn_s_sleep(TILE_DELAY2);
    E.sk = *(const GAS v4u*)((const GAS char*)P.shk + (u32)so4);
    E.dd = *(const GAS u32*)((const GAS char*)P.dsk + ((u32)so4 >> 2));
    __builtin_amdgcn_sched_barrier(0);
    LAS char* kv = P.sm + L_KV + wave * 5120;
    __builtin_amdgcn_raw_ptr_buffer_load_lds(rk, kv, 16, vo0, 0, 0, NT_KV);
    __builtin_amdgcn_raw_ptr_buffer_load_lds(rk, kv + 1024, 16, vo, row0, 0, NT_KV);
    __builtin_amdgcn_raw_ptr_buffer_load_lds(rk, kv + 2048, 16, vo, row0 + 4 * (D * 2), 0, NT_KV);
    __builtin_amdgcn_raw_ptr_buffer_load_lds(rv, kv + 3072, 16, vo, row0, 0, NT_KV);
    __builtin_amdgcn_raw_ptr_buffer_load_lds(rv, kv + 4096, 16, vo, row0 + 4 * (D * 2), 0, NT_KV);
  }
  asm volatile("" : "+s"(P.wave));
  if (P.wave == 5) {
    const v4u g4 = {GMAGIC, GMAGIC, GMAGIC, GMAGIC};
    __builtin_amdgcn_raw_buffer_store_b128(g4, P.rws, (lane < 8 * NGATE && (b >> 4) < NREP) ? gate_off(lane >> 3, b >> 4, b & 15) + (lane & 7) * 16 : OOB, 0, AUX_SC1);
  }
  if (BAR_LATE) asm volatile("s_barrier" ::: "memory");
  const int role = P.is_conv ? 0 : (is_moe ? 1 : 2);
  asm volatile("" : "+s"(P.wave));
  STAMP(10);
  if (P.wave == 7) router_stats(P, E, X, role);
  asm volatile("" : "+s"(P.wave));
  if (is_moe && P.wave >= 6) {
    GateUp gu;
    if (P.wave == 6) {
      if (W6_DIRECT) { __builtin_amdgcn_s_sleep(W6_DIRECT); gu.fetch(P, 1); }
      aux_wave(P, 1);
    } else {
      router_wave(P, E, 1);
    }
    asm volatile("" : "+s"(P.wave));
    routed_rows(P, gu);
  } else if (P.wave != 7) {
    if (P.wave >= 5) aux_wave(P, role);
    else tile_wave(P, E, is_moe);
  } else {
    router_wave(P, E, role);
  }
  FLUSH();
}

}

extern "C" void kernel_launch(void* const* d_in, const int* in_sizes, int n_in, void* d_out, int out_size, void* d_ws,
                              size_t ws_size, hipStream_t stream) {
  (void)in_sizes; (void)n_in; (void)out_size; (void)ws_size;
  (void)hipFuncSetAttribute((const void*)fused_kernel, hipFuncAttributeMaxDynamicSharedMemorySize, L_TOTAL);
  fused_kernel<<<NWG, NTHR, L_TOTAL, stream>>>(
      (const u16*)d_in[0], (const u16*)d_in[1], (const u16*)d_in[2], (const u16*)d_in[3], (const u16*)d_in[4],
      (const u16*)d_in[5], (const u16*)d_in[7], (const u16*)d_in[8], (const u16*)d_in[9], (const u16*)d_in[10],
      (u16*)d_out, (char*)d_ws + (WS_AT_END ? ((ws_size - WS_END) & ~(size_t)0xFFFF) : 0));
}
```
